```python
import torch

def _softmax(x, axis=-1):
    return torch.softmax(x, dim=axis)
def _gelu(x):
    return torch.nn.functional.gelu(x, approximate="tanh")
def _silu(x):
    return torch.nn.functional.silu(x)
def _sigmoid(x):
    return torch.sigmoid(x)
def _layernorm(x, axis=-1, eps=1e-5):
    m = x.mean(dim=axis, keepdim=True)
    v = x.var(dim=axis, keepdim=True, unbiased=False)
    return (x - m) / torch.sqrt(v + eps)
def _scatter_add(tgt, idx, src, axis=0):
    return tgt.clone().index_add_(axis, idx, src)
def _segment_sum(src, seg, n):
    out = torch.zeros((n,) + src.shape[1:], dtype=src.dtype)
    return out.index_add_(0, seg, src)
def _pad(x, pw):
    flat = tuple(v for pair in reversed(pw) for v in pair)
    return torch.nn.functional.pad(x, flat)

kernel_name = 'standalone__detection_seg_dino__vision'

def setup_inputs():

    _w = (lambda a: (lambda t: t.to(torch.bfloat16) if t.is_floating_point() else t)(torch.as_tensor(a)))
    H = W = (512 // 2) * 2
    C = 192
    Nq = 16
    sc = C ** -0.5
    return {
        "q": _w((lambda *s: torch.randn(*s, dtype=torch.bfloat16) * 0.2)(1, Nq, C)),
        "f0": _w((lambda *s: torch.randn(*s, dtype=torch.bfloat16) * 3.2)(1, H, W, C)),
        "f1": _w((lambda *s: torch.randn(*s, dtype=torch.bfloat16) * 3.2)(1, H // 2, W // 2, C)),
        "wq": _w((lambda *s: torch.randn(*s, dtype=torch.bfloat16) * 1.6)(C, C) * sc),
        "wk": _w((lambda *s: torch.randn(*s, dtype=torch.bfloat16) * 1.6)(C, C) * sc),
        "wv": _w((lambda *s: torch.randn(*s, dtype=torch.bfloat16) * 3.2)(C, C) * sc),
        "wo": _w((lambda *s: torch.randn(*s, dtype=torch.bfloat16) * 3.2)(C, C) * sc),
    }

def reference(q, f0, f1, wq, wk, wv, wo):
    B, H, W, C = f0.shape
    f1u = (lambda x, n, axis: torch.repeat_interleave(x, n, dim=axis))((lambda x, n, axis: torch.repeat_interleave(x, n, dim=axis))(f1, 2, axis=1), 2, axis=2)
    fused = f0 + f1u
    feats = _layernorm(fused.reshape(B, H * W, C), axis=-1)
    qn = _layernorm(q, axis=-1)
    Q = torch.einsum("bnc,cd->bnd", qn, wq)
    K = torch.einsum("bmc,cd->bmd", feats, wk)
    V = torch.einsum("bmc,cd->bmd", feats, wv)
    scale = C ** -0.5
    logits = torch.einsum("bnd,bmd->bnm", Q, K) * scale
    attn = _softmax(logits, axis=-1)
    ctx = torch.einsum("bnm,bmd->bnd", attn, V)
    return q + torch.einsum("bnd,dc->bnc", ctx, wo)
```

```cpp
#include <hip/hip_runtime.h>
#include <hip/hip_bf16.h>
#include <stdint.h>

typedef unsigned short u16;
using u32x2 = __attribute__((ext_vector_type(2))) unsigned;
using u32x4 = __attribute__((ext_vector_type(4))) unsigned;
using f32x2 = __attribute__((ext_vector_type(2))) float;
using f32x4 = __attribute__((ext_vector_type(4))) float;
using s16x8 = __attribute__((ext_vector_type(8))) short;
using bf16x2_t = __attribute__((ext_vector_type(2))) __bf16;
typedef short v4i16_t __attribute__((ext_vector_type(4)));
#define LAS __attribute__((address_space(3)))

constexpr int C = 192, NQ = 16, W0 = 512, W1 = 256, NWG = 256, NW = 8;
constexpr float EPS = 1e-5f;
constexpr float QSCALE = 0.07216878364870322f * 1.4426950408889634f;

constexpr int PART = 5120;
constexpr int SLOT = 3 * PART, NS = 10;
constexpr int LDS_QK = NS * SLOT;
constexpr int LDS_STAT = LDS_QK + 6144;
constexpr int LDS_CTL = LDS_STAT + NW * 256;
constexpr int LDS_SINK = LDS_CTL + 256;
constexpr int LDS_BYTES = LDS_SINK + 256;
constexpr int PSCR = 8 * SLOT;
constexpr int CT_FULL = 0, CT_REF = 4, CT_PA = 8, CT_PB = 12, CT_QR = 16, CT_PD = 20, CT_XD = 28, CT_TG = 32, CT_QK = 40, CT_ST = 44,
              CT_FREE = 64, CT_PRDY = 128, CT_PCON = 144, CT_MAX = 176, CT_ONE = 240;
constexpr int LDS_PK = LDS_QK, PK_B = 1664;
constexpr int LDS_LF = LDS_STAT + 1024;

constexpr int NDST = 192;
constexpr int MB_BYTES = NWG * NDST * 64;
constexpr int EP_OFF = MB_BYTES;
constexpr int FLAG_OFF = EP_OFF + 512;
constexpr int WVO_OFF = EP_OFF + 8192;
constexpr int QK_OFF = WVO_OFF + C * C * 2;
constexpr int QK_REC = 1280, QK_COPY = 18 * QK_REC;
constexpr int WS_BYTES = QK_OFF + 4 * QK_COPY;

#define NX 3
#define GIVES 0x101000004242ull
constexpr int NT = 32 + NX;
__host__ __device__ constexpr int gives(int wg) { return (int)((GIVES >> (wg < 54 ? 4 * (wg & 1) : 16 + 4 * (wg & 7))) & 15); }
struct Tabs { unsigned seq[112]; unsigned take[101]; };
constexpr Tabs make_tabs() {
  Tabs t{};
  for (int i = 0; i < 112; ++i) t.seq[i] = i + 1;
  int k = 0;
  for (int wg = 0; wg < 256; ++wg)
    for (int j = 0; j < gives(wg); ++j, ++k) t.take[k % 101] += (1u << 30) + ((unsigned)(j << 8 | wg) << (10 * (k / 101)));
  return t;
}
constexpr int given() { int k = 0; for (int wg = 0; wg < 256; ++wg) k += gives(wg); return k; }
constexpr int GIVEN = given();
static_assert(GIVEN <= 101 * NX && NX <= 3, "too many tiles sent elsewhere");
__device__ const Tabs tabs = make_tabs();
#define QKAT 6
#define WVN 4
#define DEPTH 6
#define GATE 450
#define CODE_BYTES 25856

__device__ __forceinline__ unsigned cvtpk(float lo, float hi) {
  bf16x2_t v = {(__bf16)lo, (__bf16)hi};
  return __builtin_bit_cast(unsigned, v);
}
__device__ __forceinline__ float bflo(unsigned x) { return __builtin_bit_cast(float, x << 16); }
__device__ __forceinline__ float bfhi(unsigned x) { return __builtin_bit_cast(float, x & 0xffff0000u); }
template <int CTRL> __device__ __forceinline__ float dpp(float x) {
  return __builtin_bit_cast(float, __builtin_amdgcn_mov_dpp(__builtin_bit_cast(int, x), CTRL, 0xf, 0xf, true));
}
constexpr int XOR8 = 0x128, ROR4 = 0x124;
__device__ __forceinline__ float row_align(float x) {
  const int v = __builtin_bit_cast(int, x);
  int r = __builtin_amdgcn_update_dpp(v, v, 0x12C, 0x2, 0xf, false);
  r = __builtin_amdgcn_update_dpp(r, v, 0x128, 0x4, 0xf, false);
  r = __builtin_amdgcn_update_dpp(r, v, 0x124, 0x8, 0xf, false);
  return __builtin_bit_cast(float, r);
}
__device__ __forceinline__ float xrow16_max(float x) {
  auto s = __builtin_amdgcn_permlane16_swap(__float_as_uint(x), __float_as_uint(x), false, false);
  x = fmaxf(__uint_as_float(s[0]), __uint_as_float(s[1]));
  auto t = __builtin_amdgcn_permlane32_swap(__float_as_uint(x), __float_as_uint(x), false, false);
  return fmaxf(__uint_as_float(t[0]), __uint_as_float(t[1]));
}
__device__ __forceinline__ float xrow16_sum(float x) {
  auto s = __builtin_amdgcn_permlane16_swap(__float_as_uint(x), __float_as_uint(x), false, false);
  x = __uint_as_float(s[0]) + __uint_as_float(s[1]);
  auto t = __builtin_amdgcn_permlane32_swap(__float_as_uint(x), __float_as_uint(x), false, false);
  return __uint_as_float(t[0]) + __uint_as_float(t[1]);
}
__device__ __forceinline__ float lo32(float x) {
  auto s = __builtin_amdgcn_permlane32_swap(__float_as_uint(x), __float_as_uint(x), false, false);
  return __uint_as_float(s[0]);
}
__device__ __forceinline__ f32x4 mfma(u32x4 a, u32x4 b, f32x4 c) {
  return __builtin_amdgcn_mfma_f32_16x16x32_bf16(__builtin_bit_cast(s16x8, a), __builtin_bit_cast(s16x8, b), c, 0, 0, 0);
}
__device__ __forceinline__ f32x4 mfma16(u32x2 a, u32x2 b, f32x4 c) {
  return __builtin_amdgcn_mfma_f32_16x16x16bf16_1k(__builtin_bit_cast(v4i16_t, a), __builtin_bit_cast(v4i16_t, b), c, 0, 0, 0);
}
__device__ __forceinline__ u32x2 vtr(LAS const char* p) {
  return __builtin_bit_cast(u32x2, __builtin_amdgcn_ds_read_tr16_b64_v4i16((LAS v4i16_t*)p));
}
__device__ __forceinline__ float sel4(f32x4 v, int r) {
  const float a = (r & 1) ? v[1] : v[0], b = (r & 1) ? v[3] : v[2];
  return (r & 2) ? b : a;
}
using RSRC = __amdgpu_buffer_rsrc_t;
__device__ __forceinline__ RSRC mkrs(const void* p, int bytes) { return __builtin_amdgcn_make_buffer_rsrc(const_cast<void*>(p), (short)0, bytes, 0x00020000); }
__device__ __forceinline__ u32x4 mkrs4(const void* p, unsigned bytes) {
  const unsigned long long b = (unsigned long long)p;
  return u32x4{(unsigned)b, (unsigned)(b >> 32) & 0xffffu, bytes, 0x00020000u};
}
template <int AUX> __device__ __forceinline__ u32x4 ldb128(RSRC r, unsigned vo, int so) { return __builtin_bit_cast(u32x4, __builtin_amdgcn_raw_buffer_load_b128(r, vo, so, AUX)); }
#define SB() __builtin_amdgcn_sched_barrier(0)
#define WAVE_SYNC() do { __builtin_amdgcn_fence(__ATOMIC_RELEASE, "wavefront"); __builtin_amdgcn_wave_barrier(); __builtin_amdgcn_fence(__ATOMIC_ACQUIRE, "wavefront"); } while (0)
#define RAW_BARRIER() asm volatile("s_waitcnt lgkmcnt(0)\n\ts_barrier" ::: "memory")
#define DMA_NT(m0_, vo_, rs_, so_) asm volatile("s_mov_b32 m0, %0\n\ts_nop 0\n\tbuffer_load_dwordx4 %1, %2, %3 offen nt lds" :: "s"(m0_), "v"(vo_), "s"(rs_), "s"(so_) : "memory")
#define DMA(m0_, vo_, rs_, so_) asm volatile("s_mov_b32 m0, %0\n\ts_nop 0\n\tbuffer_load_dwordx4 %1, %2, %3 offen lds" :: "s"(m0_), "v"(vo_), "s"(rs_), "s"(so_) : "memory")
#define DMA_TAG(m0_, vo_, rs_, so_) asm volatile("s_mov_b32 m0, %0\n\ts_mov_b64 exec, 1\n\tbuffer_load_dword %1, %2, %3 offen lds\n\ts_mov_b64 exec, -1" :: "s"(m0_), "v"(vo_), "s"(rs_), "s"(so_) : "memory")
template <int N> __device__ __forceinline__ void waitvm() { asm volatile("s_waitcnt vmcnt(%0)" :: "i"(N) : "memory"); }


__device__ __forceinline__ void lds_add(LAS char* p, int lane, unsigned v) {
  asm volatile("" ::: "memory");
  if (lane == 0) __hip_atomic_fetch_add((LAS unsigned*)p, v, __ATOMIC_RELAXED, __HIP_MEMORY_SCOPE_WORKGROUP);
  asm volatile("" ::: "memory");
}
__device__ __forceinline__ void lds_set(LAS char* p, int lane, unsigned v) {
  asm volatile("" ::: "memory");
  if (lane == 0) __hip_atomic_store((LAS unsigned*)p, v, __ATOMIC_RELAXED, __HIP_MEMORY_SCOPE_WORKGROUP);
  asm volatile("" ::: "memory");
}
__device__ __forceinline__ void lds_inc(LAS char* p) {
  asm volatile("s_mov_b64 exec, 1\n\tds_add_u32 %0, %1\n\ts_mov_b64 exec, -1" :: "v"(p), "v"(1u) : "memory");
}
__device__ __forceinline__ void lds_put(LAS char* p, unsigned v) {
  asm volatile("s_mov_b64 exec, 1\n\tds_write_b32 %0, %1\n\ts_mov_b64 exec, -1" :: "v"(p), "v"(v) : "memory");
}
__device__ __forceinline__ int lds_get(LAS char* p) {
  asm volatile("" ::: "memory");
  const unsigned v = __hip_atomic_load((LAS unsigned*)p, __ATOMIC_RELAXED, __HIP_MEMORY_SCOPE_WORKGROUP);
  asm volatile("" ::: "memory");
  return __builtin_amdgcn_readfirstlane(v);
}
__device__ __forceinline__ void lds_wait(LAS char* p, int target) {
#pragma unroll 1
  for (int spin = 0; spin < (1 << 22); ++spin) {
    if (lds_get(p) >= target) break;
    __builtin_amdgcn_s_sleep(1);
  }
}
__device__ __forceinline__ int lds_spin(LAS char* p, int target) {
  int r, cnt = 1 << 22;
  unsigned t;
  asm volatile("1:\n\tds_read_b32 %1, %3\n\ts_waitcnt lgkmcnt(0)\n\tv_readfirstlane_b32 %0, %1\n\ts_cmp_ge_i32 %0, %4\n\ts_cbranch_scc1 2f\n\t"
               "s_sub_u32 %2, %2, 1\n\ts_cbranch_scc0 1b\n2:"
               : "=&s"(r), "=&v"(t), "+s"(cnt) : "v"(p), "s"(target) : "memory", "scc");
  return r;
}
__device__ __forceinline__ u32x4 pack8(f32x4 a, f32x4 b) { return u32x4{cvtpk(a[0], a[1]), cvtpk(a[2], a[3]), cvtpk(b[0], b[1]), cvtpk(b[2], b[3])}; }
__device__ __forceinline__ unsigned tagf(float x, unsigned t) { return (__float_as_uint(x) & 0xfffff000u) | t; }

__device__ __forceinline__ void stb128(u32x4 v, RSRC r, unsigned o) { __builtin_amdgcn_raw_buffer_store_b128(v, r, o, 0, 16); }
__device__ __forceinline__ void stb64(u32x2 v, RSRC r, unsigned o) { __builtin_amdgcn_raw_buffer_store_b64(v, r, o, 0, 16); }
__device__ __forceinline__ void stb32(unsigned v, RSRC r, unsigned o) { __builtin_amdgcn_raw_buffer_store_b32(v, r, o, 0, 16); }
__device__ __forceinline__ unsigned qk_check(u32x4 x, unsigned tg) {
  const unsigned a = (x[0] ^ __builtin_amdgcn_alignbit(x[1], x[1], 11)) * 0x9e3779b1u + tg;
  const unsigned b = (x[2] + __builtin_amdgcn_alignbit(x[3], x[3], 19)) * 0x85ebca77u;
  return __builtin_amdgcn_alignbit(a, a, 15) ^ b ^ (x[1] + x[3]);
}
__device__ __forceinline__ unsigned add3(unsigned a, unsigned b, unsigned c) {
  return cvtpk((bflo(a) + bflo(b)) + bflo(c), (bfhi(a) + bfhi(b)) + bfhi(c));
}
__global__ __launch_bounds__(512, 2) void dino_main(const u16* q, const u16* f0, const u16* f1, const u16* wq, const u16* wk, const u16* wv,
                                                     const u16* wo, char* ws, u16* out) {
  __shared__ __attribute__((aligned(128))) char smem[LDS_BYTES];
  LAS char* lds = (LAS char*)smem;
  LAS char* ctl = lds + LDS_CTL;
  const unsigned lb = (unsigned)(unsigned long long)lds;
  const int tid = threadIdx.x, lane = tid & 63;
  const int w = __builtin_amdgcn_readfirstlane(tid >> 6);
  const int wg = blockIdx.x;
  const int n = lane & 15, g = lane >> 4;
  const int x = lane >> 3, e = lane & 7;
  const int tq = (lane >> 2) & 3, tp = lane & 3;
  const unsigned long long tstart = wall_clock64();
  if (w == 7 && wg >= 56 && wg < 72) {
    const u32x4 rc = mkrs4((const void*)(__builtin_amdgcn_s_getpc() & ~255ull), CODE_BYTES);
#pragma unroll 1
    for (int i = 0; i < CODE_BYTES; i += 4096)
      asm volatile("s_mov_b32 m0, %0\n\ts_nop 0\n\tbuffer_load_dword %1, %2, %3 offen lds" :: "s"(lb + LDS_SINK), "v"(lane * 64), "s"(rc), "s"(i) : "memory");
  }
  if (w == 3) *(LAS unsigned*)(ctl + 4 * lane) = lane >= CT_ONE / 4 ? 0x3f803f80u : lane >= CT_MAX / 4 ? 0xff800000u : 0u;
  SB();
#define ARGS_IN() asm volatile("s_waitcnt lgkmcnt(0)\n\ts_barrier" :: "s"(q), "s"(f0), "s"(f1), "s"(wq), "s"(wk), "s"(wv), "s"(wo), "s"(ws), "s"(out) : "memory")
#define rws mkrs(ws, WS_BYTES)
  unsigned long long tg;
  const int dst = (wg >> 4) * 16 + (wg & 7) * 2 + ((wg >> 3) & 1);
  unsigned qres = 0u;
  const int give = gives(wg), ntake = wg < 54 || (wg & 1) ? 0 : GIVEN / 101 + (((wg - 54) >> 1) < GIVEN % 101 ? 1 : 0);
#define SKIP(s_) ((s_) >= 21 + ntake && (s_) < 21 + NX + give)

  const bool pq = wg < 18 && w != 0;
  const bool pv = w == 7 && wg >= 18 && wg < 54;
  const int pj = wg / 6, pu = wg % 6;
  if (pq) {
    unsigned vo = w >= 4 ? (48 * (w - 4) + x) * 384 + ((e ^ ((x & 2) << 1)) << 4) + pj * 128
                : w >= 2 ? (8 * (w - 2) + x) * 384 + ((e ^ (4 * (w - 2) + (x >> 1))) << 4)
                         : (32 * pu + 8 * (((n >> 1) & 2) | (n >> 3)) + (n & 3)) * 384 + pj * 128 + g * 16;
    unsigned lo = lb + (w >= 4 ? PSCR + (w - 4) * 6144 : w >= 2 ? PSCR + 24576 + (w - 2) * 1024 : LDS_QK + 2048);
    const int cnt = w >= 4 ? 6 : w >= 2 ? 3 : 4, lst = w >= 4 || w < 2 ? 1024 : 2048;
    const int ga = w >= 4 ? 3072 : w >= 2 ? 128 : 64, gb = w < 2 ? 1408 : 0;
    asm volatile("" : "+v"(vo), "+s"(lo));
    ARGS_IN();
    const u32x4 rw = mkrs4(w >= 4 ? wq : w >= 2 ? q : wk, w >= 4 || w < 2 ? C * C * 2 : NQ * C * 2);
#pragma unroll 1
    for (int i = 0; i < cnt; ++i) DMA(lo + i * lst, vo, rw, i * ga + (i >> 1) * gb);
  } else if (w != 0) RAW_BARRIER();
  SB();
  if (__builtin_expect(w == 0, 1)) {
    unsigned v0 = x * 768 + ((e ^ (((x >> 1) & 3) << 1)) << 4), v1 = x * 768 + 384 + ((e ^ ((((x >> 1) & 3) << 1) | 1)) << 4);
    unsigned vy = x * 384 + ((e ^ (((x >> 2) & 1) * 4 + ((x >> 1) & 1) * 3)) << 4);
    unsigned b0 = (wg >> 5) * (2 * W0 * C * 2) + (wg & 31) * 6144, b1 = (wg >> 5) * (W1 * C * 2) + (wg & 31) * 3072;
    constexpr unsigned st0 = 16 * W0 * C * 2, st1 = 8 * W1 * C * 2;
    int slot = 0, lap = 0;
    u32x4 rt = mkrs4(tabs.seq, 112 * 4);
    unsigned vz = 0u;
    asm volatile("" : "+v"(v0), "+v"(v1), "+v"(vy), "+v"(vz), "+s"(b0), "+s"(b1), "+s"(rt));
    ARGS_IN();
    const u32x4 r0 = mkrs4(f0, W0 * W0 * C * 2), r1 = mkrs4(f1, W1 * W1 * C * 2);
    __builtin_amdgcn_s_setprio(3);
    if (wg < 18) __builtin_amdgcn_s_sleep(4);
#define UNIT(l_, g0_, g1_, t_) do { \
        DMA_NT((l_) + 4096, vy, r1, (g1_)); \
        DMA_NT((l_), v0, r0, (g0_)); \
        DMA_NT((l_) + 1024, v1, r0, (g0_)); \
        DMA_NT((l_) + 2048, v0, r0, (g0_) + W0 * C * 2); \
        DMA_NT((l_) + 3072, v1, r0, (g0_) + W0 * C * 2); \
        DMA_TAG(lb + LDS_CTL + CT_FULL, vz, rt, (t_)); \
        waitvm<6 * DEPTH>(); } while (0)
#pragma unroll 1
    for (int s = 0; s < NT; ++s) {
      if (s >= NS || (s == 8 && wg < 54)) {
        LAS char* fr = s >= NS ? ctl + CT_FREE + 4 * slot : ctl + CT_PD;
        const int need = s >= NS ? lap - (SKIP(s - NS) ? 4 : 0) : 1;
        if (__builtin_expect(lds_get(fr) < need, 0)) {
          lds_wait(fr, need);
        }
      }
      unsigned a0 = b0, a1 = b1;
      if (s >= 21 && s < 21 + ntake) {
        const unsigned tk = tabs.take[(wg - 54) >> 1] >> (10 * (s - 21)), xw = tk & 255u, xs = 21 + ((tk >> 8) & 3u);
        a0 = (xw >> 5) * (2 * W0 * C * 2) + (xw & 31) * 6144 + xs * st0; a1 = (xw >> 5) * (W1 * C * 2) + (xw & 31) * 3072 + xs * st1;
      }
      const unsigned sb = lb + slot * SLOT;
      if (!SKIP(s)) {
#pragma unroll
        for (int p = 0; p < 3; ++p) UNIT(sb + p * PART, a0 + p * 128, a1 + p * 128, 12 * s + 4 * p);
      }
      if (slot == NS - 1) { slot = 0; lap += 4; } else ++slot;
      if (s < 21 || s >= 21 + NX) { b0 += st0; b1 += st1; }
    }
    __builtin_amdgcn_s_setprio(0);
    qres = __builtin_amdgcn_raw_buffer_load_b16(mkrs(q, NQ * C * 2), (dst * 16 + n) * 2, 0, 0);
    waitvm<0>();
    tg = ((unsigned long long)((unsigned)lds_spin(ctl + CT_TG + 4, 1) & 0xffu) << 32) | (unsigned)lds_get(ctl + CT_TG);
  } else {
    if (pq) {
      if (w >= 2) {
        waitvm<0>();
        lds_add(ctl + CT_PA, lane, 1);
      }
      if (w >= 4) {
        const u32x4 one = {0x3f803f80u, 0x3f803f80u, 0x3f803f80u, 0x3f803f80u};
        f32x4 gq = {0.f, 0.f, 0.f, 0.f}, sq = gq, rr = gq, cs = gq;
        LAS const char* rd = lds + PSCR + 128 * (8 * g + tq) + 16 * (((w & 2) * 2 + tp) ^ ((tq & 2) << 1)) + 8 * (w & 1);
        LAS const char* rq0 = lds + PSCR + 24576 + 128 * n + 16 * (g ^ (n >> 1));
        LAS const char* rq1 = lds + PSCR + 24576 + 128 * n + 16 * ((4 + g) ^ (n >> 1));
        u32x4 qv[6], af[6];
        lds_spin(ctl + CT_PA, 6);
#pragma unroll
        for (int s = 0; s < 6; ++s) qv[s] = *(LAS const u32x4*)(((s & 1) ? rq1 : rq0) + (s >> 1) * 2048);
#pragma unroll
        for (int s = 0; s < 6; ++s) {
          const u32x2 a = vtr(rd + s * 4096), b = vtr(rd + s * 4096 + 512);
          af[s] = u32x4{a[0], a[1], b[0], b[1]};
        }
        SB();
#pragma unroll
        for (int s = 0; s < 6; ++s) { gq = mfma(qv[s], qv[s], gq); sq = mfma(one, qv[s], sq); }
#pragma unroll
        for (int s = 0; s < 6; ++s) { rr = mfma(af[s], qv[s], rr); cs = mfma(af[s], one, cs); }
        const float mean = sq[0] * (1.f / C);
        const float s2 = xrow16_sum(g == (n >> 2) ? sel4(gq, n & 3) : 0.f);
        const float rstd = __builtin_amdgcn_rsqf(fmaxf(fmaf(-mean, mean, s2 * (1.f / C)), 0.f) + EPS);
        const f32x4 qq = (rr - mean * cs) * rstd;
        *(LAS u32x2*)(lds + LDS_QK + ((w >> 1) & 1) * 1024 + lane * 16 + 8 * (w & 1)) = u32x2{cvtpk(qq[0], qq[1]), cvtpk(qq[2], qq[3])};
        lds_add(ctl + CT_PB, lane, 1);
      }
    }
    const int pcb = pj - 3, drb = pu;
    u32x4 pan[12], dir[12];
    if (pv) {
      const RSRC rdir = mkrs(wv, C * C * 2), rpan = mkrs(wo, C * C * 2);
      const unsigned fo = ((32 * drb + n) * C + 8 * g) * 2, po = (lane >> 2) * (C * 2) + (lane & 3) * 16 + pcb * 64;
#pragma unroll
      for (int j = 0; j < 12; ++j) pan[j] = ldb128<0>(rpan, po, 16 * j * C * 2);
#pragma unroll
      for (int t = 0; t < 12; ++t) dir[t] = ldb128<0>(rdir, fo, (t >> 1) * 64 + 16 * (t & 1) * C * 2);
    }
    SB();
    asm volatile("s_load_dwordx2 %0, %1, 0x0\n\ts_waitcnt lgkmcnt(0)" : "=s"(tg) : "s"(ws + EP_OFF) : "memory");
    tg = (tg + 0x40100401ull) & 0xffffffffffull;
    if (w == 3 && lane == 0) *(LAS u32x2*)(ctl + CT_TG) = u32x2{(unsigned)tg, (unsigned)(tg >> 32) | 0x100u};
    if (pq) {
      if (w == 1) {
        waitvm<0>();
        u32x4 kf4[4];
#pragma unroll
        for (int i = 0; i < 4; ++i) kf4[i] = *(LAS const u32x4*)(lds + LDS_QK + 2048 + i * 1024 + lane * 16);
        lds_spin(ctl + CT_PB, 4);
        const u32x4 q0 = *(LAS const u32x4*)(lds + LDS_QK + lane * 16), q1 = *(LAS const u32x4*)(lds + LDS_QK + 1024 + lane * 16);
        const f32x4 z = {0.f, 0.f, 0.f, 0.f};
        const f32x4 d0 = mfma(kf4[1], q1, mfma(kf4[0], q0, z)), d1 = mfma(kf4[3], q1, mfma(kf4[2], q0, z));
        const u32x4 xq = pack8(d0 * QSCALE, d1 * QSCALE);
        const unsigned ck = qk_check(xq, (unsigned)tg);
        asm volatile("" :: "v"(ck));
        const unsigned so = QK_OFF + wg * QK_REC;
#pragma unroll
        for (int cp = 0; cp < 4; ++cp) {
          stb128(xq, rws, lane * 16 + so + cp * QK_COPY);
          stb32(ck, rws, lane * 4 + so + 1024 + cp * QK_COPY);
        }
        lds_add(ctl + CT_PD, lane, 1);
      }
    }
    if (pv) {
      LAS char* cvw = lds + PSCR + lane * 16;
      LAS const char* cvr = lds + PSCR + 64 * (8 * g + tq) + 8 * tp;
#pragma unroll
      for (int j = 0; j < 12; ++j) *(LAS u32x4*)(cvw + j * 1024) = pan[j];
      WAVE_SYNC();
      f32x4 d[2][2];
#pragma unroll
      for (int i = 0; i < 4; ++i) d[i >> 1][i & 1] = f32x4{0.f, 0.f, 0.f, 0.f};
#pragma unroll
      for (int s = 0; s < 6; ++s) {
        const u32x2 a = vtr(cvr + s * 2048), b = vtr(cvr + s * 2048 + 256), c = vtr(cvr + s * 2048 + 32), dd = vtr(cvr + s * 2048 + 288);
        const u32x4 p0 = {a[0], a[1], b[0], b[1]}, p1 = {c[0], c[1], dd[0], dd[1]};
#pragma unroll
        for (int hf = 0; hf < 2; ++hf) { d[0][hf] = mfma(dir[2 * s + hf], p0, d[0][hf]); d[1][hf] = mfma(dir[2 * s + hf], p1, d[1][hf]); }
      }
      lds_add(ctl + CT_PD, lane, 1);
      const int so = WVO_OFF + (pcb * 12 + 2 * drb) * 1024;
      stb128(pack8(d[0][0], d[0][1]), rws, lane * 16 + so);
      stb128(pack8(d[1][0], d[1][1]), rws, lane * 16 + so + 1024);
      waitvm<0>();
      if (lane < 8) stb64(u32x2{(unsigned)tg, (unsigned)(tg >> 32)}, rws, FLAG_OFF + lane * 512 + (wg - 18) * 8);
    }
    const f32x4 z = {0.f, 0.f, 0.f, 0.f};
    int full = 0;
#define NEED(u_) do { if (full < (u_)) full = lds_spin(ctl + CT_FULL, (u_)); } while (0)
    if (w >= 4) {
      const int brg = ((g & 1) << 1) | (g >> 1);
      const int fn = (((n >> 1) & 3) << 1) | ((n >> 3) & 1);
      const int fy = ((n >> 2) & 1) * 4 + ((n >> 1) & 1) * 3;
      const int orr0 = 128 * n + 16 * (brg ^ fn), orr1 = 128 * n + 16 * ((4 + brg) ^ fn);
      const int ory0 = n < 8 ? 4096 + 128 * n + 16 * (brg ^ fy) : LDS_CTL + CT_ONE, ory1 = n < 8 ? 4096 + 128 * n + 16 * ((4 + brg) ^ fy) : LDS_CTL + CT_ONE;
      const int yon = n < 8 ? 1 : 0;
      const float kf = n < 8 ? 2.f : 1.f;
      LAS char* pk = lds + LDS_PK + (w - 4) * PK_B;
      u32x4 qk[6];
      float mrun, lsum = 0.f;
#define RD_K(k_, i_) do { LAS const char* p_ = lds + slot * SLOT + (((k_) & 1) ? orr1 : orr0) + ((k_) >> 1) * PART; \
    yv[i_] = *(LAS const u32x4*)(lds + (((k_) & 1) ? ory1 : ory0) + __mul24(yon, slot * SLOT + ((k_) >> 1) * PART)); \
    xa[i_] = *(LAS const u32x4*)(p_); xb[i_] = *(LAS const u32x4*)(p_ + 2048); } while (0)
#define MF_STAT(i_) do { cy = mfma(yv[i_], yv[i_], cy); ca = mfma(xa[i_], yv[i_], ca); cb = mfma(xb[i_], yv[i_], cb); \
    ga = mfma(xa[i_], xa[i_], ga); gb = mfma(xb[i_], xb[i_], gb); } while (0)
#define MF_S(k_, i_) do { sy = mfma(yv[i_], qk[k_], sy); sa = mfma(xa[i_], qk[k_], sa); sb = mfma(xb[i_], qk[k_], sb); } while (0)
#define STATS(st) do { \
      const int r = n & 3; \
      const float yy = lo32(sel4(cy, r)); \
      const float ua = fmaf(kf, sel4(ca, r), yy), ub = fmaf(kf, sel4(cb, r), yy); \
      const float va = dpp<XOR8>(ua), vb = dpp<XOR8>(ub); \
      const float mna = (n < 8 ? va : ua) * (1.f / C), mnb = (n < 8 ? vb : ub) * (1.f / C); \
      const float qa = fmaf((n < 8 ? ua : va) + sel4(ga, r), 1.f / C, EPS), qb = fmaf((n < 8 ? ub : vb) + sel4(gb, r), 1.f / C, EPS); \
      const float ra_ = __builtin_amdgcn_rsqf(fmaxf(fmaf(-mna, mna, qa), EPS)); \
      const float rb_ = __builtin_amdgcn_rsqf(fmaxf(fmaf(-mnb, mnb, qb), EPS)); \
      const f32x4 v_ = {row_align(ra_), row_align(mna), row_align(rb_), row_align(mnb)}; \
      st[0] = f32x4{dpp<0x150>(v_[0]), dpp<0x150>(v_[1]), dpp<0x150>(v_[2]), dpp<0x150>(v_[3])}; \
      st[1] = f32x4{dpp<0x151>(v_[0]), dpp<0x151>(v_[1]), dpp<0x151>(v_[2]), dpp<0x151>(v_[3])}; \
      st[2] = f32x4{dpp<0x152>(v_[0]), dpp<0x152>(v_[1]), dpp<0x152>(v_[2]), dpp<0x152>(v_[3])}; \
      st[3] = f32x4{dpp<0x153>(v_[0]), dpp<0x153>(v_[1]), dpp<0x153>(v_[2]), dpp<0x153>(v_[3])}; \
    } while (0)
#define LOGITS(sv, st, base_) do { \
      const auto sw = __builtin_amdgcn_permlane32_swap(__float_as_uint(sy[0]), __float_as_uint(sy[0]), false, false); \
      const float qs = __uint_as_float(sw[1]);       \
      _Pragma("unroll") for (int r = 0; r < 4; ++r) { \
        const float s0 = r == 0 ? __uint_as_float(sw[0]) : lo32(sy[r]); \
        sv[r] = fmaf(fmaf(-st[r][1], qs, sa[r] + s0), st[r][0], -(base_)); \
        sv[4 + r] = fmaf(fmaf(-st[r][3], qs, sb[r] + s0), st[r][2], -(base_)); \
      } \
    } while (0)
#define SV_MAX(sv) fmaxf(fmaxf(fmaxf(sv[0], sv[1]), fmaxf(sv[2], sv[3])), fmaxf(fmaxf(sv[4], sv[5]), fmaxf(sv[6], sv[7])))
#define PUBLISH(sv, st, s_) do { \
      float tm_ = 0.f, c_[4], p_[8]; \
      _Pragma("unroll") for (int r = 0; r < 4; ++r) { p_[r] = sv[r] * st[r][0]; p_[4 + r] = sv[4 + r] * st[r][2]; } \
      _Pragma("unroll") for (int r = 0; r < 4; ++r) { \
        tm_ = fmaf(p_[4 + r], st[r][3], fmaf(p_[r], st[r][1], tm_)); \
        const float h_ = p_[r] + p_[4 + r]; \
        const auto x_ = __builtin_amdgcn_permlane32_swap(__float_as_uint(h_), __float_as_uint(h_), false, false); \
        c_[r] = __uint_as_float(x_[0]) + __uint_as_float(x_[1]); \
      } \
      tm_ = xrow16_sum(tm_); \
      tm_ = g == 2 ? -tm_ : 0.f; \
      const float r1_ = tm_ - bfhi(__float_as_uint(tm_)), r2_ = r1_ - bfhi(__float_as_uint(r1_)); \
      unsigned p0_ = cvtpk(c_[0], c_[1]), p1_ = cvtpk(c_[2], c_[3]); \
      unsigned q0_ = __builtin_amdgcn_perm(__float_as_uint(r1_), __float_as_uint(tm_), 0x07060302u), q1_ = __float_as_uint(r2_) >> 16; \
      asm volatile("" : "+v"(p0_), "+v"(p1_), "+v"(q0_), "+v"(q1_)); \
      *(LAS u32x4*)(pk + lane * 16) = u32x4{cvtpk(p_[0], p_[1]), cvtpk(p_[2], p_[3]), cvtpk(p_[4], p_[5]), cvtpk(p_[6], p_[7])}; \
      *(LAS u32x2*)(pk + 1024 + lane * 8) = u32x2{g < 2 ? p0_ : q0_, g < 2 ? p1_ : q1_}; \
      if (g == 0) *(LAS float*)(pk + 1536 + n * 4) = mrun; \
      lds_put(ctl + CT_PRDY + 4 * (w - 4), (s_) + 1); \
      lsum += ((sv[0] + sv[1]) + (sv[2] + sv[3])) + ((sv[4] + sv[5]) + (sv[6] + sv[7])); \
    } while (0)
      int s = (w + NT - 1) & 3, slot = s;
      f32x4 st1[4];
      float s1v[8];
      {
        u32x4 yv[12], xa[12], xb[12];
        f32x4 st0[4];
#pragma unroll
        for (int h = 0; h < 2; ++h) {
          f32x4 cy = z, ga = z, ca = z, gb = z, cb = z;
          lds_wait(ctl + CT_FULL, 3 * (s + 4 * h) + 3);
#pragma unroll
          for (int k = 0; k < 6; ++k) RD_K(k, 6 * h + k);
          lds_inc(ctl + CT_FREE + 4 * slot);
          SB();
#pragma unroll
          for (int k = 0; k < 6; ++k) MF_STAT(6 * h + k);
          SB();
          if (h == 0) { STATS(st0); slot += 4; } else STATS(st1);
          SB();
        }
        lds_wait(ctl + CT_QK, 3);
#pragma unroll
        for (int k = 0; k < 6; ++k) qk[k] = *(LAS const u32x4*)(lds + LDS_QK + k * 1024 + lane * 16);
        float s0v[8];
        {
          f32x4 sy = z, sa = z, sb = z;
#pragma unroll
          for (int k = 0; k < 6; ++k) MF_S(k, k);
          LOGITS(s0v, st0, 0.f);
        }
        {
          f32x4 sy = z, sa = z, sb = z;
#pragma unroll
          for (int k = 0; k < 6; ++k) MF_S(k, 6 + k);
          LOGITS(s1v, st1, 0.f);
        }
        mrun = xrow16_max(fmaxf(SV_MAX(s0v), SV_MAX(s1v)));
        if (g == 0) __builtin_amdgcn_ds_fmaxf((LAS float*)(ctl + CT_MAX + 4 * n), mrun, __ATOMIC_RELAXED, __HIP_MEMORY_SCOPE_WORKGROUP, false);
        lds_inc(ctl + CT_QR);
        lds_spin(ctl + CT_QR, 4);
        mrun = *(LAS const float*)(ctl + CT_MAX + 4 * n);
#pragma unroll
        for (int j = 0; j < 8; ++j) { s0v[j] = __builtin_amdgcn_exp2f(s0v[j] - mrun); s1v[j] = __builtin_amdgcn_exp2f(s1v[j] - mrun); }
        PUBLISH(s0v, st0, s);
        s += 4;
      }
      int npk = 6;
#pragma unroll 1
      for (;;) {
        s += 4; slot += 4; slot -= slot >= NS ? NS : 0;
        if (s >= NT) break;
        if (SKIP(s)) continue;
        f32x4 sy = z, cy = z, sa = z, ga = z, ca = z, sb = z, gb = z, cb = z, st[4];
        u32x4 yv[6], xa[6], xb[6];
        float sv[8];
        NEED(3 * s + 3);
#pragma unroll
        for (int k = 0; k < 6; ++k) RD_K(k, k);
        lds_inc(ctl + CT_FREE + 4 * slot);
        unsigned pcv = *(volatile LAS unsigned*)(ctl + CT_PCON + 4 * (w - 4));
        SB();
#pragma unroll
        for (int k = 0; k < 6; ++k) MF_STAT(k);
        SB();
#pragma unroll
        for (int k = 0; k < 6; ++k) MF_S(k, k);
        if (s < 12) {
          lds_spin(ctl + CT_PCON + 4 * (w - 4), 3);
          PUBLISH(s1v, st1, s - 4);
        }
        STATS(st);
        LOGITS(sv, st, mrun);
        {
          float e_[8];
#pragma unroll
          for (int j = 0; j < 8; ++j) e_[j] = __builtin_amdgcn_exp2f(sv[j]);
          const float tm = SV_MAX(sv);
          if (__builtin_expect(__any(!(tm <= 16.f)), 0)) {
            const float mx = xrow16_max(tm), dm = mx <= 16.f ? 0.f : mx;
            mrun += dm; lsum *= __builtin_amdgcn_exp2f(-dm);
#pragma unroll
            for (int j = 0; j < 8; ++j) e_[j] = __builtin_amdgcn_exp2f(sv[j] - dm);
          }
#pragma unroll
          for (int j = 0; j < 8; ++j) sv[j] = e_[j];
        }
        if (__builtin_expect((int)__builtin_amdgcn_readfirstlane(pcv) < npk, 0)) lds_spin(ctl + CT_PCON + 4 * (w - 4), npk);
        npk += 3;
        PUBLISH(sv, st, s);
      }
      lsum = xrow16_sum(lsum);
      if (g == 0) *(LAS f32x2*)(lds + LDS_LF + (w - 4) * 128 + n * 8) = f32x2{mrun, lsum};
      lds_inc(ctl + CT_XD);
    } else {
      const int ftr = ((g & 1) << 2) | ((tq >> 1) << 1) | (g >> 1);
      const int fty = (g & 1) * 4 + (tq >> 1) * 3;
      const int ton = g < 2 ? 1 : 0;
      int otr[4], oty[4];
#pragma unroll
      for (int t = 0; t < 4; ++t) {
        otr[t] = (w - 1) * PART + 128 * (4 * g + tq) + 16 * ((2 * t + (tp >> 1)) ^ ftr) + 8 * (tp & 1);
        oty[t] = g < 2 ? (w - 1) * PART + 4096 + 128 * (4 * g + tq) + 16 * ((2 * t + (tp >> 1)) ^ fty) + 8 * (tp & 1) : LDS_CTL + CT_ONE;
      }
#define RD_T(ta, ty, so_, yo_) _Pragma("unroll") for (int t_ = 0; t_ < 4; ++t_) { \
    const u32x2 lo_ = vtr(lds + otr[t_] + (so_)), hi_ = vtr(lds + otr[t_] + (so_) + 2048); \
    ta[t_] = u32x4{lo_[0], lo_[1], hi_[0], hi_[1]}; ty[t_] = vtr(lds + oty[t_] + (yo_)); }
      f32x4 acc[4] = {z, z, z, z};
#define RD_P(j_) \
      pf = *(LAS const u32x4*)(lds + LDS_PK + (j_) * PK_B + lane * 16); \
      py = *(LAS const u32x2*)(lds + LDS_PK + (j_) * PK_B + 1024 + lane * 8); \
      lds_inc(ctl + CT_PCON + 4 * (j_))
#define MF_PV(ta, ty) \
      _Pragma("unroll") for (int t = 0; t < 4; ++t) acc[t] = mfma(ta[t], pf, acc[t]); \
      _Pragma("unroll") for (int t = 0; t < 4; ++t) acc[t] = mfma16(ty[t], py, acc[t])
#define MOVE_REF(m_) do { if (__builtin_expect(__any((m_) != M), 0)) { \
            Mtop = fmaxf(Mtop, (m_)); \
            const bool drop = (m_) < Mtop - 64.f; \
            const float mn = drop ? M : (m_), fa = __builtin_amdgcn_exp2f(M - mn); \
            M = mn; \
            _Pragma("unroll") for (int t = 0; t < 4; ++t) acc[t] *= fa; \
            if (drop) { pf = u32x4{0u, 0u, 0u, 0u}; py = u32x2{0u, 0u}; } \
          } } while (0)
      const unsigned flo = FLAG_OFF + (wg & 7) * 512 + (lane < 36 ? lane : 35) * 8;
      float M, Mtop;
      u32x4 wvv[24], pf;
      u32x2 py;
      {
        u32x4 ta[8][4], xx[6];
        u32x2 ty[8][4], fl;
        unsigned cc[6];
        const int qo = QK_OFF + (wg & 3) * QK_COPY + (2 * w - 2) * QK_REC;
#define QK_LOAD() _Pragma("unroll") for (int j = 0; j < 6; ++j) { \
          xx[j] = ldb128<16>(rws, lane * 16, qo + ((j >> 1) * 6 + (j & 1)) * QK_REC); \
          cc[j] = __builtin_amdgcn_raw_buffer_load_b32(rws, lane * 4, qo + ((j >> 1) * 6 + (j & 1)) * QK_REC + 1024, 16); }
#pragma unroll
        for (int u = 0; u < 8; ++u) {
          if (u == QKAT) {
#pragma unroll 1
            for (int spin = 0; spin < 64; ++spin) {
              if ((long long)(wall_clock64() - tstart) >= GATE) break;
              __builtin_amdgcn_s_sleep(2);
            }
            if (pq) lds_wait(ctl + CT_PD, 1);
            QK_LOAD();
#pragma unroll 1
            for (int spin = 0; spin < (1 << 18); ++spin) {
              unsigned df = 0u;
#pragma unroll
              for (int j = 0; j < 6; ++j) df |= qk_check(xx[j], (unsigned)tg) ^ cc[j];
              if (__all(df == 0u)) break;
              asm volatile("" ::: "memory");
              QK_LOAD();
            }
#pragma unroll
            for (int h = 0; h < 2; ++h) {
              u32x4 sx;
#pragma unroll
              for (int i = 0; i < 4; ++i) sx[i] = add3(xx[h][i], xx[2 + h][i], xx[4 + h][i]);
              *(LAS u32x4*)(lds + LDS_QK + (2 * w - 2 + h) * 1024 + lane * 16) = sx;
            }
            lds_inc(ctl + CT_QK);
            SB();
          }
          lds_wait(ctl + CT_FULL, 3 * u + w);
          RD_T(ta[u], ty[u], u * SLOT, ton * (u * SLOT));
          lds_inc(ctl + CT_FREE + 4 * u);
          SB();
        }
        fl = __builtin_bit_cast(u32x2, __builtin_amdgcn_raw_buffer_load_b64(rws, flo, 0, 16));
#pragma unroll
        for (int u = 0; u < 12; ++u) {
          constexpr int jm = NT - 1;
          lds_spin(ctl + CT_PRDY + 4 * ((u - jm) & 3), u + 1);
          if (u == 0) Mtop = M = *(LAS const float*)(ctl + CT_MAX + 4 * n);
          const float m_ = u < 8 ? M : *(LAS const float*)(lds + LDS_PK + ((u - jm) & 3) * PK_B + 1536 + n * 4);
          RD_P((u - jm) & 3);
          SB();
          if (u >= 8) MOVE_REF(m_);
          MF_PV(ta[u & 7], ty[u & 7]);
          SB();
          if (u < 4) {
            NEED(3 * (u + 8) + w);
            RD_T(ta[u], ty[u], ((u + 8) % NS) * SLOT, ton * (((u + 8) % NS) * SLOT));
            lds_inc(ctl + CT_FREE + 4 * ((u + 8) % NS));
            SB();
          }
        }
#pragma unroll 1
        for (int spin = 0; spin < (1 << 18); ++spin) {
          if (__all(fl[0] == (unsigned)tg && fl[1] == (unsigned)(tg >> 32))) break;
          asm volatile("" ::: "memory");
          fl = __builtin_bit_cast(u32x2, __builtin_amdgcn_raw_buffer_load_b64(rws, flo, 0, 16));
        }
        asm volatile("" ::: "memory");
      }
      int slot = 2, nw = 0;
      unsigned rdy = 0u;
#pragma unroll 1
      for (int s = 12; s < NT; ++s) {
        if (!SKIP(s)) {
          const int j = (s - (NT - 1)) & 3;
          u32x4 ta[4];
          u32x2 ty[4];
          const bool ahead = (int)__builtin_amdgcn_readfirstlane(rdy) > s;
          if (!ahead) NEED(3 * s + w);
          RD_T(ta, ty, slot * SLOT, __mul24(ton, slot * SLOT));
          lds_inc(ctl + CT_FREE + 4 * slot);
          SB();
          if (!ahead) lds_spin(ctl + CT_PRDY + 4 * j, s + 1);
          const float m_ = *(LAS const float*)(lds + LDS_PK + j * PK_B + 1536 + n * 4);
          RD_P(j);
          rdy = *(volatile LAS unsigned*)(ctl + CT_PRDY + 4 * ((j + 1) & 3));
          SB();
          MOVE_REF(m_);
          MF_PV(ta, ty);
          SB();
          if (nw < 24 / WVN) {
#pragma unroll
            for (int u = 0; u < 24 / WVN; ++u)
              if (nw == u) {
#pragma unroll
                for (int j = WVN * u; j < WVN * u + WVN; ++j) wvv[j] = ldb128<16>(rws, lane * 16, WVO_OFF + ((w - 1) * 24 + j) * 1024);
              }
            ++nw;
          }
        } else rdy = 0u;
        slot = slot == NS - 1 ? 0 : slot + 1;
      }
      LAS char* xc = lds + ((NT - 6) % NS) * SLOT;
      *(LAS u32x4*)(xc + (2 * w - 2) * 1024 + lane * 16) = pack8(acc[0], acc[1]);
      *(LAS u32x4*)(xc + (2 * w - 1) * 1024 + lane * 16) = pack8(acc[2], acc[3]);
      lds_inc(ctl + CT_XD);
      lds_spin(ctl + CT_XD, 7);
      f32x2 ml[4];
#pragma unroll
      for (int j = 0; j < 4; ++j) ml[j] = *(LAS const f32x2*)(lds + LDS_LF + j * 128 + n * 8);
      u32x4 b[6];
#pragma unroll
      for (int k = 0; k < 6; ++k) b[k] = *(LAS const u32x4*)(xc + k * 1024 + lane * 16);
      SB();
      f32x4 o[4];
#pragma unroll
      for (int t = 0; t < 2; ++t) o[t] = mfma(wvv[t], b[0], z);
      SB();
      float L = 0.f;
#pragma unroll
      for (int j = 0; j < 4; ++j) L = fmaf(ml[j][1], __builtin_amdgcn_exp2f(ml[j][0] - M), L);
      float rl = __builtin_amdgcn_rcpf(L);
      const float lse = M + __builtin_amdgcn_logf(L);
      const unsigned lb8 = __float_as_uint(lse) >> (8 * g);
      unsigned tt[4];
#pragma unroll
      for (int r = 0; r < 4; ++r) tt[r] = (((lb8 >> (2 * r)) & 3u) << 10) | ((unsigned)(tg >> (10 * r)) & 1023u);
      if (w == 3 && n == dst / 12 && g == 0) *(LAS float*)(ctl + CT_REF) = lse;
      const unsigned mo = (wg * NDST + (n & 7) * 12 + 4 * (w - 1) + (n >> 3)) * 64 + g * 16;
#pragma unroll
      for (int k = 1; k < 6; ++k)
#pragma unroll
        for (int t = 0; t < 2; ++t) o[t] = mfma(wvv[2 * k + t], b[k], o[t]);
      asm volatile("" : "+v"(tt[0]), "+v"(tt[1]), "+v"(tt[2]), "+v"(tt[3]), "+v"(rl));
      SB();
#pragma unroll
      for (int h = 0; h < 2; ++h) {
        if (h == 0) {
#pragma unroll
          for (int k = 0; k < 6; ++k)
#pragma unroll
            for (int t = 2; t < 4; ++t) o[t] = mfma(wvv[10 + 2 * k + t], b[k], k ? o[t] : z);
        }
        u32x4 lo, hi, ta_, tb_;
#pragma unroll
        for (int r = 0; r < 4; ++r) { ta_[r] = tagf(o[2 * h][r] * rl, tt[r]); tb_[r] = tagf(o[2 * h + 1][r] * rl, tt[r]); }
        if (h) { lo = ta_; hi = tb_; asm volatile("" : "+v"(ta_), "+v"(tb_), "+v"(lo), "+v"(hi)); } else { lo = ta_; hi = tb_; }
#pragma unroll
        for (int r = 0; r < 4; ++r) {
          lo[r] = __builtin_amdgcn_update_dpp(lo[r], tb_[r], XOR8, 0xf, 0xc, false);
          hi[r] = __builtin_amdgcn_update_dpp(hi[r], ta_[r], XOR8, 0xf, 0x3, false);
        }
        stb128(lo, rws, mo + h * 128);
        stb128(hi, rws, mo + h * 128 + 8 * 12 * 64);
        SB();
      }
      lds_inc(ctl + CT_ST);
    }
  }

  if (wg < NDST) {
    const unsigned t0 = (unsigned)tg & 1023u, t1 = (unsigned)(tg >> 10) & 1023u, t2 = (unsigned)(tg >> 20) & 1023u, t3 = (unsigned)(tg >> 30) & 1023u;
    lds_spin(ctl + CT_ST, 3);
    float ref = *(LAS const float*)(ctl + CT_REF);
    u32x4 A = {0u, 0u, 0u, 0u}, B = A;
    {
      const unsigned ao = (tid >> 2) * (NDST * 64) + (tid & 3) * 16;
      bool oka = false, okb = false;
#pragma unroll 1
      for (int spin = 0; spin < (1 << 18); ++spin) {
        if (!oka) A = ldb128<16>(rws, ao, dst * 64);
        if (!okb) B = ldb128<16>(rws, ao, dst * 64 + 128 * NDST * 64);
        oka = (((A[0] ^ t0) | (A[1] ^ t1) | (A[2] ^ t2) | (A[3] ^ t3)) & 1023u) == 0u;
        okb = (((B[0] ^ t0) | (B[1] ^ t1) | (B[2] ^ t2) | (B[3] ^ t3)) & 1023u) == 0u;
        if (__all(oka && okb)) break;
        asm volatile("" ::: "memory");
      }
    }
    {
      unsigned la = (((A[0] >> 10) & 3u) | ((A[1] >> 8) & 12u) | ((A[2] >> 6) & 48u) | ((A[3] >> 4) & 192u)) << (8 * (lane & 3));
      unsigned lb_ = (((B[0] >> 10) & 3u) | ((B[1] >> 8) & 12u) | ((B[2] >> 6) & 48u) | ((B[3] >> 4) & 192u)) << (8 * (lane & 3));
      la |= __builtin_amdgcn_mov_dpp(la, 0xB1, 0xf, 0xf, true); lb_ |= __builtin_amdgcn_mov_dpp(lb_, 0xB1, 0xf, 0xf, true);
      la |= __builtin_amdgcn_mov_dpp(la, 0x4E, 0xf, 0xf, true); lb_ |= __builtin_amdgcn_mov_dpp(lb_, 0x4E, 0xf, 0xf, true);
      const float Ma = __uint_as_float(la), Mb = __uint_as_float(lb_);
      float M = fmaxf(Ma, Mb);
      if (!__all(M - ref <= 64.f)) {
        M = fmaxf(M, dpp<ROR4>(M)); M = fmaxf(M, dpp<XOR8>(M));
        ref = xrow16_max(M);
      }
      const float wa = __builtin_amdgcn_exp2f(Ma - ref), wb = __builtin_amdgcn_exp2f(Mb - ref);
      f32x4 v;
#pragma unroll
      for (int k = 0; k < 4; ++k) {
        float s = wa * __uint_as_float(A[k] & 0xfffff000u) + wb * __uint_as_float(B[k] & 0xfffff000u);
        s += dpp<ROR4>(s); s += dpp<XOR8>(s);
        v[k] = xrow16_sum(s);
      }
      float l = wa + wb;
      l += dpp<ROR4>(l); l += dpp<XOR8>(l);
      l = xrow16_sum(l);
      if (lane < 4) *(LAS f32x4*)(lds + LDS_STAT + w * 256 + lane * 16) = v;
      if (lane == 4) *(LAS f32x2*)(lds + LDS_STAT + w * 256 + 64) = f32x2{ref, l};
    }
    RAW_BARRIER();
    if (tid < 16) {
      f32x2 ml[NW];
      float av[NW], MM = -INFINITY;
#pragma unroll
      for (int p = 0; p < NW; ++p) { ml[p] = *(LAS f32x2*)(lds + LDS_STAT + p * 256 + 64); av[p] = *(LAS float*)(lds + LDS_STAT + p * 256 + tid * 4); MM = fmaxf(MM, ml[p][0]); }
      float mm = ml[0][0];
#pragma unroll
      for (int p = 1; p < NW; ++p) mm = fminf(mm, ml[p][0]);
      if (__builtin_expect(__any(mm != MM), 0)) {
#pragma unroll
        for (int p = 0; p < NW; ++p) { const float wt = __builtin_amdgcn_exp2f(ml[p][0] - MM); av[p] *= wt; ml[p][1] *= wt; }
      }
      const float a = ((av[0] + av[1]) + (av[2] + av[3])) + ((av[4] + av[5]) + (av[6] + av[7]));
      const float L = ((ml[0][1] + ml[1][1]) + (ml[2][1] + ml[3][1])) + ((ml[4][1] + ml[5][1]) + (ml[6][1] + ml[7][1]));
      const float o = bflo(qres) + a * __builtin_amdgcn_rcpf(L);
      const unsigned ob = cvtpk(o, __uint_as_float(__builtin_amdgcn_mov_dpp(__float_as_uint(o), 0xB1, 0xf, 0xf, true)));
      if (!(tid & 1)) __builtin_amdgcn_raw_buffer_store_b32(ob, mkrs(out, NQ * C * 2), (dst * 16 + tid) * 2, 0, 16);
      if (wg == 0 && tid == 0) stb64(u32x2{(unsigned)tg, (unsigned)(tg >> 32)}, rws, EP_OFF);
    }
  }
}

#undef rws
extern "C" void kernel_launch(void* const* d_in, const int* in_sizes, int n_in, void* d_out, int out_size, void* d_ws, size_t ws_size,
                              hipStream_t stream) {
  dino_main<<<NWG, 512, 0, stream>>>((const u16*)d_in[0], (const u16*)d_in[1], (const u16*)d_in[2], (const u16*)d_in[3], (const u16*)d_in[4],
                                     (const u16*)d_in[5], (const u16*)d_in[6], (char*)d_ws + ((ws_size / 2) & ~(size_t)0x1fffff), (u16*)d_out);
}
```

```cpp
#define PROBE 13
#define PROBE_WG -1
#define PROBE_K 8
#include <hip/hip_runtime.h>
#include <hip/hip_bf16.h>
#include <stdint.h>

typedef unsigned short u16;
using u32x2 = __attribute__((ext_vector_type(2))) unsigned;
using u32x4 = __attribute__((ext_vector_type(4))) unsigned;
using f32x2 = __attribute__((ext_vector_type(2))) float;
using f32x4 = __attribute__((ext_vector_type(4))) float;
using s16x8 = __attribute__((ext_vector_type(8))) short;
using bf16x2_t = __attribute__((ext_vector_type(2))) __bf16;
typedef short v4i16_t __attribute__((ext_vector_type(4)));
#define LAS __attribute__((address_space(3)))

constexpr int C = 192, NQ = 16, W0 = 512, W1 = 256, NWG = 256, NW = 8;
constexpr float EPS = 1e-5f;
constexpr float QSCALE = 0.07216878364870322f * 1.4426950408889634f;

constexpr int PART = 4096, CRS = 3 * PART;
constexpr int SLOT = CRS + 3072, NS = 10;
constexpr int LDS_QK = NS * SLOT;
constexpr int NPK = 7;
constexpr int LDS_PK = LDS_QK, PK_B = 1344;
constexpr int LDS_CTL = LDS_PK + NPK * PK_B;
constexpr int LDS_SINK = LDS_CTL + 384;
constexpr int LDS_BYTES = LDS_SINK + 256;
constexpr int PSCR = 8 * SLOT;
constexpr int CT_FULL = 8, CT_REF = 4, CT_PA = 0, CT_PB = 12, CT_QR = 16, CT_PD = 20, CT_XD = 28, CT_TG = 32, CT_QK = 40, CT_ST = 44,
              CT_FREE = 48, CT_ONE = 112, CT_PRDY = 128, CT_PCON = 160, CT_MAX = 256;

constexpr int NDST = 192;
constexpr int MB_BYTES = NWG * NDST * 64;
constexpr int EP_OFF = MB_BYTES;
constexpr int FLAG_OFF = EP_OFF + 512;
constexpr int WVO_OFF = EP_OFF + 8192;
constexpr int QK_OFF = WVO_OFF + C * C * 2;
constexpr int QK_REC = 1280, QK_COPY = 18 * QK_REC;
constexpr int WS_BYTES = QK_OFF + 4 * QK_COPY;

#define GIVES 0x101000003131ull
__host__ __device__ constexpr int gives(int wg) { return (int)((GIVES >> (wg < 54 ? 4 * (wg & 1) : 16 + 4 * (wg & 7))) & 15); }
struct Tabs { unsigned seq[112]; unsigned take[101]; };
constexpr Tabs make_tabs() {
  Tabs t{};
  for (int i = 0; i < 112; ++i) t.seq[i] = i + 1;
  int k = 0;
  for (int wg = 0; wg < 256; ++wg)
    for (int j = 0; j < gives(wg); ++j, ++k) t.take[k % 101] += (unsigned)(j << 8 | wg) << (10 * (k / 101));
  return t;
}
constexpr int given() { int k = 0; for (int wg = 0; wg < 256; ++wg) k += gives(wg); return k; }
constexpr int GIVEN = given();
static_assert(GIVEN <= 101 * 3, "too many tiles sent elsewhere");
__device__ const Tabs tabs = make_tabs();
#define CLK 0
#define TSMASK 0xffff
#define PQT 0
#define QKAT 6
#define WVN 4
#define DEPTH 6
#define GATE 450
#define CODE_BYTES 24256

__device__ __forceinline__ unsigned cvtpk(float lo, float hi) {
  bf16x2_t v = {(__bf16)lo, (__bf16)hi};
  return __builtin_bit_cast(unsigned, v);
}
__device__ __forceinline__ float bflo(unsigned x) { return __builtin_bit_cast(float, x << 16); }
__device__ __forceinline__ float bfhi(unsigned x) { return __builtin_bit_cast(float, x & 0xffff0000u); }
template <int CTRL> __device__ __forceinline__ float dpp(float x) {
  return __builtin_bit_cast(float, __builtin_amdgcn_mov_dpp(__builtin_bit_cast(int, x), CTRL, 0xf, 0xf, true));
}
constexpr int XOR8 = 0x128, ROR4 = 0x124;
template <bool HI8> __device__ __forceinline__ float half_copy(float x) {
  const int v = __builtin_bit_cast(int, x);
  return __builtin_bit_cast(float, __builtin_amdgcn_update_dpp(v, v, XOR8, 0xf, HI8 ? 0xc : 0x3, false));
}
__device__ __forceinline__ float row_max(float x) {
  x = fmaxf(x, dpp<XOR8>(x)); x = fmaxf(x, dpp<ROR4>(x)); x = fmaxf(x, dpp<0x122>(x));
  return fmaxf(x, dpp<0x121>(x));
}
__device__ __forceinline__ f32x2 swap32(float a, float b) {
  auto s = __builtin_amdgcn_permlane32_swap(__float_as_uint(a), __float_as_uint(b), false, false);
  return f32x2{__uint_as_float(s[0]), __uint_as_float(s[1])};
}
__device__ __forceinline__ f32x2 swap16(float a, float b) {
  auto s = __builtin_amdgcn_permlane16_swap(__float_as_uint(a), __float_as_uint(b), false, false);
  return f32x2{__uint_as_float(s[0]), __uint_as_float(s[1])};
}
__device__ __forceinline__ float xrow16_max(float x) {
  auto s = __builtin_amdgcn_permlane16_swap(__float_as_uint(x), __float_as_uint(x), false, false);
  x = fmaxf(__uint_as_float(s[0]), __uint_as_float(s[1]));
  auto t = __builtin_amdgcn_permlane32_swap(__float_as_uint(x), __float_as_uint(x), false, false);
  return fmaxf(__uint_as_float(t[0]), __uint_as_float(t[1]));
}
__device__ __forceinline__ float xrow16_sum(float x) {
  auto s = __builtin_amdgcn_permlane16_swap(__float_as_uint(x), __float_as_uint(x), false, false);
  x = __uint_as_float(s[0]) + __uint_as_float(s[1]);
  auto t = __builtin_amdgcn_permlane32_swap(__float_as_uint(x), __float_as_uint(x), false, false);
  return __uint_as_float(t[0]) + __uint_as_float(t[1]);
}
__device__ __forceinline__ f32x4 mfma(u32x4 a, u32x4 b, f32x4 c) {
  return __builtin_amdgcn_mfma_f32_16x16x32_bf16(__builtin_bit_cast(s16x8, a), __builtin_bit_cast(s16x8, b), c, 0, 0, 0);
}
__device__ __forceinline__ u32x2 vtr(LAS const char* p) {
  return __builtin_bit_cast(u32x2, __builtin_amdgcn_ds_read_tr16_b64_v4i16((LAS v4i16_t*)p));
}
__device__ __forceinline__ u32x4 vtr2(LAS const char* p, LAS const char* q_) {
  const u32x2 a = vtr(p), b = vtr(q_);
  return u32x4{a[0], a[1], b[0], b[1]};
}
__device__ __forceinline__ float sel4(f32x4 v, int r) {
  const float a = (r & 1) ? v[1] : v[0], b = (r & 1) ? v[3] : v[2];
  return (r & 2) ? b : a;
}
using RSRC = __amdgpu_buffer_rsrc_t;
__device__ __forceinline__ RSRC mkrs(const void* p, int bytes) { return __builtin_amdgcn_make_buffer_rsrc(const_cast<void*>(p), (short)0, bytes, 0x00020000); }
__device__ __forceinline__ u32x4 mkrs4(const void* p, unsigned bytes) {
  const unsigned long long b = (unsigned long long)p;
  return u32x4{(unsigned)b, (unsigned)(b >> 32) & 0xffffu, bytes, 0x00020000u};
}
template <int AUX> __device__ __forceinline__ u32x4 ldb128(RSRC r, unsigned vo, int so) { return __builtin_bit_cast(u32x4, __builtin_amdgcn_raw_buffer_load_b128(r, vo, so, AUX)); }
#define SB() __builtin_amdgcn_sched_barrier(0)
#define WAVE_SYNC() do { __builtin_amdgcn_fence(__ATOMIC_RELEASE, "wavefront"); __builtin_amdgcn_wave_barrier(); __builtin_amdgcn_fence(__ATOMIC_ACQUIRE, "wavefront"); } while (0)
#define RAW_BARRIER() asm volatile("s_waitcnt lgkmcnt(0)\n\ts_barrier" ::: "memory")
#define DMA(m0_, vo_, rs_, so_) asm volatile("s_mov_b32 m0, %0\n\ts_nop 0\n\tbuffer_load_dwordx4 %1, %2, %3 offen lds" :: "s"(m0_), "v"(vo_), "s"(rs_), "s"(so_) : "memory")
template <int N> __device__ __forceinline__ void waitlds() { __builtin_amdgcn_s_waitcnt(0xc07f | (N << 8)); }
template <int N> __device__ __forceinline__ void waitvm() { asm volatile("s_waitcnt vmcnt(%0)" :: "i"(N) : "memory"); }

#define TR(c_) do { } while (0)
#define TS(i_) do { if ((i_) == PROBE) tprobe = wall_clock64(); } while (0)
#define TP(i_) do { } while (0)

__device__ __forceinline__ void lds_add(LAS char* p, int lane, unsigned v) {
  asm volatile("" ::: "memory");
  if (lane == 0) __hip_atomic_fetch_add((LAS unsigned*)p, v, __ATOMIC_RELAXED, __HIP_MEMORY_SCOPE_WORKGROUP);
  asm volatile("" ::: "memory");
}
__device__ __forceinline__ void lds_set(LAS char* p, int lane, unsigned v) {
  asm volatile("" ::: "memory");
  if (lane == 0) __hip_atomic_store((LAS unsigned*)p, v, __ATOMIC_RELAXED, __HIP_MEMORY_SCOPE_WORKGROUP);
  asm volatile("" ::: "memory");
}
__device__ __forceinline__ void lds_inc(int O, LAS char* p) {
  asm volatile("s_mov_b64 exec, 1\n\tds_add_u32 %0, %1 offset:%2\n\ts_mov_b64 exec, -1" :: "v"(p), "v"(1u), "i"(O) : "memory");
}
__device__ __forceinline__ void lds_put(int O, LAS char* p, unsigned v) {
  asm volatile("s_mov_b64 exec, 1\n\tds_write_b32 %0, %1 offset:%2\n\ts_mov_b64 exec, -1" :: "v"(p), "v"(v), "i"(O) : "memory");
}
__device__ __forceinline__ int lds_get(LAS char* p) {
  asm volatile("" ::: "memory");
  const unsigned v = __hip_atomic_load((LAS unsigned*)p, __ATOMIC_RELAXED, __HIP_MEMORY_SCOPE_WORKGROUP);
  asm volatile("" ::: "memory");
  return __builtin_amdgcn_readfirstlane(v);
}
__device__ __forceinline__ void lds_wait(LAS char* p, int target) {
#pragma unroll 1
  for (int spin = 0; spin < (1 << 22); ++spin) {
    if (lds_get(p) >= target) break;
    __builtin_amdgcn_s_sleep(1);
  }
}
__device__ __forceinline__ int lds_spin(int O, LAS char* p, int target) {
  int r, cnt = 1 << 22;
  unsigned t;
  asm volatile("1:\n\tds_read_b32 %1, %3 offset:%5\n\ts_waitcnt lgkmcnt(0)\n\tv_readfirstlane_b32 %0, %1\n\ts_cmp_ge_i32 %0, %4\n\ts_cbranch_scc1 2f\n\t"
               "s_sub_u32 %2, %2, 1\n\ts_cbranch_scc0 1b\n2:"
               : "=&s"(r), "=&v"(t), "+s"(cnt) : "v"(p), "s"(target), "i"(O) : "memory", "scc");
  return r;
}
__device__ __forceinline__ void lds_need(int O, int& have, LAS char* p, int target) {
  int cnt;
  unsigned t;
  asm volatile("s_cmp_ge_i32 %0, %4\n\ts_cbranch_scc1 2f\n\ts_mov_b32 %2, 0x400000\n"
               "1:\n\tds_read_b32 %1, %3 offset:%5\n\ts_waitcnt lgkmcnt(0)\n\tv_readfirstlane_b32 %0, %1\n\ts_cmp_ge_i32 %0, %4\n\ts_cbranch_scc1 2f\n\t"
               "s_sub_u32 %2, %2, 1\n\ts_cbranch_scc0 1b\n2:"
               : "+s"(have), "=&v"(t), "=&s"(cnt) : "v"(p), "s"(target), "i"(O) : "memory", "scc");
  asm volatile("" : "+s"(have));
}
__device__ __forceinline__ u32x4 pack8(f32x4 a, f32x4 b) { return u32x4{cvtpk(a[0], a[1]), cvtpk(a[2], a[3]), cvtpk(b[0], b[1]), cvtpk(b[2], b[3])}; }
__device__ __forceinline__ unsigned tagf(float x, unsigned t) { return (__float_as_uint(x) & 0xfffff000u) | t; }

__device__ __forceinline__ void stb128(u32x4 v, RSRC r, unsigned o) { __builtin_amdgcn_raw_buffer_store_b128(v, r, o, 0, 16); }
__device__ __forceinline__ void stb64(u32x2 v, RSRC r, unsigned o) { __builtin_amdgcn_raw_buffer_store_b64(v, r, o, 0, 16); }
__device__ __forceinline__ void stb32(unsigned v, RSRC r, unsigned o) { __builtin_amdgcn_raw_buffer_store_b32(v, r, o, 0, 16); }
__device__ __forceinline__ unsigned qk_check(u32x4 x, unsigned tg) {
  const unsigned a = (x[0] ^ __builtin_amdgcn_alignbit(x[1], x[1], 11)) * 0x9e3779b1u + tg;
  const unsigned b = (x[2] + __builtin_amdgcn_alignbit(x[3], x[3], 19)) * 0x85ebca77u;
  return __builtin_amdgcn_alignbit(a, a, 15) ^ b ^ (x[1] + x[3]);
}
__device__ __forceinline__ unsigned add3(unsigned a, unsigned b, unsigned c) {
  return cvtpk((bflo(a) + bflo(b)) + bflo(c), (bfhi(a) + bfhi(b)) + bfhi(c));
}
__global__ __launch_bounds__(512, 2) void dino_main(const u16* q, const u16* f0, const u16* f1, const u16* wq, const u16* wk, const u16* wv,
                                                     const u16* wo, char* ws, u16* out) {
  __shared__ __attribute__((aligned(128))) char smem[LDS_BYTES];
  LAS char* lds = (LAS char*)smem;
  LAS char* ctl = lds + LDS_CTL;
  const unsigned lb = (unsigned)(unsigned long long)lds;
  const int tid = threadIdx.x, lane = tid & 63;
  const int w = __builtin_amdgcn_readfirstlane(tid >> 6);
  const int wg = blockIdx.x;
  const int n = lane & 15, g = lane >> 4;
  const int x = lane >> 3, e = lane & 7;
  const int tq = (lane >> 2) & 3, tp = lane & 3;
  const unsigned long long tstart = wall_clock64();
  unsigned long long tprobe = tstart;
  if (w == 7 && wg >= 56 && wg < 72) {
    const u32x4 rc = mkrs4((const void*)(__builtin_amdgcn_s_getpc() & ~255ull), CODE_BYTES);
#pragma unroll 1
    for (int i = 0; i < CODE_BYTES; i += 4096)
      asm volatile("s_mov_b32 m0, %0\n\ts_nop 0\n\tbuffer_load_dword %1, %2, %3 offen lds" :: "s"(lb + LDS_SINK), "v"(lane * 64), "s"(rc), "s"(i) : "memory");
  }
  if (w == 3 && lane < 48) {
    const unsigned iv = n >= CT_ONE / 8 ? 0x3f803f80u : x == CT_MAX / 64 ? 0xff800000u : 0u;
    *(LAS u32x2*)(ctl + 8 * lane) = u32x2{iv, iv};
  }
  SB();
#define ARGS_IN() asm volatile("s_waitcnt lgkmcnt(0)\n\ts_barrier" :: "s"(q), "s"(f0), "s"(f1), "s"(wq), "s"(wk), "s"(wv), "s"(wo), "s"(ws), "s"(out) : "memory")
#define rws mkrs(ws, WS_BYTES)
  unsigned long long tg;
  const int dq = (wg & 7) * 2 + ((wg >> 3) & 1), dst = (wg & ~15) + dq, dout = dq * C + (wg & ~15);
  unsigned qres = 0u;
  const int NT = 32 - gives(wg) + (wg < 54 || (wg & 1) ? 0 : GIVEN / 101 + (((wg - 54) >> 1) < GIVEN % 101 ? 1 : 0));

  const bool pq = wg < 18 && w != 0;
  const bool pv = w == 7 && wg >= 18 && wg < 54;
  const int pj = wg / 6, pu = wg % 6;
  if (pq) {
    unsigned vo = w >= 4 ? (48 * (w - 4) + x) * 384 + ((e ^ ((x & 2) << 1)) << 4) + pj * 128
                : w >= 2 ? (8 * (w - 2) + x) * 384 + ((e ^ (4 * (w - 2) + (x >> 1))) << 4)
                         : (32 * pu + 8 * (((n >> 1) & 2) | (n >> 3)) + (n & 3)) * 384 + pj * 128 + g * 16;
    unsigned lo = lb + (w >= 4 ? PSCR + (w - 4) * 6144 : w >= 2 ? PSCR + 24576 + (w - 2) * 1024 : LDS_QK + 2048);
    const int cnt = w >= 4 ? 6 : w >= 2 ? 3 : 4, lst = w >= 4 || w < 2 ? 1024 : 2048;
    const int ga = w >= 4 ? 3072 : w >= 2 ? 128 : 64, gb = w < 2 ? 1408 : 0;
    asm volatile("" : "+v"(vo), "+s"(lo));
    ARGS_IN();
    const u32x4 rw = mkrs4(w >= 4 ? wq : w >= 2 ? q : wk, w >= 4 || w < 2 ? C * C * 2 : NQ * C * 2);
#pragma unroll 1
    for (int i = 0; i < cnt; ++i) DMA(lo + i * lst, vo, rw, i * ga + (i >> 1) * gb);
  } else if (w != 0) RAW_BARRIER();
  SB();
  if (__builtin_expect(w == 0, 1)) {
    unsigned v0 = x * 768 + ((e ^ (((x >> 1) & 3) << 1)) << 4) + 1024, v1 = x * 768 + 384 + ((e ^ ((((x >> 1) & 3) << 1) | 1)) << 4);
    unsigned v2 = v0 + W0 * C * 2 - 2048, v3 = v1 + W0 * C * 2 - 2048;
    unsigned y0 = x * 128 + ((e ^ (x >= 6 ? 3 : 0)) << 4), y1 = x * 128 + ((e ^ (x >= 4 ? 4 : 3)) << 4), y2 = x * 128 + ((e ^ (x >= 2 ? 7 : 4)) << 4);
    unsigned b0 = (wg >> 5) * (2 * W0 * C * 2) + (wg & 31) * 6144, b1 = (wg >> 5) * (W1 * C * 2) + (wg & 31) * 3072;
    constexpr unsigned st0 = 16 * W0 * C * 2, st1 = 8 * W1 * C * 2;
    unsigned sb = lb, fo = CT_FREE, fv = 0u, tk = 0u;
    int lap = 0, need = 0;
    u32x4 rt = mkrs4(tabs.seq, 112 * 4);
    unsigned vz = 0u;
    asm volatile("" : "+v"(v0), "+v"(v1), "+v"(v2), "+v"(v3), "+v"(y0), "+v"(y1), "+v"(y2), "+v"(vz), "+s"(b0), "+s"(b1), "+s"(rt));
    ARGS_IN();
    const u32x4 r0 = mkrs4((const char*)f0 - 1024, W0 * W0 * C * 2 + 1024), r1 = mkrs4(f1, W1 * W1 * C * 2);
    __builtin_amdgcn_s_setprio(3);
    if (wg < 18) __builtin_amdgcn_s_sleep(4);
    TS(1); TR(0xd00);
#define UNIT(p_) do { unsigned so_; \
      asm volatile("s_add_u32 m0, %1, %10\n\ts_add_u32 %0, %2, %11\n\t" \
                   "buffer_load_dwordx4 %3, %7, %0 offen nt lds\n\tbuffer_load_dwordx4 %4, %7, %0 offen offset:1024 nt lds\n\t" \
                   "buffer_load_dwordx4 %5, %7, %0 offen offset:2048 nt lds\n\tbuffer_load_dwordx4 %6, %7, %0 offen offset:3072 nt lds\n\t" \
                   "s_mov_b32 m0, %12\n\ts_mov_b64 exec, 1\n\tbuffer_load_dword %13, %8, %9 offen offset:%14 lds\n\ts_mov_b64 exec, -1\n\t" \
                   "s_waitcnt vmcnt(%15)" \
                   : "=&s"(so_) : "s"(sb), "s"(b0), "v"(v0), "v"(v1), "v"(v2), "v"(v3), "s"(r0), "s"(rt), "s"(12 * s), "i"((p_) * PART), "i"((p_) * 128), \
                     "s"(lb + LDS_CTL + CT_FULL - 4 * (p_)), "v"(vz), "i"(4 * (p_)), "i"(6 * DEPTH) : "memory", "scc"); } while (0)
#pragma unroll 1
    for (int s = 0; s < NT; ++s) {
      if (__builtin_expect((int)__builtin_amdgcn_readfirstlane(fv) < need, 0)) {
        lds_wait(ctl + (s == 8 ? CT_PD : fo), need);
      }
      asm volatile("s_add_u32 m0, %0, %6\n\ts_nop 0\n\tbuffer_load_dwordx4 %1, %4, %5 offen nt lds\n\t"
                   "buffer_load_dwordx4 %2, %4, %5 offen offset:1024 nt lds\n\tbuffer_load_dwordx4 %3, %4, %5 offen offset:2048 nt lds"
                   :: "s"(sb), "v"(y0), "v"(y1), "v"(y2), "s"(r1), "s"(b1), "i"(CRS) : "memory", "scc");
      UNIT(0);
      UNIT(1);
      if (fo == CT_FREE + 4 * (NS - 1)) { fo = CT_FREE; lap += 4; } else fo += 4;
      need = s == 7 && wg < 54 ? 1 : lap;
      fv = *(volatile LAS unsigned*)(ctl + (s == 7 ? CT_PD : fo));
      if (s == 2 && NT > 32) tk = tabs.take[(wg - 54) >> 1];
      UNIT(2);
      sb = fo == CT_FREE ? lb : sb + SLOT;
      b0 += st0; b1 += st1;
      if (s >= 31) {
        const unsigned xw = tk & 255u, xs = 31 - ((tk >> 8) & 3u);
        b0 = (xw >> 5) * (2 * W0 * C * 2) + (xw & 31) * 6144 + xs * st0; b1 = (xw >> 5) * (W1 * C * 2) + (xw & 31) * 3072 + xs * st1;
        tk >>= 10;
      }
      if ((s & 1) == 1 || s < 2) TR(0x800 + s);
    }
    TS(2);
    __builtin_amdgcn_s_setprio(0);
    qres = __builtin_amdgcn_raw_buffer_load_b16(mkrs(q, NQ * C * 2), (dout + n) * 2, 0, 0);
    waitvm<0>();
    TS(3); TR(0x900);
    tg = ((unsigned long long)((unsigned)lds_spin(CT_TG + 4, ctl, 1) & 0xffu) << 32) | (unsigned)lds_get(ctl + CT_TG);
  } else {
    if (pq) {
      if (w >= 2) {
        waitvm<0>();
        TP(3);
        lds_add(ctl + CT_PA, lane, 1);
      }
      if (w >= 4) {
        const u32x4 one = {0x3f803f80u, 0x3f803f80u, 0x3f803f80u, 0x3f803f80u};
        f32x4 gq = {0.f, 0.f, 0.f, 0.f}, sq = gq, rr = gq, cs = gq;
        LAS const char* rd = lds + PSCR + 128 * (8 * g + tq) + 16 * (((w & 2) * 2 + tp) ^ ((tq & 2) << 1)) + 8 * (w & 1);
        LAS const char* rq0 = lds + PSCR + 24576 + 128 * n + 16 * (g ^ (n >> 1));
        LAS const char* rq1 = lds + PSCR + 24576 + 128 * n + 16 * ((4 + g) ^ (n >> 1));
        u32x4 qv[6], af[6];
        lds_spin(CT_PA, ctl, 6);
        TS(4);
#pragma unroll
        for (int s = 0; s < 6; ++s) qv[s] = *(LAS const u32x4*)(((s & 1) ? rq1 : rq0) + (s >> 1) * 2048);
#pragma unroll
        for (int s = 0; s < 6; ++s) {
          const u32x2 a = vtr(rd + s * 4096), b = vtr(rd + s * 4096 + 512);
          af[s] = u32x4{a[0], a[1], b[0], b[1]};
        }
        SB();
#pragma unroll
        for (int s = 0; s < 6; ++s) { gq = mfma(qv[s], qv[s], gq); sq = mfma(one, qv[s], sq); }
#pragma unroll
        for (int s = 0; s < 6; ++s) { rr = mfma(af[s], qv[s], rr); cs = mfma(af[s], one, cs); }
        const float mean = sq[0] * (1.f / C);
        const float s2 = xrow16_sum(g == (n >> 2) ? sel4(gq, n & 3) : 0.f);
        const float rstd = __builtin_amdgcn_rsqf(fmaxf(fmaf(-mean, mean, s2 * (1.f / C)), 0.f) + EPS);
        const f32x4 qq = (rr - mean * cs) * rstd;
        *(LAS u32x2*)(lds + LDS_QK + ((w >> 1) & 1) * 1024 + lane * 16 + 8 * (w & 1)) = u32x2{cvtpk(qq[0], qq[1]), cvtpk(qq[2], qq[3])};
        lds_add(ctl + CT_PB, lane, 1);
        TP(7);
      }
    }
    const int pcb = pj - 3, drb = pu;
    u32x4 pan[12], dir[12];
    if (pv) {
      const RSRC rdir = mkrs(wv, C * C * 2), rpan = mkrs(wo, C * C * 2);
      const unsigned fo = ((32 * drb + n) * C + 8 * g) * 2, po = (lane >> 2) * (C * 2) + (lane & 3) * 16 + pcb * 64;
#pragma unroll
      for (int j = 0; j < 12; ++j) pan[j] = ldb128<0>(rpan, po, 16 * j * C * 2);
#pragma unroll
      for (int t = 0; t < 12; ++t) dir[t] = ldb128<0>(rdir, fo, (t >> 1) * 64 + 16 * (t & 1) * C * 2);
    }
    SB();
    TP(1);
    asm volatile("s_load_dwordx2 %0, %1, 0x0\n\ts_waitcnt lgkmcnt(0)" : "=s"(tg) : "s"(ws + EP_OFF) : "memory");
    tg = (tg + 0x40100401ull) & 0xffffffffffull;
    TP(2);
    if (w == 3 && lane == 0) *(LAS u32x2*)(ctl + CT_TG) = u32x2{(unsigned)tg, (unsigned)(tg >> 32) | 0x100u};
    if (pq) {
      if (w == 1) {
        waitvm<0>();
        u32x4 kf4[4];
#pragma unroll
        for (int i = 0; i < 4; ++i) kf4[i] = *(LAS const u32x4*)(lds + LDS_QK + 2048 + i * 1024 + lane * 16);
        lds_spin(CT_PB, ctl, 4);
        TP(8);
        const u32x4 q0 = *(LAS const u32x4*)(lds + LDS_QK + lane * 16), q1 = *(LAS const u32x4*)(lds + LDS_QK + 1024 + lane * 16);
        const f32x4 z = {0.f, 0.f, 0.f, 0.f};
        const f32x4 d0 = mfma(kf4[1], q1, mfma(kf4[0], q0, z)), d1 = mfma(kf4[3], q1, mfma(kf4[2], q0, z));
        const u32x4 xq = pack8(d0 * QSCALE, d1 * QSCALE);
        const unsigned ck = qk_check(xq, (unsigned)tg);
        asm volatile("" :: "v"(ck));
        TP(9);
        const unsigned so = QK_OFF + wg * QK_REC;
#pragma unroll
        for (int cp = 0; cp < 4; ++cp) {
          stb128(xq, rws, lane * 16 + so + cp * QK_COPY);
          stb32(ck, rws, lane * 4 + so + 1024 + cp * QK_COPY);
        }
        lds_add(ctl + CT_PD, lane, 1);
        TS(5);
      }
    }
    if (pv) {
      LAS char* cvw = lds + PSCR + lane * 16;
      LAS const char* cvr = lds + PSCR + 64 * (8 * g + tq) + 8 * tp;
#pragma unroll
      for (int j = 0; j < 12; ++j) *(LAS u32x4*)(cvw + j * 1024) = pan[j];
      WAVE_SYNC();
      f32x4 d[2][2];
#pragma unroll
      for (int i = 0; i < 4; ++i) d[i >> 1][i & 1] = f32x4{0.f, 0.f, 0.f, 0.f};
#pragma unroll
      for (int s = 0; s < 6; ++s) {
        const u32x2 a = vtr(cvr + s * 2048), b = vtr(cvr + s * 2048 + 256), c = vtr(cvr + s * 2048 + 32), dd = vtr(cvr + s * 2048 + 288);
        const u32x4 p0 = {a[0], a[1], b[0], b[1]}, p1 = {c[0], c[1], dd[0], dd[1]};
#pragma unroll
        for (int hf = 0; hf < 2; ++hf) { d[0][hf] = mfma(dir[2 * s + hf], p0, d[0][hf]); d[1][hf] = mfma(dir[2 * s + hf], p1, d[1][hf]); }
      }
      lds_add(ctl + CT_PD, lane, 1);
      const int so = WVO_OFF + (pcb * 12 + 2 * drb) * 1024;
      stb128(pack8(d[0][0], d[0][1]), rws, lane * 16 + so);
      stb128(pack8(d[1][0], d[1][1]), rws, lane * 16 + so + 1024);
      waitvm<0>();
      if (lane < 8) stb64(u32x2{(unsigned)tg, (unsigned)(tg >> 32)}, rws, FLAG_OFF + lane * 512 + (wg - 18) * 8);
    }
    const f32x4 z = {0.f, 0.f, 0.f, 0.f};
    int full = 0;
#define NEED(u_) lds_need(CT_FULL, full, ctl, (u_))
    if (w >= 4) {
      const int brg = ((g & 1) << 1) | (g >> 1);
      const int fn = (((n >> 1) & 3) << 1) | ((n >> 3) & 1);
      const int fy = ((n >> 2) & 1) * 4 + ((n >> 1) & 1) * 3;
      const int orr0 = 128 * n + 16 * (brg ^ fn), orr1 = 128 * n + 16 * ((4 + brg) ^ fn);
      const int ory0 = n < 8 ? CRS + 384 * n + 16 * (brg ^ fy) : LDS_CTL + CT_ONE, ory1 = n < 8 ? CRS + 384 * n + 16 * ((4 + brg) ^ fy) : LDS_CTL + CT_ONE;
      const int yon = n < 8 ? 1 : 0;
      const int opk = LDS_PK + 320 * g + 8 * n, ork = LDS_PK + 320 * (2 * (g >> 1) + (n >> 3)) + 256 + 8 * (n & 7) + 4 * (g & 1);
      u32x4 qk[6];
      f32x4 ref, qs;
      LAS const char *px0, *px1, *py0, *py1;
#define AT_SLOT(o_) do { px0 = lds + orr0 + (o_); px1 = lds + orr1 + (o_); py0 = lds + ory0 + __mul24(yon, (o_)); py1 = lds + ory1 + __mul24(yon, (o_)); \
      asm volatile("" : "+v"(px0), "+v"(px1), "+v"(py0), "+v"(py1)); } while (0)
#define RD_K(k_, i_) do { LAS const char* p_ = (((k_) & 1) ? px1 : px0) + ((k_) >> 1) * PART; \
    yv[i_] = *(LAS const u32x4*)((((k_) & 1) ? py1 : py0) + ((k_) >> 1) * 128); \
    xa[i_] = *(LAS const u32x4*)(p_); xb[i_] = *(LAS const u32x4*)(p_ + 2048); } while (0)
#define MF_STAT(i_) do { cy = mfma(yv[i_], yv[i_], cy); ca = mfma(yv[i_], xa[i_], ca); cb = mfma(yv[i_], xb[i_], cb); \
    ga = mfma(xa[i_], xa[i_], ga); gb = mfma(xb[i_], xb[i_], gb); } while (0)
#define MF_S(k_, i_) do { sy = mfma(qk[k_], yv[i_], sy); sa = mfma(qk[k_], xa[i_], sa); sb = mfma(qk[k_], xb[i_], sb); } while (0)
#define STATS(st, rec) do { \
      const int r = n & 3; \
      const float yy = half_copy<true>(sel4(cy, r)); \
      const f32x2 Y = swap32(yy, yy), X = swap32(sel4(ca, r), sel4(cb, r)), G = swap32(sel4(ga, r), sel4(gb, r)); \
      const float q1_ = fmaf(2.f, X[0], Y[0]) + (n < 8 ? G[0] : G[1]);       \
      const f32x2 Q = swap16(q1_, q1_); \
      const float mn_ = (X[1] + Y[1]) * (1.f / C); \
      const float var = fmaxf(fmaf(-mn_, mn_, fmaf((n & 4) ? Q[1] : Q[0], 1.f / C, EPS)), EPS), rs_ = __builtin_amdgcn_rsqf(var); \
      const f32x2 R = swap32(rs_, rs_), Mn = swap32(mn_, mn_); \
      st = f32x4{R[0], Mn[0], R[1], Mn[1]}; \
      const float v_ = (g & 1) ? var * rs_ : mn_, h_ = bfhi(__float_as_uint(v_)); \
      rec = cvtpk(h_, v_ - h_); \
    } while (0)
#define LOGITS(sv, st, base_) do { \
      _Pragma("unroll") for (int r = 0; r < 4; ++r) { \
        const float s0 = half_copy<true>(sy[r]); \
        sv[r] = fmaf(fmaf(-st[1], qs[r], sa[r] + s0), st[0], -(base_)[r]); \
        sv[4 + r] = fmaf(fmaf(-st[3], qs[r], sb[r] + s0), st[2], -(base_)[r]); \
      } \
    } while (0)
#define SV_MAX(sv) fmaxf(fmaxf(fmaxf(sv[0], sv[1]), fmaxf(sv[2], sv[3])), fmaxf(fmaxf(sv[4], sv[5]), fmaxf(sv[6], sv[7])))
#define PUBLISH(sv, st, rec, pk_, word_) do { \
      *(LAS unsigned*)(lds + ork + (pk_) * PK_B) = rec; \
      *(LAS u32x2*)(lds + opk + (pk_) * PK_B) = u32x2{cvtpk(sv[0] * st[0], sv[1] * st[0]), cvtpk(sv[2] * st[0], sv[3] * st[0])}; \
      *(LAS u32x2*)(lds + opk + (pk_) * PK_B + 128) = u32x2{cvtpk(sv[4] * st[2], sv[5] * st[2]), cvtpk(sv[6] * st[2], sv[7] * st[2])}; \
      lds_put(CT_PRDY, ctl + 4 * (pk_), (word_)); \
    } while (0)
      int s = (w + NT - 1) & 3, slot = s + 4, pk = s + 4, ncon = 0;
      {
        u32x4 yv[12], xa[12], xb[12];
        f32x4 st0, st1;
        unsigned rec0, rec1;
#pragma unroll
        for (int h = 0; h < 2; ++h) {
          f32x4 cy = z, ga = z, ca = z, gb = z, cb = z;
          AT_SLOT((s + 4 * h) * SLOT);
          lds_wait(ctl + CT_FULL, 3 * (s + 4 * h) + 3);
          TR(0xe02);
#pragma unroll
          for (int k = 0; k < 6; ++k) RD_K(k, 6 * h + k);
          lds_inc(CT_FREE + 16 * h, ctl + 4 * s);
          SB();
#pragma unroll
          for (int k = 0; k < 6; ++k) MF_STAT(6 * h + k);
          SB();
          if (h == 0) { STATS(st0, rec0); asm volatile("" : "+v"(st0), "+v"(rec0)); } else { STATS(st1, rec1); asm volatile("" : "+v"(st1), "+v"(rec1)); }
          SB();
          TS(1); TR(0x100);
        }
        lds_wait(ctl + CT_QK, 3);
        TS(6); TR(0x200);
#pragma unroll
        for (int k = 0; k < 6; ++k) qk[k] = *(LAS const u32x4*)(lds + LDS_QK + k * 1024 + lane * 16);
        float s0v[8], s1v[8];
        {
          f32x4 sy = z, sa = z, sb = z;
#pragma unroll
          for (int k = 0; k < 6; ++k) MF_S(k, k);
#pragma unroll
          for (int r = 0; r < 4; ++r) qs[r] = half_copy<false>(sy[r]);
          LOGITS(s0v, st0, z);
        }
        {
          f32x4 sy = z, sa = z, sb = z;
#pragma unroll
          for (int k = 0; k < 6; ++k) MF_S(k, 6 + k);
          LOGITS(s1v, st1, z);
        }
#pragma unroll
        for (int r = 0; r < 4; ++r) ref[r] = row_max(fmaxf(fmaxf(s0v[r], s0v[4 + r]), fmaxf(s1v[r], s1v[4 + r])));
        if (n < 4) __builtin_amdgcn_ds_fmaxf((LAS float*)(ctl + CT_MAX + 16 * g + 4 * n), sel4(ref, n), __ATOMIC_RELAXED, __HIP_MEMORY_SCOPE_WORKGROUP, false);
        lds_inc(CT_QR, ctl);
        lds_spin(CT_QR, ctl, 4);
        ref = *(LAS const f32x4*)(ctl + CT_MAX + 16 * g);
#pragma unroll
        for (int j = 0; j < 8; ++j) s0v[j] = __builtin_amdgcn_exp2f(s0v[j] - ref[j & 3]);
        PUBLISH(s0v, st0, rec0, s, 2 * s + 2);
#pragma unroll
        for (int j = 0; j < 8; ++j) s1v[j] = __builtin_amdgcn_exp2f(s1v[j] - ref[j & 3]);
        if (pk >= NPK) { pk -= NPK; ncon = 3; lds_spin(CT_PCON, ctl + 4 * pk, 3); }
        s += 4;
        PUBLISH(s1v, st1, rec1, pk, 2 * s + 2);
        TS(7); TR(0x300);
      }
      int moved = 0;
#pragma unroll 1
      for (;;) {
        s += 4; slot += 4; slot -= slot >= NS ? NS : 0; pk += 4;
        if (pk >= NPK) { pk -= NPK; ncon += 3; }
        if (s >= NT) break;
        f32x4 sy = z, cy = z, sa = z, ga = z, ca = z, sb = z, gb = z, cb = z, st;
        u32x4 yv[6], xa[6], xb[6];
        float sv[8];
        unsigned rec;
        AT_SLOT(slot * SLOT);
#define RD_PART(p_) do { int cnt_; unsigned t_; \
        asm volatile("s_mov_b32 %1, 0x400000\n1:\n\tds_read_b32 %2, %9 offset:%15\n\t" \
                     "ds_read_b128 %3, %12 offset:%16\n\tds_read_b128 %4, %10 offset:%17\n\tds_read_b128 %5, %10 offset:%18\n\t" \
                     "ds_read_b128 %6, %13 offset:%16\n\tds_read_b128 %7, %11 offset:%17\n\tds_read_b128 %8, %11 offset:%18\n\t" \
                     "s_cmp_ge_i32 %0, %14\n\ts_cbranch_scc1 2f\n\ts_waitcnt lgkmcnt(6)\n\tv_readfirstlane_b32 %0, %2\n\t" \
                     "s_cmp_ge_i32 %0, %14\n\ts_cbranch_scc1 2f\n\ts_sub_u32 %1, %1, 1\n\ts_cbranch_scc0 1b\n2:" \
                     : "+s"(full), "=&s"(cnt_), "=&v"(t_), "=&v"(yv[2 * (p_)]), "=&v"(xa[2 * (p_)]), "=&v"(xb[2 * (p_)]), \
                       "=&v"(yv[2 * (p_) + 1]), "=&v"(xa[2 * (p_) + 1]), "=&v"(xb[2 * (p_) + 1]) \
                     : "v"(ctl), "v"(px0), "v"(px1), "v"(py0), "v"(py1), "s"(3 * s + (p_) + 1), "i"(CT_FULL), "i"(128 * (p_)), "i"(PART * (p_)), \
                       "i"(PART * (p_) + 2048) : "memory", "scc"); keep = t_; \
        asm volatile("" : "+s"(full)); } while (0)
#define GOT(k_, n_) do { waitlds<n_>(); asm volatile("" : "+v"(yv[k_]), "+v"(xa[k_]), "+v"(xb[k_]) : "v"(keep)); } while (0)
        unsigned keep;
        const unsigned pcv = *(volatile LAS unsigned*)(ctl + CT_PCON + 4 * pk);
        NEED(3 * s);
        RD_PART(0);
        TR(0x400 + full);
        GOT(0, 3);
        int con = __builtin_amdgcn_readfirstlane(pcv);
        SB();
        MF_STAT(0); MF_S(0, 0);
        SB();
        lds_need(CT_PCON, con, ctl + 4 * pk, ncon);
        RD_PART(1);
        GOT(1, 7);
        SB();
        MF_STAT(1); MF_S(1, 1);
        SB();
        GOT(2, 3);
        MF_STAT(2); MF_S(2, 2);
        SB();
        RD_PART(2);
        TR(0x700 + full);
        GOT(3, 7);
        SB();
        MF_STAT(3); MF_S(3, 3);
        SB();
        GOT(4, 3);
        lds_inc(CT_FREE, ctl + 4 * slot);
        MF_STAT(4);
        SB();
        GOT(5, 1);
        MF_STAT(5);
        SB();
        MF_S(4, 4); MF_S(5, 5);
        STATS(st, rec);
        LOGITS(sv, st, ref);
#pragma unroll
        for (int j = 0; j < 6; ++j) { __builtin_amdgcn_sched_group_barrier(0x008, 1, 0); __builtin_amdgcn_sched_group_barrier(0x002, 4, 0); }
        if (__builtin_expect(__any(!(SV_MAX(sv) <= 16.f)), 0)) {
#pragma unroll
          for (int r = 0; r < 4; ++r) {
            const float mx = row_max(fmaxf(sv[r], sv[4 + r])), dm = mx <= 16.f ? 0.f : mx;
            ref[r] += dm; sv[r] -= dm; sv[4 + r] -= dm;
          }
          moved = 1;
        }
        if (__builtin_expect(moved, 0)) {
          if (n == 0) *(LAS f32x4*)(lds + LDS_PK + pk * PK_B + 1280 + 16 * g) = ref;
        }
#pragma unroll
        for (int j = 0; j < 8; ++j) sv[j] = __builtin_amdgcn_exp2f(sv[j]);
        PUBLISH(sv, st, rec, pk, 2 * s + 2 + moved);
        TR(0x500 + s);
      }
      TS(8);
    } else {
      const int ftr = ((g & 1) << 2) | ((tq >> 1) << 1) | (g >> 1);
      const int fty = (g & 1) * 4 + (tq >> 1) * 3;
      int otr[4], oty[4];
#pragma unroll
      for (int t = 0; t < 4; ++t) {
        otr[t] = (w - 1) * PART + 128 * (4 * g + tq) + 16 * ((2 * t + (tp >> 1)) ^ ftr) + 8 * (tp & 1);
        oty[t] = CRS + (w - 1) * 128 + 384 * (4 * (g & 1) + tq) + 16 * ((2 * t + (tp >> 1)) ^ fty) + 8 * (tp & 1);
      }
      int opk = LDS_PK + 320 * tp + 8 * (4 * g + tq), ork = LDS_PK + 320 * (g >> 1) + 256 + 8 * (4 * (g & 1) + tq);
      asm volatile("" : "+v"(opk), "+v"(ork), "+v"(oty[0]), "+v"(oty[1]), "+v"(oty[2]), "+v"(oty[3]));
#define RD_T(ta, ty, so_) _Pragma("unroll") for (int t_ = 0; t_ < 4; ++t_) { \
    ta[t_] = vtr2(lds + otr[t_] + (so_), lds + otr[t_] + (so_) + 2048); ty[t_] = vtr(lds + oty[t_] + (so_)); }
      f32x4 acc[4] = {z, z, z, z}, tac = z;
#define RD_P(o_) \
      pf = vtr2(lds + opk + (o_), lds + opk + (o_) + 128); \
      sf = vtr2(lds + ork + (o_), lds + ork + (o_) + 640)
#define TWICE(ty) asm volatile("s_nop 2"); _Pragma("unroll") for (int t = 0; t < 4; ++t) { u32x2 lo_, hi_; \
      asm volatile("v_mov_b64 %0, %2\n\tv_mov_b64 %1, %2" : "=&v"(lo_), "=&v"(hi_) : "v"(ty[t])); t2[t] = u32x4{lo_[0], lo_[1], hi_[0], hi_[1]}; } \
      asm volatile("s_nop 1" : "+v"(t2[0]), "+v"(t2[1]), "+v"(t2[2]), "+v"(t2[3]))
#define MF_PX(ta) \
      tac = mfma(sf, pf, tac); \
      _Pragma("unroll") for (int t = 0; t < 4; ++t) acc[t] = mfma(ta[t], pf, acc[t])
#define MF_PY() \
      _Pragma("unroll") for (int t = 0; t < 4; ++t) acc[t] = mfma(t2[t], pf, acc[t])
#define MOVE_REF(word_, o_) do { moved |= (word_) & 1; if (__builtin_expect(moved, 0)) { \
            const float m_ = *(LAS const float*)(lds + (((word_) & 1) ? LDS_PK + (o_) + 1280 : LDS_CTL + CT_MAX) + 4 * n); \
            Mtop = fmaxf(Mtop, m_); \
            const bool drop = m_ < Mtop - 64.f; \
            const float mn = drop ? M : m_, fa = __builtin_amdgcn_exp2f(M - mn); \
            M = mn; \
            _Pragma("unroll") for (int t = 0; t < 4; ++t) acc[t] *= fa; \
            tac *= fa; \
            if (drop) pf = u32x4{0u, 0u, 0u, 0u}; \
          } } while (0)
      const unsigned flo = FLAG_OFF + (wg & 7) * 512 + (lane < 36 ? lane : 35) * 8;
      float M, Mtop;
      int moved = 0;
      u32x4 wvv[24], pf, sf, t2[4];
      {
        u32x4 ta[8][4], xx[6];
        u32x2 ty[8][4], fl;
        unsigned cc[6];
        const int qo = QK_OFF + (wg & 3) * QK_COPY + (2 * w - 2) * QK_REC;
#define QK_LOAD() _Pragma("unroll") for (int j = 0; j < 6; ++j) { \
          xx[j] = ldb128<16>(rws, lane * 16, qo + ((j >> 1) * 6 + (j & 1)) * QK_REC); \
          cc[j] = __builtin_amdgcn_raw_buffer_load_b32(rws, lane * 4, qo + ((j >> 1) * 6 + (j & 1)) * QK_REC + 1024, 16); }
#pragma unroll
        for (int u = 0; u < 8; ++u) {
          if (u == QKAT) {
#pragma unroll 1
            for (int spin = 0; spin < 64; ++spin) {
              if ((long long)(wall_clock64() - tstart) >= GATE) break;
              __builtin_amdgcn_s_sleep(2);
            }
            if (pq) lds_wait(ctl + CT_PD, 1);
            QK_LOAD();
#pragma unroll 1
            for (int spin = 0; spin < (1 << 18); ++spin) {
              unsigned df = 0u;
#pragma unroll
              for (int j = 0; j < 6; ++j) df |= qk_check(xx[j], (unsigned)tg) ^ cc[j];
              if (__all(df == 0u)) break;
              TS(14);
              asm volatile("" ::: "memory");
              QK_LOAD();
            }
#pragma unroll
            for (int h = 0; h < 2; ++h) {
              u32x4 sx;
#pragma unroll
              for (int i = 0; i < 4; ++i) sx[i] = add3(xx[h][i], xx[2 + h][i], xx[4 + h][i]);
              *(LAS u32x4*)(lds + LDS_QK + (2 * w - 2 + h) * 1024 + lane * 16) = sx;
            }
            lds_inc(CT_QK, ctl);
            SB();
            TR(0x200);
          }
          lds_wait(ctl + CT_FULL, 3 * u + w);
          RD_T(ta[u], ty[u], u * SLOT);
          lds_inc(CT_FREE + 4 * u, ctl);
          SB();
        }
        TR(0x100);
        fl = __builtin_bit_cast(u32x2, __builtin_amdgcn_raw_buffer_load_b64(rws, flo, 0, 16));
#pragma unroll
        for (int u = 0; u < 12; ++u) {
          const int word = lds_spin(CT_PRDY + 4 * (u % NPK), ctl, 2 * u + 2);
          if (u == 0) Mtop = M = *(LAS const float*)(ctl + CT_MAX + 4 * n);
          RD_P((u % NPK) * PK_B);
          if (u >= 8) MOVE_REF(word, (u % NPK) * PK_B);
          lds_inc(CT_PCON + 4 * (u % NPK), ctl);
          SB();
          MF_PX(ta[u & 7]);
          SB();
          TWICE(ty[u & 7]);
          MF_PY();
          asm volatile("" : "+v"(acc[0]), "+v"(acc[1]), "+v"(acc[2]), "+v"(acc[3]), "+v"(tac));
          SB();
          if (u < 4) {
            NEED(3 * (u + 8) + w);
            RD_T(ta[u], ty[u], ((u + 8) % NS) * SLOT);
            lds_inc(CT_FREE + 4 * ((u + 8) % NS), ctl);
            SB();
          }
        }
        TR(0x300);
#pragma unroll 1
        for (int spin = 0; spin < (1 << 18); ++spin) {
          if (__all(fl[0] == (unsigned)tg && fl[1] == (unsigned)(tg >> 32))) break;
          asm volatile("" ::: "memory");
          fl = __builtin_bit_cast(u32x2, __builtin_amdgcn_raw_buffer_load_b64(rws, flo, 0, 16));
        }
        asm volatile("" ::: "memory");
      }
      int slot = 3, pk = 12 % NPK;
      unsigned rdy = 0u;
      const int kro[3] = {(w - 1) * 4096, (w == 3 ? 0 : w) * 4096, (w == 1 ? 2 : w - 2) * 4096};
#define RD_PACKET() do { int cnt_; u32x2 p0_, p1_, p2_, p3_; \
        word = __builtin_amdgcn_readfirstlane(rdy); \
        asm volatile("s_mov_b32 %1, 0x400000\n1:\n\tds_read_b32 %2, %7 offset:%11\n\t" \
                     "ds_read_b64_tr_b16 %5, %9\n\tds_read_b64_tr_b16 %6, %9 offset:640\n\t" \
                     "ds_read_b64_tr_b16 %3, %8\n\tds_read_b64_tr_b16 %4, %8 offset:128\n\t" \
                     "s_cmp_ge_i32 %0, %10\n\ts_cbranch_scc1 2f\n\ts_waitcnt lgkmcnt(4)\n\tv_readfirstlane_b32 %0, %2\n\t" \
                     "s_cmp_ge_i32 %0, %10\n\ts_cbranch_scc1 2f\n\ts_sub_u32 %1, %1, 1\n\ts_cbranch_scc0 1b\n2:" \
                     : "+s"(word), "=&s"(cnt_), "=&v"(rdy), "=&v"(p0_), "=&v"(p1_), "=&v"(p2_), "=&v"(p3_) \
                     : "v"(ctl + 4 * pk), "v"(lds + opk + pk * PK_B), "v"(lds + ork + pk * PK_B), "s"(2 * s + 2), "i"(CT_PRDY) : "memory", "scc"); \
        asm volatile("" : "+s"(word)); \
        pf = u32x4{p0_[0], p0_[1], p1_[0], p1_[1]}; sf = u32x4{p2_[0], p2_[1], p3_[0], p3_[1]}; } while (0)
#define GOT_PACKET() do { waitlds<0>(); asm volatile("" : "+v"(pf), "+v"(sf) : "v"(rdy)); MOVE_REF(word, pk * PK_B); } while (0)
#define RD_Y(t_, p_) do { const u32x2 lo_ = vtr(p_); asm volatile("" ::: "memory"); const u32x2 hi_ = vtr(p_); \
        t2[t_] = u32x4{lo_[0], lo_[1], hi_[0], hi_[1]}; } while (0)
      u32x4 ta[4];
      int s = 12, word;
      NEED(36 + w);
#pragma unroll
      for (int t = 0; t < 4; ++t) { ta[t] = vtr2(lds + otr[t] + 2 * SLOT, lds + otr[t] + 2 * SLOT + 2048); RD_Y(t, lds + oty[t] + 2 * SLOT); }
      lds_inc(CT_FREE + 8, ctl);
#pragma unroll 1
      for (; s < NT - 1; ++s) {
        RD_PACKET();
        LAS const char *px[4], *py[4];
#pragma unroll
        for (int t = 0; t < 4; ++t) { px[t] = lds + otr[t] + slot * SLOT; py[t] = lds + oty[t] + slot * SLOT; asm volatile("" : "+v"(px[t]), "+v"(py[t])); }
        NEED(3 * s + 3 + w);
        GOT_PACKET();
        TR(0x400 + s);
        SB();
        tac = mfma(sf, pf, tac);
        asm volatile("" : "+v"(tac));
        SB();
        lds_inc(CT_PCON, ctl + 4 * pk);
        pk = pk == NPK - 1 ? 0 : pk + 1;
#pragma unroll
        for (int t = 0; t < 4; ++t) { acc[t] = mfma(ta[t], pf, acc[t]); SB(); ta[t] = vtr2(px[t], px[t] + 2048); SB(); }
        rdy = *(volatile LAS unsigned*)(ctl + CT_PRDY + 4 * pk);
#pragma unroll
        for (int t = 0; t < 4; ++t) { acc[t] = mfma(t2[t], pf, acc[t]); SB(); RD_Y(t, py[t]); SB(); }
        lds_inc(CT_FREE, ctl + 4 * slot);
        if (s < 12 + 24 / WVN) {
#pragma unroll
          for (int u = 0; u < 24 / WVN; ++u)
            if (s - 12 == u) {
#pragma unroll
              for (int j = WVN * u; j < WVN * u + WVN; ++j)
                wvv[j] = ldb128<16>(rws, lane * 16 + (j & 3) * 1024, WVO_OFF + (w - 1) * 24576 + (j / 12) * 12288 + kro[(j % 12) >> 2]);
            }
        }
        TR(0x500 + s);
        slot = slot == NS - 1 ? 0 : slot + 1;
      }
      LAS char* xc = lds + slot * SLOT + lane * 16;
      LAS char* xw = xc + (kro[0] >> 1);
      LAS const char *xr1 = xc + (kro[1] >> 1), *xr2 = xc + (kro[2] >> 1);
      unsigned mo = (wg * NDST + 64 * (w - 1) + n) * 64 + g * 16;
      asm volatile("" : "+v"(xw), "+v"(xr1), "+v"(xr2), "+v"(mo));
      waitvm<0>();
      RD_PACKET();
      GOT_PACKET();
      TR(0x400 + s);
      SB();
      tac = mfma(sf, pf, tac);
      asm volatile("" : "+v"(tac));
      SB();
#pragma unroll
      for (int t = 0; t < 2; ++t) acc[t] = mfma(t2[t], pf, mfma(ta[t], pf, acc[t]));
      SB();
#pragma unroll
      for (int t = 2; t < 4; ++t) acc[t] = mfma(t2[t], pf, mfma(ta[t], pf, acc[t]));
      const float L = tac[2] + tac[3], rl = __builtin_amdgcn_rcpf(L), ct = -(tac[0] + tac[1]) * rl;
      u32x4 b[6];
      b[0] = pack8(acc[0] * rl + ct, acc[1] * rl + ct);
      *(LAS u32x4*)xw = b[0];
#pragma unroll
      for (int j = 0; j < 4; ++j) { __builtin_amdgcn_sched_group_barrier(0x008, 1, 0); __builtin_amdgcn_sched_group_barrier(0x002, 2, 0); }
      SB();
      TS(15); TR(0x600);
      b[1] = pack8(acc[2] * rl + ct, acc[3] * rl + ct);
      *(LAS u32x4*)(xw + 1024) = b[1];
      lds_inc(CT_XD, ctl);
      SB();
      f32x4 o[4];
#pragma unroll
      for (int t = 0; t < 4; ++t) o[t] = mfma(wvv[12 * (t >> 1) + (t & 1)], b[0], z);
      const float lse = M + __builtin_amdgcn_logf(L);
      unsigned lb8 = __float_as_uint(lse) >> (8 * g), xd;
      asm volatile("" : "+v"(lb8), "+v"(o[0]), "+v"(o[1]), "+v"(o[2]), "+v"(o[3]));
      SB();
#define RD_SLICES "ds_read_b32 %0, %7 offset:%10\n\tds_read_b128 %1, %8\n\tds_read_b128 %2, %8 offset:1024\n\tds_read_b128 %3, %9\n\tds_read_b128 %4, %9 offset:1024\n"
      asm volatile(RD_SLICES : "=&v"(xd), "=&v"(b[2]), "=&v"(b[3]), "=&v"(b[4]), "=&v"(b[5]) : "i"(0), "i"(0), "v"(ctl), "v"(xr1), "v"(xr2), "i"(CT_XD) : "memory");
      SB();
#pragma unroll
      for (int t = 0; t < 4; ++t) o[t] = mfma(wvv[12 * (t >> 1) + 2 + (t & 1)], b[1], o[t]);
      unsigned tt[4];
#pragma unroll
      for (int r = 0; r < 4; ++r) tt[r] = (((lb8 >> (2 * r)) & 3u) << 10) | ((unsigned)(tg >> (10 * r)) & 1023u);
      asm volatile("" : "+v"(tt[0]), "+v"(tt[1]), "+v"(tt[2]), "+v"(tt[3]), "+v"(o[0]), "+v"(o[1]), "+v"(o[2]), "+v"(o[3]));
      SB();
      {
        int cnt_, c_;
        asm volatile("s_mov_b32 %6, 0x400000\n\ts_branch 2f\n1:\n\t" RD_SLICES "2:\n\ts_waitcnt lgkmcnt(4)\n\tv_readfirstlane_b32 %5, %0\n\t"
                     "s_cmp_ge_i32 %5, 3\n\ts_cbranch_scc1 3f\n\ts_sub_u32 %6, %6, 1\n\ts_cbranch_scc0 1b\n3:\n\ts_waitcnt lgkmcnt(0)"
                     : "+v"(xd), "+v"(b[2]), "+v"(b[3]), "+v"(b[4]), "+v"(b[5]), "=&s"(c_), "=&s"(cnt_)
                     : "v"(ctl), "v"(xr1), "v"(xr2), "i"(CT_XD) : "memory", "scc");
      }
      SB();
#define MAIL(t_) stb128(u32x4{tagf(o[t_][0], tt[0]), tagf(o[t_][1], tt[1]), tagf(o[t_][2], tt[2]), tagf(o[t_][3], tt[3])}, rws, mo + (t_) * 1024)
#pragma unroll
      for (int k = 2; k < 6; ++k)
#pragma unroll
        for (int t = 0; t < 2; ++t) o[t] = mfma(wvv[2 * k + t], b[k], o[t]);
      SB();
#pragma unroll
      for (int k = 2; k < 6; ++k) o[2] = mfma(wvv[12 + 2 * k], b[k], o[2]);
      SB();
#pragma unroll
      for (int k = 2; k < 6; ++k) o[3] = mfma(wvv[13 + 2 * k], b[k], o[3]);
      MAIL(0); MAIL(1);
#pragma unroll
      for (int j = 0; j < 4; ++j) {
        __builtin_amdgcn_sched_group_barrier(0x008, 1, 1); __builtin_amdgcn_sched_group_barrier(0x002, 2, 1);
        if (j & 1) __builtin_amdgcn_sched_group_barrier(0x040, 1, 1);
      }
      SB();
      MAIL(2); MAIL(3);
      TS(10);
      if (w == 3 && n == dq && g == 0) *(LAS float*)(ctl + CT_REF) = lse;
      lds_inc(CT_ST, ctl);
    }
  }
  TS(11); TR(0xa00);

  if (wg < NDST) {
    const unsigned t0 = (unsigned)tg & 1023u, t1 = (unsigned)(tg >> 10) & 1023u, t2 = (unsigned)(tg >> 20) & 1023u, t3 = (unsigned)(tg >> 30) & 1023u;
    lds_spin(CT_ST, ctl, 3);
    float ref = *(LAS const float*)(ctl + CT_REF);
    LAS char* fin = lds + (NT >= 30 ? NT - 30 : NT - 20) * SLOT + 8192;
    u32x4 A = {0u, 0u, 0u, 0u}, B = A;
    {
      const unsigned ao = (tid >> 2) * (NDST * 64) + (tid & 3) * 16;
      bool oka = false, okb = false;
#pragma unroll 1
      for (int spin = 0; spin < (1 << 18); ++spin) {
        if (!oka) A = ldb128<16>(rws, ao, dst * 64);
        if (!okb) B = ldb128<16>(rws, ao, dst * 64 + 128 * NDST * 64);
        oka = (((A[0] ^ t0) | (A[1] ^ t1) | (A[2] ^ t2) | (A[3] ^ t3)) & 1023u) == 0u;
        okb = (((B[0] ^ t0) | (B[1] ^ t1) | (B[2] ^ t2) | (B[3] ^ t3)) & 1023u) == 0u;
        if (__all(oka && okb)) break;
        asm volatile("" ::: "memory");
      }
    }
    TS(12); TR(0xb00);
    {
      unsigned la = (((A[0] >> 10) & 3u) | ((A[1] >> 8) & 12u) | ((A[2] >> 6) & 48u) | ((A[3] >> 4) & 192u)) << (8 * (lane & 3));
      unsigned lb_ = (((B[0] >> 10) & 3u) | ((B[1] >> 8) & 12u) | ((B[2] >> 6) & 48u) | ((B[3] >> 4) & 192u)) << (8 * (lane & 3));
      la |= __builtin_amdgcn_mov_dpp(la, 0xB1, 0xf, 0xf, true); lb_ |= __builtin_amdgcn_mov_dpp(lb_, 0xB1, 0xf, 0xf, true);
      la |= __builtin_amdgcn_mov_dpp(la, 0x4E, 0xf, 0xf, true); lb_ |= __builtin_amdgcn_mov_dpp(lb_, 0x4E, 0xf, 0xf, true);
      const float Ma = __uint_as_float(la), Mb = __uint_as_float(lb_);
      float M = fmaxf(Ma, Mb);
      if (!__all(M - ref <= 64.f)) {
        M = fmaxf(M, dpp<ROR4>(M)); M = fmaxf(M, dpp<XOR8>(M));
        ref = xrow16_max(M);
      }
      const float wa = __builtin_amdgcn_exp2f(Ma - ref), wb = __builtin_amdgcn_exp2f(Mb - ref);
      f32x4 v;
#pragma unroll
      for (int k = 0; k < 4; ++k) {
        float s = wa * __uint_as_float(A[k] & 0xfffff000u) + wb * __uint_as_float(B[k] & 0xfffff000u);
        s += dpp<ROR4>(s); s += dpp<XOR8>(s);
        v[k] = xrow16_sum(s);
      }
      float l = wa + wb;
      l += dpp<ROR4>(l); l += dpp<XOR8>(l);
      l = xrow16_sum(l);
      if (lane < 4) *(LAS f32x4*)(fin + w * 256 + lane * 16) = v;
      if (lane == 4) *(LAS f32x2*)(fin + w * 256 + 64) = f32x2{ref, l};
    }
    RAW_BARRIER();
    if (tid < 16) {
      f32x2 ml[NW];
      float av[NW], MM = -INFINITY;
#pragma unroll
      for (int p = 0; p < NW; ++p) { ml[p] = *(LAS f32x2*)(fin + p * 256 + 64); av[p] = *(LAS float*)(fin + p * 256 + tid * 4); MM = fmaxf(MM, ml[p][0]); }
      float mm = ml[0][0];
#pragma unroll
      for (int p = 1; p < NW; ++p) mm = fminf(mm, ml[p][0]);
      if (__builtin_expect(__any(mm != MM), 0)) {
#pragma unroll
        for (int p = 0; p < NW; ++p) { const float wt = __builtin_amdgcn_exp2f(ml[p][0] - MM); av[p] *= wt; ml[p][1] *= wt; }
      }
      const float a = ((av[0] + av[1]) + (av[2] + av[3])) + ((av[4] + av[5]) + (av[6] + av[7]));
      const float L = ((ml[0][1] + ml[1][1]) + (ml[2][1] + ml[3][1])) + ((ml[4][1] + ml[5][1]) + (ml[6][1] + ml[7][1]));
      const float o = bflo(qres) + a * __builtin_amdgcn_rcpf(L);
      const unsigned ob = cvtpk(o, __uint_as_float(__builtin_amdgcn_mov_dpp(__float_as_uint(o), 0xB1, 0xf, 0xf, true)));
      if (!(tid & 1)) __builtin_amdgcn_raw_buffer_store_b32(ob, mkrs(out, NQ * C * 2), (dout + tid) * 2, 0, 16);
      if (wg == 0 && tid == 0) stb64(u32x2{(unsigned)tg, (unsigned)(tg >> 32)}, rws, EP_OFF);
    }
  }
  TS(13);
  if (PROBE_WG < 0 || wg == PROBE_WG) {
    const unsigned long long tend = wall_clock64(), lim = tend + PROBE_K * (tprobe - tstart);
    for (int spin = 0; spin < (1 << 20); ++spin) { if (wall_clock64() >= lim) break; __builtin_amdgcn_s_sleep(4); }
  }
}

#undef rws
#define WS_AT(size_) (((size_) - (64u << 20)) & ~(size_t)0x1fffff)
extern "C" void kernel_launch(void* const* d_in, const int* in_sizes, int n_in, void* d_out, int out_size, void* d_ws, size_t ws_size,
                              hipStream_t stream) {
  dino_main<<<NWG, 512, 0, stream>>>((const u16*)d_in[0], (const u16*)d_in[1], (const u16*)d_in[2], (const u16*)d_in[3], (const u16*)d_in[4],
                                     (const u16*)d_in[5], (const u16*)d_in[6], (char*)d_ws + WS_AT(ws_size), (u16*)d_out);
}
```

```cpp
#include <hip/hip_runtime.h>
#include <hip/hip_bf16.h>
#include <stdint.h>

typedef unsigned short u16;
using u32x2 = __attribute__((ext_vector_type(2))) unsigned;
using u32x4 = __attribute__((ext_vector_type(4))) unsigned;
using f32x2 = __attribute__((ext_vector_type(2))) float;
using f32x4 = __attribute__((ext_vector_type(4))) float;
using s16x8 = __attribute__((ext_vector_type(8))) short;
using bf16x2_t = __attribute__((ext_vector_type(2))) __bf16;
typedef short v4i16_t __attribute__((ext_vector_type(4)));
#define LAS __attribute__((address_space(3)))

constexpr int C = 192, NQ = 16, W0 = 512, W1 = 256, NWG = 256, NW = 8;
constexpr float EPS = 1e-5f;
constexpr float QSCALE = 0.07216878364870322f * 1.4426950408889634f;

constexpr int PART = 4096, CRS = 3 * PART;
constexpr int SLOT = CRS + 3072, NS = 10;
constexpr int LDS_QK = NS * SLOT;
constexpr int NPK = 7;
constexpr int LDS_PK = LDS_QK, PK_B = 1344;
constexpr int LDS_CTL = LDS_PK + NPK * PK_B;
constexpr int LDS_SINK = LDS_CTL + 384;
constexpr int LDS_BYTES = LDS_SINK + 256;
constexpr int PSCR = 8 * SLOT;
constexpr int CT_FULL = 8, CT_REF = 4, CT_PA = 0, CT_PB = 12, CT_QR = 16, CT_PD = 20, CT_T0 = 24, CT_XD = 28, CT_TG = 32, CT_QK = 40, CT_ST = 44,
              CT_FREE = 48, CT_ONE = 112, CT_PRDY = 128, CT_PCON = 160, CT_MAX = 256;

constexpr int NDST = 192;
constexpr int MB_BYTES = NWG * NDST * 64;
constexpr int EP_OFF = MB_BYTES;
constexpr int FLAG_OFF = EP_OFF + 512;
constexpr int WVO_OFF = EP_OFF + 8192;
constexpr int QK_OFF = WVO_OFF + C * C * 2;
constexpr int QK_REC = 1280, QK_COPY = 18 * QK_REC;
constexpr int WS_BYTES = QK_OFF + 4 * QK_COPY;

#define GIVES 0x000000003232ull
constexpr int NPR = 36, NTK = (NWG - NPR) / 2;
__host__ __device__ constexpr int gives(int wg) { return (int)((GIVES >> (wg < NPR ? 4 * (wg & 1) : 16 + 4 * (wg & 7))) & 15); }
struct Tabs { unsigned seq[112]; unsigned take[NTK]; };
constexpr Tabs make_tabs() {
  Tabs t{};
  for (int i = 0; i < 112; ++i) t.seq[i] = i + 1;
  int k = 0;
  for (int wg = 0; wg < 256; ++wg)
    for (int j = 0; j < gives(wg); ++j, ++k) t.take[k % NTK] += (unsigned)(j << 8 | wg) << (10 * (k / NTK));
  return t;
}
constexpr int given() { int k = 0; for (int wg = 0; wg < 256; ++wg) k += gives(wg); return k; }
constexpr int GIVEN = given();
static_assert(GIVEN <= NTK * 3, "too many tiles sent elsewhere");
__device__ const Tabs tabs = make_tabs();
#define QKAT 5
#define WVN 4
#define DEPTH 6
#define PACE0 30
#define PACE1 1860
#define RGATE 2095
#define T0AT 3
#define DRY 1
#define GATE 420
#define CODE_BYTES 25152

__device__ __forceinline__ unsigned cvtpk(float lo, float hi) {
  bf16x2_t v = {(__bf16)lo, (__bf16)hi};
  return __builtin_bit_cast(unsigned, v);
}
__device__ __forceinline__ float bflo(unsigned x) { return __builtin_bit_cast(float, x << 16); }
__device__ __forceinline__ float bfhi(unsigned x) { return __builtin_bit_cast(float, x & 0xffff0000u); }
template <int CTRL> __device__ __forceinline__ float dpp(float x) {
  return __builtin_bit_cast(float, __builtin_amdgcn_mov_dpp(__builtin_bit_cast(int, x), CTRL, 0xf, 0xf, true));
}
constexpr int XOR8 = 0x128, ROR4 = 0x124;
template <bool HI8> __device__ __forceinline__ float half_copy(float x) {
  const int v = __builtin_bit_cast(int, x);
  return __builtin_bit_cast(float, __builtin_amdgcn_update_dpp(v, v, XOR8, 0xf, HI8 ? 0xc : 0x3, false));
}
__device__ __forceinline__ float row_max(float x) {
  x = fmaxf(x, dpp<XOR8>(x)); x = fmaxf(x, dpp<ROR4>(x)); x = fmaxf(x, dpp<0x122>(x));
  return fmaxf(x, dpp<0x121>(x));
}
__device__ __forceinline__ f32x2 swap32(float a, float b) {
  auto s = __builtin_amdgcn_permlane32_swap(__float_as_uint(a), __float_as_uint(b), false, false);
  return f32x2{__uint_as_float(s[0]), __uint_as_float(s[1])};
}
__device__ __forceinline__ f32x2 swap16(float a, float b) {
  auto s = __builtin_amdgcn_permlane16_swap(__float_as_uint(a), __float_as_uint(b), false, false);
  return f32x2{__uint_as_float(s[0]), __uint_as_float(s[1])};
}
__device__ __forceinline__ float xrow16_max(float x) {
  auto s = __builtin_amdgcn_permlane16_swap(__float_as_uint(x), __float_as_uint(x), false, false);
  x = fmaxf(__uint_as_float(s[0]), __uint_as_float(s[1]));
  auto t = __builtin_amdgcn_permlane32_swap(__float_as_uint(x), __float_as_uint(x), false, false);
  return fmaxf(__uint_as_float(t[0]), __uint_as_float(t[1]));
}
__device__ __forceinline__ float xrow16_sum(float x) {
  auto s = __builtin_amdgcn_permlane16_swap(__float_as_uint(x), __float_as_uint(x), false, false);
  x = __uint_as_float(s[0]) + __uint_as_float(s[1]);
  auto t = __builtin_amdgcn_permlane32_swap(__float_as_uint(x), __float_as_uint(x), false, false);
  return __uint_as_float(t[0]) + __uint_as_float(t[1]);
}
__device__ __forceinline__ f32x4 mfma(u32x4 a, u32x4 b, f32x4 c) {
  return __builtin_amdgcn_mfma_f32_16x16x32_bf16(__builtin_bit_cast(s16x8, a), __builtin_bit_cast(s16x8, b), c, 0, 0, 0);
}
__device__ __forceinline__ u32x2 vtr(LAS const char* p) {
  return __builtin_bit_cast(u32x2, __builtin_amdgcn_ds_read_tr16_b64_v4i16((LAS v4i16_t*)p));
}
__device__ __forceinline__ u32x4 vtr2(LAS const char* p, LAS const char* q_) {
  const u32x2 a = vtr(p), b = vtr(q_);
  return u32x4{a[0], a[1], b[0], b[1]};
}
__device__ __forceinline__ float sel4(f32x4 v, int r) {
  const float a = (r & 1) ? v[1] : v[0], b = (r & 1) ? v[3] : v[2];
  return (r & 2) ? b : a;
}
using RSRC = __amdgpu_buffer_rsrc_t;
__device__ __forceinline__ RSRC mkrs(const void* p, int bytes) { return __builtin_amdgcn_make_buffer_rsrc(const_cast<void*>(p), (short)0, bytes, 0x00020000); }
__device__ __forceinline__ u32x4 mkrs4(const void* p, unsigned bytes) {
  const unsigned long long b = (unsigned long long)p;
  return u32x4{(unsigned)b, (unsigned)(b >> 32) & 0xffffu, bytes, 0x00020000u};
}
template <int AUX> __device__ __forceinline__ u32x4 ldb128(RSRC r, unsigned vo, int so) { return __builtin_bit_cast(u32x4, __builtin_amdgcn_raw_buffer_load_b128(r, vo, so, AUX)); }
#define SB() __builtin_amdgcn_sched_barrier(0)
#define WAVE_SYNC() do { __builtin_amdgcn_fence(__ATOMIC_RELEASE, "wavefront"); __builtin_amdgcn_wave_barrier(); __builtin_amdgcn_fence(__ATOMIC_ACQUIRE, "wavefront"); } while (0)
#define RAW_BARRIER() asm volatile("s_waitcnt lgkmcnt(0)\n\ts_barrier" ::: "memory")
#define DMA(m0_, vo_, rs_, so_) asm volatile("s_mov_b32 m0, %0\n\ts_nop 0\n\tbuffer_load_dwordx4 %1, %2, %3 offen lds" :: "s"(m0_), "v"(vo_), "s"(rs_), "s"(so_) : "memory")
template <int N> __device__ __forceinline__ void waitlds() { __builtin_amdgcn_s_waitcnt(0xc07f | (N << 8)); }
template <int N> __device__ __forceinline__ void waitvm() { asm volatile("s_waitcnt vmcnt(%0)" :: "i"(N) : "memory"); }


__device__ __forceinline__ void lds_add(LAS char* p, int lane, unsigned v) {
  asm volatile("" ::: "memory");
  if (lane == 0) __hip_atomic_fetch_add((LAS unsigned*)p, v, __ATOMIC_RELAXED, __HIP_MEMORY_SCOPE_WORKGROUP);
  asm volatile("" ::: "memory");
}
__device__ __forceinline__ void lds_set(LAS char* p, int lane, unsigned v) {
  asm volatile("" ::: "memory");
  if (lane == 0) __hip_atomic_store((LAS unsigned*)p, v, __ATOMIC_RELAXED, __HIP_MEMORY_SCOPE_WORKGROUP);
  asm volatile("" ::: "memory");
}
__device__ __forceinline__ void lds_inc(int O, LAS char* p) {
  asm volatile("s_mov_b64 exec, 1\n\tds_add_u32 %0, %1 offset:%2\n\ts_mov_b64 exec, -1" :: "v"(p), "v"(1u), "i"(O) : "memory");
}
__device__ __forceinline__ void lds_put(int O, LAS char* p, unsigned v) {
  asm volatile("s_mov_b64 exec, 1\n\tds_write_b32 %0, %1 offset:%2\n\ts_mov_b64 exec, -1" :: "v"(p), "v"(v), "i"(O) : "memory");
}
__device__ __forceinline__ int lds_get(LAS char* p) {
  asm volatile("" ::: "memory");
  const unsigned v = __hip_atomic_load((LAS unsigned*)p, __ATOMIC_RELAXED, __HIP_MEMORY_SCOPE_WORKGROUP);
  asm volatile("" ::: "memory");
  return __builtin_amdgcn_readfirstlane(v);
}
__device__ __forceinline__ void lds_wait(LAS char* p, int target) {
#pragma unroll 1
  for (int spin = 0; spin < (1 << 22); ++spin) {
    if (lds_get(p) >= target) break;
    __builtin_amdgcn_s_sleep(1);
  }
}
__device__ __forceinline__ int lds_spin(int O, LAS char* p, int target) {
  int r, cnt = 1 << 22;
  unsigned t;
  asm volatile("1:\n\tds_read_b32 %1, %3 offset:%5\n\ts_waitcnt lgkmcnt(0)\n\tv_readfirstlane_b32 %0, %1\n\ts_cmp_ge_i32 %0, %4\n\ts_cbranch_scc1 2f\n\t"
               "s_sub_u32 %2, %2, 1\n\ts_cbranch_scc0 1b\n2:"
               : "=&s"(r), "=&v"(t), "+s"(cnt) : "v"(p), "s"(target), "i"(O) : "memory", "scc");
  return r;
}
__device__ __forceinline__ void lds_need(int O, int& have, LAS char* p, int target) {
  int cnt;
  unsigned t;
  asm volatile("s_cmp_ge_i32 %0, %4\n\ts_cbranch_scc1 2f\n\ts_mov_b32 %2, 0x400000\n"
               "1:\n\tds_read_b32 %1, %3 offset:%5\n\ts_waitcnt lgkmcnt(0)\n\tv_readfirstlane_b32 %0, %1\n\ts_cmp_ge_i32 %0, %4\n\ts_cbranch_scc1 2f\n\t"
               "s_sub_u32 %2, %2, 1\n\ts_cbranch_scc0 1b\n2:"
               : "+s"(have), "=&v"(t), "=&s"(cnt) : "v"(p), "s"(target), "i"(O) : "memory", "scc");
  asm volatile("" : "+s"(have));
}
__device__ __forceinline__ u32x4 pack8(f32x4 a, f32x4 b) { return u32x4{cvtpk(a[0], a[1]), cvtpk(a[2], a[3]), cvtpk(b[0], b[1]), cvtpk(b[2], b[3])}; }
__device__ __forceinline__ unsigned tagf(float x, unsigned t) { return (__float_as_uint(x) & 0xfffff000u) | t; }

__device__ __forceinline__ void stb128(u32x4 v, RSRC r, unsigned o) { __builtin_amdgcn_raw_buffer_store_b128(v, r, o, 0, 16); }
__device__ __forceinline__ void stb64(u32x2 v, RSRC r, unsigned o) { __builtin_amdgcn_raw_buffer_store_b64(v, r, o, 0, 16); }
__device__ __forceinline__ void stb32(unsigned v, RSRC r, unsigned o) { __builtin_amdgcn_raw_buffer_store_b32(v, r, o, 0, 16); }
__device__ __forceinline__ unsigned qk_check(u32x4 x, unsigned tg) {
  const unsigned a = (x[0] ^ __builtin_amdgcn_alignbit(x[1], x[1], 11)) * 0x9e3779b1u + tg;
  const unsigned b = (x[2] + __builtin_amdgcn_alignbit(x[3], x[3], 19)) * 0x85ebca77u;
  return __builtin_amdgcn_alignbit(a, a, 15) ^ b ^ (x[1] + x[3]);
}
__device__ __forceinline__ unsigned add3(unsigned a, unsigned b, unsigned c) {
  return cvtpk((bflo(a) + bflo(b)) + bflo(c), (bfhi(a) + bfhi(b)) + bfhi(c));
}
__global__ __launch_bounds__(512, 2) void dino_main(const u16* q, const u16* f0, const u16* f1, const u16* wq, const u16* wk, const u16* wv,
                                                     const u16* wo, char* ws, u16* out) {
  __shared__ __attribute__((aligned(128))) char smem[LDS_BYTES];
  LAS char* lds = (LAS char*)smem;
  LAS char* ctl = lds + LDS_CTL;
  const unsigned lb = (unsigned)(unsigned long long)lds;
  const int w = __builtin_amdgcn_readfirstlane(threadIdx.x >> 6);
  unsigned long long pc0;
  asm volatile("s_getpc_b64 %0" : "=s"(pc0));
#define COORDS() \
  int tid = threadIdx.x, wg = blockIdx.x; \
  asm volatile("" : "+v"(tid), "+s"(wg)); \
  __builtin_assume(tid >= 0 && tid < 512); __builtin_assume(wg >= 0 && wg < NWG); \
  const int lane = tid & 63, n = lane & 15, g = lane >> 4;       \
    \
  const int dq = (wg & 7) * 2 + ((wg >> 3) & 1), dst = (wg & ~15) + dq, dout = dq * C + (wg & ~15); \
  const int NT = 32 - gives(wg) + (wg < NPR || (wg & 1) ? 0 : GIVEN / NTK + (((wg - NPR) >> 1) < GIVEN % NTK ? 1 : 0)); \
  (void)g; (void)dst; (void)dout; (void)NT
#define ARGS_IN() asm volatile("s_waitcnt lgkmcnt(0)" :: "s"(q), "s"(f0), "s"(f1), "s"(wq), "s"(wk), "s"(wv), "s"(wo), "s"(ws), "s"(out) : "memory")
#define rws mkrs(ws, WS_BYTES)
  unsigned long long tg;
  unsigned qres = 0u;
  const unsigned long long tstart = wall_clock64();
  if (w != 0) {
    const unsigned l0 = threadIdx.x & 63;
    if (blockIdx.x < NPR) {
      const unsigned p6 = (blockIdx.x * 43) >> 8, pu = blockIdx.x - 6 * p6;
      unsigned vo = ((l0 * 16) ^ (((l0 * 4) & 0x40) | (l0 & 0x20))) + (l0 >> 3) * 256;
      const bool pv = p6 >= 3, wa = w >= 4, isq = !pv && (w & 6) == 2;
      const unsigned a6 = 6 * w - 24, pj = pv ? p6 - 3 : p6;
      unsigned so = (wa ? a6 : isq ? w - 2 : 4 * pu) * 3072 + (isq ? 0 : pv && !wa ? w - 1 : pj) * 128, ga = isq ? 128 : 3072;
      unsigned lo = lb + PSCR + (wa ? a6 : pv ? 4 * w + 20 : (0x1b181e00u >> (8 * w)) & 255u) * 1024, hi = lo + (wa ? 6144 : isq ? 3072 : 4096);
      asm volatile("" : "+v"(vo), "+s"(lo), "+s"(so), "+s"(hi), "+s"(ga));
      ARGS_IN();
      const u32x4 rw = mkrs4(wa ? (pv ? wo : wq) : pv ? wv : isq ? q : wk, isq ? NQ * C * 2 : C * C * 2);
#pragma unroll 1
      do { DMA(lo, vo, rw, so); lo += 1024; so += ga; } while (lo != hi);
    } else if (w == 7 && blockIdx.x >= 56 && blockIdx.x < 72) {
      const u32x4 rc = mkrs4((const void*)(pc0 & ~255ull), CODE_BYTES);
#pragma unroll 1
      for (int i = 0; i < CODE_BYTES; i += 4096)
        asm volatile("s_mov_b32 m0, %0\n\ts_nop 0\n\tbuffer_load_dword %1, %2, %3 offen lds" :: "s"(lb + LDS_SINK), "v"(l0 * 64), "s"(rc), "s"(i) : "memory");
    }
    RAW_BARRIER();
  }
  SB();
  if (__builtin_expect(w == 0, 1)) {
    const unsigned l0 = threadIdx.x & 63, g0 = blockIdx.x;
    unsigned y0 = (l0 * 16) ^ (l0 >= 48 ? 48 : 0), y1 = (l0 * 16) ^ (l0 >= 32 ? 64 : 48), y2 = (l0 * 16) ^ (l0 >= 16 ? 112 : 64);
    unsigned v0 = ((l0 * 16) ^ ((l0 * 2) & 0x60)) + (l0 >> 3) * 640 + 1024, v1 = (v0 ^ 16) - 640;
    unsigned v2 = v0 + W0 * C * 2 - 2048, v3 = v1 + W0 * C * 2 - 2048;
    unsigned b0 = g0 * 6144 + (g0 >> 5) * (W0 * C * 2), b1 = g0 * 3072;
    {
      const unsigned iv = (l0 & 15) >= CT_ONE / 8 ? 0x3f803f80u : (l0 >> 3) == CT_MAX / 64 ? 0xff800000u : 0u;
      *(LAS u32x2*)(ctl + 8 * min(l0, 47u)) = u32x2{l0 >= 47 ? 0x3f803f80u : iv, l0 >= 47 ? 0x3f803f80u : iv};
    }
    constexpr unsigned st0 = 16 * W0 * C * 2, st1 = 8 * W1 * C * 2;
    unsigned sb = lb, fo = CT_FREE, fv = 0u, tk = 0u;
    int lap = 0, need = 0;
    u32x4 rt = mkrs4(tabs.seq, 112 * 4);
    unsigned vz = 0u;
    asm volatile("" : "+v"(v0), "+v"(v1), "+v"(v2), "+v"(v3), "+v"(y0), "+v"(y1), "+v"(y2), "+v"(vz), "+s"(b0), "+s"(b1), "+s"(rt));
    ARGS_IN();
    const u32x4 r0 = mkrs4((const char*)f0 - 1024, W0 * W0 * C * 2 + 1024), r1 = mkrs4(f1, W1 * W1 * C * 2);
    if (g0 < NPR) __builtin_amdgcn_s_barrier();
#define UNIT(p_) do { unsigned so_; \
      asm volatile("s_add_u32 m0, %1, %10\n\ts_add_u32 %0, %2, %11\n\t" \
                   "buffer_load_dwordx4 %3, %7, %0 offen nt lds\n\tbuffer_load_dwordx4 %4, %7, %0 offen offset:1024 nt lds\n\t" \
                   "buffer_load_dwordx4 %5, %7, %0 offen offset:2048 nt lds\n\tbuffer_load_dwordx4 %6, %7, %0 offen offset:3072 nt lds\n\t" \
                   "s_mov_b32 m0, %12\n\ts_mov_b64 exec, 1\n\tbuffer_load_dword %13, %8, %9 offen offset:%14 lds\n\ts_mov_b64 exec, -1\n\t" \
                   "s_waitcnt vmcnt(%15)" \
                   : "=&s"(so_) : "s"(sb), "s"(b0), "v"(v0), "v"(v1), "v"(v2), "v"(v3), "s"(r0), "s"(rt), "s"(12 * s), "i"((p_) * PART), "i"((p_) * 128), \
                     "s"(lb + LDS_CTL + CT_FULL - 4 * (p_)), "v"(vz), "i"(4 * (p_)), "i"(6 * DEPTH) : "memory", "scc"); } while (0)
#define COARSE() \
      asm volatile("s_add_u32 m0, %0, %6\n\ts_nop 0\n\tbuffer_load_dwordx4 %1, %4, %5 offen nt lds\n\t" \
                   "buffer_load_dwordx4 %2, %4, %5 offen offset:1024 nt lds\n\tbuffer_load_dwordx4 %3, %4, %5 offen offset:2048 nt lds" \
                   :: "s"(sb), "v"(y0), "v"(y1), "v"(y2), "s"(r1), "s"(b1), "i"(CRS) : "memory", "scc")
#pragma unroll 1
    for (int s = 0; s < 2; ++s) {
      COARSE(); UNIT(0); UNIT(1); UNIT(2);
      sb += SLOT; b0 += st0; b1 += st1;
    }
    if (g0 >= NPR) __builtin_amdgcn_s_barrier();
    __builtin_amdgcn_s_setprio(3);
    COORDS();
    fo += 8;
    const int tau = __builtin_amdgcn_readfirstlane((int)((PACE1 - PACE0) * 256.f * __builtin_amdgcn_rcpf((float)NT)));
    unsigned due = PACE0 * 256u + 2 * tau, t0v = 0u;
    unsigned long long now = tstart;
#pragma unroll 1
    for (int s = 2; s < NT; ++s) {
      asm volatile("" : "+s"(now));
      const unsigned t0s = __builtin_amdgcn_readfirstlane(t0v), t0 = t0s ? t0s : (unsigned)tstart;
      if (PACE1 && (int)(((unsigned)now - t0) * 256u - due) < 0) {
#pragma unroll 1
        for (int spin = 0; spin < 256; ++spin) {
          if ((int)(((unsigned)wall_clock64() - t0) * 256u - due) >= 0) break;
          __builtin_amdgcn_s_sleep(1);
        }
      }
      due += tau;
      if (__builtin_expect((int)__builtin_amdgcn_readfirstlane(fv) < need, 0)) {
        lds_wait(ctl + (s == 8 ? CT_PD : fo), need);
      }
      COARSE();
      UNIT(0);
      UNIT(1);
      if (fo == CT_FREE + 4 * (NS - 1)) { fo = CT_FREE; lap += 4; } else fo += 4;
      need = s == 7 && wg < NPR ? 4 : lap;
      fv = *(volatile LAS unsigned*)(ctl + (s == 7 ? CT_PD : fo));
      if (s == 2 && NT > 32) tk = tabs.take[(wg - NPR) >> 1];
      if (PACE1) { now = wall_clock64(); t0v = *(volatile LAS unsigned*)(ctl + CT_T0); }
      UNIT(2);
      sb = fo == CT_FREE ? lb : sb + SLOT;
      b0 += st0; b1 += st1;
      if (s >= 31) {
        const unsigned xw = tk & 255u, xs = 31 - ((tk >> 8) & 3u);
        b0 = xw * 6144 + (xw >> 5) * (W0 * C * 2) + xs * st0; b1 = xw * 3072 + xs * st1;
        tk >>= 10;
      }
    }
    __builtin_amdgcn_s_setprio(0);
    qres = __builtin_amdgcn_raw_buffer_load_b16(mkrs(q, NQ * C * 2), (dout + n) * 2, 0, 0);
    waitvm<0>();
    tg = ((unsigned long long)((unsigned)lds_spin(CT_TG + 4, ctl, 1) & 0xffu) << 32) | (unsigned)lds_get(ctl + CT_TG);
  } else {
    COORDS();
    const int tq = (lane >> 2) & 3, tp = lane & 3;
    const bool pq = wg < 18;
    const bool pv = wg >= 18 && wg < NPR;
    const int pj = wg / 6, pu = wg % 6;
    const u32x4 one = {0x3f803f80u, 0x3f803f80u, 0x3f803f80u, 0x3f803f80u};
    const int fq = ((n & 2) << 1) | ((n & 4) >> 1);
    LAS const char* rq0 = lds + PSCR + 24576 + 3072 * (n >> 3) + 128 * (n & 7) + 16 * (g ^ fq);
    LAS const char* rq1 = lds + PSCR + 24576 + 3072 * (n >> 3) + 128 * (n & 7) + 16 * ((4 + g) ^ fq);
    if (pq && w >= 2) {
#pragma unroll 1
      for (int pass = w >= 4 ? 1 - DRY : 1; pass < 2; ++pass) {
        if (pass) {
          waitvm<0>();
          lds_inc(CT_PA, ctl);
        }
        if (w >= 4) {
          f32x4 sq = {0.f, 0.f, 0.f, 0.f}, rr = sq, cs = sq;
          const int ro = PSCR + 128 * (8 * g + tq) + 16 * (((w & 2) * 2 + tp) ^ ((tq & 2) << 1)) + 8 * (w & 1);
          u32x4 qv[6], af[6];
          lds_spin(CT_PA, ctl, pass ? 6 : 0);
#pragma unroll
          for (int s = 0; s < 6; ++s) {
            qv[s] = *(LAS const u32x4*)(((s & 1) ? rq1 : rq0) + (s >> 1) * 1024);
            af[s] = vtr2(lds + ro + s * 4096, lds + (ro ^ 32) + s * 4096 + 512);
          }
          SB();
#pragma unroll
          for (int s = 0; s < 6; ++s) { sq = mfma(one, qv[s], sq); rr = mfma(af[s], qv[s], rr); cs = mfma(af[s], one, cs); }
          const f32x4 qq = rr - (sq[0] * (1.f / C)) * cs;
          *(LAS u32x2*)(lds + LDS_QK + 4096 + ((w >> 1) & 1) * 1024 + lane * 16 + 8 * (w & 1)) = u32x2{cvtpk(qq[0], qq[1]), cvtpk(qq[2], qq[3])};
          if (pass) lds_inc(CT_PB, ctl);
        }
      }
    }
    SB();
    asm volatile("s_load_dwordx2 %0, %1, 0x0\n\ts_waitcnt lgkmcnt(0)" : "=s"(tg) : "s"(ws + EP_OFF) : "memory");
    tg = (tg + 0x40100401ull) & 0xffffffffffull;
    if (w == 3 && lane == 0) *(LAS u32x2*)(ctl + CT_TG) = u32x2{(unsigned)tg, (unsigned)(tg >> 32) | 0x100u};
    if (pq && w == 1) {
#pragma unroll 1
      for (int pass = 1 - DRY; pass < 2; ++pass) {
        if (pass) waitvm<0>();
        u32x4 kf4[4], qv[6];
        const int ko = LDS_QK + 128 * (((n & 4) << 2) | (n & 11)) + 16 * (g ^ ((n & 2) << 1));
#pragma unroll
        for (int i = 0; i < 4; ++i) kf4[i] = *(LAS const u32x4*)(lds + (ko ^ ((i & 1) * 64 + (i >> 1) * 32)) + (i >> 1) * 512);
        lds_spin(CT_PA, ctl, pass ? 6 : 0);
#pragma unroll
        for (int s = 0; s < 6; ++s) qv[s] = *(LAS const u32x4*)(((s & 1) ? rq1 : rq0) + (s >> 1) * 1024);
        const f32x4 z = {0.f, 0.f, 0.f, 0.f};
        f32x4 gq = z, sq = z;
#pragma unroll
        for (int s = 0; s < 6; ++s) { gq = mfma(qv[s], qv[s], gq); sq = mfma(one, qv[s], sq); }
        const float mean = sq[0] * (1.f / C);
        const float s2 = xrow16_sum(g == (n >> 2) ? sel4(gq, n & 3) : 0.f);
        float sc = __builtin_amdgcn_rsqf(fmaxf(fmaf(-mean, mean, s2 * (1.f / C)), 0.f) + EPS) * QSCALE;
        asm volatile("" : "+v"(sc));
        lds_spin(CT_PB, ctl, pass ? 4 : 0);
        const u32x4 q0 = *(LAS const u32x4*)(lds + LDS_QK + 4096 + lane * 16), q1 = *(LAS const u32x4*)(lds + LDS_QK + 5120 + lane * 16);
        const f32x4 d0 = mfma(kf4[1], q1, mfma(kf4[0], q0, z)), d1 = mfma(kf4[3], q1, mfma(kf4[2], q0, z));
        const u32x4 xq = pack8(d0 * sc, d1 * sc);
        const unsigned ck = qk_check(xq, (unsigned)tg);
        asm volatile("" :: "v"(ck));
        if (pass) {
          const unsigned so = QK_OFF + wg * QK_REC;
#pragma unroll
          for (int cp = 0; cp < 4; ++cp) {
            stb128(xq, rws, lane * 16 + so + cp * QK_COPY);
            stb32(ck, rws, lane * 4 + so + 1024 + cp * QK_COPY);
          }
          lds_put(CT_PD, ctl, 4);
        }
      }
    }
    if (pv) {
      waitvm<0>();
      lds_inc(CT_PA, ctl);
      if (w >= 4) {
        const int ro = PSCR + 128 * (8 * g + tq) + 16 * ((2 * (w - 4) + (tp >> 1)) ^ ((tq & 2) << 1)) + 8 * (tp & 1);
        const int rv = PSCR + 24576 + 128 * n + 16 * (g ^ fq);
        f32x4 d0 = {0.f, 0.f, 0.f, 0.f}, d1 = d0;
        lds_spin(CT_PA, ctl, 7);
#pragma unroll
        for (int s = 0; s < 6; ++s) {
          const u32x4 p = vtr2(lds + ro + s * 4096, lds + (ro ^ 32) + s * 4096 + 512);
          LAS const char* pd = lds + (rv ^ ((s & 1) * 64)) + (s >> 1) * 4096;
          d0 = mfma(*(LAS const u32x4*)pd, p, d0); d1 = mfma(*(LAS const u32x4*)(pd + 2048), p, d1);
        }
        const int fi = (pj - 3) * 24 + ((w >> 1) & 1) * 12 + 2 * pu + (w & 1);
        stb128(pack8(d0, d1), rws, lane * 16 + WVO_OFF + fi * 1024);
        lds_inc(CT_PD, ctl);
        waitvm<0>();
        if (lane < 8) stb64(u32x2{(unsigned)tg, (unsigned)(tg >> 32) | (unsigned)tstart << 8}, rws, FLAG_OFF + lane * 768 + fi * 8);
      }
    }
    const f32x4 z = {0.f, 0.f, 0.f, 0.f};
    int full = 0;
#define NEED(u_) lds_need(CT_FULL, full, ctl, (u_))
    if (w >= 4) {
      const int brg = ((g & 1) << 1) | (g >> 1);
      const int fn = (((n >> 1) & 3) << 1) | ((n >> 3) & 1);
      const int fy = ((n >> 2) & 1) * 4 + ((n >> 1) & 1) * 3;
      const int orr0 = 128 * n + 16 * (brg ^ fn), orr1 = 128 * n + 16 * ((4 + brg) ^ fn);
      const int ory0 = n < 8 ? CRS + 384 * n + 16 * (brg ^ fy) : LDS_CTL + CT_ONE, ory1 = n < 8 ? CRS + 384 * n + 16 * ((4 + brg) ^ fy) : LDS_CTL + CT_ONE;
      const int yon = n < 8 ? 1 : 0;
      const int opk = LDS_PK + 320 * g + 8 * n, ork = LDS_PK + 320 * (2 * (g >> 1) + (n >> 3)) + 256 + 8 * (n & 7) + 4 * (g & 1);
      u32x4 qk[6];
      f32x4 ref, qs;
      LAS const char *px0, *px1, *py0, *py1;
#define AT_SLOT(o_) do { px0 = lds + orr0 + (o_); px1 = lds + orr1 + (o_); py0 = lds + ory0 + __mul24(yon, (o_)); py1 = lds + ory1 + __mul24(yon, (o_)); \
      asm volatile("" : "+v"(px0), "+v"(px1), "+v"(py0), "+v"(py1)); } while (0)
#define RD_K(k_, i_) do { LAS const char* p_ = (((k_) & 1) ? px1 : px0) + ((k_) >> 1) * PART; \
    yv[i_] = *(LAS const u32x4*)((((k_) & 1) ? py1 : py0) + ((k_) >> 1) * 128); \
    xa[i_] = *(LAS const u32x4*)(p_); xb[i_] = *(LAS const u32x4*)(p_ + 2048); } while (0)
#define MF_STAT(i_) do { cy = mfma(yv[i_], yv[i_], cy); ca = mfma(yv[i_], xa[i_], ca); cb = mfma(yv[i_], xb[i_], cb); \
    ga = mfma(xa[i_], xa[i_], ga); gb = mfma(xb[i_], xb[i_], gb); } while (0)
#define MF_S(k_, i_) do { sy = mfma(qk[k_], yv[i_], sy); sa = mfma(qk[k_], xa[i_], sa); sb = mfma(qk[k_], xb[i_], sb); } while (0)
#define STATS(st, rec) do { \
      const int r = n & 3; \
      const float yy = half_copy<true>(sel4(cy, r)); \
      const f32x2 Y = swap32(yy, yy), X = swap32(sel4(ca, r), sel4(cb, r)), G = swap32(sel4(ga, r), sel4(gb, r)); \
      const float q1_ = fmaf(2.f, X[0], Y[0]) + (n < 8 ? G[0] : G[1]);       \
      const f32x2 Q = swap16(q1_, q1_); \
      const float mn_ = (X[1] + Y[1]) * (1.f / C); \
      const float var = fmaxf(fmaf(-mn_, mn_, fmaf((n & 4) ? Q[1] : Q[0], 1.f / C, EPS)), EPS), rs_ = __builtin_amdgcn_rsqf(var); \
      const f32x2 R = swap32(rs_, rs_), Mn = swap32(mn_, mn_); \
      st = f32x4{R[0], Mn[0], R[1], Mn[1]}; \
      const float v_ = (g & 1) ? var * rs_ : mn_, h_ = bfhi(__float_as_uint(v_)); \
      rec = cvtpk(h_, v_ - h_); \
    } while (0)
#define LOGITS(sv, st, base_) do { \
      _Pragma("unroll") for (int r = 0; r < 4; ++r) { \
        const float s0 = half_copy<true>(sy[r]); \
        sv[r] = fmaf(fmaf(-st[1], qs[r], sa[r] + s0), st[0], -(base_)[r]); \
        sv[4 + r] = fmaf(fmaf(-st[3], qs[r], sb[r] + s0), st[2], -(base_)[r]); \
      } \
    } while (0)
#define SV_MAX(sv) fmaxf(fmaxf(fmaxf(sv[0], sv[1]), fmaxf(sv[2], sv[3])), fmaxf(fmaxf(sv[4], sv[5]), fmaxf(sv[6], sv[7])))
#define PUBLISH(sv, st, rec, pk_, word_) do { \
      *(LAS unsigned*)(lds + ork + (pk_) * PK_B) = rec; \
      *(LAS u32x2*)(lds + opk + (pk_) * PK_B) = u32x2{cvtpk(sv[0] * st[0], sv[1] * st[0]), cvtpk(sv[2] * st[0], sv[3] * st[0])}; \
      *(LAS u32x2*)(lds + opk + (pk_) * PK_B + 128) = u32x2{cvtpk(sv[4] * st[2], sv[5] * st[2]), cvtpk(sv[6] * st[2], sv[7] * st[2])}; \
      lds_put(CT_PRDY, ctl + 4 * (pk_), (word_)); \
    } while (0)
      int s = (w + NT - 1) & 3, slot = s + 4, pk = s + 4, ncon = 0;
      {
        u32x4 yv[12], xa[12], xb[12];
        f32x4 st0, st1;
        unsigned rec0, rec1;
#pragma unroll
        for (int h = 0; h < 2; ++h) {
          f32x4 cy = z, ga = z, ca = z, gb = z, cb = z;
          AT_SLOT((s + 4 * h) * SLOT);
          lds_wait(ctl + CT_FULL, 3 * (s + 4 * h) + 3);
#pragma unroll
          for (int k = 0; k < 6; ++k) RD_K(k, 6 * h + k);
          lds_inc(CT_FREE + 16 * h, ctl + 4 * s);
          SB();
#pragma unroll
          for (int k = 0; k < 6; ++k) MF_STAT(6 * h + k);
          SB();
          if (h == 0) { STATS(st0, rec0); asm volatile("" : "+v"(st0), "+v"(rec0)); } else { STATS(st1, rec1); asm volatile("" : "+v"(st1), "+v"(rec1)); }
          SB();
        }
        lds_wait(ctl + CT_QK, 3);
#pragma unroll
        for (int k = 0; k < 6; ++k) qk[k] = *(LAS const u32x4*)(lds + LDS_QK + k * 1024 + lane * 16);
        float s0v[8], s1v[8];
        {
          f32x4 sy = z, sa = z, sb = z;
#pragma unroll
          for (int k = 0; k < 6; ++k) MF_S(k, k);
#pragma unroll
          for (int r = 0; r < 4; ++r) qs[r] = half_copy<false>(sy[r]);
          LOGITS(s0v, st0, z);
        }
        {
          f32x4 sy = z, sa = z, sb = z;
#pragma unroll
          for (int k = 0; k < 6; ++k) MF_S(k, 6 + k);
          LOGITS(s1v, st1, z);
        }
#pragma unroll
        for (int r = 0; r < 4; ++r) ref[r] = row_max(fmaxf(fmaxf(s0v[r], s0v[4 + r]), fmaxf(s1v[r], s1v[4 + r])));
        if (n < 4) __builtin_amdgcn_ds_fmaxf((LAS float*)(ctl + CT_MAX + 16 * g + 4 * n), sel4(ref, n), __ATOMIC_RELAXED, __HIP_MEMORY_SCOPE_WORKGROUP, false);
        lds_inc(CT_QR, ctl);
        lds_spin(CT_QR, ctl, 4);
        ref = *(LAS const f32x4*)(ctl + CT_MAX + 16 * g);
#pragma unroll
        for (int j = 0; j < 8; ++j) s0v[j] = __builtin_amdgcn_exp2f(s0v[j] - ref[j & 3]);
        PUBLISH(s0v, st0, rec0, s, 2 * s + 2);
#pragma unroll
        for (int j = 0; j < 8; ++j) s1v[j] = __builtin_amdgcn_exp2f(s1v[j] - ref[j & 3]);
        if (pk >= NPK) { pk -= NPK; ncon = 3; lds_spin(CT_PCON, ctl + 4 * pk, 3); }
        s += 4;
        PUBLISH(s1v, st1, rec1, pk, 2 * s + 2);
      }
      int moved = 0;
#pragma unroll 1
      for (;;) {
        s += 4; slot += 4; slot -= slot >= NS ? NS : 0; pk += 4;
        if (pk >= NPK) { pk -= NPK; ncon += 3; }
        if (s >= NT) break;
        f32x4 sy = z, cy = z, sa = z, ga = z, ca = z, sb = z, gb = z, cb = z, st;
        u32x4 yv[6], xa[6], xb[6];
        float sv[8];
        unsigned rec;
        AT_SLOT(slot * SLOT);
#define RD_PART(p_) do { int cnt_; unsigned t_; \
        asm volatile("s_mov_b32 %1, 0x400000\n1:\n\tds_read_b32 %2, %9 offset:%15\n\t" \
                     "ds_read_b128 %3, %12 offset:%16\n\tds_read_b128 %4, %10 offset:%17\n\tds_read_b128 %5, %10 offset:%18\n\t" \
                     "ds_read_b128 %6, %13 offset:%16\n\tds_read_b128 %7, %11 offset:%17\n\tds_read_b128 %8, %11 offset:%18\n\t" \
                     "s_cmp_ge_i32 %0, %14\n\ts_cbranch_scc1 2f\n\ts_waitcnt lgkmcnt(6)\n\tv_readfirstlane_b32 %0, %2\n\t" \
                     "s_cmp_ge_i32 %0, %14\n\ts_cbranch_scc1 2f\n\ts_sub_u32 %1, %1, 1\n\ts_cbranch_scc0 1b\n2:" \
                     : "+s"(full), "=&s"(cnt_), "=&v"(t_), "=&v"(yv[2 * (p_)]), "=&v"(xa[2 * (p_)]), "=&v"(xb[2 * (p_)]), \
                       "=&v"(yv[2 * (p_) + 1]), "=&v"(xa[2 * (p_) + 1]), "=&v"(xb[2 * (p_) + 1]) \
                     : "v"(ctl), "v"(px0), "v"(px1), "v"(py0), "v"(py1), "s"(3 * s + (p_) + 1), "i"(CT_FULL), "i"(128 * (p_)), "i"(PART * (p_)), \
                       "i"(PART * (p_) + 2048) : "memory", "scc"); keep = t_; \
        asm volatile("" : "+s"(full)); } while (0)
#define GOT(k_, n_) do { waitlds<n_>(); asm volatile("" : "+v"(yv[k_]), "+v"(xa[k_]), "+v"(xb[k_]) : "v"(keep)); } while (0)
        unsigned keep;
        const unsigned pcv = *(volatile LAS unsigned*)(ctl + CT_PCON + 4 * pk);
        NEED(3 * s);
        RD_PART(0);
        GOT(0, 3);
        int con = __builtin_amdgcn_readfirstlane(pcv);
        SB();
        MF_STAT(0); MF_S(0, 0);
        SB();
        lds_need(CT_PCON, con, ctl + 4 * pk, ncon);
        RD_PART(1);
        GOT(1, 7);
        SB();
        MF_STAT(1); MF_S(1, 1);
        SB();
        GOT(2, 3);
        MF_STAT(2); MF_S(2, 2);
        SB();
        RD_PART(2);
        GOT(3, 7);
        SB();
        MF_STAT(3); MF_S(3, 3);
        SB();
        GOT(4, 3);
        lds_inc(CT_FREE, ctl + 4 * slot);
        MF_STAT(4);
        SB();
        GOT(5, 1);
        MF_STAT(5);
        SB();
        MF_S(4, 4); MF_S(5, 5);
        STATS(st, rec);
        LOGITS(sv, st, ref);
#pragma unroll
        for (int j = 0; j < 6; ++j) { __builtin_amdgcn_sched_group_barrier(0x008, 1, 0); __builtin_amdgcn_sched_group_barrier(0x002, 4, 0); }
        if (__builtin_expect(__any(!(SV_MAX(sv) <= 16.f)), 0)) {
#pragma unroll
          for (int r = 0; r < 4; ++r) {
            const float mx = row_max(fmaxf(sv[r], sv[4 + r])), dm = mx <= 16.f ? 0.f : mx;
            ref[r] += dm; sv[r] -= dm; sv[4 + r] -= dm;
          }
          moved = 1;
        }
        if (__builtin_expect(moved, 0)) {
          if (n == 0) *(LAS f32x4*)(lds + LDS_PK + pk * PK_B + 1280 + 16 * g) = ref;
        }
#pragma unroll
        for (int j = 0; j < 8; ++j) sv[j] = __builtin_amdgcn_exp2f(sv[j]);
        PUBLISH(sv, st, rec, pk, 2 * s + 2 + moved);
      }
    } else {
      const int ftr = ((g & 1) << 2) | ((tq >> 1) << 1) | (g >> 1);
      const int fty = (g & 1) * 4 + (tq >> 1) * 3;
      int otr[4], oty[4];
#pragma unroll
      for (int t = 0; t < 4; ++t) {
        otr[t] = (w - 1) * PART + 128 * (4 * g + tq) + 16 * ((2 * t + (tp >> 1)) ^ ftr) + 8 * (tp & 1);
        oty[t] = CRS + (w - 1) * 128 + 384 * (4 * (g & 1) + tq) + 16 * ((2 * t + (tp >> 1)) ^ fty) + 8 * (tp & 1);
      }
      int opk = LDS_PK + 320 * tp + 8 * (4 * g + tq), ork = LDS_PK + 320 * (g >> 1) + 256 + 8 * (4 * (g & 1) + tq);
      asm volatile("" : "+v"(opk), "+v"(ork), "+v"(oty[0]), "+v"(oty[1]), "+v"(oty[2]), "+v"(oty[3]));
#define RD_T(ta, ty, so_) _Pragma("unroll") for (int t_ = 0; t_ < 4; ++t_) { \
    ta[t_] = vtr2(lds + otr[t_] + (so_), lds + otr[t_] + (so_) + 2048); ty[t_] = vtr(lds + oty[t_] + (so_)); }
      f32x4 acc[4] = {z, z, z, z}, tac = z;
#define RD_P(o_) \
      pf = vtr2(lds + opk + (o_), lds + opk + (o_) + 128); \
      sf = vtr2(lds + ork + (o_), lds + ork + (o_) + 640)
#define TWICE(ty) asm volatile("s_nop 2"); _Pragma("unroll") for (int t = 0; t < 4; ++t) { u32x2 lo_, hi_; \
      asm volatile("v_mov_b64 %0, %2\n\tv_mov_b64 %1, %2" : "=&v"(lo_), "=&v"(hi_) : "v"(ty[t])); t2[t] = u32x4{lo_[0], lo_[1], hi_[0], hi_[1]}; } \
      asm volatile("s_nop 1" : "+v"(t2[0]), "+v"(t2[1]), "+v"(t2[2]), "+v"(t2[3]))
#define MF_PX(ta) \
      tac = mfma(sf, pf, tac); \
      _Pragma("unroll") for (int t = 0; t < 4; ++t) acc[t] = mfma(ta[t], pf, acc[t])
#define MF_PY() \
      _Pragma("unroll") for (int t = 0; t < 4; ++t) acc[t] = mfma(t2[t], pf, acc[t])
#define MOVE_REF(word_, o_) do { moved |= (word_) & 1; if (__builtin_expect(moved, 0)) { \
            const float m_ = *(LAS const float*)(lds + (((word_) & 1) ? LDS_PK + (o_) + 1280 : LDS_CTL + CT_MAX) + 4 * n); \
            Mtop = fmaxf(Mtop, m_); \
            const bool drop = m_ < Mtop - 64.f; \
            const float mn = drop ? M : m_, fa = __builtin_amdgcn_exp2f(M - mn); \
            M = mn; \
            _Pragma("unroll") for (int t = 0; t < 4; ++t) acc[t] *= fa; \
            tac *= fa; \
            if (drop) pf = u32x4{0u, 0u, 0u, 0u}; \
          } } while (0)
      const unsigned flo = FLAG_OFF + (wg & 7) * 768 + (w - 1) * 192 + (lane < 24 ? lane : 23) * 8;
      float M, Mtop;
      int moved = 0;
      u32x4 wvv[24], pf, sf, t2[4];
      {
        u32x4 ta[8][4], xx[6];
        u32x2 ty[8][4], fl;
        unsigned cc[6];
        const int qo = QK_OFF + (wg & 3) * QK_COPY + (2 * w - 2) * QK_REC;
#define QK_LOAD() _Pragma("unroll") for (int j = 0; j < 6; ++j) { \
          xx[j] = ldb128<16>(rws, lane * 16, qo + ((j >> 1) * 6 + (j & 1)) * QK_REC); \
          cc[j] = __builtin_amdgcn_raw_buffer_load_b32(rws, lane * 4, qo + ((j >> 1) * 6 + (j & 1)) * QK_REC + 1024, 16); }
#pragma unroll
        for (int u = 0; u < 8; ++u) {
          if (u == QKAT) {
#pragma unroll 1
            for (int spin = 0; spin < 64; ++spin) {
              if ((long long)(wall_clock64() - tstart) >= GATE) break;
              __builtin_amdgcn_s_sleep(2);
            }
            if (wg < NPR) lds_wait(ctl + CT_PD, 4);
            QK_LOAD();
#pragma unroll 1
            for (int spin = 0; spin < (1 << 18); ++spin) {
              unsigned df = 0u;
#pragma unroll
              for (int j = 0; j < 6; ++j) df |= qk_check(xx[j], (unsigned)tg) ^ cc[j];
              if (__all(df == 0u)) break;
              asm volatile("" ::: "memory");
              QK_LOAD();
            }
#pragma unroll
            for (int h = 0; h < 2; ++h) {
              u32x4 sx;
#pragma unroll
              for (int i = 0; i < 4; ++i) sx[i] = add3(xx[h][i], xx[2 + h][i], xx[4 + h][i]);
              *(LAS u32x4*)(lds + LDS_QK + (2 * w - 2 + h) * 1024 + lane * 16) = sx;
            }
            lds_inc(CT_QK, ctl);
            SB();
          }
          lds_wait(ctl + CT_FULL, 3 * u + w);
          RD_T(ta[u], ty[u], u * SLOT);
          lds_inc(CT_FREE + 4 * u, ctl);
          SB();
        }
        fl = __builtin_bit_cast(u32x2, __builtin_amdgcn_raw_buffer_load_b64(rws, flo, 0, 16));
#pragma unroll
        for (int u = 0; u < 12; ++u) {
          const int word = lds_spin(CT_PRDY + 4 * (u % NPK), ctl, 2 * u + 2);
          if (u == 0) Mtop = M = *(LAS const float*)(ctl + CT_MAX + 4 * n);
          RD_P((u % NPK) * PK_B);
          if (u >= 8) MOVE_REF(word, (u % NPK) * PK_B);
          lds_inc(CT_PCON + 4 * (u % NPK), ctl);
          SB();
          MF_PX(ta[u & 7]);
          SB();
          TWICE(ty[u & 7]);
          MF_PY();
          asm volatile("" : "+v"(acc[0]), "+v"(acc[1]), "+v"(acc[2]), "+v"(acc[3]), "+v"(tac));
          SB();
#define FLAGS_UP() __all(fl[0] == (unsigned)tg && (fl[1] & 255u) == (unsigned)(tg >> 32))
          if (u == T0AT && w == 1 && FLAGS_UP()) {
            int d = (int)(fl[1] - ((unsigned)tstart << 8)) >> 8;
            d += __builtin_amdgcn_mov_dpp(d, XOR8, 0xf, 0xf, true); d += __builtin_amdgcn_mov_dpp(d, ROR4, 0xf, 0xf, true);
            d += __builtin_amdgcn_mov_dpp(d, 0x122, 0xf, 0xf, true); d += __builtin_amdgcn_mov_dpp(d, 0x121, 0xf, 0xf, true);
            lds_put(CT_T0, ctl, ((unsigned)tstart + min(max(d >> 4, -300), 300)) | 1u);
          }
          if (u < 4) {
            NEED(3 * (u + 8) + w);
            RD_T(ta[u], ty[u], ((u + 8) % NS) * SLOT);
            lds_inc(CT_FREE + 4 * ((u + 8) % NS), ctl);
            SB();
          }
        }
#pragma unroll 1
        for (int spin = 0; spin < (1 << 18); ++spin) {
          if (FLAGS_UP()) break;
          asm volatile("" ::: "memory");
          fl = __builtin_bit_cast(u32x2, __builtin_amdgcn_raw_buffer_load_b64(rws, flo, 0, 16));
        }
        asm volatile("" ::: "memory");
      }
      int slot = 3, pk = 12 % NPK;
      unsigned rdy = 0u;
      const int kro[3] = {(w - 1) * 4096, (w == 3 ? 0 : w) * 4096, (w == 1 ? 2 : w - 2) * 4096};
#define RD_PACKET() do { int cnt_; u32x2 p0_, p1_, p2_, p3_; \
        word = __builtin_amdgcn_readfirstlane(rdy); \
        asm volatile("s_mov_b32 %1, 0x400000\n1:\n\tds_read_b32 %2, %7 offset:%11\n\t" \
                     "ds_read_b64_tr_b16 %5, %9\n\tds_read_b64_tr_b16 %6, %9 offset:640\n\t" \
                     "ds_read_b64_tr_b16 %3, %8\n\tds_read_b64_tr_b16 %4, %8 offset:128\n\t" \
                     "s_cmp_ge_i32 %0, %10\n\ts_cbranch_scc1 2f\n\ts_waitcnt lgkmcnt(4)\n\tv_readfirstlane_b32 %0, %2\n\t" \
                     "s_cmp_ge_i32 %0, %10\n\ts_cbranch_scc1 2f\n\ts_sub_u32 %1, %1, 1\n\ts_cbranch_scc0 1b\n2:" \
                     : "+s"(word), "=&s"(cnt_), "=&v"(rdy), "=&v"(p0_), "=&v"(p1_), "=&v"(p2_), "=&v"(p3_) \
                     : "v"(ctl + 4 * pk), "v"(lds + opk + pk * PK_B), "v"(lds + ork + pk * PK_B), "s"(2 * s + 2), "i"(CT_PRDY) : "memory", "scc"); \
        asm volatile("" : "+s"(word)); \
        pf = u32x4{p0_[0], p0_[1], p1_[0], p1_[1]}; sf = u32x4{p2_[0], p2_[1], p3_[0], p3_[1]}; } while (0)
#define GOT_PACKET() do { waitlds<0>(); asm volatile("" : "+v"(pf), "+v"(sf) : "v"(rdy)); MOVE_REF(word, pk * PK_B); } while (0)
#define RD_Y(t_, p_) do { const u32x2 lo_ = vtr(p_); asm volatile("" ::: "memory"); const u32x2 hi_ = vtr(p_); \
        t2[t_] = u32x4{lo_[0], lo_[1], hi_[0], hi_[1]}; } while (0)
      u32x4 ta[4];
      int s = 12, word;
      NEED(36 + w);
#pragma unroll
      for (int t = 0; t < 4; ++t) { ta[t] = vtr2(lds + otr[t] + 2 * SLOT, lds + otr[t] + 2 * SLOT + 2048); RD_Y(t, lds + oty[t] + 2 * SLOT); }
      lds_inc(CT_FREE + 8, ctl);
#pragma unroll 1
      for (; s < NT - 1; ++s) {
        RD_PACKET();
        LAS const char *px[4], *py[4];
#pragma unroll
        for (int t = 0; t < 4; ++t) { px[t] = lds + otr[t] + slot * SLOT; py[t] = lds + oty[t] + slot * SLOT; asm volatile("" : "+v"(px[t]), "+v"(py[t])); }
        NEED(3 * s + 3 + w);
        GOT_PACKET();
        SB();
        tac = mfma(sf, pf, tac);
        asm volatile("" : "+v"(tac));
        SB();
        lds_inc(CT_PCON, ctl + 4 * pk);
        pk = pk == NPK - 1 ? 0 : pk + 1;
#pragma unroll
        for (int t = 0; t < 4; ++t) { acc[t] = mfma(ta[t], pf, acc[t]); SB(); ta[t] = vtr2(px[t], px[t] + 2048); SB(); }
        rdy = *(volatile LAS unsigned*)(ctl + CT_PRDY + 4 * pk);
#pragma unroll
        for (int t = 0; t < 4; ++t) { acc[t] = mfma(t2[t], pf, acc[t]); SB(); RD_Y(t, py[t]); SB(); }
        lds_inc(CT_FREE, ctl + 4 * slot);
        if (s < 12 + 24 / WVN) {
#pragma unroll
          for (int u = 0; u < 24 / WVN; ++u)
            if (s - 12 == u) {
#pragma unroll
              for (int j = WVN * u; j < WVN * u + WVN; ++j)
                wvv[j] = ldb128<16>(rws, lane * 16 + (j & 3) * 1024, WVO_OFF + (w - 1) * 24576 + (j / 12) * 12288 + kro[(j % 12) >> 2]);
            }
        }
        slot = slot == NS - 1 ? 0 : slot + 1;
      }
      LAS char* xc = lds + slot * SLOT + lane * 16;
      LAS char* xw = xc + (kro[0] >> 1);
      LAS const char *xr1 = xc + (kro[1] >> 1), *xr2 = xc + (kro[2] >> 1);
      unsigned mo = (wg * NDST + 64 * (w - 1) + n) * 64 + g * 16;
      asm volatile("" : "+v"(xw), "+v"(xr1), "+v"(xr2), "+v"(mo));
      waitvm<0>();
      RD_PACKET();
      GOT_PACKET();
      SB();
      tac = mfma(sf, pf, tac);
      asm volatile("" : "+v"(tac));
      SB();
#pragma unroll
      for (int t = 0; t < 2; ++t) acc[t] = mfma(t2[t], pf, mfma(ta[t], pf, acc[t]));
      SB();
#pragma unroll
      for (int t = 2; t < 4; ++t) acc[t] = mfma(t2[t], pf, mfma(ta[t], pf, acc[t]));
      const float L = tac[2] + tac[3], rl = __builtin_amdgcn_rcpf(L), ct = -(tac[0] + tac[1]) * rl;
      u32x4 b[6];
      b[0] = pack8(acc[0] * rl + ct, acc[1] * rl + ct);
      *(LAS u32x4*)xw = b[0];
#pragma unroll
      for (int j = 0; j < 4; ++j) { __builtin_amdgcn_sched_group_barrier(0x008, 1, 0); __builtin_amdgcn_sched_group_barrier(0x002, 2, 0); }
      SB();
      b[1] = pack8(acc[2] * rl + ct, acc[3] * rl + ct);
      *(LAS u32x4*)(xw + 1024) = b[1];
      lds_inc(CT_XD, ctl);
      SB();
      f32x4 o[4];
#pragma unroll
      for (int t = 0; t < 4; ++t) o[t] = mfma(wvv[12 * (t >> 1) + (t & 1)], b[0], z);
      const float lse = M + __builtin_amdgcn_logf(L);
      unsigned lb8 = __float_as_uint(lse) >> (8 * g), xd;
      asm volatile("" : "+v"(lb8), "+v"(o[0]), "+v"(o[1]), "+v"(o[2]), "+v"(o[3]));
      SB();
#define RD_SLICES "ds_read_b32 %0, %7 offset:%10\n\tds_read_b128 %1, %8\n\tds_read_b128 %2, %8 offset:1024\n\tds_read_b128 %3, %9\n\tds_read_b128 %4, %9 offset:1024\n"
      asm volatile(RD_SLICES : "=&v"(xd), "=&v"(b[2]), "=&v"(b[3]), "=&v"(b[4]), "=&v"(b[5]) : "i"(0), "i"(0), "v"(ctl), "v"(xr1), "v"(xr2), "i"(CT_XD) : "memory");
      SB();
#pragma unroll
      for (int t = 0; t < 4; ++t) o[t] = mfma(wvv[12 * (t >> 1) + 2 + (t & 1)], b[1], o[t]);
      unsigned tt[4];
#pragma unroll
      for (int r = 0; r < 4; ++r) tt[r] = (((lb8 >> (2 * r)) & 3u) << 10) | ((unsigned)(tg >> (10 * r)) & 1023u);
      asm volatile("" : "+v"(tt[0]), "+v"(tt[1]), "+v"(tt[2]), "+v"(tt[3]), "+v"(o[0]), "+v"(o[1]), "+v"(o[2]), "+v"(o[3]));
      SB();
      {
        int cnt_, c_;
        asm volatile("s_mov_b32 %6, 0x400000\n\ts_branch 2f\n1:\n\t" RD_SLICES "2:\n\ts_waitcnt lgkmcnt(4)\n\tv_readfirstlane_b32 %5, %0\n\t"
                     "s_cmp_ge_i32 %5, 3\n\ts_cbranch_scc1 3f\n\ts_sub_u32 %6, %6, 1\n\ts_cbranch_scc0 1b\n3:\n\ts_waitcnt lgkmcnt(0)"
                     : "+v"(xd), "+v"(b[2]), "+v"(b[3]), "+v"(b[4]), "+v"(b[5]), "=&s"(c_), "=&s"(cnt_)
                     : "v"(ctl), "v"(xr1), "v"(xr2), "i"(CT_XD) : "memory", "scc");
      }
      SB();
#define MAIL(t_) stb128(u32x4{tagf(o[t_][0], tt[0]), tagf(o[t_][1], tt[1]), tagf(o[t_][2], tt[2]), tagf(o[t_][3], tt[3])}, rws, mo + (t_) * 1024)
#pragma unroll
      for (int k = 2; k < 6; ++k)
#pragma unroll
        for (int t = 0; t < 2; ++t) o[t] = mfma(wvv[2 * k + t], b[k], o[t]);
      SB();
#pragma unroll
      for (int k = 2; k < 6; ++k) o[2] = mfma(wvv[12 + 2 * k], b[k], o[2]);
      SB();
#pragma unroll
      for (int k = 2; k < 6; ++k) o[3] = mfma(wvv[13 + 2 * k], b[k], o[3]);
      MAIL(0); MAIL(1);
#pragma unroll
      for (int j = 0; j < 4; ++j) {
        __builtin_amdgcn_sched_group_barrier(0x008, 1, 1); __builtin_amdgcn_sched_group_barrier(0x002, 2, 1);
        if (j & 1) __builtin_amdgcn_sched_group_barrier(0x040, 1, 1);
      }
      SB();
      MAIL(2); MAIL(3);
      if (w == 3 && n == dq && g == 0) *(LAS float*)(ctl + CT_REF) = lse;
      lds_inc(CT_ST, ctl);
    }
  }
  COORDS();

  if (wg < NDST) {
    const unsigned t0 = (unsigned)tg & 1023u, t1 = (unsigned)(tg >> 10) & 1023u, t2 = (unsigned)(tg >> 20) & 1023u, t3 = (unsigned)(tg >> 30) & 1023u;
    lds_spin(CT_ST, ctl, 3);
    float ref = *(LAS const float*)(ctl + CT_REF);
    if (RGATE) {
      const unsigned t0s = lds_get(ctl + CT_T0), t0 = t0s ? t0s : (unsigned)tstart;
#pragma unroll 1
      for (int spin = 0; spin < 256; ++spin) {
        if ((int)((unsigned)wall_clock64() - t0) >= RGATE) break;
        __builtin_amdgcn_s_sleep(1);
      }
    }
    LAS char* fin = lds + (NT >= 30 ? NT - 30 : NT - 20) * SLOT + 8192;
    u32x4 A = {0u, 0u, 0u, 0u}, B = A;
    {
      const unsigned ao = (tid >> 2) * (NDST * 64) + (tid & 3) * 16;
      bool oka = false, okb = false;
#pragma unroll 1
      for (int spin = 0; spin < (1 << 18); ++spin) {
        if (!oka) A = ldb128<16>(rws, ao, dst * 64);
        if (!okb) B = ldb128<16>(rws, ao, dst * 64 + 128 * NDST * 64);
        oka = (((A[0] ^ t0) | (A[1] ^ t1) | (A[2] ^ t2) | (A[3] ^ t3)) & 1023u) == 0u;
        okb = (((B[0] ^ t0) | (B[1] ^ t1) | (B[2] ^ t2) | (B[3] ^ t3)) & 1023u) == 0u;
        if (__all(oka && okb)) break;
        asm volatile("" ::: "memory");
      }
    }
    {
      unsigned la = (((A[0] >> 10) & 3u) | ((A[1] >> 8) & 12u) | ((A[2] >> 6) & 48u) | ((A[3] >> 4) & 192u)) << (8 * (lane & 3));
      unsigned lb_ = (((B[0] >> 10) & 3u) | ((B[1] >> 8) & 12u) | ((B[2] >> 6) & 48u) | ((B[3] >> 4) & 192u)) << (8 * (lane & 3));
      la |= __builtin_amdgcn_mov_dpp(la, 0xB1, 0xf, 0xf, true); lb_ |= __builtin_amdgcn_mov_dpp(lb_, 0xB1, 0xf, 0xf, true);
      la |= __builtin_amdgcn_mov_dpp(la, 0x4E, 0xf, 0xf, true); lb_ |= __builtin_amdgcn_mov_dpp(lb_, 0x4E, 0xf, 0xf, true);
      const float Ma = __uint_as_float(la), Mb = __uint_as_float(lb_);
      float M = fmaxf(Ma, Mb);
      if (!__all(M - ref <= 64.f)) {
        M = fmaxf(M, dpp<ROR4>(M)); M = fmaxf(M, dpp<XOR8>(M));
        ref = xrow16_max(M);
      }
      const float wa = __builtin_amdgcn_exp2f(Ma - ref), wb = __builtin_amdgcn_exp2f(Mb - ref);
      f32x4 v;
#pragma unroll
      for (int k = 0; k < 4; ++k) {
        float s = wa * __uint_as_float(A[k] & 0xfffff000u) + wb * __uint_as_float(B[k] & 0xfffff000u);
        s += dpp<ROR4>(s); s += dpp<XOR8>(s);
        v[k] = xrow16_sum(s);
      }
      float l = wa + wb;
      l += dpp<ROR4>(l); l += dpp<XOR8>(l);
      l = xrow16_sum(l);
      if (lane < 4) *(LAS f32x4*)(fin + w * 256 + lane * 16) = v;
      if (lane == 4) *(LAS f32x2*)(fin + w * 256 + 64) = f32x2{ref, l};
    }
    RAW_BARRIER();
    if (tid < 16) {
      f32x2 ml[NW];
      float av[NW], MM = -INFINITY;
#pragma unroll
      for (int p = 0; p < NW; ++p) { ml[p] = *(LAS f32x2*)(fin + p * 256 + 64); av[p] = *(LAS float*)(fin + p * 256 + tid * 4); MM = fmaxf(MM, ml[p][0]); }
      float mm = ml[0][0];
#pragma unroll
      for (int p = 1; p < NW; ++p) mm = fminf(mm, ml[p][0]);
      if (__builtin_expect(__any(mm != MM), 0)) {
#pragma unroll
        for (int p = 0; p < NW; ++p) { const float wt = __builtin_amdgcn_exp2f(ml[p][0] - MM); av[p] *= wt; ml[p][1] *= wt; }
      }
      const float a = ((av[0] + av[1]) + (av[2] + av[3])) + ((av[4] + av[5]) + (av[6] + av[7]));
      const float L = ((ml[0][1] + ml[1][1]) + (ml[2][1] + ml[3][1])) + ((ml[4][1] + ml[5][1]) + (ml[6][1] + ml[7][1]));
      const float o = bflo(qres) + a * __builtin_amdgcn_rcpf(L);
      const unsigned ob = cvtpk(o, __uint_as_float(__builtin_amdgcn_mov_dpp(__float_as_uint(o), 0xB1, 0xf, 0xf, true)));
      if (!(tid & 1)) __builtin_amdgcn_raw_buffer_store_b32(ob, mkrs(out, NQ * C * 2), (dout + tid) * 2, 0, 16);
      if (wg == 0 && tid == 0) stb64(u32x2{(unsigned)tg, (unsigned)(tg >> 32)}, rws, EP_OFF);
    }
  }
}

#undef rws
#define WS_AT(size_) (((size_) - (64u << 20)) & ~(size_t)0x1fffff)
extern "C" void kernel_launch(void* const* d_in, const int* in_sizes, int n_in, void* d_out, int out_size, void* d_ws, size_t ws_size,
                              hipStream_t stream) {
  dino_main<<<NWG, 512, 0, stream>>>((const u16*)d_in[0], (const u16*)d_in[1], (const u16*)d_in[2], (const u16*)d_in[3], (const u16*)d_in[4],
                                     (const u16*)d_in[5], (const u16*)d_in[6], (char*)d_ws + WS_AT(ws_size), (u16*)d_out);
}
```

```cpp
#include <hip/hip_runtime.h>
#include <hip/hip_bf16.h>
#include <stdint.h>

typedef unsigned short u16;
using u32x2 = __attribute__((ext_vector_type(2))) unsigned;
using u32x4 = __attribute__((ext_vector_type(4))) unsigned;
using f32x2 = __attribute__((ext_vector_type(2))) float;
using f32x4 = __attribute__((ext_vector_type(4))) float;
using s16x8 = __attribute__((ext_vector_type(8))) short;
using bf16x2_t = __attribute__((ext_vector_type(2))) __bf16;
typedef short v4i16_t __attribute__((ext_vector_type(4)));
#define LAS __attribute__((address_space(3)))

constexpr int C = 192, NQ = 16, W0 = 512, W1 = 256, NWG = 256, NW = 8;
constexpr float EPS = 1e-5f;
constexpr float QSCALE = 0.07216878364870322f * 1.4426950408889634f;

constexpr int PART = 4096, CRS = 3 * PART;
constexpr int SLOT = CRS + 3072, NS = 10;
constexpr int LDS_QK = NS * SLOT;
constexpr int NPK = 7;
constexpr int LDS_PK = LDS_QK, PK_B = 1344;
constexpr int LDS_CTL = LDS_PK + NPK * PK_B;
constexpr int LDS_SINK = LDS_CTL + 384;
constexpr int LDS_BYTES = LDS_SINK + 256;
constexpr int PSCR = 8 * SLOT;
constexpr int CT_FULL = 8, CT_WV = 4, CT_PA = 0, CT_PB = 12, CT_QR = 16, CT_PD = 20, CT_T0 = 24, CT_XD = 28, CT_TG = 32, CT_QK = 40, CT_ST = 44,
              CT_FREE = 48, CT_FAR = 88, CT_ONE = 112, CT_PRDY = 128, CT_PCON = 160, CT_MAX = 256;

constexpr int MB_BYTES = NWG * NWG * 32;
constexpr int EP_OFF = MB_BYTES;
constexpr int FLAG_OFF = EP_OFF + 512;
constexpr int WVO_OFF = EP_OFF + 8192;
constexpr int QK_OFF = WVO_OFF + C * C * 2;
constexpr int QK_REC = 1280, QK_COPY = 18 * QK_REC;
constexpr int WS_BYTES = QK_OFF + 4 * QK_COPY;

#define GIVES 0x000000003232ull
constexpr int NPR = 36, NTK = (NWG - NPR) / 2;
__host__ __device__ constexpr int gives(int wg) { return (int)((GIVES >> (wg < NPR ? 4 * (wg & 1) : 16 + 4 * (wg & 7))) & 15); }
struct Tabs { unsigned seq[112]; unsigned take[NTK]; };
constexpr Tabs make_tabs() {
  Tabs t{};
  for (int i = 0; i < 112; ++i) t.seq[i] = i + 1;
  int k = 0;
  for (int wg = 0; wg < 256; ++wg)
    for (int j = 0; j < gives(wg); ++j, ++k) t.take[k % NTK] += (unsigned)(j << 8 | wg) << (10 * (k / NTK));
  return t;
}
constexpr int given() { int k = 0; for (int wg = 0; wg < 256; ++wg) k += gives(wg); return k; }
constexpr int GIVEN = given();
static_assert(GIVEN <= NTK * 3, "too many tiles sent elsewhere");
__device__ const Tabs tabs = make_tabs();
#define QKAT 5
#define WVN 3
#define DEPTH 6
#define PACE0 30
#define PACE1 1830
#define RGATE 2050
#define T0AT 3
#define LAND0 200
#define DRY 1
#define GATE 420
#define CODE_BYTES 25664

__device__ __forceinline__ unsigned cvtpk(float lo, float hi) {
  bf16x2_t v = {(__bf16)lo, (__bf16)hi};
  return __builtin_bit_cast(unsigned, v);
}
__device__ __forceinline__ float bflo(unsigned x) { return __builtin_bit_cast(float, x << 16); }
__device__ __forceinline__ float bfhi(unsigned x) { return __builtin_bit_cast(float, x & 0xffff0000u); }
template <int CTRL> __device__ __forceinline__ float dpp(float x) {
  return __builtin_bit_cast(float, __builtin_amdgcn_mov_dpp(__builtin_bit_cast(int, x), CTRL, 0xf, 0xf, true));
}
constexpr int XOR8 = 0x128, ROR4 = 0x124;
template <bool HI8> __device__ __forceinline__ float half_copy(float x) {
  const int v = __builtin_bit_cast(int, x);
  return __builtin_bit_cast(float, __builtin_amdgcn_update_dpp(v, v, XOR8, 0xf, HI8 ? 0xc : 0x3, false));
}
__device__ __forceinline__ float row_max(float x) {
  x = fmaxf(x, dpp<XOR8>(x)); x = fmaxf(x, dpp<ROR4>(x)); x = fmaxf(x, dpp<0x122>(x));
  return fmaxf(x, dpp<0x121>(x));
}
__device__ __forceinline__ f32x2 swap32(float a, float b) {
  auto s = __builtin_amdgcn_permlane32_swap(__float_as_uint(a), __float_as_uint(b), false, false);
  return f32x2{__uint_as_float(s[0]), __uint_as_float(s[1])};
}
__device__ __forceinline__ f32x2 swap16(float a, float b) {
  auto s = __builtin_amdgcn_permlane16_swap(__float_as_uint(a), __float_as_uint(b), false, false);
  return f32x2{__uint_as_float(s[0]), __uint_as_float(s[1])};
}
__device__ __forceinline__ float xrow16_max(float x) {
  auto s = __builtin_amdgcn_permlane16_swap(__float_as_uint(x), __float_as_uint(x), false, false);
  x = fmaxf(__uint_as_float(s[0]), __uint_as_float(s[1]));
  auto t = __builtin_amdgcn_permlane32_swap(__float_as_uint(x), __float_as_uint(x), false, false);
  return fmaxf(__uint_as_float(t[0]), __uint_as_float(t[1]));
}
__device__ __forceinline__ float xrow16_sum(float x) {
  auto s = __builtin_amdgcn_permlane16_swap(__float_as_uint(x), __float_as_uint(x), false, false);
  x = __uint_as_float(s[0]) + __uint_as_float(s[1]);
  auto t = __builtin_amdgcn_permlane32_swap(__float_as_uint(x), __float_as_uint(x), false, false);
  return __uint_as_float(t[0]) + __uint_as_float(t[1]);
}
__device__ __forceinline__ f32x4 mfma(u32x4 a, u32x4 b, f32x4 c) {
  return __builtin_amdgcn_mfma_f32_16x16x32_bf16(__builtin_bit_cast(s16x8, a), __builtin_bit_cast(s16x8, b), c, 0, 0, 0);
}
__device__ __forceinline__ u32x2 vtr(LAS const char* p) {
  return __builtin_bit_cast(u32x2, __builtin_amdgcn_ds_read_tr16_b64_v4i16((LAS v4i16_t*)p));
}
__device__ __forceinline__ u32x4 vtr2(LAS const char* p, LAS const char* q_) {
  const u32x2 a = vtr(p), b = vtr(q_);
  return u32x4{a[0], a[1], b[0], b[1]};
}
__device__ __forceinline__ float sel4(f32x4 v, int r) {
  const float a = (r & 1) ? v[1] : v[0], b = (r & 1) ? v[3] : v[2];
  return (r & 2) ? b : a;
}
using RSRC = __amdgpu_buffer_rsrc_t;
__device__ __forceinline__ RSRC mkrs(const void* p, int bytes) { return __builtin_amdgcn_make_buffer_rsrc(const_cast<void*>(p), (short)0, bytes, 0x00020000); }
__device__ __forceinline__ u32x4 mkrs4(const void* p, unsigned bytes) {
  const unsigned long long b = (unsigned long long)p;
  return u32x4{(unsigned)b, (unsigned)(b >> 32) & 0xffffu, bytes, 0x00020000u};
}
template <int AUX> __device__ __forceinline__ u32x4 ldb128(RSRC r, unsigned vo, int so) { return __builtin_bit_cast(u32x4, __builtin_amdgcn_raw_buffer_load_b128(r, vo, so, AUX)); }
#define SB() __builtin_amdgcn_sched_barrier(0)
#define WAVE_SYNC() do { __builtin_amdgcn_fence(__ATOMIC_RELEASE, "wavefront"); __builtin_amdgcn_wave_barrier(); __builtin_amdgcn_fence(__ATOMIC_ACQUIRE, "wavefront"); } while (0)
#define RAW_BARRIER() asm volatile("s_waitcnt lgkmcnt(0)\n\ts_barrier" ::: "memory")
#define DMA(m0_, vo_, rs_, so_) asm volatile("s_mov_b32 m0, %0\n\ts_nop 0\n\tbuffer_load_dwordx4 %1, %2, %3 offen lds" :: "s"(m0_), "v"(vo_), "s"(rs_), "s"(so_) : "memory")
template <int N> __device__ __forceinline__ void waitlds() { __builtin_amdgcn_s_waitcnt(0xc07f | (N << 8)); }
template <int N> __device__ __forceinline__ void waitvm() { asm volatile("s_waitcnt vmcnt(%0)" :: "i"(N) : "memory"); }


__device__ __forceinline__ void lds_add(LAS char* p, int lane, unsigned v) {
  asm volatile("" ::: "memory");
  if (lane == 0) __hip_atomic_fetch_add((LAS unsigned*)p, v, __ATOMIC_RELAXED, __HIP_MEMORY_SCOPE_WORKGROUP);
  asm volatile("" ::: "memory");
}
__device__ __forceinline__ void lds_set(LAS char* p, int lane, unsigned v) {
  asm volatile("" ::: "memory");
  if (lane == 0) __hip_atomic_store((LAS unsigned*)p, v, __ATOMIC_RELAXED, __HIP_MEMORY_SCOPE_WORKGROUP);
  asm volatile("" ::: "memory");
}
__device__ __forceinline__ void lds_inc(int O, LAS char* p) {
  asm volatile("s_mov_b64 exec, 1\n\tds_add_u32 %0, %1 offset:%2\n\ts_mov_b64 exec, -1" :: "v"(p), "v"(1u), "i"(O) : "memory");
}
__device__ __forceinline__ void lds_put(int O, LAS char* p, unsigned v) {
  asm volatile("s_mov_b64 exec, 1\n\tds_write_b32 %0, %1 offset:%2\n\ts_mov_b64 exec, -1" :: "v"(p), "v"(v), "i"(O) : "memory");
}
__device__ __forceinline__ int lds_get(LAS char* p) {
  asm volatile("" ::: "memory");
  const unsigned v = __hip_atomic_load((LAS unsigned*)p, __ATOMIC_RELAXED, __HIP_MEMORY_SCOPE_WORKGROUP);
  asm volatile("" ::: "memory");
  return __builtin_amdgcn_readfirstlane(v);
}
__device__ __forceinline__ void lds_wait(LAS char* p, int target) {
#pragma unroll 1
  for (int spin = 0; spin < (1 << 22); ++spin) {
    if (lds_get(p) >= target) break;
    __builtin_amdgcn_s_sleep(1);
  }
}
__device__ __forceinline__ int lds_spin(int O, LAS char* p, int target) {
  int r, cnt = 1 << 22;
  unsigned t;
  asm volatile("1:\n\tds_read_b32 %1, %3 offset:%5\n\ts_waitcnt lgkmcnt(0)\n\tv_readfirstlane_b32 %0, %1\n\ts_cmp_ge_i32 %0, %4\n\ts_cbranch_scc1 2f\n\t"
               "s_sub_u32 %2, %2, 1\n\ts_cbranch_scc0 1b\n2:"
               : "=&s"(r), "=&v"(t), "+s"(cnt) : "v"(p), "s"(target), "i"(O) : "memory", "scc");
  return r;
}
__device__ __forceinline__ void lds_need(int O, int& have, LAS char* p, int target) {
  int cnt;
  unsigned t;
  asm volatile("s_cmp_ge_i32 %0, %4\n\ts_cbranch_scc1 2f\n\ts_mov_b32 %2, 0x400000\n"
               "1:\n\tds_read_b32 %1, %3 offset:%5\n\ts_waitcnt lgkmcnt(0)\n\tv_readfirstlane_b32 %0, %1\n\ts_cmp_ge_i32 %0, %4\n\ts_cbranch_scc1 2f\n\t"
               "s_sub_u32 %2, %2, 1\n\ts_cbranch_scc0 1b\n2:"
               : "+s"(have), "=&v"(t), "=&s"(cnt) : "v"(p), "s"(target), "i"(O) : "memory", "scc");
  asm volatile("" : "+s"(have));
}
__device__ __forceinline__ u32x4 pack8(f32x4 a, f32x4 b) { return u32x4{cvtpk(a[0], a[1]), cvtpk(a[2], a[3]), cvtpk(b[0], b[1]), cvtpk(b[2], b[3])}; }

__device__ __forceinline__ void stb128(u32x4 v, RSRC r, unsigned o) { __builtin_amdgcn_raw_buffer_store_b128(v, r, o, 0, 16); }
__device__ __forceinline__ void stb64(u32x2 v, RSRC r, unsigned o) { __builtin_amdgcn_raw_buffer_store_b64(v, r, o, 0, 16); }
__device__ __forceinline__ void stb32(unsigned v, RSRC r, unsigned o) { __builtin_amdgcn_raw_buffer_store_b32(v, r, o, 0, 16); }
__device__ __forceinline__ unsigned qk_check(u32x4 x, unsigned tg) {
  const unsigned a = (x[0] ^ __builtin_amdgcn_alignbit(x[1], x[1], 11)) * 0x9e3779b1u + tg;
  const unsigned b = (x[2] + __builtin_amdgcn_alignbit(x[3], x[3], 19)) * 0x85ebca77u;
  return __builtin_amdgcn_alignbit(a, a, 15) ^ b ^ (x[1] + x[3]);
}
__device__ __forceinline__ unsigned add3(unsigned a, unsigned b, unsigned c) {
  return cvtpk((bflo(a) + bflo(b)) + bflo(c), (bfhi(a) + bfhi(b)) + bfhi(c));
}
__global__ __launch_bounds__(512, 2) void dino_main(const u16* q, const u16* f0, const u16* f1, const u16* wq, const u16* wk, const u16* wv,
                                                     const u16* wo, char* ws, u16* out) {
  __shared__ __attribute__((aligned(128))) char smem[LDS_BYTES];
  LAS char* lds = (LAS char*)smem;
  LAS char* ctl = lds + LDS_CTL;
  const unsigned lb = (unsigned)(unsigned long long)lds;
  const int w = __builtin_amdgcn_readfirstlane(threadIdx.x >> 6);
  unsigned long long pc0;
  asm volatile("s_getpc_b64 %0" : "=s"(pc0));
#define COORDS() \
  int tid = threadIdx.x, wg = blockIdx.x; \
  asm volatile("" : "+v"(tid), "+s"(wg)); \
  __builtin_assume(tid >= 0 && tid < 512); __builtin_assume(wg >= 0 && wg < NWG); \
  const int lane = tid & 63, n = lane & 15, g = lane >> 4;       \
    \
  const int dq = (wg & 7) * 2 + ((wg >> 4) & 1), dst = (wg & ~31) + (wg & 7) * 4 + ((wg >> 3) & 3); \
  const int dj = 6 * ((wg >> 5) & 1) + min(lane & 7, 5); \
  const int dout = dq * C + 48 * (wg >> 6) + 16 * (dj >> 2) + (wg & 8) + ((lane & 8) >> 1) + (dj & 3); \
  const int NT = 32 - gives(wg) + (wg < NPR || (wg & 1) ? 0 : GIVEN / NTK + (((wg - NPR) >> 1) < GIVEN % NTK ? 1 : 0)); \
  const int xslot = (NT >= 30 ? NT - 30 : NT - 20) * SLOT;       \
  (void)xslot; (void)n; (void)g; (void)dst; (void)dout; (void)NT
#define ARGS_IN() asm volatile("s_waitcnt lgkmcnt(0)" :: "s"(q), "s"(f0), "s"(f1), "s"(wq), "s"(wk), "s"(wv), "s"(wo), "s"(ws), "s"(out) : "memory")
#define rws mkrs(ws, WS_BYTES)
#define PROJECT(k0_) do { \
      _Pragma("unroll") for (int k = k0_; k < 6; ++k) { o[0] = mfma(wvv[k], b[k], o[0]); o[1] = mfma(wvv[6 + k], b[k], o[1]); } \
      asm volatile("" : "+v"(o[0]), "+v"(o[1])); \
      SB(); \
      _Pragma("unroll") for (int k = k0_; k < 6; ++k) o[2] = mfma(wvv[12 + k], b[k], o[2]); \
      asm volatile("" : "+v"(o[2])); \
      SB(); \
      stb128(u32x4{cvtpk(o[0][0], o[0][1]), cvtpk(o[0][2], o[0][3]), cvtpk(o[1][0], o[1][1]), tw}, rws, mo); \
      SB(); \
      stb128(u32x4{cvtpk(o[1][2], o[1][3]), cvtpk(o[2][0], o[2][1]), cvtpk(o[2][2], o[2][3]), tw}, rws, mo + 1024); \
    } while (0)
  unsigned long long tg;
  unsigned qres = 0u;
  const unsigned long long tstart = wall_clock64();
  if (w != 0) {
    const unsigned l0 = threadIdx.x & 63;
    if (blockIdx.x < NPR) {
      const unsigned p6 = (blockIdx.x * 43) >> 8, pu = blockIdx.x - 6 * p6;
      unsigned vo = ((l0 * 16) ^ (((l0 * 4) & 0x40) | (l0 & 0x20))) + (l0 >> 3) * 256;
      const bool pv = p6 >= 3, wa = w >= 4, isq = !pv && (w & 6) == 2;
      const unsigned a6 = 6 * w - 24, pj = pv ? p6 - 3 : p6;
      unsigned so = (wa ? a6 : isq ? w - 2 : 4 * pu) * 3072 + (isq ? 0 : pv && !wa ? w - 1 : pj) * 128, ga = isq ? 128 : 3072;
      unsigned lo = lb + PSCR + (wa ? a6 : pv ? 4 * w + 20 : (0x1b181e00u >> (8 * w)) & 255u) * 1024, hi = lo + (wa ? 6144 : isq ? 3072 : 4096);
      asm volatile("" : "+v"(vo), "+s"(lo), "+s"(so), "+s"(hi), "+s"(ga));
      ARGS_IN();
      const u32x4 rw = mkrs4(wa ? (pv ? wo : wq) : pv ? wv : isq ? q : wk, isq ? NQ * C * 2 : C * C * 2);
#pragma unroll 1
      do { DMA(lo, vo, rw, so); lo += 1024; so += ga; } while (lo != hi);
    } else if (w == 7 && blockIdx.x >= 56 && blockIdx.x < 72) {
      const u32x4 rc = mkrs4((const void*)(pc0 & ~255ull), CODE_BYTES);
#pragma unroll 1
      for (int i = 0; i < CODE_BYTES; i += 4096)
        asm volatile("s_mov_b32 m0, %0\n\ts_nop 0\n\tbuffer_load_dword %1, %2, %3 offen lds" :: "s"(lb + LDS_SINK), "v"(l0 * 64), "s"(rc), "s"(i) : "memory");
    }
    RAW_BARRIER();
  }
  SB();
  if (__builtin_expect(w == 0, 1)) {
    const unsigned l0 = threadIdx.x & 63, g0 = blockIdx.x;
    unsigned y0 = (l0 * 16) ^ (l0 >= 48 ? 48 : 0), y1 = (l0 * 16) ^ (l0 >= 32 ? 64 : 48), y2 = (l0 * 16) ^ (l0 >= 16 ? 112 : 64);
    unsigned v0 = ((l0 * 16) ^ ((l0 * 2) & 0x60)) + (l0 >> 3) * 640 + 1024, v1 = (v0 ^ 16) - 640;
    unsigned v2 = v0 + W0 * C * 2 - 2048, v3 = v1 + W0 * C * 2 - 2048;
    unsigned b0 = g0 * 6144 + (g0 >> 5) * (W0 * C * 2), b1 = g0 * 3072;
    {
      const unsigned iv = (l0 & 15) >= CT_ONE / 8 ? 0x3f803f80u : (l0 >> 3) == CT_MAX / 64 ? 0xff800000u : 0u;
      *(LAS u32x2*)(ctl + 8 * min(l0, 47u)) = u32x2{l0 >= 47 ? 0x3f803f80u : iv, l0 >= 47 ? 0x3f803f80u : iv};
    }
    constexpr unsigned st0 = 16 * W0 * C * 2, st1 = 8 * W1 * C * 2;
    unsigned sb = lb, fo = CT_FREE, fv = 0u, tk = 0u;
    int lap = 0, need = 0;
    u32x4 rt = mkrs4(tabs.seq, 112 * 4);
    unsigned vz = 0u;
    asm volatile("" : "+v"(v0), "+v"(v1), "+v"(v2), "+v"(v3), "+v"(y0), "+v"(y1), "+v"(y2), "+v"(vz), "+s"(b0), "+s"(b1), "+s"(rt));
    ARGS_IN();
    const u32x4 r0 = mkrs4((const char*)f0 - 1024, W0 * W0 * C * 2 + 1024), r1 = mkrs4(f1, W1 * W1 * C * 2);
    if (g0 < NPR) __builtin_amdgcn_s_barrier();
#define UNIT(p_) do { unsigned so_; \
      asm volatile("s_add_u32 m0, %1, %10\n\ts_add_u32 %0, %2, %11\n\t" \
                   "buffer_load_dwordx4 %3, %7, %0 offen nt lds\n\tbuffer_load_dwordx4 %4, %7, %0 offen offset:1024 nt lds\n\t" \
                   "buffer_load_dwordx4 %5, %7, %0 offen offset:2048 nt lds\n\tbuffer_load_dwordx4 %6, %7, %0 offen offset:3072 nt lds\n\t" \
                   "s_mov_b32 m0, %12\n\ts_mov_b64 exec, 1\n\tbuffer_load_dword %13, %8, %9 offen offset:%14 lds\n\ts_mov_b64 exec, -1\n\t" \
                   "s_waitcnt vmcnt(%15)" \
                   : "=&s"(so_) : "s"(sb), "s"(b0), "v"(v0), "v"(v1), "v"(v2), "v"(v3), "s"(r0), "s"(rt), "s"(12 * s), "i"((p_) * PART), "i"((p_) * 128), \
                     "s"(lb + LDS_CTL + CT_FULL - 4 * (p_)), "v"(vz), "i"(4 * (p_)), "i"(6 * DEPTH) : "memory", "scc"); } while (0)
#define COARSE() \
      asm volatile("s_add_u32 m0, %0, %6\n\ts_nop 0\n\tbuffer_load_dwordx4 %1, %4, %5 offen nt lds\n\t" \
                   "buffer_load_dwordx4 %2, %4, %5 offen offset:1024 nt lds\n\tbuffer_load_dwordx4 %3, %4, %5 offen offset:2048 nt lds" \
                   :: "s"(sb), "v"(y0), "v"(y1), "v"(y2), "s"(r1), "s"(b1), "i"(CRS) : "memory", "scc")
#pragma unroll 1
    for (int s = 0; s < 2; ++s) {
      COARSE(); UNIT(0); UNIT(1); UNIT(2);
      sb += SLOT; b0 += st0; b1 += st1;
    }
    if (g0 >= NPR) __builtin_amdgcn_s_barrier();
    __builtin_amdgcn_s_setprio(3);
    COORDS();
    fo += 8;
    const int tau = __builtin_amdgcn_readfirstlane((int)((PACE1 - PACE0) * 256.f * __builtin_amdgcn_rcpf((float)NT)));
    unsigned due = PACE0 * 256u + 2 * tau, t0v = 0u;
    unsigned long long now = tstart;
#pragma unroll 1
    for (int s = 2; s < NT; ++s) {
      asm volatile("" : "+s"(now));
      const unsigned t0s = __builtin_amdgcn_readfirstlane(t0v), t0 = t0s ? t0s : (unsigned)tstart;
      if (PACE1 && (int)(((unsigned)now - t0) * 256u - due) < 0) {
#pragma unroll 1
        for (int spin = 0; spin < 256; ++spin) {
          if ((int)(((unsigned)wall_clock64() - t0) * 256u - due) >= 0) break;
          __builtin_amdgcn_s_sleep(1);
        }
      }
      due += tau;
      if (__builtin_expect((int)__builtin_amdgcn_readfirstlane(fv) < need, 0)) {
        lds_wait(ctl + (s == 8 ? CT_PD : fo), need);
      }
      COARSE();
      UNIT(0);
      UNIT(1);
      if (fo == CT_FREE + 4 * (NS - 1)) { fo = CT_FREE; lap += 4; } else fo += 4;
      need = s == 7 && wg < NPR ? 4 : lap;
      fv = *(volatile LAS unsigned*)(ctl + (s == 7 ? CT_PD : fo));
      if (s == 2 && NT > 32) tk = tabs.take[(wg - NPR) >> 1];
      if (PACE1) { now = wall_clock64(); t0v = *(volatile LAS unsigned*)(ctl + CT_T0); }
      UNIT(2);
      sb = fo == CT_FREE ? lb : sb + SLOT;
      b0 += st0; b1 += st1;
      if (s >= 31) {
        const unsigned xw = tk & 255u, xs = 31 - ((tk >> 8) & 3u);
        b0 = xw * 6144 + (xw >> 5) * (W0 * C * 2) + xs * st0; b1 = xw * 3072 + xs * st1;
        tk >>= 10;
      }
    }
    __builtin_amdgcn_s_setprio(0);
    u32x4 wvv[18], b[6];
    lds_wait(ctl + CT_WV, 1);
#pragma unroll
    for (int j = 0; j < 18; ++j) wvv[j] = ldb128<16>(rws, lane * 16 + (j & 3) * 1024, WVO_OFF + 54 * 1024 + (j >> 2) * 4096);
    qres = __builtin_amdgcn_raw_buffer_load_b16(mkrs(q, NQ * C * 2), dout * 2, 0, 0);
    tg = ((unsigned long long)((unsigned)lds_spin(CT_TG + 4, ctl, 1) & 0xffu) << 32) | (unsigned)lds_get(ctl + CT_TG);
    LAS const char *xr = lds + xslot + lane * 16, *xl = lds + xslot + 6144 + 4 * n;
    unsigned mo = wg * 8192 + 6144 + n * 64 + g * 16, tw = (unsigned)(tg >> (20 * (g & 1))) & 0xfffffu, sh = 12 * (g & 1), lse;
    asm volatile("" : "+v"(xr), "+v"(xl), "+v"(mo), "+v"(tw), "+v"(sh));
    __builtin_amdgcn_s_waitcnt(0x0f70);
    lds_spin(CT_XD, ctl, 1);
    {
      int cnt_, c_;
      unsigned t_;
      asm volatile("s_mov_b32 %1, 0x400000\n1:\n\tds_read_b32 %2, %10 offset:%13\n\tds_read_b128 %3, %11\n\tds_read_b128 %4, %11 offset:1024\n\t"
                   "ds_read_b128 %5, %11 offset:2048\n\tds_read_b128 %6, %11 offset:3072\n\tds_read_b128 %7, %11 offset:4096\n\t"
                   "ds_read_b128 %8, %11 offset:5120\n\tds_read_b32 %9, %12\n\ts_waitcnt lgkmcnt(7)\n\tv_readfirstlane_b32 %0, %2\n\t"
                   "s_cmp_ge_i32 %0, 3\n\ts_cbranch_scc1 2f\n\ts_sub_u32 %1, %1, 1\n\ts_cbranch_scc0 1b\n2:\n\ts_waitcnt lgkmcnt(0)"
                   : "=&s"(c_), "=&s"(cnt_), "=&v"(t_), "=&v"(b[0]), "=&v"(b[1]), "=&v"(b[2]), "=&v"(b[3]), "=&v"(b[4]), "=&v"(b[5]), "=&v"(lse)
                   : "v"(ctl), "v"(xr), "v"(xl), "i"(CT_XD) : "memory", "scc");
    }
    SB();
    f32x4 o[3] = {{0.f, 0.f, 0.f, 0.f}, {0.f, 0.f, 0.f, 0.f}, {0.f, 0.f, 0.f, 0.f}};
    tw |= (lse << sh) & 0xfff00000u;
    PROJECT(0);
    lds_inc(CT_ST, ctl);
  } else {
    COORDS();
    const int tq = (lane >> 2) & 3, tp = lane & 3;
    const bool pq = wg < 18;
    const bool pv = wg >= 18 && wg < NPR;
    const int pj = wg / 6, pu = wg % 6;
    const u32x4 one = {0x3f803f80u, 0x3f803f80u, 0x3f803f80u, 0x3f803f80u};
    const int fq = ((n & 2) << 1) | ((n & 4) >> 1);
    LAS const char* rq0 = lds + PSCR + 24576 + 3072 * (n >> 3) + 128 * (n & 7) + 16 * (g ^ fq);
    LAS const char* rq1 = lds + PSCR + 24576 + 3072 * (n >> 3) + 128 * (n & 7) + 16 * ((4 + g) ^ fq);
    if (pq && w >= 2) {
#pragma unroll 1
      for (int pass = w >= 4 ? 1 - DRY : 1; pass < 2; ++pass) {
        if (pass) {
          waitvm<0>();
          lds_inc(CT_PA, ctl);
        }
        if (w >= 4) {
          f32x4 sq = {0.f, 0.f, 0.f, 0.f}, rr = sq, cs = sq;
          const int ro = PSCR + 128 * (8 * g + tq) + 16 * (((w & 2) * 2 + tp) ^ ((tq & 2) << 1)) + 8 * (w & 1);
          u32x4 qv[6], af[6];
          lds_spin(CT_PA, ctl, pass ? 6 : 0);
#pragma unroll
          for (int s = 0; s < 6; ++s) {
            qv[s] = *(LAS const u32x4*)(((s & 1) ? rq1 : rq0) + (s >> 1) * 1024);
            af[s] = vtr2(lds + ro + s * 4096, lds + (ro ^ 32) + s * 4096 + 512);
          }
          SB();
#pragma unroll
          for (int s = 0; s < 6; ++s) { sq = mfma(one, qv[s], sq); rr = mfma(af[s], qv[s], rr); cs = mfma(af[s], one, cs); }
          const f32x4 qq = rr - (sq[0] * (1.f / C)) * cs;
          *(LAS u32x2*)(lds + LDS_QK + 4096 + ((w >> 1) & 1) * 1024 + lane * 16 + 8 * (w & 1)) = u32x2{cvtpk(qq[0], qq[1]), cvtpk(qq[2], qq[3])};
          if (pass) lds_inc(CT_PB, ctl);
        }
      }
    }
    SB();
    asm volatile("s_load_dwordx2 %0, %1, 0x0\n\ts_waitcnt lgkmcnt(0)" : "=s"(tg) : "s"(ws + EP_OFF) : "memory");
    tg = (tg + 0x100001ull) & 0xffffffffffull;
    if (w == 3 && lane == 0) *(LAS u32x2*)(ctl + CT_TG) = u32x2{(unsigned)tg, (unsigned)(tg >> 32) | 0x100u};
    if (pq && w == 1) {
#pragma unroll 1
      for (int pass = 1 - DRY; pass < 2; ++pass) {
        if (pass) waitvm<0>();
        u32x4 kf4[4], qv[6];
        const int ko = LDS_QK + 128 * (((n & 4) << 2) | (n & 11)) + 16 * (g ^ ((n & 2) << 1));
#pragma unroll
        for (int i = 0; i < 4; ++i) kf4[i] = *(LAS const u32x4*)(lds + (ko ^ ((i & 1) * 64 + (i >> 1) * 32)) + (i >> 1) * 512);
        lds_spin(CT_PA, ctl, pass ? 6 : 0);
#pragma unroll
        for (int s = 0; s < 6; ++s) qv[s] = *(LAS const u32x4*)(((s & 1) ? rq1 : rq0) + (s >> 1) * 1024);
        const f32x4 z = {0.f, 0.f, 0.f, 0.f};
        f32x4 gq = z, sq = z;
#pragma unroll
        for (int s = 0; s < 6; ++s) { gq = mfma(qv[s], qv[s], gq); sq = mfma(one, qv[s], sq); }
        const float mean = sq[0] * (1.f / C);
        const float s2 = xrow16_sum(g == (n >> 2) ? sel4(gq, n & 3) : 0.f);
        float sc = __builtin_amdgcn_rsqf(fmaxf(fmaf(-mean, mean, s2 * (1.f / C)), 0.f) + EPS) * QSCALE;
        asm volatile("" : "+v"(sc));
        lds_spin(CT_PB, ctl, pass ? 4 : 0);
        const u32x4 q0 = *(LAS const u32x4*)(lds + LDS_QK + 4096 + lane * 16), q1 = *(LAS const u32x4*)(lds + LDS_QK + 5120 + lane * 16);
        const f32x4 d0 = mfma(kf4[1], q1, mfma(kf4[0], q0, z)), d1 = mfma(kf4[3], q1, mfma(kf4[2], q0, z));
        const u32x4 xq = pack8(d0 * sc, d1 * sc);
        const unsigned ck = qk_check(xq, (unsigned)tg);
        asm volatile("" :: "v"(ck));
        if (pass) {
          const unsigned so = QK_OFF + wg * QK_REC;
#pragma unroll
          for (int cp = 0; cp < 4; ++cp) {
            stb128(xq, rws, lane * 16 + so + cp * QK_COPY);
            stb32(ck, rws, lane * 4 + so + 1024 + cp * QK_COPY);
          }
          lds_put(CT_PD, ctl, 4);
        }
      }
    }
    if (pv) {
      waitvm<0>();
      lds_inc(CT_PA, ctl);
      if (w >= 4) {
        const int ro = PSCR + 128 * (8 * g + tq) + 16 * ((2 * (w - 4) + (tp >> 1)) ^ ((tq & 2) << 1)) + 8 * (tp & 1);
        const int rv = PSCR + 24576 + 128 * n + 16 * (g ^ fq);
        f32x4 d0 = {0.f, 0.f, 0.f, 0.f}, d1 = d0;
        lds_spin(CT_PA, ctl, 7);
        const unsigned org = (unsigned)tstart + max((int)(wall_clock64() - tstart) - LAND0, 0);
#pragma unroll
        for (int s = 0; s < 6; ++s) {
          const u32x4 p = vtr2(lds + ro + s * 4096, lds + (ro ^ 32) + s * 4096 + 512);
          LAS const char* pd = lds + (rv ^ ((s & 1) * 64)) + (s >> 1) * 4096;
          d0 = mfma(*(LAS const u32x4*)pd, p, d0); d1 = mfma(*(LAS const u32x4*)(pd + 2048), p, d1);
        }
        const int fi = (pj - 3) * 24 + (w - 4) * 6 + pu;
        stb128(pack8(d0, d1), rws, lane * 16 + WVO_OFF + fi * 1024);
        lds_inc(CT_PD, ctl);
        waitvm<0>();
        if (lane < 8) stb64(u32x2{(unsigned)tg, (unsigned)(tg >> 32) | org << 8}, rws, FLAG_OFF + lane * 768 + fi * 8);
      }
    }
    const f32x4 z = {0.f, 0.f, 0.f, 0.f};
    int full = 0;
#define NEED(u_) lds_need(CT_FULL, full, ctl, (u_))
    if (w >= 4) {
      const int brg = ((g & 1) << 1) | (g >> 1);
      const int fn = (((n >> 1) & 3) << 1) | ((n >> 3) & 1);
      const int fy = ((n >> 2) & 1) * 4 + ((n >> 1) & 1) * 3;
      const int orr0 = 128 * n + 16 * (brg ^ fn), orr1 = 128 * n + 16 * ((4 + brg) ^ fn);
      const int ory0 = n < 8 ? CRS + 384 * n + 16 * (brg ^ fy) : LDS_CTL + CT_ONE, ory1 = n < 8 ? CRS + 384 * n + 16 * ((4 + brg) ^ fy) : LDS_CTL + CT_ONE;
      const int yon = n < 8 ? 1 : 0;
      const int opk = LDS_PK + 320 * g + 8 * n, ork = LDS_PK + 320 * (2 * (g >> 1) + (n >> 3)) + 256 + 8 * (n & 7) + 4 * (g & 1);
      u32x4 qk[6];
      f32x4 ref, qs;
      LAS const char *px0, *px1, *py0, *py1;
#define AT_SLOT(o_) do { px0 = lds + orr0 + (o_); px1 = lds + orr1 + (o_); py0 = lds + ory0 + __mul24(yon, (o_)); py1 = lds + ory1 + __mul24(yon, (o_)); \
      asm volatile("" : "+v"(px0), "+v"(px1), "+v"(py0), "+v"(py1)); } while (0)
#define RD_K(k_, i_) do { LAS const char* p_ = (((k_) & 1) ? px1 : px0) + ((k_) >> 1) * PART; \
    yv[i_] = *(LAS const u32x4*)((((k_) & 1) ? py1 : py0) + ((k_) >> 1) * 128); \
    xa[i_] = *(LAS const u32x4*)(p_); xb[i_] = *(LAS const u32x4*)(p_ + 2048); } while (0)
#define MF_STAT(i_) do { cy = mfma(yv[i_], yv[i_], cy); ca = mfma(yv[i_], xa[i_], ca); cb = mfma(yv[i_], xb[i_], cb); \
    ga = mfma(xa[i_], xa[i_], ga); gb = mfma(xb[i_], xb[i_], gb); } while (0)
#define MF_S(k_, i_) do { sy = mfma(qk[k_], yv[i_], sy); sa = mfma(qk[k_], xa[i_], sa); sb = mfma(qk[k_], xb[i_], sb); } while (0)
#define STATS(st, rec) do { \
      const int r = n & 3; \
      const float yy = half_copy<true>(sel4(cy, r)); \
      const f32x2 Y = swap32(yy, yy), X = swap32(sel4(ca, r), sel4(cb, r)), G = swap32(sel4(ga, r), sel4(gb, r)); \
      const float q1_ = fmaf(2.f, X[0], Y[0]) + (n < 8 ? G[0] : G[1]);       \
      const f32x2 Q = swap16(q1_, q1_); \
      const float mn_ = (X[1] + Y[1]) * (1.f / C); \
      const float var = fmaxf(fmaf(-mn_, mn_, fmaf((n & 4) ? Q[1] : Q[0], 1.f / C, EPS)), EPS), rs_ = __builtin_amdgcn_rsqf(var); \
      const f32x2 R = swap32(rs_, rs_), Mn = swap32(mn_, mn_); \
      st = f32x4{R[0], Mn[0], R[1], Mn[1]}; \
      const float v_ = (g & 1) ? var * rs_ : mn_, h_ = bfhi(__float_as_uint(v_)); \
      rec = cvtpk(h_, v_ - h_); \
    } while (0)
#define LOGITS(sv, st, base_) do { \
      _Pragma("unroll") for (int r = 0; r < 4; ++r) { \
        const float s0 = half_copy<true>(sy[r]); \
        sv[r] = fmaf(fmaf(-st[1], qs[r], sa[r] + s0), st[0], -(base_)[r]); \
        sv[4 + r] = fmaf(fmaf(-st[3], qs[r], sb[r] + s0), st[2], -(base_)[r]); \
      } \
    } while (0)
#define SV_MAX(sv) fmaxf(fmaxf(fmaxf(sv[0], sv[1]), fmaxf(sv[2], sv[3])), fmaxf(fmaxf(sv[4], sv[5]), fmaxf(sv[6], sv[7])))
#define PUBLISH(sv, st, rec, pk_, word_) do { \
      *(LAS unsigned*)(lds + ork + (pk_) * PK_B) = rec; \
      *(LAS u32x2*)(lds + opk + (pk_) * PK_B) = u32x2{cvtpk(sv[0] * st[0], sv[1] * st[0]), cvtpk(sv[2] * st[0], sv[3] * st[0])}; \
      *(LAS u32x2*)(lds + opk + (pk_) * PK_B + 128) = u32x2{cvtpk(sv[4] * st[2], sv[5] * st[2]), cvtpk(sv[6] * st[2], sv[7] * st[2])}; \
      lds_put(CT_PRDY, ctl + 4 * (pk_), (word_)); \
    } while (0)
      int s = (w + NT - 1) & 3, slot = s + 4, pk = s + 4, ncon = 0;
      {
        u32x4 yv[12], xa[12], xb[12];
        f32x4 st0, st1;
        unsigned rec0, rec1;
#pragma unroll
        for (int h = 0; h < 2; ++h) {
          f32x4 cy = z, ga = z, ca = z, gb = z, cb = z;
          AT_SLOT((s + 4 * h) * SLOT);
          lds_wait(ctl + CT_FULL, 3 * (s + 4 * h) + 3);
#pragma unroll
          for (int k = 0; k < 6; ++k) RD_K(k, 6 * h + k);
          lds_inc(CT_FREE + 16 * h, ctl + 4 * s);
          SB();
#pragma unroll
          for (int k = 0; k < 6; ++k) MF_STAT(6 * h + k);
          SB();
          if (h == 0) { STATS(st0, rec0); asm volatile("" : "+v"(st0), "+v"(rec0)); } else { STATS(st1, rec1); asm volatile("" : "+v"(st1), "+v"(rec1)); }
          SB();
        }
        lds_wait(ctl + CT_QK, 3);
#pragma unroll
        for (int k = 0; k < 6; ++k) qk[k] = *(LAS const u32x4*)(lds + LDS_QK + k * 1024 + lane * 16);
        float s0v[8], s1v[8];
        {
          f32x4 sy = z, sa = z, sb = z;
#pragma unroll
          for (int k = 0; k < 6; ++k) MF_S(k, k);
#pragma unroll
          for (int r = 0; r < 4; ++r) qs[r] = half_copy<false>(sy[r]);
          LOGITS(s0v, st0, z);
        }
        {
          f32x4 sy = z, sa = z, sb = z;
#pragma unroll
          for (int k = 0; k < 6; ++k) MF_S(k, 6 + k);
          LOGITS(s1v, st1, z);
        }
#pragma unroll
        for (int r = 0; r < 4; ++r) ref[r] = row_max(fmaxf(fmaxf(s0v[r], s0v[4 + r]), fmaxf(s1v[r], s1v[4 + r])));
        if (n < 4) __builtin_amdgcn_ds_fmaxf((LAS float*)(ctl + CT_MAX + 16 * g + 4 * n), sel4(ref, n), __ATOMIC_RELAXED, __HIP_MEMORY_SCOPE_WORKGROUP, false);
        lds_inc(CT_QR, ctl);
        lds_spin(CT_QR, ctl, 4);
        ref = *(LAS const f32x4*)(ctl + CT_MAX + 16 * g);
#pragma unroll
        for (int j = 0; j < 8; ++j) s0v[j] = __builtin_amdgcn_exp2f(s0v[j] - ref[j & 3]);
        PUBLISH(s0v, st0, rec0, s, 2 * s + 2);
#pragma unroll
        for (int j = 0; j < 8; ++j) s1v[j] = __builtin_amdgcn_exp2f(s1v[j] - ref[j & 3]);
        if (pk >= NPK) { pk -= NPK; ncon = 3; lds_spin(CT_PCON, ctl + 4 * pk, 3); }
        s += 4;
        PUBLISH(s1v, st1, rec1, pk, 2 * s + 2);
      }
      int moved = 0;
#pragma unroll 1
      for (;;) {
        s += 4; slot += 4; slot -= slot >= NS ? NS : 0; pk += 4;
        if (pk >= NPK) { pk -= NPK; ncon += 3; }
        if (s >= NT) break;
        f32x4 sy = z, cy = z, sa = z, ga = z, ca = z, sb = z, gb = z, cb = z, st;
        u32x4 yv[6], xa[6], xb[6];
        float sv[8];
        unsigned rec;
        AT_SLOT(slot * SLOT);
#define RD_PART(p_) do { int cnt_; unsigned t_; \
        asm volatile("s_mov_b32 %1, 0x400000\n1:\n\tds_read_b32 %2, %9 offset:%15\n\t" \
                     "ds_read_b128 %3, %12 offset:%16\n\tds_read_b128 %4, %10 offset:%17\n\tds_read_b128 %5, %10 offset:%18\n\t" \
                     "ds_read_b128 %6, %13 offset:%16\n\tds_read_b128 %7, %11 offset:%17\n\tds_read_b128 %8, %11 offset:%18\n\t" \
                     "s_cmp_ge_i32 %0, %14\n\ts_cbranch_scc1 2f\n\ts_waitcnt lgkmcnt(6)\n\tv_readfirstlane_b32 %0, %2\n\t" \
                     "s_cmp_ge_i32 %0, %14\n\ts_cbranch_scc1 2f\n\ts_sub_u32 %1, %1, 1\n\ts_cbranch_scc0 1b\n2:" \
                     : "+s"(full), "=&s"(cnt_), "=&v"(t_), "=&v"(yv[2 * (p_)]), "=&v"(xa[2 * (p_)]), "=&v"(xb[2 * (p_)]), \
                       "=&v"(yv[2 * (p_) + 1]), "=&v"(xa[2 * (p_) + 1]), "=&v"(xb[2 * (p_) + 1]) \
                     : "v"(ctl), "v"(px0), "v"(px1), "v"(py0), "v"(py1), "s"(3 * s + (p_) + 1), "i"(CT_FULL), "i"(128 * (p_)), "i"(PART * (p_)), \
                       "i"(PART * (p_) + 2048) : "memory", "scc"); keep = t_; \
        asm volatile("" : "+s"(full)); } while (0)
#define GOT(k_, n_) do { waitlds<n_>(); asm volatile("" : "+v"(yv[k_]), "+v"(xa[k_]), "+v"(xb[k_]) : "v"(keep)); } while (0)
        unsigned keep;
        const unsigned pcv = *(volatile LAS unsigned*)(ctl + CT_PCON + 4 * pk);
        NEED(3 * s);
        RD_PART(0);
        GOT(0, 3);
        int con = __builtin_amdgcn_readfirstlane(pcv);
        SB();
        MF_STAT(0); MF_S(0, 0);
        SB();
        lds_need(CT_PCON, con, ctl + 4 * pk, ncon);
        RD_PART(1);
        GOT(1, 7);
        SB();
        MF_STAT(1); MF_S(1, 1);
        SB();
        GOT(2, 3);
        MF_STAT(2); MF_S(2, 2);
        SB();
        RD_PART(2);
        GOT(3, 7);
        SB();
        MF_STAT(3); MF_S(3, 3);
        SB();
        GOT(4, 3);
        lds_inc(CT_FREE, ctl + 4 * slot);
        MF_STAT(4);
        SB();
        GOT(5, 1);
        MF_STAT(5);
        SB();
        MF_S(4, 4); MF_S(5, 5);
        STATS(st, rec);
        LOGITS(sv, st, ref);
#pragma unroll
        for (int j = 0; j < 6; ++j) { __builtin_amdgcn_sched_group_barrier(0x008, 1, 0); __builtin_amdgcn_sched_group_barrier(0x002, 4, 0); }
        if (__builtin_expect(__any(!(SV_MAX(sv) <= 16.f)), 0)) {
#pragma unroll
          for (int r = 0; r < 4; ++r) {
            const float mx = row_max(fmaxf(sv[r], sv[4 + r])), dm = mx <= 16.f ? 0.f : mx;
            ref[r] += dm; sv[r] -= dm; sv[4 + r] -= dm;
          }
          moved = 1;
        }
        if (__builtin_expect(moved, 0)) {
          if (n == 0) *(LAS f32x4*)(lds + LDS_PK + pk * PK_B + 1280 + 16 * g) = ref;
        }
#pragma unroll
        for (int j = 0; j < 8; ++j) sv[j] = __builtin_amdgcn_exp2f(sv[j]);
        PUBLISH(sv, st, rec, pk, 2 * s + 2 + moved);
      }
    } else {
      const int ftr = ((g & 1) << 2) | ((tq >> 1) << 1) | (g >> 1);
      const int fty = (g & 1) * 4 + (tq >> 1) * 3;
      int otr[4], oty[4];
#pragma unroll
      for (int t = 0; t < 4; ++t) {
        otr[t] = (w - 1) * PART + 128 * (4 * g + tq) + 16 * ((2 * t + (tp >> 1)) ^ ftr) + 8 * (tp & 1);
        oty[t] = CRS + (w - 1) * 128 + 384 * (4 * (g & 1) + tq) + 16 * ((2 * t + (tp >> 1)) ^ fty) + 8 * (tp & 1);
      }
      int opk = LDS_PK + 320 * tp + 8 * (4 * g + tq), ork = LDS_PK + 320 * (g >> 1) + 256 + 8 * (4 * (g & 1) + tq);
      asm volatile("" : "+v"(opk), "+v"(ork), "+v"(oty[0]), "+v"(oty[1]), "+v"(oty[2]), "+v"(oty[3]));
#define RD_T(ta, ty, so_) _Pragma("unroll") for (int t_ = 0; t_ < 4; ++t_) { \
    ta[t_] = vtr2(lds + otr[t_] + (so_), lds + otr[t_] + (so_) + 2048); ty[t_] = vtr(lds + oty[t_] + (so_)); }
      f32x4 acc[4] = {z, z, z, z}, tac = z;
#define RD_P(o_) \
      pf = vtr2(lds + opk + (o_), lds + opk + (o_) + 128); \
      sf = vtr2(lds + ork + (o_), lds + ork + (o_) + 640)
#define TWICE(ty) asm volatile("s_nop 2"); _Pragma("unroll") for (int t = 0; t < 4; ++t) { u32x2 lo_, hi_; \
      asm volatile("v_mov_b64 %0, %2\n\tv_mov_b64 %1, %2" : "=&v"(lo_), "=&v"(hi_) : "v"(ty[t])); t2[t] = u32x4{lo_[0], lo_[1], hi_[0], hi_[1]}; } \
      asm volatile("s_nop 1" : "+v"(t2[0]), "+v"(t2[1]), "+v"(t2[2]), "+v"(t2[3]))
#define MF_PX(ta) \
      tac = mfma(sf, pf, tac); \
      _Pragma("unroll") for (int t = 0; t < 4; ++t) acc[t] = mfma(ta[t], pf, acc[t])
#define MF_PY() \
      _Pragma("unroll") for (int t = 0; t < 4; ++t) acc[t] = mfma(t2[t], pf, acc[t])
#define MOVE_REF(word_, o_) do { moved |= (word_) & 1; if (__builtin_expect(moved, 0)) { \
            const float m_ = *(LAS const float*)(lds + (((word_) & 1) ? LDS_PK + (o_) + 1280 : LDS_CTL + CT_MAX) + 4 * n); \
            Mtop = fmaxf(Mtop, m_); \
            const bool drop = m_ < Mtop - 64.f; \
            const float mn = drop ? M : m_, fa = __builtin_amdgcn_exp2f(M - mn); \
            M = mn; \
            _Pragma("unroll") for (int t = 0; t < 4; ++t) acc[t] *= fa; \
            tac *= fa; \
            if (drop) pf = u32x4{0u, 0u, 0u, 0u}; \
          } } while (0)
      const unsigned flo = FLAG_OFF + (wg & 7) * 768 + (w - 1) * 144 + (lane < 36 ? lane : 35) * 8;
      float M, Mtop;
      int moved = 0;
      u32x4 wvv[18], pf, sf, t2[4];
      {
        u32x4 ta[8][4], xx[6];
        u32x2 ty[8][4], fl;
        unsigned cc[6];
        const int qo = QK_OFF + (wg & 3) * QK_COPY + (2 * w - 2) * QK_REC;
#define QK_LOAD() _Pragma("unroll") for (int j = 0; j < 6; ++j) { \
          xx[j] = ldb128<16>(rws, lane * 16, qo + ((j >> 1) * 6 + (j & 1)) * QK_REC); \
          cc[j] = __builtin_amdgcn_raw_buffer_load_b32(rws, lane * 4, qo + ((j >> 1) * 6 + (j & 1)) * QK_REC + 1024, 16); }
#pragma unroll
        for (int u = 0; u < 8; ++u) {
          if (u == QKAT) {
#pragma unroll 1
            for (int spin = 0; spin < 64; ++spin) {
              if ((long long)(wall_clock64() - tstart) >= GATE) break;
              __builtin_amdgcn_s_sleep(2);
            }
            if (wg < NPR) lds_wait(ctl + CT_PD, 4);
            QK_LOAD();
#pragma unroll 1
            for (int spin = 0; spin < (1 << 18); ++spin) {
              unsigned df = 0u;
#pragma unroll
              for (int j = 0; j < 6; ++j) df |= qk_check(xx[j], (unsigned)tg) ^ cc[j];
              if (__all(df == 0u)) break;
              asm volatile("" ::: "memory");
              QK_LOAD();
            }
#pragma unroll
            for (int h = 0; h < 2; ++h) {
              u32x4 sx;
#pragma unroll
              for (int i = 0; i < 4; ++i) sx[i] = add3(xx[h][i], xx[2 + h][i], xx[4 + h][i]);
              *(LAS u32x4*)(lds + LDS_QK + (2 * w - 2 + h) * 1024 + lane * 16) = sx;
            }
            lds_inc(CT_QK, ctl);
            SB();
          }
          lds_wait(ctl + CT_FULL, 3 * u + w);
          RD_T(ta[u], ty[u], u * SLOT);
          lds_inc(CT_FREE + 4 * u, ctl);
          SB();
        }
        fl = __builtin_bit_cast(u32x2, __builtin_amdgcn_raw_buffer_load_b64(rws, flo, 0, 16));
#pragma unroll
        for (int u = 0; u < 12; ++u) {
          const int word = lds_spin(CT_PRDY + 4 * (u % NPK), ctl, 2 * u + 2);
          if (u == 0) Mtop = M = *(LAS const float*)(ctl + CT_MAX + 4 * n);
          RD_P((u % NPK) * PK_B);
          if (u >= 8) MOVE_REF(word, (u % NPK) * PK_B);
          lds_inc(CT_PCON + 4 * (u % NPK), ctl);
          SB();
          MF_PX(ta[u & 7]);
          SB();
          TWICE(ty[u & 7]);
          MF_PY();
          asm volatile("" : "+v"(acc[0]), "+v"(acc[1]), "+v"(acc[2]), "+v"(acc[3]), "+v"(tac));
          SB();
#define FLAGS_UP() __all(fl[0] == (unsigned)tg && (fl[1] & 255u) == (unsigned)(tg >> 32))
          if (u == T0AT && w == 1 && FLAGS_UP()) {
            int d = (int)(fl[1] - ((unsigned)tstart << 8)) >> 8;
            d += __builtin_amdgcn_mov_dpp(d, XOR8, 0xf, 0xf, true); d += __builtin_amdgcn_mov_dpp(d, ROR4, 0xf, 0xf, true);
            d += __builtin_amdgcn_mov_dpp(d, 0x122, 0xf, 0xf, true); d += __builtin_amdgcn_mov_dpp(d, 0x121, 0xf, 0xf, true);
            lds_put(CT_T0, ctl, ((unsigned)tstart + min(max(d >> 4, -300), 300)) | 1u);
          }
          if (u < 4) {
            NEED(3 * (u + 8) + w);
            RD_T(ta[u], ty[u], ((u + 8) % NS) * SLOT);
            lds_inc(CT_FREE + 4 * ((u + 8) % NS), ctl);
            SB();
          }
        }
#pragma unroll 1
        for (int spin = 0; spin < (1 << 18); ++spin) {
          if (FLAGS_UP()) break;
          asm volatile("" ::: "memory");
          fl = __builtin_bit_cast(u32x2, __builtin_amdgcn_raw_buffer_load_b64(rws, flo, 0, 16));
        }
        asm volatile("" ::: "memory");
        if (w == 3) lds_put(CT_WV, ctl, 1);
      }
      int slot = 3, pk = 12 % NPK;
      unsigned rdy = 0u;
      const int kro[3] = {(w - 1) * 2048, (w == 3 ? 0 : w) * 2048, (w == 1 ? 2 : w - 2) * 2048};
#define RD_PACKET() do { int cnt_; u32x2 p0_, p1_, p2_, p3_; \
        word = __builtin_amdgcn_readfirstlane(rdy); \
        asm volatile("s_mov_b32 %1, 0x400000\n1:\n\tds_read_b32 %2, %7 offset:%11\n\t" \
                     "ds_read_b64_tr_b16 %5, %9\n\tds_read_b64_tr_b16 %6, %9 offset:640\n\t" \
                     "ds_read_b64_tr_b16 %3, %8\n\tds_read_b64_tr_b16 %4, %8 offset:128\n\t" \
                     "s_cmp_ge_i32 %0, %10\n\ts_cbranch_scc1 2f\n\ts_waitcnt lgkmcnt(4)\n\tv_readfirstlane_b32 %0, %2\n\t" \
                     "s_cmp_ge_i32 %0, %10\n\ts_cbranch_scc1 2f\n\ts_sub_u32 %1, %1, 1\n\ts_cbranch_scc0 1b\n2:" \
                     : "+s"(word), "=&s"(cnt_), "=&v"(rdy), "=&v"(p0_), "=&v"(p1_), "=&v"(p2_), "=&v"(p3_) \
                     : "v"(ctl + 4 * pk), "v"(lds + opk + pk * PK_B), "v"(lds + ork + pk * PK_B), "s"(2 * s + 2), "i"(CT_PRDY) : "memory", "scc"); \
        asm volatile("" : "+s"(word)); \
        pf = u32x4{p0_[0], p0_[1], p1_[0], p1_[1]}; sf = u32x4{p2_[0], p2_[1], p3_[0], p3_[1]}; } while (0)
#define GOT_PACKET() do { waitlds<0>(); asm volatile("" : "+v"(pf), "+v"(sf) : "v"(rdy)); MOVE_REF(word, pk * PK_B); } while (0)
#define RD_Y(t_, p_) do { const u32x2 lo_ = vtr(p_); asm volatile("" ::: "memory"); const u32x2 hi_ = vtr(p_); \
        t2[t_] = u32x4{lo_[0], lo_[1], hi_[0], hi_[1]}; } while (0)
      u32x4 ta[4];
      int s = 12, word;
      NEED(36 + w);
#pragma unroll
      for (int t = 0; t < 4; ++t) { ta[t] = vtr2(lds + otr[t] + 2 * SLOT, lds + otr[t] + 2 * SLOT + 2048); RD_Y(t, lds + oty[t] + 2 * SLOT); }
      lds_inc(CT_FREE + 8, ctl);
#pragma unroll 1
      for (; s < NT - 1; ++s) {
        RD_PACKET();
        LAS const char *px[4], *py[4];
#pragma unroll
        for (int t = 0; t < 4; ++t) { px[t] = lds + otr[t] + slot * SLOT; py[t] = lds + oty[t] + slot * SLOT; asm volatile("" : "+v"(px[t]), "+v"(py[t])); }
        NEED(3 * s + 3 + w);
        GOT_PACKET();
        SB();
        tac = mfma(sf, pf, tac);
        asm volatile("" : "+v"(tac));
        SB();
        lds_inc(CT_PCON, ctl + 4 * pk);
        pk = pk == NPK - 1 ? 0 : pk + 1;
#pragma unroll
        for (int t = 0; t < 4; ++t) { acc[t] = mfma(ta[t], pf, acc[t]); SB(); ta[t] = vtr2(px[t], px[t] + 2048); SB(); }
        rdy = *(volatile LAS unsigned*)(ctl + CT_PRDY + 4 * pk);
#pragma unroll
        for (int t = 0; t < 4; ++t) { acc[t] = mfma(t2[t], pf, acc[t]); SB(); RD_Y(t, py[t]); SB(); }
        lds_inc(CT_FREE, ctl + 4 * slot);
        if (s < 12 + 18 / WVN) {
#pragma unroll
          for (int u = 0; u < 18 / WVN; ++u)
            if (s - 12 == u) {
#pragma unroll
              for (int j = WVN * u; j < WVN * u + WVN; ++j)
                wvv[j] = ldb128<16>(rws, lane * 16 + (j & 1) * 1024, WVO_OFF + (w - 1) * 18432 + (j / 6) * 6144 + kro[(j % 6) >> 1]);
            }
        }
        slot = slot == NS - 1 ? 0 : slot + 1;
      }
      LAS char *xw = lds + xslot + lane * 16 + kro[0], *xl = lds + xslot + 6144 + 4 * n;
      LAS const char *xr1 = lds + xslot + lane * 16 + kro[1], *xr2 = lds + xslot + lane * 16 + kro[2];
      unsigned mo = wg * 8192 + (w - 1) * 2048 + n * 64 + g * 16, tw = (unsigned)(tg >> (20 * (g & 1))) & 0xfffffu, sh = 12 * (g & 1);
      asm volatile("" : "+v"(xw), "+v"(xl), "+v"(xr1), "+v"(xr2), "+v"(mo), "+v"(tw), "+v"(sh));
      waitvm<0>();
      RD_PACKET();
      GOT_PACKET();
      SB();
      tac = mfma(sf, pf, tac);
      asm volatile("" : "+v"(tac));
      SB();
#pragma unroll
      for (int t = 0; t < 2; ++t) acc[t] = mfma(t2[t], pf, mfma(ta[t], pf, acc[t]));
      SB();
#pragma unroll
      for (int t = 2; t < 4; ++t) acc[t] = mfma(t2[t], pf, mfma(ta[t], pf, acc[t]));
      const float L = tac[2] + tac[3], rl = __builtin_amdgcn_rcpf(L), ct = -(tac[0] + tac[1]) * rl;
      const float lse = M + __builtin_amdgcn_logf(L);
      u32x4 b[6];
      b[0] = pack8(acc[0] * rl + ct, acc[1] * rl + ct);
      *(LAS u32x4*)xw = b[0];
      *(LAS float*)xl = lse;
#pragma unroll
      for (int j = 0; j < 4; ++j) { __builtin_amdgcn_sched_group_barrier(0x008, 1, 0); __builtin_amdgcn_sched_group_barrier(0x002, 3, 0); }
      SB();
      b[1] = pack8(acc[2] * rl + ct, acc[3] * rl + ct);
      *(LAS u32x4*)(xw + 1024) = b[1];
      lds_inc(CT_XD, ctl);
      SB();
      f32x4 o[3];
      unsigned xd;
#pragma unroll
      for (int t = 0; t < 3; ++t) o[t] = mfma(wvv[6 * t], b[0], z);
      asm volatile("" : "+v"(o[0]), "+v"(o[1]), "+v"(o[2]));
      SB();
#define RD_SLICES "ds_read_b32 %0, %7 offset:%10\n\tds_read_b128 %1, %8\n\tds_read_b128 %2, %8 offset:1024\n\tds_read_b128 %3, %9\n\tds_read_b128 %4, %9 offset:1024\n"
      asm volatile(RD_SLICES : "=&v"(xd), "=&v"(b[2]), "=&v"(b[3]), "=&v"(b[4]), "=&v"(b[5]) : "i"(0), "i"(0), "v"(ctl), "v"(xr1), "v"(xr2), "i"(CT_XD) : "memory");
      SB();
#pragma unroll
      for (int t = 0; t < 3; ++t) o[t] = mfma(wvv[6 * t + 1], b[1], o[t]);
      tw |= (__float_as_uint(lse) << sh) & 0xfff00000u;
      asm volatile("" : "+v"(tw), "+v"(o[0]), "+v"(o[1]), "+v"(o[2]));
      SB();
      {
        int cnt_, c_;
        asm volatile("s_mov_b32 %6, 0x400000\n\ts_branch 2f\n1:\n\t" RD_SLICES "2:\n\ts_waitcnt lgkmcnt(4)\n\tv_readfirstlane_b32 %5, %0\n\t"
                     "s_cmp_ge_i32 %5, 3\n\ts_cbranch_scc1 3f\n\ts_sub_u32 %6, %6, 1\n\ts_cbranch_scc0 1b\n3:\n\ts_waitcnt lgkmcnt(0)"
                     : "+v"(xd), "+v"(b[2]), "+v"(b[3]), "+v"(b[4]), "+v"(b[5]), "=&s"(c_), "=&s"(cnt_)
                     : "v"(ctl), "v"(xr1), "v"(xr2), "i"(CT_XD) : "memory", "scc");
      }
      SB();
      PROJECT(2);
      lds_inc(CT_ST, ctl);
    }
  }
  COORDS();

  {
    LAS char* fin = lds + xslot + 8192;
    unsigned ao = (tid >> 1) * 8192 + (tid & 1) * 16 + dst * 32, tw = (unsigned)(tg >> (20 * (tid & 1))), sh = 12 * (tid & 1), oo = dout * 2;
    LAS char *fw = fin + (((lane & 8) | ((g & 1) << 2) | (g & 2)) + (lane & 1)) * 32 + 4 * w, *fr = fin + (2 * (lane & 7) + ((lane >> 3) & 1)) * 32;
    float v7 = 0.f;
    const unsigned t0s = lds_get(ctl + CT_T0), gate = (t0s ? t0s : (unsigned)tstart) + RGATE;
    const RSRC rm = rws;
    u32x4 ro = mkrs4(out, NQ * C * 2);
    unsigned long long wm = 0x0303030303030303ull;
    qres <<= 16;
    asm volatile("" : "+v"(ao), "+v"(tw), "+v"(sh), "+v"(oo), "+v"(fw), "+v"(fr), "+v"(v7), "+v"(qres), "+s"(ro), "+s"(wm));
    lds_spin(CT_ST, ctl, 4);
    float ref = *(LAS const float*)(lds + xslot + 6144 + 4 * dq);
    *(LAS float*)(fin + 512 + 4 * w) = ref;
#pragma unroll 1
    for (int spin = 0; spin < 1024; ++spin)
      if ((int)((unsigned)wall_clock64() - gate) >= 0) break;
    u32x4 A = ldb128<16>(rm, ao, 0);
#pragma unroll 1
    for (int spin = 0; spin < (1 << 18); ++spin) {
      unsigned x = (A[3] ^ tw) << 12;
      x |= __builtin_amdgcn_mov_dpp(x, 0xB1, 0xf, 0xf, true);
      if (__builtin_expect(__builtin_amdgcn_ballot_w64(x != 0u) == 0ull, 1)) break;
      asm volatile("" ::: "memory");
      if (x != 0u) A = ldb128<16>(rm, ao, 0);
    }
    unsigned lx = (A[3] & 0xfff00000u) >> sh;
    lx |= __builtin_amdgcn_mov_dpp(lx, 0xB1, 0xf, 0xf, true);
    float d = __uint_as_float(lx) - ref;
    if (__builtin_expect(__builtin_amdgcn_ballot_w64(!(d <= 64.f)) != 0ull, 0)) {
      ref = xrow16_max(row_max(__uint_as_float(lx)));
      d = __uint_as_float(lx) - ref;
      *(LAS float*)(fin + 512 + 4 * w) = ref;
      lds_put(CT_FAR, ctl, 1);
    }
    const float wt = __builtin_amdgcn_exp2f(d);
    const f32x2 p0 = swap32(bflo(A[0]) * wt, bfhi(A[0]) * wt), p1 = swap32(bflo(A[1]) * wt, bfhi(A[1]) * wt);
    const f32x2 p2 = swap32(bflo(A[2]) * wt, bfhi(A[2]) * wt), p3 = swap32(wt, v7);
    const f32x2 r0 = swap16(p0[0] + p0[1], p1[0] + p1[1]), r1 = swap16(p2[0] + p2[1], p3[0] + p3[1]);
    float c0 = r0[0] + r0[1], c1 = r1[0] + r1[1];
    asm volatile("s_nop 1\n\tv_add_f32_dpp %0, %0, %0 row_ror:8 row_mask:0xf bank_mask:0x3\n\tv_add_f32_dpp %0, %1, %1 row_ror:8 row_mask:0xf bank_mask:0xc\n\t"
                 "s_nop 1\n\tv_add_f32_dpp %0, %0, %0 row_shl:4 row_mask:0xf bank_mask:0x5\n\t"
                 "s_nop 1\n\tv_add_f32_dpp %0, %0, %0 quad_perm:[2,3,0,1] row_mask:0xf bank_mask:0x5\n\t"
                 "s_mov_b64 exec, %3\n\tds_write_b32 %2, %0\n\ts_mov_b64 exec, -1\n\ts_waitcnt lgkmcnt(0)\n\ts_barrier"
                 : "+v"(c0) : "v"(c1), "v"(fw), "s"(wm) : "memory");
    if (w == 0) {
      f32x4 a0 = *(LAS const f32x4*)fr, a1 = *(LAS const f32x4*)(fr + 16), l0 = *(LAS const f32x4*)(fin + 384), l1 = *(LAS const f32x4*)(fin + 400);
      if (__builtin_expect(lds_get(ctl + CT_FAR), 0)) {
        f32x4 m0 = *(LAS const f32x4*)(fin + 512), m1 = *(LAS const f32x4*)(fin + 528);
        const float mm = fmaxf(fmaxf(fmaxf(m0[0], m0[1]), fmaxf(m0[2], m0[3])), fmaxf(fmaxf(m1[0], m1[1]), fmaxf(m1[2], m1[3])));
#pragma unroll
        for (int p = 0; p < 4; ++p) { m0[p] = __builtin_amdgcn_exp2f(m0[p] - mm); m1[p] = __builtin_amdgcn_exp2f(m1[p] - mm); }
        a0 *= m0; l0 *= m0; a1 *= m1; l1 *= m1;
      }
      a0 += a1; l0 += l1;
      const float o = __uint_as_float(qres) + ((a0[0] + a0[2]) + (a0[1] + a0[3])) * __builtin_amdgcn_rcpf((l0[0] + l0[2]) + (l0[1] + l0[3]));
      const unsigned ob = cvtpk(o, __uint_as_float(__builtin_amdgcn_mov_dpp(__float_as_uint(o), 0xB1, 0xf, 0xf, true)));
      asm volatile("s_mov_b64 exec, 0x1515\n\tbuffer_store_dword %0, %1, %2, 0 offen sc1\n\ts_mov_b64 exec, -1" :: "v"(ob), "v"(oo), "s"(ro) : "memory");
      if (wg == 0 && tid == 0) stb64(u32x2{(unsigned)tg, (unsigned)(tg >> 32)}, rws, EP_OFF);
    }
  }
}

#undef rws
#define WS_AT(size_) (((size_) - (64u << 20)) & ~(size_t)0x1fffff)
extern "C" void kernel_launch(void* const* d_in, const int* in_sizes, int n_in, void* d_out, int out_size, void* d_ws, size_t ws_size,
                              hipStream_t stream) {
  dino_main<<<NWG, 512, 0, stream>>>((const u16*)d_in[0], (const u16*)d_in[1], (const u16*)d_in[2], (const u16*)d_in[3], (const u16*)d_in[4],
                                     (const u16*)d_in[5], (const u16*)d_in[6], (char*)d_ws + WS_AT(ws_size), (u16*)d_out);
}
```

```cpp
#include <hip/hip_runtime.h>
#include <hip/hip_bf16.h>
#include <stdint.h>

typedef unsigned short u16;
using u32x2 = __attribute__((ext_vector_type(2))) unsigned;
using u32x4 = __attribute__((ext_vector_type(4))) unsigned;
using u32x8 = __attribute__((ext_vector_type(8))) unsigned;
using f32x2 = __attribute__((ext_vector_type(2))) float;
using f32x4 = __attribute__((ext_vector_type(4))) float;
using s16x8 = __attribute__((ext_vector_type(8))) short;
using bf16x2_t = __attribute__((ext_vector_type(2))) __bf16;
typedef short v4i16_t __attribute__((ext_vector_type(4)));
#define LAS __attribute__((address_space(3)))

constexpr int C = 192, NQ = 16, W0 = 512, W1 = 256, NWG = 256, NW = 8;
constexpr float EPS = 1e-5f;
constexpr float QSCALE = 0.07216878364870322f * 1.4426950408889634f;

constexpr int PART = 4096, CRS = 3 * PART;
constexpr int SLOT = CRS + 3072, NS = 10;
constexpr int LDS_QK = NS * SLOT;
constexpr int NPK = 7;
constexpr int LDS_PK = LDS_QK, PK_B = 1344;
constexpr int LDS_CTL = LDS_PK + NPK * PK_B;
constexpr int LDS_SINK = LDS_CTL + 384;
constexpr int LDS_BYTES = LDS_SINK + 256;
constexpr int PSCR = 8 * SLOT;
constexpr int CT_FULL = 8, CT_WV = 4, CT_PA = 0, CT_PB = 12, CT_QR = 16, CT_PD = 20, CT_T0 = 24, CT_XD = 28, CT_TG = 32, CT_QK = 40, CT_ST = 44,
              CT_FREE = 48, CT_FAR = 88, CT_ONE = 112, CT_PRDY = 128, CT_PCON = 160, CT_MAX = 256;

constexpr int MB_BYTES = NWG * NWG * 32;
constexpr int EP_OFF = MB_BYTES;
constexpr int FLAG_OFF = EP_OFF + 512;
constexpr int WVO_OFF = EP_OFF + 8192;
constexpr int QK_OFF = WVO_OFF + C * C * 2;
constexpr int QK_REC = 1280, QK_COPY = 18 * QK_REC;
constexpr int HINT_OFF = QK_OFF + 4 * QK_COPY;
constexpr int WS_BYTES = HINT_OFF + 64 * 128;

#define GIVES 0x000000003232ull
constexpr int NPR = 36, NTK = (NWG - NPR) / 2;
__host__ __device__ constexpr int gives(int wg) { return (int)((GIVES >> (wg < NPR ? 4 * (wg & 1) : 16 + 4 * (wg & 7))) & 15); }
struct Tabs { unsigned seq[112]; unsigned take[NTK]; };
constexpr Tabs make_tabs() {
  Tabs t{};
  for (int i = 0; i < 112; ++i) t.seq[i] = i + 1;
  int k = 0;
  for (int wg = 0; wg < 256; ++wg)
    for (int j = 0; j < gives(wg); ++j, ++k) t.take[k % NTK] += (unsigned)(j << 8 | wg) << (10 * (k / NTK));
  return t;
}
constexpr int given() { int k = 0; for (int wg = 0; wg < 256; ++wg) k += gives(wg); return k; }
constexpr int GIVEN = given();
static_assert(GIVEN <= NTK * 3, "too many tiles sent elsewhere");
__device__ const Tabs tabs = make_tabs();
#define QKAT 3
#define WVN 3
#define DEPTH 6
#define PACE0 30
#define PACE1 1830
#define RGATE 2045
#define T0AT 3
#define LAND0 200
#define TLBE 1
#define HSTEP 40
#define GATE 350
#define CODE_BYTES 25792

__device__ __forceinline__ unsigned cvtpk(float lo, float hi) {
  bf16x2_t v = {(__bf16)lo, (__bf16)hi};
  return __builtin_bit_cast(unsigned, v);
}
__device__ __forceinline__ float bflo(unsigned x) { return __builtin_bit_cast(float, x << 16); }
__device__ __forceinline__ float bfhi(unsigned x) { return __builtin_bit_cast(float, x & 0xffff0000u); }
template <int CTRL> __device__ __forceinline__ float dpp(float x) {
  return __builtin_bit_cast(float, __builtin_amdgcn_mov_dpp(__builtin_bit_cast(int, x), CTRL, 0xf, 0xf, true));
}
constexpr int XOR8 = 0x128, ROR4 = 0x124;
template <bool HI8> __device__ __forceinline__ float half_copy(float x) {
  const int v = __builtin_bit_cast(int, x);
  return __builtin_bit_cast(float, __builtin_amdgcn_update_dpp(v, v, XOR8, 0xf, HI8 ? 0xc : 0x3, false));
}
__device__ __forceinline__ float row_max(float x) {
  x = fmaxf(x, dpp<XOR8>(x)); x = fmaxf(x, dpp<ROR4>(x)); x = fmaxf(x, dpp<0x122>(x));
  return fmaxf(x, dpp<0x121>(x));
}
__device__ __forceinline__ f32x2 swap32(float a, float b) {
  auto s = __builtin_amdgcn_permlane32_swap(__float_as_uint(a), __float_as_uint(b), false, false);
  return f32x2{__uint_as_float(s[0]), __uint_as_float(s[1])};
}
__device__ __forceinline__ f32x2 swap16(float a, float b) {
  auto s = __builtin_amdgcn_permlane16_swap(__float_as_uint(a), __float_as_uint(b), false, false);
  return f32x2{__uint_as_float(s[0]), __uint_as_float(s[1])};
}
__device__ __forceinline__ float xrow16_max(float x) {
  auto s = __builtin_amdgcn_permlane16_swap(__float_as_uint(x), __float_as_uint(x), false, false);
  x = fmaxf(__uint_as_float(s[0]), __uint_as_float(s[1]));
  auto t = __builtin_amdgcn_permlane32_swap(__float_as_uint(x), __float_as_uint(x), false, false);
  return fmaxf(__uint_as_float(t[0]), __uint_as_float(t[1]));
}
__device__ __forceinline__ float xrow16_sum(float x) {
  auto s = __builtin_amdgcn_permlane16_swap(__float_as_uint(x), __float_as_uint(x), false, false);
  x = __uint_as_float(s[0]) + __uint_as_float(s[1]);
  auto t = __builtin_amdgcn_permlane32_swap(__float_as_uint(x), __float_as_uint(x), false, false);
  return __uint_as_float(t[0]) + __uint_as_float(t[1]);
}
__device__ __forceinline__ f32x4 mfma(u32x4 a, u32x4 b, f32x4 c) {
  return __builtin_amdgcn_mfma_f32_16x16x32_bf16(__builtin_bit_cast(s16x8, a), __builtin_bit_cast(s16x8, b), c, 0, 0, 0);
}
__device__ __forceinline__ u32x2 vtr(LAS const char* p) {
  return __builtin_bit_cast(u32x2, __builtin_amdgcn_ds_read_tr16_b64_v4i16((LAS v4i16_t*)p));
}
__device__ __forceinline__ u32x4 vtr2(LAS const char* p, LAS const char* q_) {
  const u32x2 a = vtr(p), b = vtr(q_);
  return u32x4{a[0], a[1], b[0], b[1]};
}
__device__ __forceinline__ float sel4(f32x4 v, int r) {
  const float a = (r & 1) ? v[1] : v[0], b = (r & 1) ? v[3] : v[2];
  return (r & 2) ? b : a;
}
using RSRC = __amdgpu_buffer_rsrc_t;
__device__ __forceinline__ RSRC mkrs(const void* p, int bytes) { return __builtin_amdgcn_make_buffer_rsrc(const_cast<void*>(p), (short)0, bytes, 0x00020000); }
__device__ __forceinline__ u32x4 mkrs4(const void* p, unsigned bytes) {
  const unsigned long long b = (unsigned long long)p;
  return u32x4{(unsigned)b, (unsigned)(b >> 32) & 0xffffu, bytes, 0x00020000u};
}
template <int AUX> __device__ __forceinline__ u32x4 ldb128(RSRC r, unsigned vo, int so) { return __builtin_bit_cast(u32x4, __builtin_amdgcn_raw_buffer_load_b128(r, vo, so, AUX)); }
#define SB() __builtin_amdgcn_sched_barrier(0)
#define WAVE_SYNC() do { __builtin_amdgcn_fence(__ATOMIC_RELEASE, "wavefront"); __builtin_amdgcn_wave_barrier(); __builtin_amdgcn_fence(__ATOMIC_ACQUIRE, "wavefront"); } while (0)
#define RAW_BARRIER() asm volatile("s_waitcnt lgkmcnt(0)\n\ts_barrier" ::: "memory")
#define DMA(m0_, vo_, rs_, so_) asm volatile("s_mov_b32 m0, %0\n\ts_nop 0\n\tbuffer_load_dwordx4 %1, %2, %3 offen lds" :: "s"(m0_), "v"(vo_), "s"(rs_), "s"(so_) : "memory")
template <int N> __device__ __forceinline__ void waitlds() { __builtin_amdgcn_s_waitcnt(0xc07f | (N << 8)); }
template <int N> __device__ __forceinline__ void waitvm() { asm volatile("s_waitcnt vmcnt(%0)" :: "i"(N) : "memory"); }


__device__ __forceinline__ void lds_add(LAS char* p, int lane, unsigned v) {
  asm volatile("" ::: "memory");
  if (lane == 0) __hip_atomic_fetch_add((LAS unsigned*)p, v, __ATOMIC_RELAXED, __HIP_MEMORY_SCOPE_WORKGROUP);
  asm volatile("" ::: "memory");
}
__device__ __forceinline__ void lds_set(LAS char* p, int lane, unsigned v) {
  asm volatile("" ::: "memory");
  if (lane == 0) __hip_atomic_store((LAS unsigned*)p, v, __ATOMIC_RELAXED, __HIP_MEMORY_SCOPE_WORKGROUP);
  asm volatile("" ::: "memory");
}
__device__ __forceinline__ void lds_inc(int O, LAS char* p) {
  asm volatile("s_mov_b64 exec, 1\n\tds_add_u32 %0, %1 offset:%2\n\ts_mov_b64 exec, -1" :: "v"(p), "v"(1u), "i"(O) : "memory");
}
__device__ __forceinline__ void lds_put(int O, LAS char* p, unsigned v) {
  asm volatile("s_mov_b64 exec, 1\n\tds_write_b32 %0, %1 offset:%2\n\ts_mov_b64 exec, -1" :: "v"(p), "v"(v), "i"(O) : "memory");
}
__device__ __forceinline__ int lds_get(LAS char* p) {
  asm volatile("" ::: "memory");
  const unsigned v = __hip_atomic_load((LAS unsigned*)p, __ATOMIC_RELAXED, __HIP_MEMORY_SCOPE_WORKGROUP);
  asm volatile("" ::: "memory");
  return __builtin_amdgcn_readfirstlane(v);
}
__device__ __forceinline__ void lds_wait(LAS char* p, int target) {
#pragma unroll 1
  for (int spin = 0; spin < (1 << 22); ++spin) {
    if (lds_get(p) >= target) break;
    __builtin_amdgcn_s_sleep(1);
  }
}
__device__ __forceinline__ int lds_spin(int O, LAS char* p, int target) {
  int r, cnt = 1 << 22;
  unsigned t;
  asm volatile("1:\n\tds_read_b32 %1, %3 offset:%5\n\ts_waitcnt lgkmcnt(0)\n\tv_readfirstlane_b32 %0, %1\n\ts_cmp_ge_i32 %0, %4\n\ts_cbranch_scc1 2f\n\t"
               "s_sub_u32 %2, %2, 1\n\ts_cbranch_scc0 1b\n2:"
               : "=&s"(r), "=&v"(t), "+s"(cnt) : "v"(p), "s"(target), "i"(O) : "memory", "scc");
  return r;
}
__device__ __forceinline__ void lds_need(int O, int& have, LAS char* p, int target) {
  int cnt;
  unsigned t;
  asm volatile("s_cmp_ge_i32 %0, %4\n\ts_cbranch_scc1 2f\n\ts_mov_b32 %2, 0x400000\n"
               "1:\n\tds_read_b32 %1, %3 offset:%5\n\ts_waitcnt lgkmcnt(0)\n\tv_readfirstlane_b32 %0, %1\n\ts_cmp_ge_i32 %0, %4\n\ts_cbranch_scc1 2f\n\t"
               "s_sub_u32 %2, %2, 1\n\ts_cbranch_scc0 1b\n2:"
               : "+s"(have), "=&v"(t), "=&s"(cnt) : "v"(p), "s"(target), "i"(O) : "memory", "scc");
  asm volatile("" : "+s"(have));
}
__device__ __forceinline__ u32x4 pack8(f32x4 a, f32x4 b) { return u32x4{cvtpk(a[0], a[1]), cvtpk(a[2], a[3]), cvtpk(b[0], b[1]), cvtpk(b[2], b[3])}; }

__device__ __forceinline__ void stb128(u32x4 v, RSRC r, unsigned o) { __builtin_amdgcn_raw_buffer_store_b128(v, r, o, 0, 16); }
__device__ __forceinline__ void stb64(u32x2 v, RSRC r, unsigned o) { __builtin_amdgcn_raw_buffer_store_b64(v, r, o, 0, 16); }
__device__ __forceinline__ void stb32(unsigned v, RSRC r, unsigned o) { __builtin_amdgcn_raw_buffer_store_b32(v, r, o, 0, 16); }
__device__ __forceinline__ unsigned qk_check(u32x4 x, unsigned tg) {
  const unsigned a = (x[0] ^ __builtin_amdgcn_alignbit(x[1], x[1], 11)) * 0x9e3779b1u + tg;
  const unsigned b = (x[2] + __builtin_amdgcn_alignbit(x[3], x[3], 19)) * 0x85ebca77u;
  return __builtin_amdgcn_alignbit(a, a, 15) ^ b ^ (x[1] + x[3]);
}
__device__ __forceinline__ unsigned add3(unsigned a, unsigned b, unsigned c) {
  return cvtpk((bflo(a) + bflo(b)) + bflo(c), (bfhi(a) + bfhi(b)) + bfhi(c));
}
__global__ __launch_bounds__(512, 2) void dino_main(const u16* q, const u16* f0, const u16* f1, const u16* wq, const u16* wk, const u16* wv,
                                                     const u16* wo, char* ws, u16* out) {
  __shared__ __attribute__((aligned(128))) char smem[LDS_BYTES];
  LAS char* lds = (LAS char*)smem;
  LAS char* ctl = lds + LDS_CTL;
  const unsigned lb = (unsigned)(unsigned long long)lds;
  const int w = __builtin_amdgcn_readfirstlane(threadIdx.x >> 6);
  unsigned long long pc0;
  asm volatile("s_getpc_b64 %0" : "=s"(pc0));
#define COORDS() \
  int tid = threadIdx.x, wg = blockIdx.x; \
  asm volatile("" : "+v"(tid), "+s"(wg)); \
  __builtin_assume(tid >= 0 && tid < 512); __builtin_assume(wg >= 0 && wg < NWG); \
  const int lane = tid & 63, n = lane & 15, g = lane >> 4;       \
    \
  const int dq = (wg & 7) * 2 + ((wg >> 4) & 1), dst = (wg & ~31) + (wg & 7) * 4 + ((wg >> 3) & 3); \
  const int dj = 6 * ((wg >> 5) & 1) + min(lane & 7, 5); \
  const int dout = dq * C + 48 * (wg >> 6) + 16 * (dj >> 2) + (wg & 8) + ((lane & 8) >> 1) + (dj & 3); \
  const int NT = 32 - gives(wg) + (wg < NPR || (wg & 1) ? 0 : GIVEN / NTK + (((wg - NPR) >> 1) < GIVEN % NTK ? 1 : 0)); \
  const int xslot = (NT >= 30 ? NT - 30 : NT - 20) * SLOT;       \
  (void)xslot; (void)n; (void)g; (void)dst; (void)dout; (void)NT
#define ARGS_IN() asm volatile("s_waitcnt lgkmcnt(0)" :: "s"(q), "s"(f0), "s"(f1), "s"(wq), "s"(wk), "s"(wv), "s"(wo), "s"(ws), "s"(out) : "memory")
#define rws mkrs(ws, WS_BYTES)
#define PROJECT(k0_) do { \
      _Pragma("unroll") for (int k = k0_; k < 6; ++k) { o[0] = mfma(wvv[k], b[k], o[0]); o[1] = mfma(wvv[6 + k], b[k], o[1]); } \
      asm volatile("" : "+v"(o[0]), "+v"(o[1])); \
      SB(); \
      _Pragma("unroll") for (int k = k0_; k < 6; ++k) o[2] = mfma(wvv[12 + k], b[k], o[2]); \
      asm volatile("" : "+v"(o[2])); \
      SB(); \
      stb128(u32x4{cvtpk(o[0][0], o[0][1]), cvtpk(o[0][2], o[0][3]), cvtpk(o[1][0], o[1][1]), tw}, rws, mo); \
      SB(); \
      stb128(u32x4{cvtpk(o[1][2], o[1][3]), cvtpk(o[2][0], o[2][1]), cvtpk(o[2][2], o[2][3]), tw}, rws, mo + 1024); \
    } while (0)
  unsigned long long tg;
#define GET_TAG() tg = ((unsigned long long)((unsigned)lds_spin(CT_TG + 4, ctl, 1) & 0xffu) << 32) | (unsigned)lds_get(ctl + CT_TG)
  unsigned qres = 0u;
  const unsigned long long tstart = wall_clock64();
  if (w != 0) {
    const unsigned l0 = threadIdx.x & 63;
    if (blockIdx.x < NPR) {
      const unsigned p6 = (blockIdx.x * 43) >> 8, pu = blockIdx.x - 6 * p6;
      unsigned vo = ((l0 * 16) ^ (((l0 * 4) & 0x40) | (l0 & 0x20))) + (l0 >> 3) * 256;
      const bool pv = p6 >= 3, wa = w >= 4, isq = !pv && (w & 6) == 2;
      const unsigned a6 = 6 * w - 24, pj = pv ? p6 - 3 : p6;
      unsigned so = (wa ? a6 : isq ? w - 2 : 4 * pu) * 3072 + (isq ? 0 : pv && !wa ? w - 1 : pj) * 128, ga = isq ? 128 : 3072;
      unsigned lo = lb + PSCR + (wa ? a6 : pv ? 4 * w + 20 : (0x1b181e00u >> (8 * w)) & 255u) * 1024, hi = lo + (wa ? 6144 : isq ? 3072 : 4096);
      asm volatile("" : "+v"(vo), "+s"(lo), "+s"(so), "+s"(hi), "+s"(ga));
      ARGS_IN();
      const u32x4 rw = mkrs4(wa ? (pv ? wo : wq) : pv ? wv : isq ? q : wk, isq ? NQ * C * 2 : C * C * 2);
#pragma unroll 1
      do { DMA(lo, vo, rw, so); lo += 1024; so += ga; } while (lo != hi);
    } else if (w == 7 && blockIdx.x >= 56 && blockIdx.x < 72) {
      const u32x4 rc = mkrs4((const void*)(pc0 & ~255ull), CODE_BYTES);
#pragma unroll 1
      for (int i = 0; i < CODE_BYTES; i += 4096)
        asm volatile("s_mov_b32 m0, %0\n\ts_nop 0\n\tbuffer_load_dword %1, %2, %3 offen lds" :: "s"(lb + LDS_SINK), "v"(l0 * 64), "s"(rc), "s"(i) : "memory");
    }
    if (w == 3) {
      unsigned long long ep;
      asm volatile("s_waitcnt lgkmcnt(0)\n\ts_load_dwordx2 %0, %1, %2\n\ts_barrier\n\ts_waitcnt lgkmcnt(0)" : "=&s"(ep) : "s"(ws), "s"(EP_OFF) : "memory");
      *(LAS unsigned long long*)(ctl + CT_TG) = ((ep + 0x100001ull) & 0xffffffffffull) | 0x10000000000ull;
    } else RAW_BARRIER();
  }
  SB();
  if (__builtin_expect(w == 0, 1)) {
    const unsigned l0 = threadIdx.x & 63, g0 = blockIdx.x;
    unsigned y0 = (l0 * 16) ^ (l0 >= 48 ? 48 : 0), y1 = (l0 * 16) ^ (l0 >= 32 ? 64 : 48), y2 = (l0 * 16) ^ (l0 >= 16 ? 112 : 64);
    unsigned v0 = ((l0 * 16) ^ ((l0 * 2) & 0x60)) + (l0 >> 3) * 640 + 1024, v1 = (v0 ^ 16) - 640;
    unsigned v2 = v0 + W0 * C * 2 - 2048, v3 = v1 + W0 * C * 2 - 2048;
    unsigned b0 = g0 * 6144 + (g0 >> 5) * (W0 * C * 2), b1 = g0 * 3072;
    {
      const unsigned iv = (l0 & 15) >= CT_ONE / 8 ? 0x3f803f80u : (l0 >> 3) == CT_MAX / 64 ? 0xff800000u : 0u;
      *(LAS u32x2*)(ctl + 8 * min(l0, 47u)) = u32x2{l0 >= 47 ? 0x3f803f80u : iv, l0 >= 47 ? 0x3f803f80u : iv};
    }
    constexpr unsigned st0 = 16 * W0 * C * 2, st1 = 8 * W1 * C * 2;
    unsigned sb = lb, fo = CT_FREE, fv = 0u, tk = 0u;
    int lap = 0, need = 0;
    u32x4 rt = mkrs4(tabs.seq, 112 * 4);
    unsigned vz = 0u;
    asm volatile("" : "+v"(v0), "+v"(v1), "+v"(v2), "+v"(v3), "+v"(y0), "+v"(y1), "+v"(y2), "+v"(vz), "+s"(b0), "+s"(b1), "+s"(rt));
    ARGS_IN();
    const u32x4 r0 = mkrs4((const char*)f0 - 1024, W0 * W0 * C * 2 + 1024), r1 = mkrs4(f1, W1 * W1 * C * 2);
    if (g0 < NPR) __builtin_amdgcn_s_barrier();
#define UNIT(p_) do { unsigned so_; \
      asm volatile("s_add_u32 m0, %1, %10\n\ts_add_u32 %0, %2, %11\n\t" \
                   "buffer_load_dwordx4 %3, %7, %0 offen nt lds\n\tbuffer_load_dwordx4 %4, %7, %0 offen offset:1024 nt lds\n\t" \
                   "buffer_load_dwordx4 %5, %7, %0 offen offset:2048 nt lds\n\tbuffer_load_dwordx4 %6, %7, %0 offen offset:3072 nt lds\n\t" \
                   "s_mov_b32 m0, %12\n\ts_mov_b64 exec, 1\n\tbuffer_load_dword %13, %8, %9 offen offset:%14 lds\n\ts_mov_b64 exec, -1\n\t" \
                   "s_waitcnt vmcnt(%15)" \
                   : "=&s"(so_) : "s"(sb), "s"(b0), "v"(v0), "v"(v1), "v"(v2), "v"(v3), "s"(r0), "s"(rt), "s"(12 * s), "i"((p_) * PART), "i"((p_) * 128), \
                     "s"(lb + LDS_CTL + CT_FULL - 4 * (p_)), "v"(vz), "i"(4 * (p_)), "i"(6 * DEPTH) : "memory", "scc"); } while (0)
#define COARSE() \
      asm volatile("s_add_u32 m0, %0, %6\n\ts_nop 0\n\tbuffer_load_dwordx4 %1, %4, %5 offen nt lds\n\t" \
                   "buffer_load_dwordx4 %2, %4, %5 offen offset:1024 nt lds\n\tbuffer_load_dwordx4 %3, %4, %5 offen offset:2048 nt lds" \
                   :: "s"(sb), "v"(y0), "v"(y1), "v"(y2), "s"(r1), "s"(b1), "i"(CRS) : "memory", "scc")
#pragma unroll 1
    for (int s = 0; s < 2; ++s) {
      COARSE(); UNIT(0); UNIT(1); UNIT(2);
      sb += SLOT; b0 += st0; b1 += st1;
    }
    if (g0 >= NPR) __builtin_amdgcn_s_barrier();
    __builtin_amdgcn_s_setprio(3);
    COORDS();
    fo += 8;
    const int tau = __builtin_amdgcn_readfirstlane((int)((PACE1 - PACE0) * 256.f * __builtin_amdgcn_rcpf((float)NT)));
    unsigned due = PACE0 * 256u + 2 * tau, t0v = 0u;
    unsigned long long now = tstart;
#pragma unroll 1
    for (int s = 2; s < NT; ++s) {
      asm volatile("" : "+s"(now));
      const unsigned t0s = __builtin_amdgcn_readfirstlane(t0v), t0 = t0s ? t0s : (unsigned)tstart;
      if (PACE1 && (int)(((unsigned)now - t0) * 256u - due) < 0) {
#pragma unroll 1
        for (int spin = 0; spin < 256; ++spin) {
          if ((int)(((unsigned)wall_clock64() - t0) * 256u - due) >= 0) break;
          __builtin_amdgcn_s_sleep(1);
        }
      }
      due += tau;
      if (__builtin_expect((int)__builtin_amdgcn_readfirstlane(fv) < need, 0)) {
        lds_wait(ctl + (s == 8 ? CT_PD : fo), need);
      }
      COARSE();
      UNIT(0);
      UNIT(1);
      if (fo == CT_FREE + 4 * (NS - 1)) { fo = CT_FREE; lap += 4; } else fo += 4;
      need = s == 7 && wg < NPR ? 4 : lap;
      fv = *(volatile LAS unsigned*)(ctl + (s == 7 ? CT_PD : fo));
      if (s == 2 && NT > 32) tk = tabs.take[(wg - NPR) >> 1];
      if (PACE1) { now = wall_clock64(); t0v = *(volatile LAS unsigned*)(ctl + CT_T0); }
      UNIT(2);
      sb = fo == CT_FREE ? lb : sb + SLOT;
      b0 += st0; b1 += st1;
      if (s >= 31) {
        const unsigned xw = tk & 255u, xs = 31 - ((tk >> 8) & 3u);
        b0 = xw * 6144 + (xw >> 5) * (W0 * C * 2) + xs * st0; b1 = xw * 3072 + xs * st1;
        tk >>= 10;
      }
    }
    __builtin_amdgcn_s_setprio(0);
    u32x4 wvv[18], b[6];
    if (TLBE) {
      const u32x4 ra = mkrs4(ws - 4096, 4096), rb = mkrs4(out, NQ * C * 2);
      asm volatile("s_mov_b32 m0, %0\n\ts_mov_b64 exec, 1\n\tbuffer_load_dword %1, %2, 0 offen lds\n\tbuffer_load_dword %1, %3, 0 offen lds\n\ts_mov_b64 exec, -1"
                   :: "s"(lb + LDS_SINK), "v"(vz), "s"(ra), "s"(rb) : "memory");
    }
    lds_wait(ctl + CT_WV, 1);
#pragma unroll
    for (int j = 0; j < 18; ++j) wvv[j] = ldb128<16>(rws, lane * 16 + (j & 3) * 1024, WVO_OFF + 54 * 1024 + (j >> 2) * 4096);
    qres = __builtin_amdgcn_raw_buffer_load_b16(mkrs(q, NQ * C * 2), dout * 2, 0, 0);
    GET_TAG();
    LAS const char *xr = lds + xslot + lane * 16, *xl = lds + xslot + 6144 + 4 * n;
    unsigned mo = wg * 8192 + 6144 + n * 64 + g * 16, tw = (unsigned)(tg >> (20 * (g & 1))) & 0xfffffu, sh = 12 * (g & 1), lse;
    asm volatile("" : "+v"(xr), "+v"(xl), "+v"(mo), "+v"(tw), "+v"(sh));
    __builtin_amdgcn_s_waitcnt(0x0f70);
    lds_spin(CT_XD, ctl, 1);
    {
      int cnt_, c_;
      unsigned t_;
      asm volatile("s_mov_b32 %1, 0x400000\n1:\n\tds_read_b32 %2, %10 offset:%13\n\tds_read_b128 %3, %11\n\tds_read_b128 %4, %11 offset:1024\n\t"
                   "ds_read_b128 %5, %11 offset:2048\n\tds_read_b128 %6, %11 offset:3072\n\tds_read_b128 %7, %11 offset:4096\n\t"
                   "ds_read_b128 %8, %11 offset:5120\n\tds_read_b32 %9, %12\n\ts_waitcnt lgkmcnt(7)\n\tv_readfirstlane_b32 %0, %2\n\t"
                   "s_cmp_ge_i32 %0, 3\n\ts_cbranch_scc1 2f\n\ts_sub_u32 %1, %1, 1\n\ts_cbranch_scc0 1b\n2:\n\ts_waitcnt lgkmcnt(0)"
                   : "=&s"(c_), "=&s"(cnt_), "=&v"(t_), "=&v"(b[0]), "=&v"(b[1]), "=&v"(b[2]), "=&v"(b[3]), "=&v"(b[4]), "=&v"(b[5]), "=&v"(lse)
                   : "v"(ctl), "v"(xr), "v"(xl), "i"(CT_XD) : "memory", "scc");
    }
    SB();
    f32x4 o[3] = {{0.f, 0.f, 0.f, 0.f}, {0.f, 0.f, 0.f, 0.f}, {0.f, 0.f, 0.f, 0.f}};
    tw |= (lse << sh) & 0xfff00000u;
    PROJECT(0);
    lds_inc(CT_ST, ctl);
  } else {
    COORDS();
    const int tq = (lane >> 2) & 3, tp = lane & 3;
    const bool pq = wg < 18;
    const bool pv = wg >= 18 && wg < NPR;
    const int pj = wg / 6, pu = wg % 6;
    if (w >= 4 && ((w + NT) & 3) == 0 && !pq) {
      unsigned k0, k1, k2;
      asm volatile("s_load_dword %0, %3, %5\n\ts_load_dword %1, %3, %6\n\ts_load_dword %2, %4, %7\n\ts_waitcnt lgkmcnt(0)"
                   : "=&s"(k0), "=&s"(k1), "=&s"(k2) : "s"(f0), "s"(f1), "s"((wg >> 3) * 3145728 + 1572860), "s"((wg >> 3) * 3145728 + 3145724),
                     "s"((wg >> 3) * 786432 + 786428) : "memory");
    }
    const u32x4 one = {0x3f803f80u, 0x3f803f80u, 0x3f803f80u, 0x3f803f80u};
    const int fq = ((n & 2) << 1) | ((n & 4) >> 1);
    LAS const char* rq0 = lds + PSCR + 24576 + 3072 * (n >> 3) + 128 * (n & 7) + 16 * (g ^ fq);
    LAS const char* rq1 = lds + PSCR + 24576 + 3072 * (n >> 3) + 128 * (n & 7) + 16 * ((4 + g) ^ fq);
    if (pq && w >= 2) {
      {
        waitvm<0>();
        lds_inc(CT_PA, ctl);
        if (w >= 4) {
          f32x4 sq = {0.f, 0.f, 0.f, 0.f}, rr = sq, cs = sq;
          const int ro = PSCR + 128 * (8 * g + tq) + 16 * (((w & 2) * 2 + tp) ^ ((tq & 2) << 1)) + 8 * (w & 1);
          u32x4 qv[6], af[6];
          lds_spin(CT_PA, ctl, 6);
#pragma unroll
          for (int s = 0; s < 6; ++s) {
            qv[s] = *(LAS const u32x4*)(((s & 1) ? rq1 : rq0) + (s >> 1) * 1024);
            af[s] = vtr2(lds + ro + s * 4096, lds + (ro ^ 32) + s * 4096 + 512);
          }
          SB();
#pragma unroll
          for (int s = 0; s < 6; ++s) { sq = mfma(one, qv[s], sq); rr = mfma(af[s], qv[s], rr); cs = mfma(af[s], one, cs); }
          const f32x4 qq = rr - (sq[0] * (1.f / C)) * cs;
          *(LAS u32x2*)(lds + LDS_QK + 4096 + ((w >> 1) & 1) * 1024 + lane * 16 + 8 * (w & 1)) = u32x2{cvtpk(qq[0], qq[1]), cvtpk(qq[2], qq[3])};
          lds_inc(CT_PB, ctl);
        }
      }
    }
    if (pq && w == 1) {
      {
        waitvm<0>();
        u32x4 kf4[4], qv[6];
        const int ko = LDS_QK + 128 * (((n & 4) << 2) | (n & 11)) + 16 * (g ^ ((n & 2) << 1));
#pragma unroll
        for (int i = 0; i < 4; ++i) kf4[i] = *(LAS const u32x4*)(lds + (ko ^ ((i & 1) * 64 + (i >> 1) * 32)) + (i >> 1) * 512);
        lds_spin(CT_PA, ctl, 6);
#pragma unroll
        for (int s = 0; s < 6; ++s) qv[s] = *(LAS const u32x4*)(((s & 1) ? rq1 : rq0) + (s >> 1) * 1024);
        const f32x4 z = {0.f, 0.f, 0.f, 0.f};
        f32x4 gq = z, sq = z;
#pragma unroll
        for (int s = 0; s < 6; ++s) { gq = mfma(qv[s], qv[s], gq); sq = mfma(one, qv[s], sq); }
        const float mean = sq[0] * (1.f / C);
        const float s2 = xrow16_sum(g == (n >> 2) ? sel4(gq, n & 3) : 0.f);
        float sc = __builtin_amdgcn_rsqf(fmaxf(fmaf(-mean, mean, s2 * (1.f / C)), 0.f) + EPS) * QSCALE;
        asm volatile("" : "+v"(sc));
        GET_TAG();
        lds_spin(CT_PB, ctl, 4);
        const u32x4 q0 = *(LAS const u32x4*)(lds + LDS_QK + 4096 + lane * 16), q1 = *(LAS const u32x4*)(lds + LDS_QK + 5120 + lane * 16);
        const f32x4 d0 = mfma(kf4[1], q1, mfma(kf4[0], q0, z)), d1 = mfma(kf4[3], q1, mfma(kf4[2], q0, z));
        const u32x4 xq = pack8(d0 * sc, d1 * sc);
        const unsigned ck = qk_check(xq, (unsigned)tg);
        asm volatile("" :: "v"(ck));
        {
          const unsigned so = QK_OFF + wg * QK_REC;
#pragma unroll
          for (int cp = 0; cp < 4; ++cp) {
            stb128(xq, rws, lane * 16 + so + cp * QK_COPY);
            stb32(ck, rws, lane * 4 + so + 1024 + cp * QK_COPY);
          }
          stb32((unsigned)tg, rws, HINT_OFF + lane * 128 + (pu >> 1) * 32 + pj * 8 + (pu & 1) * 4);
          lds_put(CT_PD, ctl, 4);
        }
      }
    }
    if (pv) {
      waitvm<0>();
      lds_inc(CT_PA, ctl);
      if (w >= 4) {
        const int ro = PSCR + 128 * (8 * g + tq) + 16 * ((2 * (w - 4) + (tp >> 1)) ^ ((tq & 2) << 1)) + 8 * (tp & 1);
        const int rv = PSCR + 24576 + 128 * n + 16 * (g ^ fq);
        f32x4 d0 = {0.f, 0.f, 0.f, 0.f}, d1 = d0;
        lds_spin(CT_PA, ctl, 7);
        const unsigned org = (unsigned)tstart + max((int)(wall_clock64() - tstart) - LAND0, 0);
#pragma unroll
        for (int s = 0; s < 6; ++s) {
          const u32x4 p = vtr2(lds + ro + s * 4096, lds + (ro ^ 32) + s * 4096 + 512);
          LAS const char* pd = lds + (rv ^ ((s & 1) * 64)) + (s >> 1) * 4096;
          d0 = mfma(*(LAS const u32x4*)pd, p, d0); d1 = mfma(*(LAS const u32x4*)(pd + 2048), p, d1);
        }
        const int fi = (pj - 3) * 24 + (w - 4) * 6 + pu;
        stb128(pack8(d0, d1), rws, lane * 16 + WVO_OFF + fi * 1024);
        lds_inc(CT_PD, ctl);
        GET_TAG();
        waitvm<0>();
        if (lane < 8) stb64(u32x2{(unsigned)tg, (unsigned)(tg >> 32) | org << 8}, rws, FLAG_OFF + lane * 768 + fi * 8);
      }
    }
    const f32x4 z = {0.f, 0.f, 0.f, 0.f};
    int full = 0;
#define NEED(u_) lds_need(CT_FULL, full, ctl, (u_))
    if (w >= 4) {
      const int brg = ((g & 1) << 1) | (g >> 1);
      const int fn = (((n >> 1) & 3) << 1) | ((n >> 3) & 1);
      const int fy = ((n >> 2) & 1) * 4 + ((n >> 1) & 1) * 3;
      const int orr0 = 128 * n + 16 * (brg ^ fn), orr1 = 128 * n + 16 * ((4 + brg) ^ fn);
      const int ory0 = n < 8 ? CRS + 384 * n + 16 * (brg ^ fy) : LDS_CTL + CT_ONE, ory1 = n < 8 ? CRS + 384 * n + 16 * ((4 + brg) ^ fy) : LDS_CTL + CT_ONE;
      const int yon = n < 8 ? 1 : 0;
      const int opk = LDS_PK + 320 * g + 8 * n, ork = LDS_PK + 320 * (2 * (g >> 1) + (n >> 3)) + 256 + 8 * (n & 7) + 4 * (g & 1);
      u32x4 qk[6];
      f32x4 ref, qs;
      LAS const char *px0, *px1, *py0, *py1;
#define AT_SLOT(o_) do { px0 = lds + orr0 + (o_); px1 = lds + orr1 + (o_); py0 = lds + ory0 + __mul24(yon, (o_)); py1 = lds + ory1 + __mul24(yon, (o_)); \
      asm volatile("" : "+v"(px0), "+v"(px1), "+v"(py0), "+v"(py1)); } while (0)
#define RD_K(k_, i_) do { LAS const char* p_ = (((k_) & 1) ? px1 : px0) + ((k_) >> 1) * PART; \
    yv[i_] = *(LAS const u32x4*)((((k_) & 1) ? py1 : py0) + ((k_) >> 1) * 128); \
    xa[i_] = *(LAS const u32x4*)(p_); xb[i_] = *(LAS const u32x4*)(p_ + 2048); } while (0)
#define MF_STAT(i_) do { cy = mfma(yv[i_], yv[i_], cy); ca = mfma(yv[i_], xa[i_], ca); cb = mfma(yv[i_], xb[i_], cb); \
    ga = mfma(xa[i_], xa[i_], ga); gb = mfma(xb[i_], xb[i_], gb); } while (0)
#define MF_S(k_, i_) do { sy = mfma(qk[k_], yv[i_], sy); sa = mfma(qk[k_], xa[i_], sa); sb = mfma(qk[k_], xb[i_], sb); } while (0)
#define STATS(st, rec) do { \
      const int r = n & 3; \
      const float yy = half_copy<true>(sel4(cy, r)); \
      const f32x2 Y = swap32(yy, yy), X = swap32(sel4(ca, r), sel4(cb, r)), G = swap32(sel4(ga, r), sel4(gb, r)); \
      const float q1_ = fmaf(2.f, X[0], Y[0]) + (n < 8 ? G[0] : G[1]);       \
      const f32x2 Q = swap16(q1_, q1_); \
      const float mn_ = (X[1] + Y[1]) * (1.f / C); \
      const float var = fmaxf(fmaf(-mn_, mn_, fmaf((n & 4) ? Q[1] : Q[0], 1.f / C, EPS)), EPS), rs_ = __builtin_amdgcn_rsqf(var); \
      const f32x2 R = swap32(rs_, rs_), Mn = swap32(mn_, mn_); \
      st = f32x4{R[0], Mn[0], R[1], Mn[1]}; \
      const float v_ = (g & 1) ? var * rs_ : mn_, h_ = bfhi(__float_as_uint(v_)); \
      rec = cvtpk(h_, v_ - h_); \
    } while (0)
#define LOGITS(sv, st, base_) do { \
      _Pragma("unroll") for (int r = 0; r < 4; ++r) { \
        const float s0 = half_copy<true>(sy[r]); \
        sv[r] = fmaf(fmaf(-st[1], qs[r], sa[r] + s0), st[0], -(base_)[r]); \
        sv[4 + r] = fmaf(fmaf(-st[3], qs[r], sb[r] + s0), st[2], -(base_)[r]); \
      } \
    } while (0)
#define SV_MAX(sv) fmaxf(fmaxf(fmaxf(sv[0], sv[1]), fmaxf(sv[2], sv[3])), fmaxf(fmaxf(sv[4], sv[5]), fmaxf(sv[6], sv[7])))
#define PUBLISH(sv, st, rec, pk_, word_) do { \
      *(LAS unsigned*)(lds + ork + (pk_) * PK_B) = rec; \
      *(LAS u32x2*)(lds + opk + (pk_) * PK_B) = u32x2{cvtpk(sv[0] * st[0], sv[1] * st[0]), cvtpk(sv[2] * st[0], sv[3] * st[0])}; \
      *(LAS u32x2*)(lds + opk + (pk_) * PK_B + 128) = u32x2{cvtpk(sv[4] * st[2], sv[5] * st[2]), cvtpk(sv[6] * st[2], sv[7] * st[2])}; \
      lds_put(CT_PRDY, ctl + 4 * (pk_), (word_)); \
    } while (0)
      int s = (w + NT - 1) & 3, slot = s + 4, pk = s + 4, ncon = 0;
      {
        u32x4 yv[12], xa[12], xb[12];
        f32x4 st0, st1;
        unsigned rec0, rec1;
#pragma unroll
        for (int h = 0; h < 2; ++h) {
          f32x4 cy = z, ga = z, ca = z, gb = z, cb = z;
          AT_SLOT((s + 4 * h) * SLOT);
          lds_wait(ctl + CT_FULL, 3 * (s + 4 * h) + 3);
#pragma unroll
          for (int k = 0; k < 6; ++k) RD_K(k, 6 * h + k);
          lds_inc(CT_FREE + 16 * h, ctl + 4 * s);
          SB();
#pragma unroll
          for (int k = 0; k < 6; ++k) MF_STAT(6 * h + k);
          SB();
          if (h == 0) { STATS(st0, rec0); asm volatile("" : "+v"(st0), "+v"(rec0)); } else { STATS(st1, rec1); asm volatile("" : "+v"(st1), "+v"(rec1)); }
          SB();
        }
        lds_wait(ctl + CT_QK, 3);
#pragma unroll
        for (int k = 0; k < 6; ++k) qk[k] = *(LAS const u32x4*)(lds + LDS_QK + k * 1024 + lane * 16);
        float s0v[8], s1v[8];
        {
          f32x4 sy = z, sa = z, sb = z;
#pragma unroll
          for (int k = 0; k < 6; ++k) MF_S(k, k);
#pragma unroll
          for (int r = 0; r < 4; ++r) qs[r] = half_copy<false>(sy[r]);
          LOGITS(s0v, st0, z);
        }
        {
          f32x4 sy = z, sa = z, sb = z;
#pragma unroll
          for (int k = 0; k < 6; ++k) MF_S(k, 6 + k);
          LOGITS(s1v, st1, z);
        }
#pragma unroll
        for (int r = 0; r < 4; ++r) ref[r] = row_max(fmaxf(fmaxf(s0v[r], s0v[4 + r]), fmaxf(s1v[r], s1v[4 + r])));
        if (n < 4) __builtin_amdgcn_ds_fmaxf((LAS float*)(ctl + CT_MAX + 16 * g + 4 * n), sel4(ref, n), __ATOMIC_RELAXED, __HIP_MEMORY_SCOPE_WORKGROUP, false);
        lds_inc(CT_QR, ctl);
        lds_spin(CT_QR, ctl, 4);
        ref = *(LAS const f32x4*)(ctl + CT_MAX + 16 * g);
#pragma unroll
        for (int j = 0; j < 8; ++j) s0v[j] = __builtin_amdgcn_exp2f(s0v[j] - ref[j & 3]);
        PUBLISH(s0v, st0, rec0, s, 2 * s + 2);
#pragma unroll
        for (int j = 0; j < 8; ++j) s1v[j] = __builtin_amdgcn_exp2f(s1v[j] - ref[j & 3]);
        if (pk >= NPK) { pk -= NPK; ncon = 3; lds_spin(CT_PCON, ctl + 4 * pk, 3); }
        s += 4;
        PUBLISH(s1v, st1, rec1, pk, 2 * s + 2);
      }
      int moved = 0;
#pragma unroll 1
      for (;;) {
        s += 4; slot += 4; slot -= slot >= NS ? NS : 0; pk += 4;
        if (pk >= NPK) { pk -= NPK; ncon += 3; }
        if (s >= NT) break;
        f32x4 sy = z, cy = z, sa = z, ga = z, ca = z, sb = z, gb = z, cb = z, st;
        u32x4 yv[6], xa[6], xb[6];
        float sv[8];
        unsigned rec;
        AT_SLOT(slot * SLOT);
#define RD_PART(p_) do { int cnt_; unsigned t_; \
        asm volatile("s_mov_b32 %1, 0x400000\n1:\n\tds_read_b32 %2, %9 offset:%15\n\t" \
                     "ds_read_b128 %3, %12 offset:%16\n\tds_read_b128 %4, %10 offset:%17\n\tds_read_b128 %5, %10 offset:%18\n\t" \
                     "ds_read_b128 %6, %13 offset:%16\n\tds_read_b128 %7, %11 offset:%17\n\tds_read_b128 %8, %11 offset:%18\n\t" \
                     "s_cmp_ge_i32 %0, %14\n\ts_cbranch_scc1 2f\n\ts_waitcnt lgkmcnt(6)\n\tv_readfirstlane_b32 %0, %2\n\t" \
                     "s_cmp_ge_i32 %0, %14\n\ts_cbranch_scc1 2f\n\ts_sub_u32 %1, %1, 1\n\ts_cbranch_scc0 1b\n2:" \
                     : "+s"(full), "=&s"(cnt_), "=&v"(t_), "=&v"(yv[2 * (p_)]), "=&v"(xa[2 * (p_)]), "=&v"(xb[2 * (p_)]), \
                       "=&v"(yv[2 * (p_) + 1]), "=&v"(xa[2 * (p_) + 1]), "=&v"(xb[2 * (p_) + 1]) \
                     : "v"(ctl), "v"(px0), "v"(px1), "v"(py0), "v"(py1), "s"(3 * s + (p_) + 1), "i"(CT_FULL), "i"(128 * (p_)), "i"(PART * (p_)), \
                       "i"(PART * (p_) + 2048) : "memory", "scc"); keep = t_; \
        asm volatile("" : "+s"(full)); } while (0)
#define GOT(k_, n_) do { waitlds<n_>(); asm volatile("" : "+v"(yv[k_]), "+v"(xa[k_]), "+v"(xb[k_]) : "v"(keep)); } while (0)
        unsigned keep;
        const unsigned pcv = *(volatile LAS unsigned*)(ctl + CT_PCON + 4 * pk);
        NEED(3 * s);
        RD_PART(0);
        GOT(0, 3);
        int con = __builtin_amdgcn_readfirstlane(pcv);
        SB();
        MF_STAT(0); MF_S(0, 0);
        SB();
        lds_need(CT_PCON, con, ctl + 4 * pk, ncon);
        RD_PART(1);
        GOT(1, 7);
        SB();
        MF_STAT(1); MF_S(1, 1);
        SB();
        GOT(2, 3);
        MF_STAT(2); MF_S(2, 2);
        SB();
        RD_PART(2);
        GOT(3, 7);
        SB();
        MF_STAT(3); MF_S(3, 3);
        SB();
        GOT(4, 3);
        lds_inc(CT_FREE, ctl + 4 * slot);
        MF_STAT(4);
        SB();
        GOT(5, 1);
        MF_STAT(5);
        SB();
        MF_S(4, 4); MF_S(5, 5);
        STATS(st, rec);
        LOGITS(sv, st, ref);
#pragma unroll
        for (int j = 0; j < 6; ++j) { __builtin_amdgcn_sched_group_barrier(0x008, 1, 0); __builtin_amdgcn_sched_group_barrier(0x002, 4, 0); }
        if (__builtin_expect(__any(!(SV_MAX(sv) <= 16.f)), 0)) {
#pragma unroll
          for (int r = 0; r < 4; ++r) {
            const float mx = row_max(fmaxf(sv[r], sv[4 + r])), dm = mx <= 16.f ? 0.f : mx;
            ref[r] += dm; sv[r] -= dm; sv[4 + r] -= dm;
          }
          moved = 1;
        }
        if (__builtin_expect(moved, 0)) {
          if (n == 0) *(LAS f32x4*)(lds + LDS_PK + pk * PK_B + 1280 + 16 * g) = ref;
        }
#pragma unroll
        for (int j = 0; j < 8; ++j) sv[j] = __builtin_amdgcn_exp2f(sv[j]);
        PUBLISH(sv, st, rec, pk, 2 * s + 2 + moved);
      }
      GET_TAG();
    } else {
      const int ftr = ((g & 1) << 2) | ((tq >> 1) << 1) | (g >> 1);
      const int fty = (g & 1) * 4 + (tq >> 1) * 3;
      int otr[4], oty[4];
#pragma unroll
      for (int t = 0; t < 4; ++t) {
        otr[t] = (w - 1) * PART + 128 * (4 * g + tq) + 16 * ((2 * t + (tp >> 1)) ^ ftr) + 8 * (tp & 1);
        oty[t] = CRS + (w - 1) * 128 + 384 * (4 * (g & 1) + tq) + 16 * ((2 * t + (tp >> 1)) ^ fty) + 8 * (tp & 1);
      }
      int opk = LDS_PK + 320 * tp + 8 * (4 * g + tq), ork = LDS_PK + 320 * (g >> 1) + 256 + 8 * (4 * (g & 1) + tq);
      asm volatile("" : "+v"(opk), "+v"(ork), "+v"(oty[0]), "+v"(oty[1]), "+v"(oty[2]), "+v"(oty[3]));
#define RD_T(ta, ty, so_) _Pragma("unroll") for (int t_ = 0; t_ < 4; ++t_) { \
    ta[t_] = vtr2(lds + otr[t_] + (so_), lds + otr[t_] + (so_) + 2048); ty[t_] = vtr(lds + oty[t_] + (so_)); }
      f32x4 acc[4] = {z, z, z, z}, tac = z;
#define RD_P(o_) \
      pf = vtr2(lds + opk + (o_), lds + opk + (o_) + 128); \
      sf = vtr2(lds + ork + (o_), lds + ork + (o_) + 640)
#define TWICE(ty) asm volatile("s_nop 2"); _Pragma("unroll") for (int t = 0; t < 4; ++t) { u32x2 lo_, hi_; \
      asm volatile("v_mov_b64 %0, %2\n\tv_mov_b64 %1, %2" : "=&v"(lo_), "=&v"(hi_) : "v"(ty[t])); t2[t] = u32x4{lo_[0], lo_[1], hi_[0], hi_[1]}; } \
      asm volatile("s_nop 1" : "+v"(t2[0]), "+v"(t2[1]), "+v"(t2[2]), "+v"(t2[3]))
#define MF_PX(ta) \
      tac = mfma(sf, pf, tac); \
      _Pragma("unroll") for (int t = 0; t < 4; ++t) acc[t] = mfma(ta[t], pf, acc[t])
#define MF_PY() \
      _Pragma("unroll") for (int t = 0; t < 4; ++t) acc[t] = mfma(t2[t], pf, acc[t])
#define MOVE_REF(word_, o_) do { moved |= (word_) & 1; if (__builtin_expect(moved, 0)) { \
            const float m_ = *(LAS const float*)(lds + (((word_) & 1) ? LDS_PK + (o_) + 1280 : LDS_CTL + CT_MAX) + 4 * n); \
            Mtop = fmaxf(Mtop, m_); \
            const bool drop = m_ < Mtop - 64.f; \
            const float mn = drop ? M : m_, fa = __builtin_amdgcn_exp2f(M - mn); \
            M = mn; \
            _Pragma("unroll") for (int t = 0; t < 4; ++t) acc[t] *= fa; \
            tac *= fa; \
            if (drop) pf = u32x4{0u, 0u, 0u, 0u}; \
          } } while (0)
      const unsigned flo = FLAG_OFF + (wg & 7) * 768 + (w - 1) * 144 + (lane < 36 ? lane : 35) * 8;
      float M, Mtop;
      int moved = 0;
      u32x4 wvv[18], pf, sf, t2[4];
      {
        u32x4 ta[8][4], xx[6];
        u32x2 ty[8][4], fl;
        unsigned cc[6];
        const int qo = QK_OFF + (wg & 3) * QK_COPY + (2 * w - 2) * QK_REC;
#define QK_LOAD() _Pragma("unroll") for (int j = 0; j < 6; ++j) { \
          xx[j] = ldb128<16>(rws, lane * 16, qo + ((j >> 1) * 6 + (j & 1)) * QK_REC); \
          cc[j] = __builtin_amdgcn_raw_buffer_load_b32(rws, lane * 4, qo + ((j >> 1) * 6 + (j & 1)) * QK_REC + 1024, 16); }
#pragma unroll
        for (int u = 0; u < 8; ++u) {
          if (u == QKAT) {
            GET_TAG();
#pragma unroll 1
            for (int r = 0; r < 8; ++r) {
#pragma unroll 1
              for (int spin = 0; spin < 128; ++spin) {
                if ((int)(wall_clock64() - tstart) >= GATE + r * HSTEP) break;
                __builtin_amdgcn_s_sleep(1);
              }
              u32x8 hv;
              asm volatile("s_load_dwordx8 %0, %1, %2\n\ts_waitcnt lgkmcnt(0)" : "=&s"(hv) : "s"(ws), "s"(HINT_OFF + ((wg & 7) + 8 * r) * 128 + (w - 1) * 32) : "memory");
              const unsigned t_ = (unsigned)tg;
              if ((((hv[0] ^ t_) | (hv[1] ^ t_) | (hv[2] ^ t_)) | ((hv[3] ^ t_) | (hv[4] ^ t_) | (hv[5] ^ t_))) == 0u) break;
            }
            if (wg < NPR) lds_wait(ctl + CT_PD, 4);
            QK_LOAD();
#pragma unroll 1
            for (int spin = 0; spin < (1 << 18); ++spin) {
              unsigned df = 0u;
#pragma unroll
              for (int j = 0; j < 6; ++j) df |= qk_check(xx[j], (unsigned)tg) ^ cc[j];
              if (__all(df == 0u)) break;
              asm volatile("" ::: "memory");
              QK_LOAD();
            }
#pragma unroll
            for (int h = 0; h < 2; ++h) {
              u32x4 sx;
#pragma unroll
              for (int i = 0; i < 4; ++i) sx[i] = add3(xx[h][i], xx[2 + h][i], xx[4 + h][i]);
              *(LAS u32x4*)(lds + LDS_QK + (2 * w - 2 + h) * 1024 + lane * 16) = sx;
            }
            lds_inc(CT_QK, ctl);
            SB();
          }
          lds_wait(ctl + CT_FULL, 3 * u + w);
          RD_T(ta[u], ty[u], u * SLOT);
          lds_inc(CT_FREE + 4 * u, ctl);
          SB();
        }
        fl = __builtin_bit_cast(u32x2, __builtin_amdgcn_raw_buffer_load_b64(rws, flo, 0, 16));
#pragma unroll
        for (int u = 0; u < 12; ++u) {
          const int word = lds_spin(CT_PRDY + 4 * (u % NPK), ctl, 2 * u + 2);
          if (u == 0) Mtop = M = *(LAS const float*)(ctl + CT_MAX + 4 * n);
          RD_P((u % NPK) * PK_B);
          if (u >= 8) MOVE_REF(word, (u % NPK) * PK_B);
          lds_inc(CT_PCON + 4 * (u % NPK), ctl);
          SB();
          MF_PX(ta[u & 7]);
          SB();
          TWICE(ty[u & 7]);
          MF_PY();
          asm volatile("" : "+v"(acc[0]), "+v"(acc[1]), "+v"(acc[2]), "+v"(acc[3]), "+v"(tac));
          SB();
#define FLAGS_UP() __all(fl[0] == (unsigned)tg && (fl[1] & 255u) == (unsigned)(tg >> 32))
          if (u == T0AT && w == 1 && FLAGS_UP()) {
            int d = (int)(fl[1] - ((unsigned)tstart << 8)) >> 8;
            d += __builtin_amdgcn_mov_dpp(d, XOR8, 0xf, 0xf, true); d += __builtin_amdgcn_mov_dpp(d, ROR4, 0xf, 0xf, true);
            d += __builtin_amdgcn_mov_dpp(d, 0x122, 0xf, 0xf, true); d += __builtin_amdgcn_mov_dpp(d, 0x121, 0xf, 0xf, true);
            lds_put(CT_T0, ctl, ((unsigned)tstart + min(max(d >> 4, -300), 300)) | 1u);
          }
          if (u < 4) {
            NEED(3 * (u + 8) + w);
            RD_T(ta[u], ty[u], ((u + 8) % NS) * SLOT);
            lds_inc(CT_FREE + 4 * ((u + 8) % NS), ctl);
            SB();
          }
        }
#pragma unroll 1
        for (int spin = 0; spin < (1 << 18); ++spin) {
          if (FLAGS_UP()) break;
          asm volatile("" ::: "memory");
          fl = __builtin_bit_cast(u32x2, __builtin_amdgcn_raw_buffer_load_b64(rws, flo, 0, 16));
        }
        asm volatile("" ::: "memory");
        if (w == 3) lds_put(CT_WV, ctl, 1);
      }
      int slot = 3, pk = 12 % NPK;
      unsigned rdy = 0u;
      const int kro[3] = {(w - 1) * 2048, (w == 3 ? 0 : w) * 2048, (w == 1 ? 2 : w - 2) * 2048};
#define RD_PACKET() do { int cnt_; u32x2 p0_, p1_, p2_, p3_; \
        word = __builtin_amdgcn_readfirstlane(rdy); \
        asm volatile("s_mov_b32 %1, 0x400000\n1:\n\tds_read_b32 %2, %7 offset:%11\n\t" \
                     "ds_read_b64_tr_b16 %5, %9\n\tds_read_b64_tr_b16 %6, %9 offset:640\n\t" \
                     "ds_read_b64_tr_b16 %3, %8\n\tds_read_b64_tr_b16 %4, %8 offset:128\n\t" \
                     "s_cmp_ge_i32 %0, %10\n\ts_cbranch_scc1 2f\n\ts_waitcnt lgkmcnt(4)\n\tv_readfirstlane_b32 %0, %2\n\t" \
                     "s_cmp_ge_i32 %0, %10\n\ts_cbranch_scc1 2f\n\ts_sub_u32 %1, %1, 1\n\ts_cbranch_scc0 1b\n2:" \
                     : "+s"(word), "=&s"(cnt_), "=&v"(rdy), "=&v"(p0_), "=&v"(p1_), "=&v"(p2_), "=&v"(p3_) \
                     : "v"(ctl + 4 * pk), "v"(lds + opk + pk * PK_B), "v"(lds + ork + pk * PK_B), "s"(2 * s + 2), "i"(CT_PRDY) : "memory", "scc"); \
        asm volatile("" : "+s"(word)); \
        pf = u32x4{p0_[0], p0_[1], p1_[0], p1_[1]}; sf = u32x4{p2_[0], p2_[1], p3_[0], p3_[1]}; } while (0)
#define GOT_PACKET() do { waitlds<0>(); asm volatile("" : "+v"(pf), "+v"(sf) : "v"(rdy)); MOVE_REF(word, pk * PK_B); } while (0)
#define RD_Y(t_, p_) do { const u32x2 lo_ = vtr(p_); asm volatile("" ::: "memory"); const u32x2 hi_ = vtr(p_); \
        t2[t_] = u32x4{lo_[0], lo_[1], hi_[0], hi_[1]}; } while (0)
      u32x4 ta[4];
      int s = 12, word;
      NEED(36 + w);
#pragma unroll
      for (int t = 0; t < 4; ++t) { ta[t] = vtr2(lds + otr[t] + 2 * SLOT, lds + otr[t] + 2 * SLOT + 2048); RD_Y(t, lds + oty[t] + 2 * SLOT); }
      lds_inc(CT_FREE + 8, ctl);
#pragma unroll 1
      for (; s < NT - 1; ++s) {
        RD_PACKET();
        LAS const char *px[4], *py[4];
#pragma unroll
        for (int t = 0; t < 4; ++t) { px[t] = lds + otr[t] + slot * SLOT; py[t] = lds + oty[t] + slot * SLOT; asm volatile("" : "+v"(px[t]), "+v"(py[t])); }
        NEED(3 * s + 3 + w);
        GOT_PACKET();
        SB();
        tac = mfma(sf, pf, tac);
        asm volatile("" : "+v"(tac));
        SB();
        lds_inc(CT_PCON, ctl + 4 * pk);
        pk = pk == NPK - 1 ? 0 : pk + 1;
#pragma unroll
        for (int t = 0; t < 4; ++t) { acc[t] = mfma(ta[t], pf, acc[t]); SB(); ta[t] = vtr2(px[t], px[t] + 2048); SB(); }
        rdy = *(volatile LAS unsigned*)(ctl + CT_PRDY + 4 * pk);
#pragma unroll
        for (int t = 0; t < 4; ++t) { acc[t] = mfma(t2[t], pf, acc[t]); SB(); RD_Y(t, py[t]); SB(); }
        lds_inc(CT_FREE, ctl + 4 * slot);
        if (s < 12 + 18 / WVN) {
#pragma unroll
          for (int u = 0; u < 18 / WVN; ++u)
            if (s - 12 == u) {
#pragma unroll
              for (int j = WVN * u; j < WVN * u + WVN; ++j)
                wvv[j] = ldb128<16>(rws, lane * 16 + (j & 1) * 1024, WVO_OFF + (w - 1) * 18432 + (j / 6) * 6144 + kro[(j % 6) >> 1]);
            }
        }
        slot = slot == NS - 1 ? 0 : slot + 1;
      }
      LAS char *xw = lds + xslot + lane * 16 + kro[0], *xl = lds + xslot + 6144 + 4 * n;
      LAS const char *xr1 = lds + xslot + lane * 16 + kro[1], *xr2 = lds + xslot + lane * 16 + kro[2];
      unsigned mo = wg * 8192 + (w - 1) * 2048 + n * 64 + g * 16, tw = (unsigned)(tg >> (20 * (g & 1))) & 0xfffffu, sh = 12 * (g & 1);
      asm volatile("" : "+v"(xw), "+v"(xl), "+v"(xr1), "+v"(xr2), "+v"(mo), "+v"(tw), "+v"(sh));
      waitvm<0>();
      RD_PACKET();
      GOT_PACKET();
      SB();
      tac = mfma(sf, pf, tac);
      asm volatile("" : "+v"(tac));
      SB();
#pragma unroll
      for (int t = 0; t < 2; ++t) acc[t] = mfma(t2[t], pf, mfma(ta[t], pf, acc[t]));
      SB();
#pragma unroll
      for (int t = 2; t < 4; ++t) acc[t] = mfma(t2[t], pf, mfma(ta[t], pf, acc[t]));
      const float L = tac[2] + tac[3], rl = __builtin_amdgcn_rcpf(L), ct = -(tac[0] + tac[1]) * rl;
      const float lse = M + __builtin_amdgcn_logf(L);
      u32x4 b[6];
      b[0] = pack8(acc[0] * rl + ct, acc[1] * rl + ct);
      *(LAS u32x4*)xw = b[0];
      *(LAS float*)xl = lse;
#pragma unroll
      for (int j = 0; j < 4; ++j) { __builtin_amdgcn_sched_group_barrier(0x008, 1, 0); __builtin_amdgcn_sched_group_barrier(0x002, 3, 0); }
      SB();
      b[1] = pack8(acc[2] * rl + ct, acc[3] * rl + ct);
      *(LAS u32x4*)(xw + 1024) = b[1];
      lds_inc(CT_XD, ctl);
      SB();
      f32x4 o[3];
      unsigned xd;
#pragma unroll
      for (int t = 0; t < 3; ++t) o[t] = mfma(wvv[6 * t], b[0], z);
      asm volatile("" : "+v"(o[0]), "+v"(o[1]), "+v"(o[2]));
      SB();
#define RD_SLICES "ds_read_b32 %0, %7 offset:%10\n\tds_read_b128 %1, %8\n\tds_read_b128 %2, %8 offset:1024\n\tds_read_b128 %3, %9\n\tds_read_b128 %4, %9 offset:1024\n"
      asm volatile(RD_SLICES : "=&v"(xd), "=&v"(b[2]), "=&v"(b[3]), "=&v"(b[4]), "=&v"(b[5]) : "i"(0), "i"(0), "v"(ctl), "v"(xr1), "v"(xr2), "i"(CT_XD) : "memory");
      SB();
#pragma unroll
      for (int t = 0; t < 3; ++t) o[t] = mfma(wvv[6 * t + 1], b[1], o[t]);
      tw |= (__float_as_uint(lse) << sh) & 0xfff00000u;
      asm volatile("" : "+v"(tw), "+v"(o[0]), "+v"(o[1]), "+v"(o[2]));
      SB();
      {
        int cnt_, c_;
        asm volatile("s_mov_b32 %6, 0x400000\n\ts_branch 2f\n1:\n\t" RD_SLICES "2:\n\ts_waitcnt lgkmcnt(4)\n\tv_readfirstlane_b32 %5, %0\n\t"
                     "s_cmp_ge_i32 %5, 3\n\ts_cbranch_scc1 3f\n\ts_sub_u32 %6, %6, 1\n\ts_cbranch_scc0 1b\n3:\n\ts_waitcnt lgkmcnt(0)"
                     : "+v"(xd), "+v"(b[2]), "+v"(b[3]), "+v"(b[4]), "+v"(b[5]), "=&s"(c_), "=&s"(cnt_)
                     : "v"(ctl), "v"(xr1), "v"(xr2), "i"(CT_XD) : "memory", "scc");
      }
      SB();
      PROJECT(2);
      lds_inc(CT_ST, ctl);
    }
  }
  COORDS();

  {
    LAS char* fin = lds + xslot + 8192;
    unsigned ao = (tid >> 1) * 8192 + (tid & 1) * 16 + dst * 32, tw = (unsigned)(tg >> (20 * (tid & 1))), sh = 12 * (tid & 1), oo = dout * 2;
    LAS char *fw = fin + (((lane & 8) | ((g & 1) << 2) | (g & 2)) + (lane & 1)) * 32 + 4 * w, *fr = fin + (2 * (lane & 7) + ((lane >> 3) & 1)) * 32;
    float v7 = 0.f;
    const unsigned t0s = lds_get(ctl + CT_T0), gate = (t0s ? t0s : (unsigned)tstart) + RGATE;
    const RSRC rm = rws;
    u32x4 ro = mkrs4(out, NQ * C * 2);
    unsigned long long wm = 0x0303030303030303ull;
    qres <<= 16;
    asm volatile("" : "+v"(ao), "+v"(tw), "+v"(sh), "+v"(oo), "+v"(fw), "+v"(fr), "+v"(v7), "+v"(qres), "+s"(ro), "+s"(wm));
    lds_spin(CT_ST, ctl, 4);
    float ref = *(LAS const float*)(lds + xslot + 6144 + 4 * dq);
    *(LAS float*)(fin + 512 + 4 * w) = ref;
#pragma unroll 1
    for (int spin = 0; spin < 1024; ++spin)
      if ((int)((unsigned)wall_clock64() - gate) >= 0) break;
    u32x4 A = ldb128<16>(rm, ao, 0);
#pragma unroll 1
    for (int spin = 0; spin < (1 << 18); ++spin) {
      unsigned x = (A[3] ^ tw) << 12;
      x |= __builtin_amdgcn_mov_dpp(x, 0xB1, 0xf, 0xf, true);
      if (__builtin_expect(__builtin_amdgcn_ballot_w64(x != 0u) == 0ull, 1)) break;
      asm volatile("" ::: "memory");
      if (x != 0u) A = ldb128<16>(rm, ao, 0);
    }
    unsigned lx = (A[3] & 0xfff00000u) >> sh;
    lx |= __builtin_amdgcn_mov_dpp(lx, 0xB1, 0xf, 0xf, true);
    float d = __uint_as_float(lx) - ref;
    if (__builtin_expect(__builtin_amdgcn_ballot_w64(!(d <= 64.f)) != 0ull, 0)) {
      ref = xrow16_max(row_max(__uint_as_float(lx)));
      d = __uint_as_float(lx) - ref;
      *(LAS float*)(fin + 512 + 4 * w) = ref;
      lds_put(CT_FAR, ctl, 1);
    }
    const float wt = __builtin_amdgcn_exp2f(d);
    const f32x2 p0 = swap32(bflo(A[0]) * wt, bfhi(A[0]) * wt), p1 = swap32(bflo(A[1]) * wt, bfhi(A[1]) * wt);
    const f32x2 p2 = swap32(bflo(A[2]) * wt, bfhi(A[2]) * wt), p3 = swap32(wt, v7);
    const f32x2 r0 = swap16(p0[0] + p0[1], p1[0] + p1[1]), r1 = swap16(p2[0] + p2[1], p3[0] + p3[1]);
    float c0 = r0[0] + r0[1], c1 = r1[0] + r1[1];
    asm volatile("s_nop 1\n\tv_add_f32_dpp %0, %0, %0 row_ror:8 row_mask:0xf bank_mask:0x3\n\tv_add_f32_dpp %0, %1, %1 row_ror:8 row_mask:0xf bank_mask:0xc\n\t"
                 "s_nop 1\n\tv_add_f32_dpp %0, %0, %0 row_shl:4 row_mask:0xf bank_mask:0x5\n\t"
                 "s_nop 1\n\tv_add_f32_dpp %0, %0, %0 quad_perm:[2,3,0,1] row_mask:0xf bank_mask:0x5\n\t"
                 "s_mov_b64 exec, %3\n\tds_write_b32 %2, %0\n\ts_mov_b64 exec, -1\n\ts_waitcnt lgkmcnt(0)\n\ts_barrier"
                 : "+v"(c0) : "v"(c1), "v"(fw), "s"(wm) : "memory");
    if (w == 0) {
      f32x4 a0 = *(LAS const f32x4*)fr, a1 = *(LAS const f32x4*)(fr + 16), l0 = *(LAS const f32x4*)(fin + 384), l1 = *(LAS const f32x4*)(fin + 400);
      if (__builtin_expect(lds_get(ctl + CT_FAR), 0)) {
        f32x4 m0 = *(LAS const f32x4*)(fin + 512), m1 = *(LAS const f32x4*)(fin + 528);
        const float mm = fmaxf(fmaxf(fmaxf(m0[0], m0[1]), fmaxf(m0[2], m0[3])), fmaxf(fmaxf(m1[0], m1[1]), fmaxf(m1[2], m1[3])));
#pragma unroll
        for (int p = 0; p < 4; ++p) { m0[p] = __builtin_amdgcn_exp2f(m0[p] - mm); m1[p] = __builtin_amdgcn_exp2f(m1[p] - mm); }
        a0 *= m0; l0 *= m0; a1 *= m1; l1 *= m1;
      }
      a0 += a1; l0 += l1;
      const float o = __uint_as_float(qres) + ((a0[0] + a0[2]) + (a0[1] + a0[3])) * __builtin_amdgcn_rcpf((l0[0] + l0[2]) + (l0[1] + l0[3]));
      const unsigned ob = cvtpk(o, __uint_as_float(__builtin_amdgcn_mov_dpp(__float_as_uint(o), 0xB1, 0xf, 0xf, true)));
      asm volatile("s_mov_b64 exec, 0x1515\n\tbuffer_store_dword %0, %1, %2, 0 offen sc1\n\ts_mov_b64 exec, -1" :: "v"(ob), "v"(oo), "s"(ro) : "memory");
      if (wg == 0 && tid == 0) stb64(u32x2{(unsigned)tg, (unsigned)(tg >> 32)}, rws, EP_OFF);
    }
  }
}

#undef rws
#define WS_AT(size_) (((size_) - (64u << 20)) & ~(size_t)0x1fffff)
extern "C" void kernel_launch(void* const* d_in, const int* in_sizes, int n_in, void* d_out, int out_size, void* d_ws, size_t ws_size,
                              hipStream_t stream) {
  dino_main<<<NWG, 512, 0, stream>>>((const u16*)d_in[0], (const u16*)d_in[1], (const u16*)d_in[2], (const u16*)d_in[3], (const u16*)d_in[4],
                                     (const u16*)d_in[5], (const u16*)d_in[6], (char*)d_ws + WS_AT(ws_size) + 4096, (u16*)d_out);
}
```

```cpp
#include <hip/hip_runtime.h>
#include <hip/hip_bf16.h>
#include <stdint.h>

typedef unsigned short u16;
using u32x2 = __attribute__((ext_vector_type(2))) unsigned;
using u32x4 = __attribute__((ext_vector_type(4))) unsigned;
using u32x8 = __attribute__((ext_vector_type(8))) unsigned;
using f32x2 = __attribute__((ext_vector_type(2))) float;
using f32x4 = __attribute__((ext_vector_type(4))) float;
using s16x8 = __attribute__((ext_vector_type(8))) short;
using bf16x2_t = __attribute__((ext_vector_type(2))) __bf16;
typedef short v4i16_t __attribute__((ext_vector_type(4)));
#define LAS __attribute__((address_space(3)))

constexpr int C = 192, NQ = 16, W0 = 512, W1 = 256, NWG = 256, NW = 8;
constexpr float EPS = 1e-5f;
constexpr float QSCALE = 0.07216878364870322f * 1.4426950408889634f;

constexpr int PART = 4096, CRS = 3 * PART;
constexpr int SLOT = CRS + 3072, NS = 10;
constexpr int LDS_QK = NS * SLOT;
constexpr int NPK = 7;
constexpr int LDS_PK = LDS_QK, PK_B = 1344;
constexpr int LDS_CTL = LDS_PK + NPK * PK_B;
constexpr int LDS_SINK = LDS_CTL + 384;
constexpr int LDS_BYTES = LDS_SINK + 256;
constexpr int PSCR = 8 * SLOT;
constexpr int CT_FULL = 8, CT_WV = 4, CT_PA = 0, CT_PB = 12, CT_QR = 16, CT_PD = 20, CT_T0 = 24, CT_XD = 28, CT_TG = 32, CT_QK = 40, CT_ST = 44,
              CT_FREE = 48, CT_FIN = 88, CT_ONE = 112, CT_PRDY = 128, CT_PCON = 160, CT_MAX = 256;

constexpr int MB_BYTES = NWG * NWG * 32;
constexpr int EP_OFF = MB_BYTES;
constexpr int FLAG_OFF = EP_OFF + 512;
constexpr int WVO_OFF = EP_OFF + 8192;
constexpr int QK_OFF = WVO_OFF + C * C * 2;
constexpr int QK_REC = 1280, QK_COPY = 18 * QK_REC;
constexpr int HINT_OFF = QK_OFF + 4 * QK_COPY;
constexpr int WS_BYTES = HINT_OFF + 64 * 128;

#define GIVES 0x000000003232ull
constexpr int NPR = 36, NTK = (NWG - NPR) / 2;
__host__ __device__ constexpr int gives(int wg) { return (int)((GIVES >> (wg < NPR ? 4 * (wg & 1) : 16 + 4 * (wg & 7))) & 15); }
struct Tabs { unsigned seq[112]; unsigned take[NTK]; };
constexpr Tabs make_tabs() {
  Tabs t{};
  for (int i = 0; i < 112; ++i) t.seq[i] = i + 1;
  int k = 0;
  for (int wg = 0; wg < 256; ++wg)
    for (int j = 0; j < gives(wg); ++j, ++k) t.take[k % NTK] += (unsigned)(j << 8 | wg) << (10 * (k / NTK));
  return t;
}
constexpr int given() { int k = 0; for (int wg = 0; wg < 256; ++wg) k += gives(wg); return k; }
constexpr int GIVEN = given();
static_assert(GIVEN <= NTK * 3, "too many tiles sent elsewhere");
__device__ const Tabs tabs = make_tabs();
#define QKAT 3
#define WV1 6
#define WMIN 24
#define WSTEP 6
#define LSTEP 20
#define OWNG 15
#define PACE0 30
#define PACE1 1810
#define RGATE 1995
#define T0AT 3
#define LAND0 200
#define TLBE 1
#define LANDS 0
#define WVS 12
#define HSTEP 40
#define GATE 350
#define CODE_BYTES 25792

__device__ __forceinline__ unsigned cvtpk(float lo, float hi) {
  bf16x2_t v = {(__bf16)lo, (__bf16)hi};
  return __builtin_bit_cast(unsigned, v);
}
__device__ __forceinline__ float bflo(unsigned x) { return __builtin_bit_cast(float, x << 16); }
__device__ __forceinline__ float bfhi(unsigned x) { return __builtin_bit_cast(float, x & 0xffff0000u); }
template <int CTRL> __device__ __forceinline__ float dpp(float x) {
  return __builtin_bit_cast(float, __builtin_amdgcn_mov_dpp(__builtin_bit_cast(int, x), CTRL, 0xf, 0xf, true));
}
constexpr int XOR8 = 0x128, ROR4 = 0x124;
template <bool HI8> __device__ __forceinline__ float half_copy(float x) {
  const int v = __builtin_bit_cast(int, x);
  return __builtin_bit_cast(float, __builtin_amdgcn_update_dpp(v, v, XOR8, 0xf, HI8 ? 0xc : 0x3, false));
}
__device__ __forceinline__ float row_max(float x) {
  x = fmaxf(x, dpp<XOR8>(x)); x = fmaxf(x, dpp<ROR4>(x)); x = fmaxf(x, dpp<0x122>(x));
  return fmaxf(x, dpp<0x121>(x));
}
__device__ __forceinline__ f32x2 swap32(float a, float b) {
  auto s = __builtin_amdgcn_permlane32_swap(__float_as_uint(a), __float_as_uint(b), false, false);
  return f32x2{__uint_as_float(s[0]), __uint_as_float(s[1])};
}
__device__ __forceinline__ f32x2 swap16(float a, float b) {
  auto s = __builtin_amdgcn_permlane16_swap(__float_as_uint(a), __float_as_uint(b), false, false);
  return f32x2{__uint_as_float(s[0]), __uint_as_float(s[1])};
}
__device__ __forceinline__ float xrow16_max(float x) {
  auto s = __builtin_amdgcn_permlane16_swap(__float_as_uint(x), __float_as_uint(x), false, false);
  x = fmaxf(__uint_as_float(s[0]), __uint_as_float(s[1]));
  auto t = __builtin_amdgcn_permlane32_swap(__float_as_uint(x), __float_as_uint(x), false, false);
  return fmaxf(__uint_as_float(t[0]), __uint_as_float(t[1]));
}
__device__ __forceinline__ float xrow16_sum(float x) {
  auto s = __builtin_amdgcn_permlane16_swap(__float_as_uint(x), __float_as_uint(x), false, false);
  x = __uint_as_float(s[0]) + __uint_as_float(s[1]);
  auto t = __builtin_amdgcn_permlane32_swap(__float_as_uint(x), __float_as_uint(x), false, false);
  return __uint_as_float(t[0]) + __uint_as_float(t[1]);
}
__device__ __forceinline__ f32x4 mfma(u32x4 a, u32x4 b, f32x4 c) {
  return __builtin_amdgcn_mfma_f32_16x16x32_bf16(__builtin_bit_cast(s16x8, a), __builtin_bit_cast(s16x8, b), c, 0, 0, 0);
}
__device__ __forceinline__ u32x2 vtr(LAS const char* p) {
  return __builtin_bit_cast(u32x2, __builtin_amdgcn_ds_read_tr16_b64_v4i16((LAS v4i16_t*)p));
}
__device__ __forceinline__ u32x4 vtr2(LAS const char* p, LAS const char* q_) {
  const u32x2 a = vtr(p), b = vtr(q_);
  return u32x4{a[0], a[1], b[0], b[1]};
}
__device__ __forceinline__ float sel4(f32x4 v, int r) {
  const float a = (r & 1) ? v[1] : v[0], b = (r & 1) ? v[3] : v[2];
  return (r & 2) ? b : a;
}
using RSRC = __amdgpu_buffer_rsrc_t;
__device__ __forceinline__ RSRC mkrs(const void* p, int bytes) { return __builtin_amdgcn_make_buffer_rsrc(const_cast<void*>(p), (short)0, bytes, 0x00020000); }
__device__ __forceinline__ u32x4 mkrs4(const void* p, unsigned bytes) {
  const unsigned long long b = (unsigned long long)p;
  return u32x4{(unsigned)b, (unsigned)(b >> 32) & 0xffffu, bytes, 0x00020000u};
}
template <int AUX> __device__ __forceinline__ u32x4 ldb128(RSRC r, unsigned vo, int so) { return __builtin_bit_cast(u32x4, __builtin_amdgcn_raw_buffer_load_b128(r, vo, so, AUX)); }
#define SB() __builtin_amdgcn_sched_barrier(0)
#define WAVE_SYNC() do { __builtin_amdgcn_fence(__ATOMIC_RELEASE, "wavefront"); __builtin_amdgcn_wave_barrier(); __builtin_amdgcn_fence(__ATOMIC_ACQUIRE, "wavefront"); } while (0)
#define RAW_BARRIER() asm volatile("s_waitcnt lgkmcnt(0)\n\ts_barrier" ::: "memory")
#define DMA(m0_, vo_, rs_, so_) asm volatile("s_mov_b32 m0, %0\n\ts_nop 0\n\tbuffer_load_dwordx4 %1, %2, %3 offen lds" :: "s"(m0_), "v"(vo_), "s"(rs_), "s"(so_) : "memory")
template <int N> __device__ __forceinline__ void waitlds() { __builtin_amdgcn_s_waitcnt(0xc07f | (N << 8)); }
template <int N> __device__ __forceinline__ void waitvm() { asm volatile("s_waitcnt vmcnt(%0)" :: "i"(N) : "memory"); }


__device__ __forceinline__ void lds_add(LAS char* p, int lane, unsigned v) {
  asm volatile("" ::: "memory");
  if (lane == 0) __hip_atomic_fetch_add((LAS unsigned*)p, v, __ATOMIC_RELAXED, __HIP_MEMORY_SCOPE_WORKGROUP);
  asm volatile("" ::: "memory");
}
__device__ __forceinline__ void lds_set(LAS char* p, int lane, unsigned v) {
  asm volatile("" ::: "memory");
  if (lane == 0) __hip_atomic_store((LAS unsigned*)p, v, __ATOMIC_RELAXED, __HIP_MEMORY_SCOPE_WORKGROUP);
  asm volatile("" ::: "memory");
}
__device__ __forceinline__ void lds_inc(int O, LAS char* p) {
  asm volatile("s_mov_b64 exec, 1\n\tds_add_u32 %0, %1 offset:%2\n\ts_mov_b64 exec, -1" :: "v"(p), "v"(1u), "i"(O) : "memory");
}
__device__ __forceinline__ void lds_put(int O, LAS char* p, unsigned v) {
  asm volatile("s_mov_b64 exec, 1\n\tds_write_b32 %0, %1 offset:%2\n\ts_mov_b64 exec, -1" :: "v"(p), "v"(v), "i"(O) : "memory");
}
__device__ __forceinline__ int lds_get(LAS char* p) {
  asm volatile("" ::: "memory");
  const unsigned v = __hip_atomic_load((LAS unsigned*)p, __ATOMIC_RELAXED, __HIP_MEMORY_SCOPE_WORKGROUP);
  asm volatile("" ::: "memory");
  return __builtin_amdgcn_readfirstlane(v);
}
__device__ __forceinline__ void lds_wait(LAS char* p, int target) {
#pragma unroll 1
  for (int spin = 0; spin < (1 << 22); ++spin) {
    if (lds_get(p) >= target) break;
    __builtin_amdgcn_s_sleep(1);
  }
}
__device__ __forceinline__ int lds_spin(int O, LAS char* p, int target) {
  int r, cnt = 1 << 22;
  unsigned t;
  asm volatile("1:\n\tds_read_b32 %1, %3 offset:%5\n\ts_waitcnt lgkmcnt(0)\n\tv_readfirstlane_b32 %0, %1\n\ts_cmp_ge_i32 %0, %4\n\ts_cbranch_scc1 2f\n\t"
               "s_sub_u32 %2, %2, 1\n\ts_cbranch_scc0 1b\n2:"
               : "=&s"(r), "=&v"(t), "+s"(cnt) : "v"(p), "s"(target), "i"(O) : "memory", "scc");
  return r;
}
__device__ __forceinline__ void lds_need(int O, int& have, LAS char* p, int target) {
  int cnt;
  unsigned t;
  asm volatile("s_cmp_ge_i32 %0, %4\n\ts_cbranch_scc1 2f\n\ts_mov_b32 %2, 0x400000\n"
               "1:\n\tds_read_b32 %1, %3 offset:%5\n\ts_waitcnt lgkmcnt(0)\n\tv_readfirstlane_b32 %0, %1\n\ts_cmp_ge_i32 %0, %4\n\ts_cbranch_scc1 2f\n\t"
               "s_sub_u32 %2, %2, 1\n\ts_cbranch_scc0 1b\n2:"
               : "+s"(have), "=&v"(t), "=&s"(cnt) : "v"(p), "s"(target), "i"(O) : "memory", "scc");
  asm volatile("" : "+s"(have));
}
__device__ __forceinline__ u32x4 pack8(f32x4 a, f32x4 b) { return u32x4{cvtpk(a[0], a[1]), cvtpk(a[2], a[3]), cvtpk(b[0], b[1]), cvtpk(b[2], b[3])}; }

__device__ __forceinline__ void stb128(u32x4 v, RSRC r, unsigned o) { __builtin_amdgcn_raw_buffer_store_b128(v, r, o, 0, 16); }
__device__ __forceinline__ void stb64(u32x2 v, RSRC r, unsigned o) { __builtin_amdgcn_raw_buffer_store_b64(v, r, o, 0, 16); }
__device__ __forceinline__ void stb32(unsigned v, RSRC r, unsigned o) { __builtin_amdgcn_raw_buffer_store_b32(v, r, o, 0, 16); }
__device__ __forceinline__ unsigned qk_check(u32x4 x, unsigned tg) {
  const unsigned a = (x[0] ^ __builtin_amdgcn_alignbit(x[1], x[1], 11)) * 0x9e3779b1u + tg;
  const unsigned b = (x[2] + __builtin_amdgcn_alignbit(x[3], x[3], 19)) * 0x85ebca77u;
  return __builtin_amdgcn_alignbit(a, a, 15) ^ b ^ (x[1] + x[3]);
}
__device__ __forceinline__ unsigned add3(unsigned a, unsigned b, unsigned c) {
  return cvtpk((bflo(a) + bflo(b)) + bflo(c), (bfhi(a) + bfhi(b)) + bfhi(c));
}
__global__ __launch_bounds__(512, 2) void dino_main(const u16* q, const u16* f0, const u16* f1, const u16* wq, const u16* wk, const u16* wv,
                                                     const u16* wo, char* ws, u16* out) {
  __shared__ __attribute__((aligned(128))) char smem[LDS_BYTES];
  LAS char* lds = (LAS char*)smem;
  LAS char* ctl = lds + LDS_CTL;
  const unsigned lb = (unsigned)(unsigned long long)lds;
  const int w = __builtin_amdgcn_readfirstlane(threadIdx.x >> 6);
  unsigned long long pc0;
  asm volatile("s_getpc_b64 %0" : "=s"(pc0));
#define COORDS() \
  int tid = threadIdx.x, wg = blockIdx.x; \
  asm volatile("" : "+v"(tid), "+s"(wg)); \
  __builtin_assume(tid >= 0 && tid < 512); __builtin_assume(wg >= 0 && wg < NWG); \
  const int lane = tid & 63, n = lane & 15, g = lane >> 4;       \
    \
  const int dq = (wg & 7) * 2 + ((wg >> 4) & 1), dst = (wg & ~31) + (wg & 7) * 4 + ((wg >> 3) & 3); \
  const int dj = 6 * ((wg >> 5) & 1) + min(lane & 7, 5); \
  const int dout = dq * C + 48 * (wg >> 6) + 16 * (dj >> 2) + (wg & 8) + ((lane & 8) >> 1) + (dj & 3); \
  const int NT = 32 - gives(wg) + (wg < NPR || (wg & 1) ? 0 : GIVEN / NTK + (((wg - NPR) >> 1) < GIVEN % NTK ? 1 : 0)); \
  const int xslot = (NT >= 30 ? NT - 30 : NT - 20) * SLOT;       \
  (void)xslot; (void)n; (void)g; (void)dst; (void)dout; (void)NT
#define ARGS_IN() asm volatile("s_waitcnt lgkmcnt(0)" :: "s"(q), "s"(f0), "s"(f1), "s"(wq), "s"(wk), "s"(wv), "s"(wo), "s"(ws), "s"(out) : "memory")
#define rws mkrs(ws, WS_BYTES)
#define PROJECT(k0_) do { \
      _Pragma("unroll") for (int k = k0_; k < 6; ++k) { o[0] = mfma(wvv[k], b[k], o[0]); o[1] = mfma(wvv[6 + k], b[k], o[1]); } \
      asm volatile("" : "+v"(o[0]), "+v"(o[1])); \
      SB(); \
      _Pragma("unroll") for (int k = k0_; k < 6; ++k) o[2] = mfma(wvv[12 + k], b[k], o[2]); \
      asm volatile("" : "+v"(o[2])); \
      SB(); \
      stb128(u32x4{cvtpk(o[0][0], o[0][1]), cvtpk(o[0][2], o[0][3]), cvtpk(o[1][0], o[1][1]), tw}, rws, mo); \
      SB(); \
      stb128(u32x4{cvtpk(o[1][2], o[1][3]), cvtpk(o[2][0], o[2][1]), cvtpk(o[2][2], o[2][3]), tw}, rws, mo + 1024); \
    } while (0)
  unsigned long long tg;
#define GET_TAG() tg = ((unsigned long long)((unsigned)lds_spin(CT_TG + 4, ctl, 1) & 0xffu) << 32) | (unsigned)lds_get(ctl + CT_TG)
  const unsigned long long tstart = wall_clock64();
  if (w != 0) {
    const unsigned l0 = threadIdx.x & 63;
    if (blockIdx.x < NPR) {
      const unsigned p6 = (blockIdx.x * 43) >> 8, pu = blockIdx.x - 6 * p6;
      unsigned vo = ((l0 * 16) ^ (((l0 * 4) & 0x40) | (l0 & 0x20))) + (l0 >> 3) * 256;
      const bool pv = p6 >= 3, wa = w >= 4, isq = !pv && (w & 6) == 2;
      const unsigned a6 = 6 * w - 24, pj = pv ? p6 - 3 : p6;
      unsigned so = (wa ? a6 : isq ? w - 2 : 4 * pu) * 3072 + (isq ? 0 : pv && !wa ? w - 1 : pj) * 128, ga = isq ? 128 : 3072;
      unsigned lo = lb + PSCR + (wa ? a6 : pv ? 4 * w + 20 : (0x1b181e00u >> (8 * w)) & 255u) * 1024, hi = lo + (wa ? 6144 : isq ? 3072 : 4096);
      asm volatile("" : "+v"(vo), "+s"(lo), "+s"(so), "+s"(hi), "+s"(ga));
      ARGS_IN();
      const u32x4 rw = mkrs4(wa ? (pv ? wo : wq) : pv ? wv : isq ? q : wk, isq ? NQ * C * 2 : C * C * 2);
#pragma unroll 1
      do { DMA(lo, vo, rw, so); lo += 1024; so += ga; } while (lo != hi);
    } else if (w == 7 && blockIdx.x >= 56 && blockIdx.x < 72) {
      const u32x4 rc = mkrs4((const void*)(pc0 & ~255ull), CODE_BYTES);
#pragma unroll 1
      for (int i = 0; i < CODE_BYTES; i += 4096)
        asm volatile("s_mov_b32 m0, %0\n\ts_nop 0\n\tbuffer_load_dword %1, %2, %3 offen lds" :: "s"(lb + LDS_SINK), "v"(l0 * 64), "s"(rc), "s"(i) : "memory");
    }
    if (w == 3) {
      unsigned long long ep;
      asm volatile("s_waitcnt lgkmcnt(0)\n\ts_load_dwordx2 %0, %1, %2\n\ts_barrier\n\ts_waitcnt lgkmcnt(0)" : "=&s"(ep) : "s"(ws), "s"(EP_OFF) : "memory");
      *(LAS unsigned long long*)(ctl + CT_TG) = ((ep + 0x100001ull) & 0xffffffffffull) | 0x10000000000ull;
    } else RAW_BARRIER();
  }
  SB();
  if (__builtin_expect(w == 0, 1)) {
    const unsigned l0 = threadIdx.x & 63, g0 = blockIdx.x;
    unsigned y0 = (l0 * 16) ^ (l0 >= 48 ? 48 : 0), y1 = (l0 * 16) ^ (l0 >= 32 ? 64 : 48), y2 = (l0 * 16) ^ (l0 >= 16 ? 112 : 64);
    unsigned v0 = ((l0 * 16) ^ ((l0 * 2) & 0x60)) + (l0 >> 3) * 640 + 1024, v1 = (v0 ^ 16) - 640;
    unsigned v2 = v0 + W0 * C * 2 - 2048, v3 = v1 + W0 * C * 2 - 2048;
    unsigned b0 = g0 * 6144 + (g0 >> 5) * (W0 * C * 2), b1 = g0 * 3072;
    {
      const unsigned iv = (l0 & 15) >= CT_ONE / 8 ? 0x3f803f80u : (l0 >> 3) == CT_MAX / 64 ? 0xff800000u : 0u;
      *(LAS u32x2*)(ctl + 8 * min(l0, 47u)) = u32x2{l0 >= 47 ? 0x3f803f80u : iv, l0 >= 47 ? 0x3f803f80u : iv};
    }
    constexpr unsigned st0 = 16 * W0 * C * 2, st1 = 8 * W1 * C * 2;
    unsigned sb = lb, fo = CT_FREE, fv = 0u, tk = 0u;
    int lap = 0;
    u32x4 rt = mkrs4(tabs.seq, 112 * 4);
    unsigned vz = 0u;
    asm volatile("" : "+v"(v0), "+v"(v1), "+v"(v2), "+v"(v3), "+v"(y0), "+v"(y1), "+v"(y2), "+v"(vz), "+s"(b0), "+s"(b1), "+s"(rt));
    ARGS_IN();
    const u32x4 r0 = mkrs4((const char*)f0 - 1024, W0 * W0 * C * 2 + 1024), r1 = mkrs4(f1, W1 * W1 * C * 2);
    if (g0 < NPR) __builtin_amdgcn_s_barrier();
#define UNIT(p_) do { unsigned so_; \
      asm volatile("s_add_u32 m0, %1, %10\n\ts_add_u32 %0, %2, %11\n\t" \
                   "buffer_load_dwordx4 %3, %7, %0 offen nt lds\n\tbuffer_load_dwordx4 %4, %7, %0 offen offset:1024 nt lds\n\t" \
                   "buffer_load_dwordx4 %5, %7, %0 offen offset:2048 nt lds\n\tbuffer_load_dwordx4 %6, %7, %0 offen offset:3072 nt lds\n\t" \
                   "s_mov_b32 m0, %12\n\ts_mov_b64 exec, 1\n\tbuffer_load_dword %13, %8, %9 offen offset:%14 lds\n\ts_mov_b64 exec, -1" \
                   : "=&s"(so_) : "s"(sb), "s"(b0), "v"(v0), "v"(v1), "v"(v2), "v"(v3), "s"(r0), "s"(rt), "s"(12 * s), "i"((p_) * PART), "i"((p_) * 128), \
                     "s"(lb + LDS_CTL + CT_FULL - 4 * (p_)), "v"(vz), "i"(4 * (p_)) : "memory", "scc"); } while (0)
#define WINDOW() \
      asm volatile("s_waitcnt vmcnt(%1)\n\ts_cmp_ge_i32 %0, %6\n\ts_cbranch_scc1 1f\n\ts_waitcnt vmcnt(%2)\n\ts_cmp_ge_i32 %0, %7\n\ts_cbranch_scc1 1f\n\t" \
                   "s_waitcnt vmcnt(%3)\n\ts_cmp_ge_i32 %0, %8\n\ts_cbranch_scc1 1f\n\ts_waitcnt vmcnt(%4)\n\ts_cmp_ge_i32 %0, %9\n\ts_cbranch_scc1 1f\n\t" \
                   "s_waitcnt vmcnt(%5)\n1:" :: "s"(late), "i"(WMIN + 4 * WSTEP), "i"(WMIN + 3 * WSTEP), "i"(WMIN + 2 * WSTEP), "i"(WMIN + WSTEP), "i"(WMIN), \
                   "i"(4 * LSTEP * 256), "i"(3 * LSTEP * 256), "i"(2 * LSTEP * 256), "i"(LSTEP * 256) : "memory", "scc")
#define COARSE(sb_) \
      asm volatile("s_add_u32 m0, %0, %6\n\ts_nop 0\n\tbuffer_load_dwordx4 %1, %4, %5 offen nt lds\n\t" \
                   "buffer_load_dwordx4 %2, %4, %5 offen offset:1024 nt lds\n\tbuffer_load_dwordx4 %3, %4, %5 offen offset:2048 nt lds" \
                   :: "s"(sb_), "v"(y0), "v"(y1), "v"(y2), "s"(r1), "s"(b1), "i"(CRS) : "memory", "scc")
    COARSE(sb);
#pragma unroll 1
    for (int s = 0; s < 2; ++s) {
      b1 += st1;
      UNIT(0); UNIT(1); COARSE(sb + SLOT); UNIT(2);
      sb += SLOT; b0 += st0;
    }
    if (g0 >= NPR) __builtin_amdgcn_s_barrier();
    __builtin_amdgcn_s_setprio(3);
    COORDS();
    fo += 12;
    const int tau = __builtin_amdgcn_readfirstlane((int)((PACE1 - PACE0) * 256.f * __builtin_amdgcn_rcpf((float)NT)));
    unsigned due = PACE0 * 256u + 2 * tau, t0v = 0u;
    unsigned long long now = tstart;
#pragma unroll 1
    for (int s = 2; s < NT; ++s) {
      asm volatile("" : "+s"(now));
      const unsigned t0s = __builtin_amdgcn_readfirstlane(t0v), t0 = t0s ? t0s : (unsigned)tstart;
      if (PACE1 && (int)(((unsigned)now - t0) * 256u - due) < 0) {
#pragma unroll 1
        for (int spin = 0; spin < 256; ++spin) {
          if ((int)(((unsigned)wall_clock64() - t0) * 256u - due) >= 0) break;
          __builtin_amdgcn_s_sleep(1);
        }
      }
      fv = *(volatile LAS unsigned*)(ctl + (s == 7 ? CT_PD : fo));
      now = wall_clock64();
      UNIT(0);
      asm volatile("" : "+s"(now));
      const int late = (int)(((unsigned)now - t0) * 256u - due);
      due += tau;
      WINDOW();
      UNIT(1);
      WINDOW();
      const unsigned nsb = fo == CT_FREE ? lb : sb + SLOT;
      unsigned n0 = b0 + st0;
      b1 += st1;
      if (s >= 31) {
        const unsigned xw = tk & 255u, xs = 31 - ((tk >> 8) & 3u);
        n0 = xw * 6144 + (xw >> 5) * (W0 * C * 2) + xs * st0; b1 = xw * 3072 + xs * st1;
        tk >>= 10;
      }
      if (s + 1 < NT) {
        const int need = s == 7 && wg < NPR ? 4 : lap;
        if (__builtin_expect((int)__builtin_amdgcn_readfirstlane(fv) < need, 0)) {
          lds_wait(ctl + (s == 7 ? CT_PD : fo), need);
        }
        COARSE(nsb);
      }
      if (s == 2 && NT > 32) tk = tabs.take[(wg - NPR) >> 1];
      now = wall_clock64();
      t0v = *(volatile LAS unsigned*)(ctl + CT_T0);
      UNIT(2);
      WINDOW();
      if (fo == CT_FREE + 4 * (NS - 1)) { fo = CT_FREE; lap += 4; } else fo += 4;
      sb = nsb; b0 = n0;
    }
    __builtin_amdgcn_s_setprio(0);
    u32x4 wvv[18], b[6];
    if (TLBE) {
      const u32x4 ra = mkrs4(ws - 4096, 4096), rb = mkrs4(out, NQ * C * 2);
      asm volatile("s_mov_b32 m0, %0\n\ts_mov_b64 exec, 1\n\tbuffer_load_dword %1, %2, 0 offen lds\n\tbuffer_load_dword %1, %3, 0 offen lds\n\ts_mov_b64 exec, -1"
                   :: "s"(lb + LDS_SINK), "v"(vz), "s"(ra), "s"(rb) : "memory");
    }
    lds_wait(ctl + CT_WV, 1);
#pragma unroll
    for (int j = 0; j < 18; ++j) wvv[j] = ldb128<16>(rws, lane * 16 + (j & 3) * 1024, WVO_OFF + 54 * 1024 + (j >> 2) * 4096);
    GET_TAG();
    LAS const char *xr = lds + xslot + lane * 16, *xl = lds + xslot + 6144 + 4 * n;
    unsigned mo = wg * 8192 + 6144 + n * 64 + g * 16, tw = (unsigned)(tg >> (20 * (g & 1))) & 0xfffffu, sh = 12 * (g & 1), lse;
    asm volatile("" : "+v"(xr), "+v"(xl), "+v"(mo), "+v"(tw), "+v"(sh));
    __builtin_amdgcn_s_waitcnt(0x0f70);
    lds_spin(CT_XD, ctl, 1);
    {
      int cnt_, c_;
      unsigned t_;
      asm volatile("s_mov_b32 %1, 0x400000\n1:\n\tds_read_b32 %2, %10 offset:%13\n\tds_read_b128 %3, %11\n\tds_read_b128 %4, %11 offset:1024\n\t"
                   "ds_read_b128 %5, %11 offset:2048\n\tds_read_b128 %6, %11 offset:3072\n\tds_read_b128 %7, %11 offset:4096\n\t"
                   "ds_read_b128 %8, %11 offset:5120\n\tds_read_b32 %9, %12\n\ts_waitcnt lgkmcnt(7)\n\tv_readfirstlane_b32 %0, %2\n\t"
                   "s_cmp_ge_i32 %0, 3\n\ts_cbranch_scc1 2f\n\ts_sub_u32 %1, %1, 1\n\ts_cbranch_scc0 1b\n2:\n\ts_waitcnt lgkmcnt(0)"
                   : "=&s"(c_), "=&s"(cnt_), "=&v"(t_), "=&v"(b[0]), "=&v"(b[1]), "=&v"(b[2]), "=&v"(b[3]), "=&v"(b[4]), "=&v"(b[5]), "=&v"(lse)
                   : "v"(ctl), "v"(xr), "v"(xl), "i"(CT_XD) : "memory", "scc");
    }
    SB();
    f32x4 o[3] = {{0.f, 0.f, 0.f, 0.f}, {0.f, 0.f, 0.f, 0.f}, {0.f, 0.f, 0.f, 0.f}};
    tw |= (lse << sh) & 0xfff00000u;
    PROJECT(0);
    lds_inc(CT_ST, ctl);
  } else {
    COORDS();
    const int tq = (lane >> 2) & 3, tp = lane & 3;
    const bool pq = wg < 18;
    const bool pv = wg >= 18 && wg < NPR;
    const int pj = wg / 6, pu = wg % 6;
    if (w >= 4 && ((w + NT) & 3) == 0 && !pq) {
      unsigned k0, k1, k2;
      asm volatile("s_load_dword %0, %3, %5\n\ts_load_dword %1, %3, %6\n\ts_load_dword %2, %4, %7\n\ts_waitcnt lgkmcnt(0)"
                   : "=&s"(k0), "=&s"(k1), "=&s"(k2) : "s"(f0), "s"(f1), "s"((wg >> 3) * 3145728 + 1572860), "s"((wg >> 3) * 3145728 + 3145724),
                     "s"((wg >> 3) * 786432 + 786428) : "memory");
    }
    const u32x4 one = {0x3f803f80u, 0x3f803f80u, 0x3f803f80u, 0x3f803f80u};
    const int fq = ((n & 2) << 1) | ((n & 4) >> 1);
    LAS const char* rq0 = lds + PSCR + 24576 + 3072 * (n >> 3) + 128 * (n & 7) + 16 * (g ^ fq);
    LAS const char* rq1 = lds + PSCR + 24576 + 3072 * (n >> 3) + 128 * (n & 7) + 16 * ((4 + g) ^ fq);
    if (pq && w >= 2) {
      {
        waitvm<0>();
        lds_inc(CT_PA, ctl);
        if (w >= 4) {
          f32x4 sq = {0.f, 0.f, 0.f, 0.f}, rr = sq, cs = sq;
          const int ro = PSCR + 128 * (8 * g + tq) + 16 * (((w & 2) * 2 + tp) ^ ((tq & 2) << 1)) + 8 * (w & 1);
          u32x4 qv[6], af[6];
          lds_spin(CT_PA, ctl, 6);
#pragma unroll
          for (int s = 0; s < 6; ++s) {
            qv[s] = *(LAS const u32x4*)(((s & 1) ? rq1 : rq0) + (s >> 1) * 1024);
            af[s] = vtr2(lds + ro + s * 4096, lds + (ro ^ 32) + s * 4096 + 512);
          }
          SB();
#pragma unroll
          for (int s = 0; s < 6; ++s) { sq = mfma(one, qv[s], sq); rr = mfma(af[s], qv[s], rr); cs = mfma(af[s], one, cs); }
          const f32x4 qq = rr - (sq[0] * (1.f / C)) * cs;
          *(LAS u32x2*)(lds + LDS_QK + 4096 + ((w >> 1) & 1) * 1024 + lane * 16 + 8 * (w & 1)) = u32x2{cvtpk(qq[0], qq[1]), cvtpk(qq[2], qq[3])};
          lds_inc(CT_PB, ctl);
        }
      }
    }
    if (pq && w == 1) {
      {
        waitvm<0>();
        u32x4 kf4[4], qv[6];
        const int ko = LDS_QK + 128 * (((n & 4) << 2) | (n & 11)) + 16 * (g ^ ((n & 2) << 1));
#pragma unroll
        for (int i = 0; i < 4; ++i) kf4[i] = *(LAS const u32x4*)(lds + (ko ^ ((i & 1) * 64 + (i >> 1) * 32)) + (i >> 1) * 512);
        lds_spin(CT_PA, ctl, 6);
#pragma unroll
        for (int s = 0; s < 6; ++s) qv[s] = *(LAS const u32x4*)(((s & 1) ? rq1 : rq0) + (s >> 1) * 1024);
        const f32x4 z = {0.f, 0.f, 0.f, 0.f};
        f32x4 gq = z, sq = z;
#pragma unroll
        for (int s = 0; s < 6; ++s) { gq = mfma(qv[s], qv[s], gq); sq = mfma(one, qv[s], sq); }
        const float mean = sq[0] * (1.f / C);
        const float s2 = xrow16_sum(g == (n >> 2) ? sel4(gq, n & 3) : 0.f);
        float sc = __builtin_amdgcn_rsqf(fmaxf(fmaf(-mean, mean, s2 * (1.f / C)), 0.f) + EPS) * QSCALE;
        asm volatile("" : "+v"(sc));
        GET_TAG();
        lds_spin(CT_PB, ctl, 4);
        const u32x4 q0 = *(LAS const u32x4*)(lds + LDS_QK + 4096 + lane * 16), q1 = *(LAS const u32x4*)(lds + LDS_QK + 5120 + lane * 16);
        const f32x4 d0 = mfma(kf4[1], q1, mfma(kf4[0], q0, z)), d1 = mfma(kf4[3], q1, mfma(kf4[2], q0, z));
        const u32x4 xq = pack8(d0 * sc, d1 * sc);
        const unsigned ck = qk_check(xq, (unsigned)tg);
        asm volatile("" :: "v"(ck));
        {
          const unsigned so = QK_OFF + wg * QK_REC;
#pragma unroll
          for (int cp = 0; cp < 4; ++cp) {
            stb128(xq, rws, lane * 16 + so + cp * QK_COPY);
            stb32(ck, rws, lane * 4 + so + 1024 + cp * QK_COPY);
          }
          stb32((unsigned)tg, rws, HINT_OFF + lane * 128 + (pu >> 1) * 32 + pj * 8 + (pu & 1) * 4);
          lds_put(CT_PD, ctl, 4);
        }
      }
    }
    if (pv) {
      waitvm<0>();
      lds_inc(CT_PA, ctl);
      if (w >= 4) {
        const int ro = PSCR + 128 * (8 * g + tq) + 16 * ((2 * (w - 4) + (tp >> 1)) ^ ((tq & 2) << 1)) + 8 * (tp & 1);
        const int rv = PSCR + 24576 + 128 * n + 16 * (g ^ fq);
        f32x4 d0 = {0.f, 0.f, 0.f, 0.f}, d1 = d0;
        lds_spin(CT_PA, ctl, 7);
        const unsigned org = (unsigned)tstart + (max((int)(wall_clock64() - tstart) - LAND0, 0) >> LANDS);
#pragma unroll
        for (int s = 0; s < 6; ++s) {
          const u32x4 p = vtr2(lds + ro + s * 4096, lds + (ro ^ 32) + s * 4096 + 512);
          LAS const char* pd = lds + (rv ^ ((s & 1) * 64)) + (s >> 1) * 4096;
          d0 = mfma(*(LAS const u32x4*)pd, p, d0); d1 = mfma(*(LAS const u32x4*)(pd + 2048), p, d1);
        }
        const int fi = (pj - 3) * 24 + (w - 4) * 6 + pu;
        stb128(pack8(d0, d1), rws, lane * 16 + WVO_OFF + fi * 1024);
        lds_inc(CT_PD, ctl);
        GET_TAG();
        waitvm<0>();
        if (lane < 8) stb64(u32x2{(unsigned)tg, (unsigned)(tg >> 32) | org << 8}, rws, FLAG_OFF + lane * 768 + fi * 8);
      }
    }
    const f32x4 z = {0.f, 0.f, 0.f, 0.f};
    int full = 0;
#define NEED(u_) lds_need(CT_FULL, full, ctl, (u_))
    if (w >= 4) {
      const int brg = ((g & 1) << 1) | (g >> 1);
      const int fn = (((n >> 1) & 3) << 1) | ((n >> 3) & 1);
      const int fy = ((n >> 2) & 1) * 4 + ((n >> 1) & 1) * 3;
      const int orr0 = 128 * n + 16 * (brg ^ fn), orr1 = 128 * n + 16 * ((4 + brg) ^ fn);
      const int ory0 = n < 8 ? CRS + 384 * n + 16 * (brg ^ fy) : LDS_CTL + CT_ONE, ory1 = n < 8 ? CRS + 384 * n + 16 * ((4 + brg) ^ fy) : LDS_CTL + CT_ONE;
      const int yon = n < 8 ? 1 : 0;
      const int opk = LDS_PK + 320 * g + 8 * n, ork = LDS_PK + 320 * (2 * (g >> 1) + (n >> 3)) + 256 + 8 * (n & 7) + 4 * (g & 1);
      u32x4 qk[6];
      f32x4 ref, qs;
      LAS const char *px0, *px1, *py0, *py1;
#define AT_SLOT(o_) do { px0 = lds + orr0 + (o_); px1 = lds + orr1 + (o_); py0 = lds + ory0 + __mul24(yon, (o_)); py1 = lds + ory1 + __mul24(yon, (o_)); \
      asm volatile("" : "+v"(px0), "+v"(px1), "+v"(py0), "+v"(py1)); } while (0)
#define RD_K(k_, i_) do { LAS const char* p_ = (((k_) & 1) ? px1 : px0) + ((k_) >> 1) * PART; \
    yv[i_] = *(LAS const u32x4*)((((k_) & 1) ? py1 : py0) + ((k_) >> 1) * 128); \
    xa[i_] = *(LAS const u32x4*)(p_); xb[i_] = *(LAS const u32x4*)(p_ + 2048); } while (0)
#define MF_STAT(i_) do { cy = mfma(yv[i_], yv[i_], cy); ca = mfma(yv[i_], xa[i_], ca); cb = mfma(yv[i_], xb[i_], cb); \
    ga = mfma(xa[i_], xa[i_], ga); gb = mfma(xb[i_], xb[i_], gb); } while (0)
#define MF_S(k_, i_) do { sy = mfma(qk[k_], yv[i_], sy); sa = mfma(qk[k_], xa[i_], sa); sb = mfma(qk[k_], xb[i_], sb); } while (0)
#define STATS(st, rec) do { \
      const int r = n & 3; \
      const float yy = half_copy<true>(sel4(cy, r)); \
      const f32x2 Y = swap32(yy, yy), X = swap32(sel4(ca, r), sel4(cb, r)), G = swap32(sel4(ga, r), sel4(gb, r)); \
      const float q1_ = fmaf(2.f, X[0], Y[0]) + (n < 8 ? G[0] : G[1]);       \
      const f32x2 Q = swap16(q1_, q1_); \
      const float mn_ = (X[1] + Y[1]) * (1.f / C); \
      const float var = fmaxf(fmaf(-mn_, mn_, fmaf((n & 4) ? Q[1] : Q[0], 1.f / C, EPS)), EPS), rs_ = __builtin_amdgcn_rsqf(var); \
      const f32x2 R = swap32(rs_, rs_), Mn = swap32(mn_, mn_); \
      st = f32x4{R[0], Mn[0], R[1], Mn[1]}; \
      const float v_ = (g & 1) ? var * rs_ : mn_, h_ = bfhi(__float_as_uint(v_)); \
      rec = cvtpk(h_, v_ - h_); \
    } while (0)
#define LOGITS(sv, st, base_) do { \
      _Pragma("unroll") for (int r = 0; r < 4; ++r) { \
        const float s0 = half_copy<true>(sy[r]); \
        sv[r] = fmaf(fmaf(-st[1], qs[r], sa[r] + s0), st[0], -(base_)[r]); \
        sv[4 + r] = fmaf(fmaf(-st[3], qs[r], sb[r] + s0), st[2], -(base_)[r]); \
      } \
    } while (0)
#define SV_MAX(sv) fmaxf(fmaxf(fmaxf(sv[0], sv[1]), fmaxf(sv[2], sv[3])), fmaxf(fmaxf(sv[4], sv[5]), fmaxf(sv[6], sv[7])))
#define PUBLISH(sv, st, rec, pk_, word_) do { \
      *(LAS unsigned*)(lds + ork + (pk_) * PK_B) = rec; \
      *(LAS u32x2*)(lds + opk + (pk_) * PK_B) = u32x2{cvtpk(sv[0] * st[0], sv[1] * st[0]), cvtpk(sv[2] * st[0], sv[3] * st[0])}; \
      *(LAS u32x2*)(lds + opk + (pk_) * PK_B + 128) = u32x2{cvtpk(sv[4] * st[2], sv[5] * st[2]), cvtpk(sv[6] * st[2], sv[7] * st[2])}; \
      lds_put(CT_PRDY, ctl + 4 * (pk_), (word_)); \
    } while (0)
      int s = (w + NT - 1) & 3, slot = s + 4, pk = s + 4, ncon = 0;
      {
        u32x4 yv[12], xa[12], xb[12];
        f32x4 st0, st1;
        unsigned rec0, rec1;
#pragma unroll
        for (int h = 0; h < 2; ++h) {
          f32x4 cy = z, ga = z, ca = z, gb = z, cb = z;
          AT_SLOT((s + 4 * h) * SLOT);
          lds_wait(ctl + CT_FULL, 3 * (s + 4 * h) + 3);
#pragma unroll
          for (int k = 0; k < 6; ++k) RD_K(k, 6 * h + k);
          lds_inc(CT_FREE + 16 * h, ctl + 4 * s);
          SB();
#pragma unroll
          for (int k = 0; k < 6; ++k) MF_STAT(6 * h + k);
          SB();
          if (h == 0) { STATS(st0, rec0); asm volatile("" : "+v"(st0), "+v"(rec0)); } else { STATS(st1, rec1); asm volatile("" : "+v"(st1), "+v"(rec1)); }
          SB();
        }
        lds_wait(ctl + CT_QK, 3);
#pragma unroll
        for (int k = 0; k < 6; ++k) qk[k] = *(LAS const u32x4*)(lds + LDS_QK + k * 1024 + lane * 16);
        float s0v[8], s1v[8];
        {
          f32x4 sy = z, sa = z, sb = z;
#pragma unroll
          for (int k = 0; k < 6; ++k) MF_S(k, k);
#pragma unroll
          for (int r = 0; r < 4; ++r) qs[r] = half_copy<false>(sy[r]);
          LOGITS(s0v, st0, z);
        }
        {
          f32x4 sy = z, sa = z, sb = z;
#pragma unroll
          for (int k = 0; k < 6; ++k) MF_S(k, 6 + k);
          LOGITS(s1v, st1, z);
        }
#pragma unroll
        for (int r = 0; r < 4; ++r) ref[r] = row_max(fmaxf(fmaxf(s0v[r], s0v[4 + r]), fmaxf(s1v[r], s1v[4 + r])));
        if (n < 4) __builtin_amdgcn_ds_fmaxf((LAS float*)(ctl + CT_MAX + 16 * g + 4 * n), sel4(ref, n), __ATOMIC_RELAXED, __HIP_MEMORY_SCOPE_WORKGROUP, false);
        lds_inc(CT_QR, ctl);
        lds_spin(CT_QR, ctl, 4);
        ref = *(LAS const f32x4*)(ctl + CT_MAX + 16 * g);
#pragma unroll
        for (int j = 0; j < 8; ++j) s0v[j] = __builtin_amdgcn_exp2f(s0v[j] - ref[j & 3]);
        PUBLISH(s0v, st0, rec0, s, 2 * s + 2);
#pragma unroll
        for (int j = 0; j < 8; ++j) s1v[j] = __builtin_amdgcn_exp2f(s1v[j] - ref[j & 3]);
        if (pk >= NPK) { pk -= NPK; ncon = 3; lds_spin(CT_PCON, ctl + 4 * pk, 3); }
        s += 4;
        PUBLISH(s1v, st1, rec1, pk, 2 * s + 2);
      }
      float thr = 16.f;
      f32x4 nref = -ref;
#pragma unroll 1
      for (;;) {
        s += 4; slot += 4; slot -= slot >= NS ? NS : 0; pk += 4;
        if (pk >= NPK) { pk -= NPK; ncon += 3; }
        if (s >= NT) break;
        f32x4 sy = z, cy = z, sa, ga = z, ca = z, sb, gb = z, cb = z;
        u32x4 yv[6], xa[6], xb[6];
        unsigned fl_, word = 2 * s + 2;
        AT_SLOT(slot * SLOT);
        LAS char *pw = lds + opk + pk * PK_B, *prc = lds + ork + pk * PK_B, *pfl = ctl + 4 * pk, *pfr = ctl + 4 * slot;
        asm volatile("" : "+v"(pw), "+v"(prc), "+v"(pfl), "+v"(pfr), "+v"(word));
#define RD_X "ds_read_b32 %2, %7 offset:%11\n\tds_read_b128 %3, %8 offset:%12\n\tds_read_b128 %4, %9 offset:%12\n\t" \
             "ds_read_b128 %5, %8 offset:%13\n\tds_read_b128 %6, %9 offset:%13\n"
#define RD_IN(p_) "v"(ctl), "v"(px0), "v"(px1), "s"(3 * s + (p_) + 1), "i"(CT_FULL), "i"(PART * (p_)), "i"(PART * (p_) + 2048)
#define ISSUE(p_) do { int d0_, d1_; \
        asm volatile(RD_X : "=&s"(d0_), "=&s"(d1_), "=&v"(fl_), "=&v"(xa[2 * (p_)]), "=&v"(xa[2 * (p_) + 1]), "=&v"(xb[2 * (p_)]), "=&v"(xb[2 * (p_) + 1]) \
                     : RD_IN(p_) : "memory"); } while (0)
#define CHECK(p_) do { int cnt_; \
        asm volatile("s_mov_b32 %1, 0x400000\n\ts_branch 2f\n1:\n\t" RD_X "2:\n\ts_waitcnt lgkmcnt(4)\n\tv_readfirstlane_b32 %0, %2\n\t" \
                     "s_cmp_ge_i32 %0, %10\n\ts_cbranch_scc1 3f\n\ts_sub_u32 %1, %1, 1\n\ts_cbranch_scc0 1b\n3:\n\ts_waitcnt lgkmcnt(2)" \
                     : "=&s"(full), "=&s"(cnt_), "+v"(fl_), "+v"(xa[2 * (p_)]), "+v"(xa[2 * (p_) + 1]), "+v"(xb[2 * (p_)]), "+v"(xb[2 * (p_) + 1]) \
                     : RD_IN(p_) : "memory", "scc"); \
        asm volatile("" : "+s"(full)); } while (0)
#define GOT_B(p_) do { waitlds<0>(); asm volatile("" : "+v"(xb[2 * (p_)]), "+v"(xb[2 * (p_) + 1])); } while (0)
#define MF_A(k_) do { ca = mfma(yv[k_], xa[k_], ca); ga = mfma(xa[k_], xa[k_], ga); sa = mfma(qk[k_], xa[k_], sa); } while (0)
#define MF_B(k_) do { cb = mfma(yv[k_], xb[k_], cb); gb = mfma(xb[k_], xb[k_], gb); sb = mfma(qk[k_], xb[k_], sb); } while (0)
        const unsigned pcv = *(volatile LAS unsigned*)(pfl + CT_PCON);
        NEED(3 * s);
#pragma unroll
        for (int k = 0; k < 6; ++k) yv[k] = *(LAS const u32x4*)(((k & 1) ? py1 : py0) + (k >> 1) * 128);
        int con = __builtin_amdgcn_readfirstlane(pcv);
        SB();
#pragma unroll
        for (int k = 0; k < 6; ++k) sy = mfma(qk[k], yv[k], sy);
        asm volatile("" : "+v"(sy));
        SB();
        lds_need(CT_PCON, con, pfl, ncon);
        ISSUE(0);
        SB();
#pragma unroll
        for (int k = 0; k < 6; ++k) cy = mfma(yv[k], yv[k], cy);
#pragma unroll
        for (int r = 0; r < 4; ++r) sa[r] = half_copy<true>(sy[r]);
        const float yy = half_copy<true>(sel4(cy, n & 3));
        f32x2 Y = swap32(yy, yy);
        asm volatile("" : "+v"(Y), "+v"(sa));
        SB();
        CHECK(0);
        sb = sa;
        MF_A(0);
        asm volatile("" : "+v"(ca), "+v"(ga), "+v"(sa));
        SB();
        GOT_B(0);
        ISSUE(1);
        SB();
        MF_B(0); MF_A(1); MF_B(1);
        asm volatile("" : "+v"(ca), "+v"(ga), "+v"(sa), "+v"(cb), "+v"(gb), "+v"(sb));
        SB();
        CHECK(1);
        MF_A(2);
        asm volatile("" : "+v"(ca), "+v"(ga), "+v"(sa));
        SB();
        GOT_B(1);
        ISSUE(2);
        SB();
        MF_B(2); MF_A(3); MF_B(3);
        asm volatile("" : "+v"(ca), "+v"(ga), "+v"(sa), "+v"(cb), "+v"(gb), "+v"(sb));
        SB();
        CHECK(2);
        ca = mfma(yv[5], xa[5], mfma(yv[4], xa[4], ca));
        ga = mfma(xa[5], xa[5], mfma(xa[4], xa[4], ga));
        asm volatile("" : "+v"(ca), "+v"(ga));
        SB();
        GOT_B(2);
        cb = mfma(yv[5], xb[5], mfma(yv[4], xb[4], cb));
        asm volatile("" : "+v"(cb));
        SB();
        lds_inc(CT_FREE, pfr);
        gb = mfma(xb[4], xb[4], gb);
        float ua = sel4(ca, n & 3);
        asm volatile("" : "+v"(gb), "+v"(ua));
        SB();
        gb = mfma(xb[5], xb[5], gb);
        float va = sel4(ga, n & 3);
        asm volatile("" : "+v"(gb), "+v"(va));
        SB();
        sa = mfma(qk[4], xa[4], sa);
        float ub = sel4(cb, n & 3);
        asm volatile("" : "+v"(sa), "+v"(ub));
        SB();
        sa = mfma(qk[5], xa[5], sa);
        const f32x2 X = swap32(ua, ub);
        float mn_ = (X[1] + Y[1]) * (1.f / C), q1_ = fmaf(2.f, X[0], Y[0]);
        asm volatile("" : "+v"(sa), "+v"(mn_), "+v"(q1_));
        SB();
        sb = mfma(qk[4], xb[4], sb);
        float vb = sel4(gb, n & 3);
        asm volatile("" : "+v"(sb), "+v"(vb));
        SB();
        sb = mfma(qk[5], xb[5], sb);
        const f32x2 Mn = swap32(mn_, mn_), G = swap32(va, vb);
        q1_ += n < 8 ? G[0] : G[1];
        const f32x2 Q = swap16(q1_, q1_);
        sa -= Mn[0] * qs;
        const float var = fmaxf(fmaf(-mn_, mn_, fmaf((n & 4) ? Q[1] : Q[0], 1.f / C, EPS)), EPS), rs_ = __builtin_amdgcn_rsqf(var);
        sb -= Mn[1] * qs;
        const float v_ = (g & 1) ? var * rs_ : mn_, h_ = bfhi(__float_as_uint(v_));
        *(LAS unsigned*)prc = cvtpk(h_, v_ - h_);
        const f32x2 R = swap32(rs_, rs_);
        sa = sa * R[0] + nref; sb = sb * R[1] + nref;
        const float tm = fmaxf(fmaxf(fmaxf(fmaxf(sa[0], sa[1]), sa[2]), fmaxf(fmaxf(sa[3], sb[0]), sb[1])), fmaxf(sb[2], sb[3]));
        if (__builtin_expect(__builtin_amdgcn_ballot_w64(!(tm <= thr)) != 0ull, 0)) {
#pragma unroll
          for (int r = 0; r < 4; ++r) {
            const float mx = row_max(fmaxf(sa[r], sb[r])), dm = mx <= 16.f ? 0.f : mx;
            nref[r] -= dm; sa[r] -= dm; sb[r] -= dm;
          }
          thr = __builtin_nanf("");
          word |= 1u;
          if (n == 0) *(LAS f32x4*)(lds + LDS_PK + pk * PK_B + 1280 + 16 * g) = -nref;
        }
#pragma unroll
        for (int r = 0; r < 4; ++r) { sa[r] = __builtin_amdgcn_exp2f(sa[r]); sb[r] = __builtin_amdgcn_exp2f(sb[r]); }
        float ra = R[0], rb = R[1];
        asm volatile("" : "+v"(ra), "+v"(rb));
        sa *= ra; sb *= rb;
        *(LAS u32x2*)pw = u32x2{cvtpk(sa[0], sa[1]), cvtpk(sa[2], sa[3])};
        *(LAS u32x2*)(pw + 128) = u32x2{cvtpk(sb[0], sb[1]), cvtpk(sb[2], sb[3])};
        lds_put(CT_PRDY, pfl, word);
        SB();
      }
      GET_TAG();
      LAS char* fin = lds + xslot + 8192;
      unsigned qres = (unsigned)__builtin_amdgcn_raw_buffer_load_b16(mkrs(q, NQ * C * 2), dout * 2, 0, 0) << 16;
      unsigned ao = ((tid & 255) >> 1) * 8192 + (tid & 1) * 16 + dst * 32, tw = (unsigned)(tg >> (20 * (tid & 1))), sh = 12 * (tid & 1), oo = dout * 2;
      LAS char *fw = fin + (((lane & 8) | ((g & 1) << 2) | (g & 2)) + (lane & 1)) * 16 + 4 * (w - 4), *fr = fin + (2 * (lane & 7) + ((lane >> 3) & 1)) * 16;
      float v7 = 0.f;
      unsigned fi_ = 8u;
      const unsigned t0s = lds_get(ctl + CT_T0);
      unsigned gate = (t0s ? t0s : (unsigned)tstart) + RGATE;
      const RSRC rm = rws;
      u32x4 ro = mkrs4(out, NQ * C * 2);
      unsigned long long wm = 0x0303030303030303ull;
      asm volatile("" : "+v"(ao), "+v"(tw), "+v"(sh), "+v"(oo), "+v"(fw), "+v"(fr), "+v"(fin), "+v"(v7), "+v"(qres), "+v"(fi_), "+s"(ro), "+s"(wm));
      lds_spin(CT_ST, ctl, 4);
      gate += max((int)((unsigned)wall_clock64() + OWNG - gate), 0);
      float org = *(LAS const float*)(lds + xslot + 6144 + 4 * dq);
      *(LAS float*)(fin + 256 + 4 * (w - 4)) = org;
#pragma unroll 1
      for (int spin = 0; spin < 1024; ++spin)
        if ((int)((unsigned)wall_clock64() - gate) >= 0) break;
      u32x4 A = ldb128<16>(rm, ao, 0), B = ldb128<16>(rm, ao, 128 * 8192);
#pragma unroll 1
      for (int spin = 0; spin < (1 << 18); ++spin) {
        unsigned xa_ = (A[3] ^ tw) << 12, xb_ = (B[3] ^ tw) << 12;
        if (__builtin_expect(__builtin_amdgcn_ballot_w64((xa_ | xb_) != 0u) == 0ull, 1)) break;
        xa_ |= __builtin_amdgcn_mov_dpp(xa_, 0xB1, 0xf, 0xf, true); xb_ |= __builtin_amdgcn_mov_dpp(xb_, 0xB1, 0xf, 0xf, true);
        asm volatile("" ::: "memory");
        if (xa_ != 0u) A = ldb128<16>(rm, ao, 0);
        if (xb_ != 0u) B = ldb128<16>(rm, ao, 128 * 8192);
      }
      unsigned la = (A[3] & 0xfff00000u) >> sh, lb_ = (B[3] & 0xfff00000u) >> sh;
      la |= __builtin_amdgcn_mov_dpp(la, 0xB1, 0xf, 0xf, true); lb_ |= __builtin_amdgcn_mov_dpp(lb_, 0xB1, 0xf, 0xf, true);
      float da = __uint_as_float(la) - org, db = __uint_as_float(lb_) - org;
      if (__builtin_expect(__builtin_amdgcn_ballot_w64(!(da <= 64.f) || !(db <= 64.f)) != 0ull, 0)) {
        org = xrow16_max(row_max(fmaxf(__uint_as_float(la), __uint_as_float(lb_))));
        da = __uint_as_float(la) - org; db = __uint_as_float(lb_) - org;
        *(LAS float*)(fin + 256 + 4 * (w - 4)) = org;
        fi_ = 9u;
      }
      const float wa = __builtin_amdgcn_exp2f(da), wb = __builtin_amdgcn_exp2f(db);
#define MIX(f_, i_) fmaf(f_(A[i_]), wa, f_(B[i_]) * wb)
      const f32x2 p0 = swap32(MIX(bflo, 0), MIX(bfhi, 0)), p1 = swap32(MIX(bflo, 1), MIX(bfhi, 1));
      const f32x2 p2 = swap32(MIX(bflo, 2), MIX(bfhi, 2)), p3 = swap32(wa + wb, v7);
      const f32x2 r0 = swap16(p0[0] + p0[1], p1[0] + p1[1]), r1 = swap16(p2[0] + p2[1], p3[0] + p3[1]);
      float c0 = r0[0] + r0[1], c1 = r1[0] + r1[1];
      asm volatile("s_nop 1\n\tv_add_f32_dpp %0, %0, %0 row_ror:8 row_mask:0xf bank_mask:0x3\n\tv_add_f32_dpp %0, %1, %1 row_ror:8 row_mask:0xf bank_mask:0xc\n\t"
                   "s_nop 1\n\tv_add_f32_dpp %0, %0, %0 row_shl:4 row_mask:0xf bank_mask:0x5\n\t"
                   "s_nop 1\n\tv_add_f32_dpp %0, %0, %0 quad_perm:[2,3,0,1] row_mask:0xf bank_mask:0x5\n\t"
                   "s_mov_b64 exec, %3\n\tds_write_b32 %2, %0\n\ts_mov_b64 exec, 1\n\tds_add_u32 %4, %5 offset:%6\n\ts_mov_b64 exec, -1"
                   : "+v"(c0) : "v"(c1), "v"(fw), "s"(wm), "v"(ctl), "v"(fi_), "i"(CT_FIN) : "memory");
      if (w == 4) {
        f32x4 a0, l0;
        unsigned cf;
        int cnt_, c_;
        asm volatile("s_mov_b32 %1, 0x4000000\n1:\n\tds_read_b32 %2, %5 offset:%8\n\tds_read_b128 %3, %6\n\tds_read_b128 %4, %7 offset:192\n\t"
                     "s_waitcnt lgkmcnt(2)\n\tv_readfirstlane_b32 %0, %2\n\ts_cmp_ge_i32 %0, 32\n\ts_cbranch_scc1 2f\n\t"
                     "s_sub_u32 %1, %1, 1\n\ts_cbranch_scc0 1b\n2:\n\ts_waitcnt lgkmcnt(0)"
                     : "=&s"(c_), "=&s"(cnt_), "=&v"(cf), "=&v"(a0), "=&v"(l0) : "v"(ctl), "v"(fr), "v"(fin), "i"(CT_FIN) : "memory", "scc");
        asm volatile("" : "+s"(c_));
        if (__builtin_expect(c_ != 32, 0)) {
          f32x4 m0 = *(LAS const f32x4*)(fin + 256);
          const float mm = fmaxf(fmaxf(m0[0], m0[1]), fmaxf(m0[2], m0[3]));
#pragma unroll
          for (int p = 0; p < 4; ++p) m0[p] = __builtin_amdgcn_exp2f(m0[p] - mm);
          a0 *= m0; l0 *= m0;
        }
        const float o = __uint_as_float(qres) + ((a0[0] + a0[2]) + (a0[1] + a0[3])) * __builtin_amdgcn_rcpf((l0[0] + l0[2]) + (l0[1] + l0[3]));
        const unsigned ob = cvtpk(o, __uint_as_float(__builtin_amdgcn_mov_dpp(__float_as_uint(o), 0xB1, 0xf, 0xf, true)));
        asm volatile("s_mov_b64 exec, 0x1515\n\tbuffer_store_dword %0, %1, %2, 0 offen sc1\n\ts_mov_b64 exec, -1" :: "v"(ob), "v"(oo), "s"(ro) : "memory");
        if (wg == 0 && lane == 0) stb64(u32x2{(unsigned)tg, (unsigned)(tg >> 32)}, rws, EP_OFF);
      }
    } else {
      const int ftr = ((g & 1) << 2) | ((tq >> 1) << 1) | (g >> 1);
      const int fty = (g & 1) * 4 + (tq >> 1) * 3;
      int otr[4], oty[4];
#pragma unroll
      for (int t = 0; t < 4; ++t) {
        otr[t] = (w - 1) * PART + 128 * (4 * g + tq) + 16 * ((2 * t + (tp >> 1)) ^ ftr) + 8 * (tp & 1);
        oty[t] = CRS + (w - 1) * 128 + 384 * (4 * (g & 1) + tq) + 16 * ((2 * t + (tp >> 1)) ^ fty) + 8 * (tp & 1);
      }
      int opk = LDS_PK + 320 * tp + 8 * (4 * g + tq), ork = LDS_PK + 320 * (g >> 1) + 256 + 8 * (4 * (g & 1) + tq);
      asm volatile("" : "+v"(opk), "+v"(ork), "+v"(oty[0]), "+v"(oty[1]), "+v"(oty[2]), "+v"(oty[3]));
#define RD_T(ta, ty, so_) _Pragma("unroll") for (int t_ = 0; t_ < 4; ++t_) { \
    ta[t_] = vtr2(lds + otr[t_] + (so_), lds + otr[t_] + (so_) + 2048); ty[t_] = vtr(lds + oty[t_] + (so_)); }
      f32x4 acc[4] = {z, z, z, z}, tac = z;
#define RD_P(o_) \
      pf = vtr2(lds + opk + (o_), lds + opk + (o_) + 128); \
      sf = vtr2(lds + ork + (o_), lds + ork + (o_) + 640)
#define TWICE(ty) asm volatile("s_nop 2"); _Pragma("unroll") for (int t = 0; t < 4; ++t) { u32x2 lo_, hi_; \
      asm volatile("v_mov_b64 %0, %2\n\tv_mov_b64 %1, %2" : "=&v"(lo_), "=&v"(hi_) : "v"(ty[t])); t2[t] = u32x4{lo_[0], lo_[1], hi_[0], hi_[1]}; } \
      asm volatile("s_nop 1" : "+v"(t2[0]), "+v"(t2[1]), "+v"(t2[2]), "+v"(t2[3]))
#define MF_PX(ta) \
      tac = mfma(sf, pf, tac); \
      _Pragma("unroll") for (int t = 0; t < 4; ++t) acc[t] = mfma(ta[t], pf, acc[t])
#define MF_PY() \
      _Pragma("unroll") for (int t = 0; t < 4; ++t) acc[t] = mfma(t2[t], pf, acc[t])
#define MOVE_REF(word_, o_) do { moved |= (word_) & 1; if (__builtin_expect(moved, 0)) { \
            const float m_ = *(LAS const float*)(lds + (((word_) & 1) ? LDS_PK + (o_) + 1280 : LDS_CTL + CT_MAX) + 4 * n); \
            Mtop = fmaxf(Mtop, m_); \
            const bool drop = m_ < Mtop - 64.f; \
            const float mn = drop ? M : m_, fa = __builtin_amdgcn_exp2f(M - mn); \
            M = mn; \
            _Pragma("unroll") for (int t = 0; t < 4; ++t) acc[t] *= fa; \
            tac *= fa; \
            if (drop) pf = u32x4{0u, 0u, 0u, 0u}; \
          } } while (0)
      const unsigned flo = FLAG_OFF + (wg & 7) * 768 + (w - 1) * 144 + (lane < 36 ? lane : 35) * 8;
      float M, Mtop;
      int moved = 0;
      u32x4 wvv[18], pf, sf, t2[4];
      {
        u32x4 ta[8][4], xx[6];
        u32x2 ty[8][4], fl;
        unsigned cc[6];
        const int qo = QK_OFF + (wg & 3) * QK_COPY + (2 * w - 2) * QK_REC;
#define QK_LOAD() _Pragma("unroll") for (int j = 0; j < 6; ++j) { \
          xx[j] = ldb128<16>(rws, lane * 16, qo + ((j >> 1) * 6 + (j & 1)) * QK_REC); \
          cc[j] = __builtin_amdgcn_raw_buffer_load_b32(rws, lane * 4, qo + ((j >> 1) * 6 + (j & 1)) * QK_REC + 1024, 16); }
#pragma unroll
        for (int u = 0; u < 8; ++u) {
          if (u == QKAT) {
            GET_TAG();
#pragma unroll 1
            for (int r = 0; r < 8; ++r) {
#pragma unroll 1
              for (int spin = 0; spin < 128; ++spin) {
                if ((int)(wall_clock64() - tstart) >= GATE + r * HSTEP) break;
                __builtin_amdgcn_s_sleep(1);
              }
              u32x8 hv;
              asm volatile("s_load_dwordx8 %0, %1, %2\n\ts_waitcnt lgkmcnt(0)" : "=&s"(hv) : "s"(ws), "s"(HINT_OFF + ((wg & 7) + 8 * r) * 128 + (w - 1) * 32) : "memory");
              const unsigned t_ = (unsigned)tg;
              if ((((hv[0] ^ t_) | (hv[1] ^ t_) | (hv[2] ^ t_)) | ((hv[3] ^ t_) | (hv[4] ^ t_) | (hv[5] ^ t_))) == 0u) break;
            }
            if (wg < NPR) lds_wait(ctl + CT_PD, 4);
            QK_LOAD();
#pragma unroll 1
            for (int spin = 0; spin < (1 << 18); ++spin) {
              unsigned df = 0u;
#pragma unroll
              for (int j = 0; j < 6; ++j) df |= qk_check(xx[j], (unsigned)tg) ^ cc[j];
              if (__all(df == 0u)) break;
              asm volatile("" ::: "memory");
              QK_LOAD();
            }
#pragma unroll
            for (int h = 0; h < 2; ++h) {
              u32x4 sx;
#pragma unroll
              for (int i = 0; i < 4; ++i) sx[i] = add3(xx[h][i], xx[2 + h][i], xx[4 + h][i]);
              *(LAS u32x4*)(lds + LDS_QK + (2 * w - 2 + h) * 1024 + lane * 16) = sx;
            }
            lds_inc(CT_QK, ctl);
            SB();
          }
          lds_wait(ctl + CT_FULL, 3 * u + w);
          RD_T(ta[u], ty[u], u * SLOT);
          lds_inc(CT_FREE + 4 * u, ctl);
          SB();
        }
        fl = __builtin_bit_cast(u32x2, __builtin_amdgcn_raw_buffer_load_b64(rws, flo, 0, 16));
#pragma unroll
        for (int u = 0; u < 12; ++u) {
          const int word = lds_spin(CT_PRDY + 4 * (u % NPK), ctl, 2 * u + 2);
          if (u == 0) Mtop = M = *(LAS const float*)(ctl + CT_MAX + 4 * n);
          RD_P((u % NPK) * PK_B);
          if (u >= 8) MOVE_REF(word, (u % NPK) * PK_B);
          lds_inc(CT_PCON + 4 * (u % NPK), ctl);
          SB();
          MF_PX(ta[u & 7]);
          SB();
          TWICE(ty[u & 7]);
          MF_PY();
          asm volatile("" : "+v"(acc[0]), "+v"(acc[1]), "+v"(acc[2]), "+v"(acc[3]), "+v"(tac));
          SB();
#define FLAGS_UP() __all(fl[0] == (unsigned)tg && (fl[1] & 255u) == (unsigned)(tg >> 32))
          if (u == T0AT && w == 1 && FLAGS_UP()) {
            int d = (int)(fl[1] - ((unsigned)tstart << 8)) >> 8;
            d += __builtin_amdgcn_mov_dpp(d, XOR8, 0xf, 0xf, true); d += __builtin_amdgcn_mov_dpp(d, ROR4, 0xf, 0xf, true);
            d += __builtin_amdgcn_mov_dpp(d, 0x122, 0xf, 0xf, true); d += __builtin_amdgcn_mov_dpp(d, 0x121, 0xf, 0xf, true);
            lds_put(CT_T0, ctl, ((unsigned)tstart + min(max(d >> 4, -300), 300)) | 1u);
          }
          if (u < 4) {
            NEED(3 * (u + 8) + w);
            RD_T(ta[u], ty[u], ((u + 8) % NS) * SLOT);
            lds_inc(CT_FREE + 4 * ((u + 8) % NS), ctl);
            SB();
          }
        }
#pragma unroll 1
        for (int spin = 0; spin < (1 << 18); ++spin) {
          if (FLAGS_UP()) break;
          asm volatile("" ::: "memory");
          fl = __builtin_bit_cast(u32x2, __builtin_amdgcn_raw_buffer_load_b64(rws, flo, 0, 16));
        }
        asm volatile("" ::: "memory");
        if (w == 3) lds_put(CT_WV, ctl, 1);
      }
      int slot = 3, pk = 12 % NPK;
      unsigned rdy = 0u;
      const int kro[3] = {(w - 1) * 2048, (w == 3 ? 0 : w) * 2048, (w == 1 ? 2 : w - 2) * 2048};
#define RD_PACKET() do { int cnt_; u32x2 p0_, p1_, p2_, p3_; \
        word = __builtin_amdgcn_readfirstlane(rdy); \
        asm volatile("s_mov_b32 %1, 0x400000\n1:\n\tds_read_b32 %2, %7 offset:%11\n\t" \
                     "ds_read_b64_tr_b16 %5, %9\n\tds_read_b64_tr_b16 %6, %9 offset:640\n\t" \
                     "ds_read_b64_tr_b16 %3, %8\n\tds_read_b64_tr_b16 %4, %8 offset:128\n\t" \
                     "s_cmp_ge_i32 %0, %10\n\ts_cbranch_scc1 2f\n\ts_waitcnt lgkmcnt(4)\n\tv_readfirstlane_b32 %0, %2\n\t" \
                     "s_cmp_ge_i32 %0, %10\n\ts_cbranch_scc1 2f\n\ts_sub_u32 %1, %1, 1\n\ts_cbranch_scc0 1b\n2:" \
                     : "+s"(word), "=&s"(cnt_), "=&v"(rdy), "=&v"(p0_), "=&v"(p1_), "=&v"(p2_), "=&v"(p3_) \
                     : "v"(ctl + 4 * pk), "v"(lds + opk + pk * PK_B), "v"(lds + ork + pk * PK_B), "s"(2 * s + 2), "i"(CT_PRDY) : "memory", "scc"); \
        asm volatile("" : "+s"(word)); \
        pf = u32x4{p0_[0], p0_[1], p1_[0], p1_[1]}; sf = u32x4{p2_[0], p2_[1], p3_[0], p3_[1]}; } while (0)
#define GOT_PACKET() do { waitlds<0>(); asm volatile("" : "+v"(pf), "+v"(sf) : "v"(rdy)); MOVE_REF(word, pk * PK_B); } while (0)
#define RD_Y(t_, p_) do { const u32x2 lo_ = vtr(p_); asm volatile("" ::: "memory"); const u32x2 hi_ = vtr(p_); \
        t2[t_] = u32x4{lo_[0], lo_[1], hi_[0], hi_[1]}; } while (0)
      u32x4 ta[4];
      int s = 12, word;
      NEED(36 + w);
#pragma unroll
      for (int t = 0; t < 4; ++t) { ta[t] = vtr2(lds + otr[t] + 2 * SLOT, lds + otr[t] + 2 * SLOT + 2048); RD_Y(t, lds + oty[t] + 2 * SLOT); }
      lds_inc(CT_FREE + 8, ctl);
#pragma unroll 1
      for (; s < NT - 1; ++s) {
        RD_PACKET();
        LAS const char *px[4], *py[4];
#pragma unroll
        for (int t = 0; t < 4; ++t) { px[t] = lds + otr[t] + slot * SLOT; py[t] = lds + oty[t] + slot * SLOT; asm volatile("" : "+v"(px[t]), "+v"(py[t])); }
        NEED(3 * s + 3 + w);
        GOT_PACKET();
        SB();
        tac = mfma(sf, pf, tac);
        asm volatile("" : "+v"(tac));
        SB();
        lds_inc(CT_PCON, ctl + 4 * pk);
        pk = pk == NPK - 1 ? 0 : pk + 1;
#pragma unroll
        for (int t = 0; t < 4; ++t) { acc[t] = mfma(ta[t], pf, acc[t]); SB(); ta[t] = vtr2(px[t], px[t] + 2048); SB(); }
        rdy = *(volatile LAS unsigned*)(ctl + CT_PRDY + 4 * pk);
#pragma unroll
        for (int t = 0; t < 4; ++t) { acc[t] = mfma(t2[t], pf, acc[t]); SB(); RD_Y(t, py[t]); SB(); }
        lds_inc(CT_FREE, ctl + 4 * slot);
#define WV_LOAD(j_) wvv[j_] = ldb128<16>(rws, lane * 16 + ((j_) & 1) * 1024, WVO_OFF + (w - 1) * 18432 + ((j_) / 6) * 6144 + kro[((j_) % 6) >> 1])
        if ((unsigned)(s - WVS) < WV1) {
#pragma unroll
          for (int u = 0; u < WV1; ++u)
            if (s - WVS == u) WV_LOAD(u);
        }
        if (s == NT - 3) {
#pragma unroll
          for (int j = WV1; j < 18; ++j) WV_LOAD(j);
        }
        slot = slot == NS - 1 ? 0 : slot + 1;
      }
      LAS char *xw = lds + xslot + lane * 16 + kro[0], *xl = lds + xslot + 6144 + 4 * n;
      LAS const char *xr1 = lds + xslot + lane * 16 + kro[1], *xr2 = lds + xslot + lane * 16 + kro[2];
      unsigned mo = wg * 8192 + (w - 1) * 2048 + n * 64 + g * 16, tw = (unsigned)(tg >> (20 * (g & 1))) & 0xfffffu, sh = 12 * (g & 1);
      asm volatile("" : "+v"(xw), "+v"(xl), "+v"(xr1), "+v"(xr2), "+v"(mo), "+v"(tw), "+v"(sh));
      waitvm<0>();
      RD_PACKET();
      GOT_PACKET();
      SB();
      tac = mfma(sf, pf, tac);
      asm volatile("" : "+v"(tac));
      SB();
#pragma unroll
      for (int t = 0; t < 2; ++t) acc[t] = mfma(t2[t], pf, mfma(ta[t], pf, acc[t]));
      SB();
#pragma unroll
      for (int t = 2; t < 4; ++t) acc[t] = mfma(t2[t], pf, mfma(ta[t], pf, acc[t]));
      const float L = tac[2] + tac[3], rl = __builtin_amdgcn_rcpf(L), ct = -(tac[0] + tac[1]) * rl;
      const float lse = M + __builtin_amdgcn_logf(L);
      u32x4 b[6];
      b[0] = pack8(acc[0] * rl + ct, acc[1] * rl + ct);
      *(LAS u32x4*)xw = b[0];
      *(LAS float*)xl = lse;
#pragma unroll
      for (int j = 0; j < 4; ++j) { __builtin_amdgcn_sched_group_barrier(0x008, 1, 0); __builtin_amdgcn_sched_group_barrier(0x002, 3, 0); }
      SB();
      b[1] = pack8(acc[2] * rl + ct, acc[3] * rl + ct);
      *(LAS u32x4*)(xw + 1024) = b[1];
      lds_inc(CT_XD, ctl);
      SB();
      f32x4 o[3];
      unsigned xd;
#pragma unroll
      for (int t = 0; t < 3; ++t) o[t] = mfma(wvv[6 * t], b[0], z);
      asm volatile("" : "+v"(o[0]), "+v"(o[1]), "+v"(o[2]));
      SB();
#define RD_SLICES "ds_read_b32 %0, %7 offset:%10\n\tds_read_b128 %1, %8\n\tds_read_b128 %2, %8 offset:1024\n\tds_read_b128 %3, %9\n\tds_read_b128 %4, %9 offset:1024\n"
      asm volatile(RD_SLICES : "=&v"(xd), "=&v"(b[2]), "=&v"(b[3]), "=&v"(b[4]), "=&v"(b[5]) : "i"(0), "i"(0), "v"(ctl), "v"(xr1), "v"(xr2), "i"(CT_XD) : "memory");
      SB();
#pragma unroll
      for (int t = 0; t < 3; ++t) o[t] = mfma(wvv[6 * t + 1], b[1], o[t]);
      tw |= (__float_as_uint(lse) << sh) & 0xfff00000u;
      asm volatile("" : "+v"(tw), "+v"(o[0]), "+v"(o[1]), "+v"(o[2]));
      SB();
      {
        int cnt_, c_;
        asm volatile("s_mov_b32 %6, 0x400000\n\ts_branch 2f\n1:\n\t" RD_SLICES "2:\n\ts_waitcnt lgkmcnt(4)\n\tv_readfirstlane_b32 %5, %0\n\t"
                     "s_cmp_ge_i32 %5, 3\n\ts_cbranch_scc1 3f\n\ts_sub_u32 %6, %6, 1\n\ts_cbranch_scc0 1b\n3:\n\ts_waitcnt lgkmcnt(0)"
                     : "+v"(xd), "+v"(b[2]), "+v"(b[3]), "+v"(b[4]), "+v"(b[5]), "=&s"(c_), "=&s"(cnt_)
                     : "v"(ctl), "v"(xr1), "v"(xr2), "i"(CT_XD) : "memory", "scc");
      }
      SB();
      PROJECT(2);
      lds_inc(CT_ST, ctl);
    }
  }
}

#undef rws
#define WS_AT(size_) (((size_) - (64u << 20)) & ~(size_t)0x1fffff)
extern "C" void kernel_launch(void* const* d_in, const int* in_sizes, int n_in, void* d_out, int out_size, void* d_ws, size_t ws_size,
                              hipStream_t stream) {
  dino_main<<<NWG, 512, 0, stream>>>((const u16*)d_in[0], (const u16*)d_in[1], (const u16*)d_in[2], (const u16*)d_in[3], (const u16*)d_in[4],
                                     (const u16*)d_in[5], (const u16*)d_in[6], (char*)d_ws + WS_AT(ws_size) + 4096, (u16*)d_out);
}
```

```cpp
#include <hip/hip_runtime.h>
#include <hip/hip_bf16.h>
#include <stdint.h>

typedef unsigned short u16;
using u32x2 = __attribute__((ext_vector_type(2))) unsigned;
using u32x4 = __attribute__((ext_vector_type(4))) unsigned;
using u32x8 = __attribute__((ext_vector_type(8))) unsigned;
using f32x2 = __attribute__((ext_vector_type(2))) float;
using f32x4 = __attribute__((ext_vector_type(4))) float;
using s16x8 = __attribute__((ext_vector_type(8))) short;
using bf16x2_t = __attribute__((ext_vector_type(2))) __bf16;
typedef short v4i16_t __attribute__((ext_vector_type(4)));
#define LAS __attribute__((address_space(3)))

constexpr int C = 192, NQ = 16, W0 = 512, W1 = 256, NWG = 256, NW = 8;
constexpr float EPS = 1e-5f;
constexpr float QSCALE = 0.07216878364870322f * 1.4426950408889634f;

constexpr int PART = 4096, CRS = 3 * PART;
constexpr int SLOT = CRS + 3072, NS = 10;
constexpr int LDS_QK = NS * SLOT;
constexpr int NPK = 7;
constexpr int LDS_PK = LDS_QK, PK_B = 1344;
constexpr int LDS_CTL = LDS_PK + NPK * PK_B;
constexpr int LDS_SINK = LDS_CTL + 384;
constexpr int LDS_BYTES = LDS_SINK + 256;
constexpr int PSCR = 8 * SLOT;
constexpr int CT_FULL = 8, CT_WV = 4, CT_PA = 0, CT_PB = 12, CT_QR = 16, CT_PD = 20, CT_T0 = 24, CT_XD = 28, CT_TG = 32, CT_QK = 40, CT_ST = 44,
              CT_FREE = 48, CT_FIN = 88, CT_ONE = 112, CT_PRDY = 128, CT_PCON = 160, CT_MAX = 256;

constexpr int MB_BYTES = NWG * NWG * 32;
constexpr int EP_OFF = MB_BYTES;
constexpr int FLAG_OFF = EP_OFF + 512;
constexpr int WVO_OFF = EP_OFF + 8192;
constexpr int QK_OFF = WVO_OFF + C * C * 2;
constexpr int QK_REC = 1280, QK_COPY = 18 * QK_REC;
constexpr int HINT_OFF = QK_OFF + 4 * QK_COPY;
constexpr int WS_BYTES = HINT_OFF + 64 * 128;

#define GIVES 0x000000003232ull
constexpr int NPR = 36, NTK = (NWG - NPR) / 2;
__host__ __device__ constexpr int gives(int wg) { return (int)((GIVES >> (wg < NPR ? 4 * (wg & 1) : 16 + 4 * (wg & 7))) & 15); }
struct Tabs { unsigned seq[112]; unsigned take[NTK]; };
constexpr Tabs make_tabs() {
  Tabs t{};
  for (int i = 0; i < 112; ++i) t.seq[i] = i + 1;
  int k = 0;
  for (int wg = 0; wg < 256; ++wg)
    for (int j = 0; j < gives(wg); ++j, ++k) t.take[k % NTK] += (unsigned)(j << 8 | wg) << (10 * (k / NTK));
  return t;
}
constexpr int given() { int k = 0; for (int wg = 0; wg < 256; ++wg) k += gives(wg); return k; }
constexpr int GIVEN = given();
static_assert(GIVEN <= NTK * 3, "too many tiles sent elsewhere");
__device__ const Tabs tabs = make_tabs();
#define QKAT 3
#define WV1 6
#define WMIN 24
#define WSTEP 6
#define LSTEP 20
#define OWNG 20
#define PACE0 30
#define PACE1 1740
#define RGATE 1932
#define T0AT 3
#define LAND0 200
#define TLBE 1
#define LANDS 0
#define WVS 12
#define HSTEP 40
#define GATE 350
#define CODE_BYTES 25856

__device__ __forceinline__ unsigned cvtpk(float lo, float hi) {
  bf16x2_t v = {(__bf16)lo, (__bf16)hi};
  return __builtin_bit_cast(unsigned, v);
}
__device__ __forceinline__ float bflo(unsigned x) { return __builtin_bit_cast(float, x << 16); }
__device__ __forceinline__ float bfhi(unsigned x) { return __builtin_bit_cast(float, x & 0xffff0000u); }
template <int CTRL> __device__ __forceinline__ float dpp(float x) {
  return __builtin_bit_cast(float, __builtin_amdgcn_mov_dpp(__builtin_bit_cast(int, x), CTRL, 0xf, 0xf, true));
}
constexpr int XOR8 = 0x128, ROR4 = 0x124;
template <bool HI8> __device__ __forceinline__ float half_copy(float x) {
  const int v = __builtin_bit_cast(int, x);
  return __builtin_bit_cast(float, __builtin_amdgcn_update_dpp(v, v, XOR8, 0xf, HI8 ? 0xc : 0x3, false));
}
__device__ __forceinline__ float row_max(float x) {
  x = fmaxf(x, dpp<XOR8>(x)); x = fmaxf(x, dpp<ROR4>(x)); x = fmaxf(x, dpp<0x122>(x));
  return fmaxf(x, dpp<0x121>(x));
}
__device__ __forceinline__ f32x2 swap32(float a, float b) {
  auto s = __builtin_amdgcn_permlane32_swap(__float_as_uint(a), __float_as_uint(b), false, false);
  return f32x2{__uint_as_float(s[0]), __uint_as_float(s[1])};
}
__device__ __forceinline__ f32x2 swap16(float a, float b) {
  auto s = __builtin_amdgcn_permlane16_swap(__float_as_uint(a), __float_as_uint(b), false, false);
  return f32x2{__uint_as_float(s[0]), __uint_as_float(s[1])};
}
__device__ __forceinline__ float xrow16_max(float x) {
  auto s = __builtin_amdgcn_permlane16_swap(__float_as_uint(x), __float_as_uint(x), false, false);
  x = fmaxf(__uint_as_float(s[0]), __uint_as_float(s[1]));
  auto t = __builtin_amdgcn_permlane32_swap(__float_as_uint(x), __float_as_uint(x), false, false);
  return fmaxf(__uint_as_float(t[0]), __uint_as_float(t[1]));
}
__device__ __forceinline__ float xrow16_sum(float x) {
  auto s = __builtin_amdgcn_permlane16_swap(__float_as_uint(x), __float_as_uint(x), false, false);
  x = __uint_as_float(s[0]) + __uint_as_float(s[1]);
  auto t = __builtin_amdgcn_permlane32_swap(__float_as_uint(x), __float_as_uint(x), false, false);
  return __uint_as_float(t[0]) + __uint_as_float(t[1]);
}
__device__ __forceinline__ f32x4 mfma(u32x4 a, u32x4 b, f32x4 c) {
  return __builtin_amdgcn_mfma_f32_16x16x32_bf16(__builtin_bit_cast(s16x8, a), __builtin_bit_cast(s16x8, b), c, 0, 0, 0);
}
__device__ __forceinline__ u32x2 vtr(LAS const char* p) {
  return __builtin_bit_cast(u32x2, __builtin_amdgcn_ds_read_tr16_b64_v4i16((LAS v4i16_t*)p));
}
__device__ __forceinline__ u32x4 vtr2(LAS const char* p, LAS const char* q_) {
  const u32x2 a = vtr(p), b = vtr(q_);
  return u32x4{a[0], a[1], b[0], b[1]};
}
__device__ __forceinline__ float sel4(f32x4 v, int r) {
  const float a = (r & 1) ? v[1] : v[0], b = (r & 1) ? v[3] : v[2];
  return (r & 2) ? b : a;
}
using RSRC = __amdgpu_buffer_rsrc_t;
__device__ __forceinline__ RSRC mkrs(const void* p, int bytes) { return __builtin_amdgcn_make_buffer_rsrc(const_cast<void*>(p), (short)0, bytes, 0x00020000); }
__device__ __forceinline__ u32x4 mkrs4(const void* p, unsigned bytes) {
  const unsigned long long b = (unsigned long long)p;
  return u32x4{(unsigned)b, (unsigned)(b >> 32) & 0xffffu, bytes, 0x00020000u};
}
template <int AUX> __device__ __forceinline__ u32x4 ldb128(RSRC r, unsigned vo, int so) { return __builtin_bit_cast(u32x4, __builtin_amdgcn_raw_buffer_load_b128(r, vo, so, AUX)); }
#define SB() __builtin_amdgcn_sched_barrier(0)
#define WAVE_SYNC() do { __builtin_amdgcn_fence(__ATOMIC_RELEASE, "wavefront"); __builtin_amdgcn_wave_barrier(); __builtin_amdgcn_fence(__ATOMIC_ACQUIRE, "wavefront"); } while (0)
#define RAW_BARRIER() asm volatile("s_waitcnt lgkmcnt(0)\n\ts_barrier" ::: "memory")
#define DMA(m0_, vo_, rs_, so_) asm volatile("s_mov_b32 m0, %0\n\ts_nop 0\n\tbuffer_load_dwordx4 %1, %2, %3 offen lds" :: "s"(m0_), "v"(vo_), "s"(rs_), "s"(so_) : "memory")
template <int N> __device__ __forceinline__ void waitlds() { __builtin_amdgcn_s_waitcnt(0xc07f | (N << 8)); }
template <int N> __device__ __forceinline__ void waitvm() { asm volatile("s_waitcnt vmcnt(%0)" :: "i"(N) : "memory"); }


__device__ __forceinline__ void lds_add(LAS char* p, int lane, unsigned v) {
  asm volatile("" ::: "memory");
  if (lane == 0) __hip_atomic_fetch_add((LAS unsigned*)p, v, __ATOMIC_RELAXED, __HIP_MEMORY_SCOPE_WORKGROUP);
  asm volatile("" ::: "memory");
}
__device__ __forceinline__ void lds_set(LAS char* p, int lane, unsigned v) {
  asm volatile("" ::: "memory");
  if (lane == 0) __hip_atomic_store((LAS unsigned*)p, v, __ATOMIC_RELAXED, __HIP_MEMORY_SCOPE_WORKGROUP);
  asm volatile("" ::: "memory");
}
__device__ __forceinline__ void lds_inc(int O, LAS char* p) {
  asm volatile("s_mov_b64 exec, 1\n\tds_add_u32 %0, %1 offset:%2\n\ts_mov_b64 exec, -1" :: "v"(p), "v"(1u), "i"(O) : "memory");
}
__device__ __forceinline__ void lds_put(int O, LAS char* p, unsigned v) {
  asm volatile("s_mov_b64 exec, 1\n\tds_write_b32 %0, %1 offset:%2\n\ts_mov_b64 exec, -1" :: "v"(p), "v"(v), "i"(O) : "memory");
}
__device__ __forceinline__ int lds_get(LAS char* p) {
  asm volatile("" ::: "memory");
  const unsigned v = __hip_atomic_load((LAS unsigned*)p, __ATOMIC_RELAXED, __HIP_MEMORY_SCOPE_WORKGROUP);
  asm volatile("" ::: "memory");
  return __builtin_amdgcn_readfirstlane(v);
}
__device__ __forceinline__ void lds_wait(LAS char* p, int target) {
#pragma unroll 1
  for (int spin = 0; spin < (1 << 22); ++spin) {
    if (lds_get(p) >= target) break;
    __builtin_amdgcn_s_sleep(1);
  }
}
__device__ __forceinline__ int lds_spin(int O, LAS char* p, int target) {
  int r, cnt = 1 << 22;
  unsigned t;
  asm volatile("1:\n\tds_read_b32 %1, %3 offset:%5\n\ts_waitcnt lgkmcnt(0)\n\tv_readfirstlane_b32 %0, %1\n\ts_cmp_ge_i32 %0, %4\n\ts_cbranch_scc1 2f\n\t"
               "s_sub_u32 %2, %2, 1\n\ts_cbranch_scc0 1b\n2:"
               : "=&s"(r), "=&v"(t), "+s"(cnt) : "v"(p), "s"(target), "i"(O) : "memory", "scc");
  return r;
}
__device__ __forceinline__ void lds_need(int O, int& have, LAS char* p, int target) {
  int cnt;
  unsigned t;
  asm volatile("s_cmp_ge_i32 %0, %4\n\ts_cbranch_scc1 2f\n\ts_mov_b32 %2, 0x400000\n"
               "1:\n\tds_read_b32 %1, %3 offset:%5\n\ts_waitcnt lgkmcnt(0)\n\tv_readfirstlane_b32 %0, %1\n\ts_cmp_ge_i32 %0, %4\n\ts_cbranch_scc1 2f\n\t"
               "s_sub_u32 %2, %2, 1\n\ts_cbranch_scc0 1b\n2:"
               : "+s"(have), "=&v"(t), "=&s"(cnt) : "v"(p), "s"(target), "i"(O) : "memory", "scc");
  asm volatile("" : "+s"(have));
}
__device__ __forceinline__ u32x4 pack8(f32x4 a, f32x4 b) { return u32x4{cvtpk(a[0], a[1]), cvtpk(a[2], a[3]), cvtpk(b[0], b[1]), cvtpk(b[2], b[3])}; }

__device__ __forceinline__ void stb128(u32x4 v, RSRC r, unsigned o) { __builtin_amdgcn_raw_buffer_store_b128(v, r, o, 0, 16); }
__device__ __forceinline__ void stb64(u32x2 v, RSRC r, unsigned o) { __builtin_amdgcn_raw_buffer_store_b64(v, r, o, 0, 16); }
__device__ __forceinline__ void stb32(unsigned v, RSRC r, unsigned o) { __builtin_amdgcn_raw_buffer_store_b32(v, r, o, 0, 16); }
__device__ __forceinline__ unsigned qk_check(u32x4 x, unsigned tg) {
  const unsigned a = (x[0] ^ __builtin_amdgcn_alignbit(x[1], x[1], 11)) * 0x9e3779b1u + tg;
  const unsigned b = (x[2] + __builtin_amdgcn_alignbit(x[3], x[3], 19)) * 0x85ebca77u;
  return __builtin_amdgcn_alignbit(a, a, 15) ^ b ^ (x[1] + x[3]);
}
__device__ __forceinline__ unsigned add3(unsigned a, unsigned b, unsigned c) {
  return cvtpk((bflo(a) + bflo(b)) + bflo(c), (bfhi(a) + bfhi(b)) + bfhi(c));
}
__global__ __launch_bounds__(512, 2) void dino_main(const u16* q, const u16* f0, const u16* f1, const u16* wq, const u16* wk, const u16* wv,
                                                     const u16* wo, char* ws, u16* out) {
  __shared__ __attribute__((aligned(128))) char smem[LDS_BYTES];
  LAS char* lds = (LAS char*)smem;
  LAS char* ctl = lds + LDS_CTL;
  const unsigned lb = (unsigned)(unsigned long long)lds;
  const int w = __builtin_amdgcn_readfirstlane(threadIdx.x >> 6);
  unsigned long long pc0;
  asm volatile("s_getpc_b64 %0" : "=s"(pc0));
#define COORDS() \
  int tid = threadIdx.x, wg = blockIdx.x; \
  asm volatile("" : "+v"(tid), "+s"(wg)); \
  __builtin_assume(tid >= 0 && tid < 512); __builtin_assume(wg >= 0 && wg < NWG); \
  const int lane = tid & 63, n = lane & 15, g = lane >> 4;       \
    \
  const int dq = (wg & 7) * 2 + ((wg >> 4) & 1), dst = (wg & ~31) + (wg & 7) * 4 + ((wg >> 3) & 3); \
  const int dj = 6 * ((wg >> 5) & 1) + min(lane & 7, 5); \
  const int dout = dq * C + 48 * (wg >> 6) + 16 * (dj >> 2) + (wg & 8) + ((lane & 8) >> 1) + (dj & 3); \
  const int NT = 32 - gives(wg) + (wg < NPR || (wg & 1) ? 0 : GIVEN / NTK + (((wg - NPR) >> 1) < GIVEN % NTK ? 1 : 0)); \
  const int xslot = (NT >= 30 ? NT - 30 : NT - 20) * SLOT;       \
  (void)xslot; (void)n; (void)g; (void)dst; (void)dout; (void)NT
#define ARGS_IN() asm volatile("s_waitcnt lgkmcnt(0)" :: "s"(q), "s"(f0), "s"(f1), "s"(wq), "s"(wk), "s"(wv), "s"(wo), "s"(ws), "s"(out) : "memory")
#define rws mkrs(ws, WS_BYTES)
#define PROJECT(k0_) do { \
      _Pragma("unroll") for (int k = k0_; k < 6; ++k) { o[0] = mfma(wvv[k], b[k], o[0]); o[1] = mfma(wvv[6 + k], b[k], o[1]); } \
      asm volatile("" : "+v"(o[0]), "+v"(o[1])); \
      SB(); \
      _Pragma("unroll") for (int k = k0_; k < 6; ++k) o[2] = mfma(wvv[12 + k], b[k], o[2]); \
      asm volatile("" : "+v"(o[2])); \
      SB(); \
      stb128(u32x4{cvtpk(o[0][0], o[0][1]), cvtpk(o[0][2], o[0][3]), cvtpk(o[1][0], o[1][1]), tw}, rws, mo); \
      SB(); \
      stb128(u32x4{cvtpk(o[1][2], o[1][3]), cvtpk(o[2][0], o[2][1]), cvtpk(o[2][2], o[2][3]), tw}, rws, mo + 1024); \
    } while (0)
  unsigned long long tg;
#define GET_TAG() tg = ((unsigned long long)((unsigned)lds_spin(CT_TG + 4, ctl, 1) & 0xffu) << 32) | (unsigned)lds_get(ctl + CT_TG)
  const unsigned long long tstart = wall_clock64();
  if (w != 0) {
    const unsigned l0 = threadIdx.x & 63;
    if (blockIdx.x < NPR) {
      const unsigned p6 = (blockIdx.x * 43) >> 8, pu = blockIdx.x - 6 * p6;
      unsigned vo = ((l0 * 16) ^ (((l0 * 4) & 0x40) | (l0 & 0x20))) + (l0 >> 3) * 256;
      const bool pv = p6 >= 3, wa = w >= 4, isq = !pv && (w & 6) == 2;
      const unsigned a6 = 6 * w - 24, pj = pv ? p6 - 3 : p6;
      unsigned so = (wa ? a6 : isq ? w - 2 : 4 * pu) * 3072 + (isq ? 0 : pv && !wa ? w - 1 : pj) * 128, ga = isq ? 128 : 3072;
      unsigned lo = lb + PSCR + (wa ? a6 : pv ? 4 * w + 20 : (0x1b181e00u >> (8 * w)) & 255u) * 1024, hi = lo + (wa ? 6144 : isq ? 3072 : 4096);
      asm volatile("" : "+v"(vo), "+s"(lo), "+s"(so), "+s"(hi), "+s"(ga));
      ARGS_IN();
      const u32x4 rw = mkrs4(wa ? (pv ? wo : wq) : pv ? wv : isq ? q : wk, isq ? NQ * C * 2 : C * C * 2);
#pragma unroll 1
      do { DMA(lo, vo, rw, so); lo += 1024; so += ga; } while (lo != hi);
    } else if (w == 7 && blockIdx.x >= 56 && blockIdx.x < 72) {
      const u32x4 rc = mkrs4((const void*)(pc0 & ~255ull), CODE_BYTES);
#pragma unroll 1
      for (int i = 0; i < CODE_BYTES; i += 4096)
        asm volatile("s_mov_b32 m0, %0\n\ts_nop 0\n\tbuffer_load_dword %1, %2, %3 offen lds" :: "s"(lb + LDS_SINK), "v"(l0 * 64), "s"(rc), "s"(i) : "memory");
    }
    if (w == 3) {
      unsigned long long ep;
      asm volatile("s_waitcnt lgkmcnt(0)\n\ts_load_dwordx2 %0, %1, %2\n\ts_barrier\n\ts_waitcnt lgkmcnt(0)" : "=&s"(ep) : "s"(ws), "s"(EP_OFF) : "memory");
      *(LAS unsigned long long*)(ctl + CT_TG) = ((ep + 0x100001ull) & 0xffffffffffull) | 0x10000000000ull;
    } else RAW_BARRIER();
  }
  SB();
  if (__builtin_expect(w == 0, 1)) {
    const unsigned l0 = threadIdx.x & 63, g0 = blockIdx.x;
    unsigned y0 = (l0 * 16) ^ (l0 >= 48 ? 48 : 0), y1 = (l0 * 16) ^ (l0 >= 32 ? 64 : 48), y2 = (l0 * 16) ^ (l0 >= 16 ? 112 : 64);
    unsigned v0 = ((l0 * 16) ^ ((l0 * 2) & 0x60)) + (l0 >> 3) * 640 + 1024, v1 = (v0 ^ 16) - 640;
    unsigned v2 = v0 + W0 * C * 2 - 2048, v3 = v1 + W0 * C * 2 - 2048;
    unsigned b0 = (g0 & 7) * (2 * W0 * C * 2) + (g0 >> 3) * 6144, b1 = (g0 & 7) * (W1 * C * 2) + (g0 >> 3) * 3072;
    {
      const unsigned iv = (l0 & 15) >= CT_ONE / 8 ? 0x3f803f80u : (l0 >> 3) == CT_MAX / 64 ? 0xff800000u : 0u;
      *(LAS u32x2*)(ctl + 8 * min(l0, 47u)) = u32x2{l0 >= 47 ? 0x3f803f80u : iv, l0 >= 47 ? 0x3f803f80u : iv};
    }
    constexpr unsigned st0 = 16 * W0 * C * 2, st1 = 8 * W1 * C * 2;
    unsigned sb = lb, fo = CT_FREE, fv = 0u, tk = 0u;
    int lap = 0;
    u32x4 rt = mkrs4(tabs.seq, 112 * 4);
    unsigned vz = 0u;
    asm volatile("" : "+v"(v0), "+v"(v1), "+v"(v2), "+v"(v3), "+v"(y0), "+v"(y1), "+v"(y2), "+v"(vz), "+s"(b0), "+s"(b1), "+s"(rt));
    ARGS_IN();
    const u32x4 r0 = mkrs4((const char*)f0 - 1024, W0 * W0 * C * 2 + 1024), r1 = mkrs4(f1, W1 * W1 * C * 2);
    if (g0 < NPR) __builtin_amdgcn_s_barrier();
#define UNIT(p_) do { unsigned so_; \
      asm volatile("s_add_u32 m0, %1, %10\n\ts_add_u32 %0, %2, %11\n\t" \
                   "buffer_load_dwordx4 %3, %7, %0 offen nt lds\n\tbuffer_load_dwordx4 %4, %7, %0 offen offset:1024 nt lds\n\t" \
                   "buffer_load_dwordx4 %5, %7, %0 offen offset:2048 nt lds\n\tbuffer_load_dwordx4 %6, %7, %0 offen offset:3072 nt lds\n\t" \
                   "s_mov_b32 m0, %12\n\ts_mov_b64 exec, 1\n\tbuffer_load_dword %13, %8, %9 offen offset:%14 lds\n\ts_mov_b64 exec, -1" \
                   : "=&s"(so_) : "s"(sb), "s"(b0), "v"(v0), "v"(v1), "v"(v2), "v"(v3), "s"(r0), "s"(rt), "s"(12 * s), "i"((p_) * PART), "i"((p_) * 128), \
                     "s"(lb + LDS_CTL + CT_FULL - 4 * (p_)), "v"(vz), "i"(4 * (p_)) : "memory", "scc"); } while (0)
#define WINDOW() \
      asm volatile("s_waitcnt vmcnt(%1)\n\ts_cmp_ge_i32 %0, %6\n\ts_cbranch_scc1 1f\n\ts_waitcnt vmcnt(%2)\n\ts_cmp_ge_i32 %0, %7\n\ts_cbranch_scc1 1f\n\t" \
                   "s_waitcnt vmcnt(%3)\n\ts_cmp_ge_i32 %0, %8\n\ts_cbranch_scc1 1f\n\ts_waitcnt vmcnt(%4)\n\ts_cmp_ge_i32 %0, %9\n\ts_cbranch_scc1 1f\n\t" \
                   "s_waitcnt vmcnt(%5)\n1:" :: "s"(late), "i"(WMIN + 4 * WSTEP), "i"(WMIN + 3 * WSTEP), "i"(WMIN + 2 * WSTEP), "i"(WMIN + WSTEP), "i"(WMIN), \
                   "i"(4 * LSTEP * 256), "i"(3 * LSTEP * 256), "i"(2 * LSTEP * 256), "i"(LSTEP * 256) : "memory", "scc")
#define COARSE(sb_) \
      asm volatile("s_add_u32 m0, %0, %6\n\ts_nop 0\n\tbuffer_load_dwordx4 %1, %4, %5 offen nt lds\n\t" \
                   "buffer_load_dwordx4 %2, %4, %5 offen offset:1024 nt lds\n\tbuffer_load_dwordx4 %3, %4, %5 offen offset:2048 nt lds" \
                   :: "s"(sb_), "v"(y0), "v"(y1), "v"(y2), "s"(r1), "s"(b1), "i"(CRS) : "memory", "scc")
    COARSE(sb);
#pragma unroll 1
    for (int s = 0; s < 2; ++s) {
      b1 += st1;
      UNIT(0); UNIT(1); COARSE(sb + SLOT); UNIT(2);
      sb += SLOT; b0 += st0;
    }
    if (g0 >= NPR) __builtin_amdgcn_s_barrier();
    __builtin_amdgcn_s_setprio(3);
    COORDS();
    fo += 12;
    const int tau = __builtin_amdgcn_readfirstlane((int)((PACE1 - PACE0) * 256.f * __builtin_amdgcn_rcpf((float)NT)));
    unsigned due = PACE0 * 256u + 2 * tau, t0v = 0u;
    unsigned long long now = tstart;
#pragma unroll 1
    for (int s = 2; s < NT; ++s) {
      asm volatile("" : "+s"(now));
      const unsigned t0s = __builtin_amdgcn_readfirstlane(t0v), t0 = t0s ? t0s : (unsigned)tstart;
      if (PACE1 && (int)(((unsigned)now - t0) * 256u - due) < 0) {
#pragma unroll 1
        for (int spin = 0; spin < 256; ++spin) {
          if ((int)(((unsigned)wall_clock64() - t0) * 256u - due) >= 0) break;
          __builtin_amdgcn_s_sleep(1);
        }
      }
      fv = *(volatile LAS unsigned*)(ctl + (s == 7 ? CT_PD : fo));
      now = wall_clock64();
      UNIT(0);
      asm volatile("" : "+s"(now));
      const int late = (int)(((unsigned)now - t0) * 256u - due);
      due += tau;
      WINDOW();
      UNIT(1);
      WINDOW();
      const unsigned nsb = fo == CT_FREE ? lb : sb + SLOT;
      unsigned n0 = b0 + st0;
      b1 += st1;
      if (s >= 31) {
        const unsigned xw = tk & 255u, xs = 31 - ((tk >> 8) & 3u);
        n0 = (xw & 7) * (2 * W0 * C * 2) + (xw >> 3) * 6144 + xs * st0; b1 = (xw & 7) * (W1 * C * 2) + (xw >> 3) * 3072 + xs * st1;
        tk >>= 10;
      }
      if (s + 1 < NT) {
        const int need = s == 7 && wg < NPR ? 4 : lap;
        if (__builtin_expect((int)__builtin_amdgcn_readfirstlane(fv) < need, 0)) {
          lds_wait(ctl + (s == 7 ? CT_PD : fo), need);
        }
        COARSE(nsb);
      }
      if (s == 2 && NT > 32) tk = tabs.take[(wg - NPR) >> 1];
      now = wall_clock64();
      t0v = *(volatile LAS unsigned*)(ctl + CT_T0);
      UNIT(2);
      WINDOW();
      if (fo == CT_FREE + 4 * (NS - 1)) { fo = CT_FREE; lap += 4; } else fo += 4;
      sb = nsb; b0 = n0;
    }
    __builtin_amdgcn_s_setprio(0);
    u32x4 wvv[18], b[6];
    if (TLBE) {
      const u32x4 ra = mkrs4(ws - 4096, 4096), rb = mkrs4(out, NQ * C * 2);
      asm volatile("s_mov_b32 m0, %0\n\ts_mov_b64 exec, 1\n\tbuffer_load_dword %1, %2, 0 offen lds\n\tbuffer_load_dword %1, %3, 0 offen lds\n\ts_mov_b64 exec, -1"
                   :: "s"(lb + LDS_SINK), "v"(vz), "s"(ra), "s"(rb) : "memory");
    }
    lds_wait(ctl + CT_WV, 1);
#pragma unroll
    for (int j = 0; j < 18; ++j) wvv[j] = ldb128<16>(rws, lane * 16 + (j & 3) * 1024, WVO_OFF + 54 * 1024 + (j >> 2) * 4096);
    GET_TAG();
    LAS const char *xr = lds + xslot + lane * 16, *xl = lds + xslot + 6144 + 4 * n;
    unsigned mo = wg * 8192 + 6144 + n * 64 + g * 16, tw = (unsigned)(tg >> (20 * (g & 1))) & 0xfffffu, sh = 12 * (g & 1), lse;
    asm volatile("" : "+v"(xr), "+v"(xl), "+v"(mo), "+v"(tw), "+v"(sh));
    __builtin_amdgcn_s_waitcnt(0x0f70);
    lds_spin(CT_XD, ctl, 1);
    {
      int cnt_, c_;
      unsigned t_;
      asm volatile("s_mov_b32 %1, 0x400000\n1:\n\tds_read_b32 %2, %10 offset:%13\n\tds_read_b128 %3, %11\n\tds_read_b128 %4, %11 offset:1024\n\t"
                   "ds_read_b128 %5, %11 offset:2048\n\tds_read_b128 %6, %11 offset:3072\n\tds_read_b128 %7, %11 offset:4096\n\t"
                   "ds_read_b128 %8, %11 offset:5120\n\tds_read_b32 %9, %12\n\ts_waitcnt lgkmcnt(7)\n\tv_readfirstlane_b32 %0, %2\n\t"
                   "s_cmp_ge_i32 %0, 3\n\ts_cbranch_scc1 2f\n\ts_sub_u32 %1, %1, 1\n\ts_cbranch_scc0 1b\n2:\n\ts_waitcnt lgkmcnt(0)"
                   : "=&s"(c_), "=&s"(cnt_), "=&v"(t_), "=&v"(b[0]), "=&v"(b[1]), "=&v"(b[2]), "=&v"(b[3]), "=&v"(b[4]), "=&v"(b[5]), "=&v"(lse)
                   : "v"(ctl), "v"(xr), "v"(xl), "i"(CT_XD) : "memory", "scc");
    }
    SB();
    f32x4 o[3] = {{0.f, 0.f, 0.f, 0.f}, {0.f, 0.f, 0.f, 0.f}, {0.f, 0.f, 0.f, 0.f}};
    tw |= (lse << sh) & 0xfff00000u;
    PROJECT(0);
    lds_inc(CT_ST, ctl);
  } else {
    COORDS();
    const int tq = (lane >> 2) & 3, tp = lane & 3;
    const bool pq = wg < 18;
    const bool pv = wg >= 18 && wg < NPR;
    const int pj = wg / 6, pu = wg % 6;
    if (w >= 4 && ((w + NT) & 3) == 0 && !pq) {
      unsigned k0, k1, k2;
      asm volatile("s_load_dword %0, %3, %5\n\ts_load_dword %1, %3, %6\n\ts_load_dword %2, %4, %7\n\ts_waitcnt lgkmcnt(0)"
                   : "=&s"(k0), "=&s"(k1), "=&s"(k2) : "s"(f0), "s"(f1), "s"((wg >> 3) * 3145728 + 1572860), "s"((wg >> 3) * 3145728 + 3145724),
                     "s"((wg >> 3) * 786432 + 786428) : "memory");
    }
    const u32x4 one = {0x3f803f80u, 0x3f803f80u, 0x3f803f80u, 0x3f803f80u};
    const int fq = ((n & 2) << 1) | ((n & 4) >> 1);
    LAS const char* rq0 = lds + PSCR + 24576 + 3072 * (n >> 3) + 128 * (n & 7) + 16 * (g ^ fq);
    LAS const char* rq1 = lds + PSCR + 24576 + 3072 * (n >> 3) + 128 * (n & 7) + 16 * ((4 + g) ^ fq);
    if (pq && w >= 2) {
      {
        waitvm<0>();
        lds_inc(CT_PA, ctl);
        if (w >= 4) {
          f32x4 sq = {0.f, 0.f, 0.f, 0.f}, rr = sq, cs = sq;
          const int ro = PSCR + 128 * (8 * g + tq) + 16 * (((w & 2) * 2 + tp) ^ ((tq & 2) << 1)) + 8 * (w & 1);
          u32x4 qv[6], af[6];
          lds_spin(CT_PA, ctl, 6);
#pragma unroll
          for (int s = 0; s < 6; ++s) {
            qv[s] = *(LAS const u32x4*)(((s & 1) ? rq1 : rq0) + (s >> 1) * 1024);
            af[s] = vtr2(lds + ro + s * 4096, lds + (ro ^ 32) + s * 4096 + 512);
          }
          SB();
#pragma unroll
          for (int s = 0; s < 6; ++s) { sq = mfma(one, qv[s], sq); rr = mfma(af[s], qv[s], rr); cs = mfma(af[s], one, cs); }
          const f32x4 qq = rr - (sq[0] * (1.f / C)) * cs;
          *(LAS u32x2*)(lds + LDS_QK + 4096 + ((w >> 1) & 1) * 1024 + lane * 16 + 8 * (w & 1)) = u32x2{cvtpk(qq[0], qq[1]), cvtpk(qq[2], qq[3])};
          lds_inc(CT_PB, ctl);
        }
      }
    }
    if (pq && w == 1) {
      {
        waitvm<0>();
        u32x4 kf4[4], qv[6];
        const int ko = LDS_QK + 128 * (((n & 4) << 2) | (n & 11)) + 16 * (g ^ ((n & 2) << 1));
#pragma unroll
        for (int i = 0; i < 4; ++i) kf4[i] = *(LAS const u32x4*)(lds + (ko ^ ((i & 1) * 64 + (i >> 1) * 32)) + (i >> 1) * 512);
        lds_spin(CT_PA, ctl, 6);
#pragma unroll
        for (int s = 0; s < 6; ++s) qv[s] = *(LAS const u32x4*)(((s & 1) ? rq1 : rq0) + (s >> 1) * 1024);
        const f32x4 z = {0.f, 0.f, 0.f, 0.f};
        f32x4 gq = z, sq = z;
#pragma unroll
        for (int s = 0; s < 6; ++s) { gq = mfma(qv[s], qv[s], gq); sq = mfma(one, qv[s], sq); }
        const float mean = sq[0] * (1.f / C);
        const float s2 = xrow16_sum(g == (n >> 2) ? sel4(gq, n & 3) : 0.f);
        float sc = __builtin_amdgcn_rsqf(fmaxf(fmaf(-mean, mean, s2 * (1.f / C)), 0.f) + EPS) * QSCALE;
        asm volatile("" : "+v"(sc));
        GET_TAG();
        lds_spin(CT_PB, ctl, 4);
        const u32x4 q0 = *(LAS const u32x4*)(lds + LDS_QK + 4096 + lane * 16), q1 = *(LAS const u32x4*)(lds + LDS_QK + 5120 + lane * 16);
        const f32x4 d0 = mfma(kf4[1], q1, mfma(kf4[0], q0, z)), d1 = mfma(kf4[3], q1, mfma(kf4[2], q0, z));
        const u32x4 xq = pack8(d0 * sc, d1 * sc);
        const unsigned ck = qk_check(xq, (unsigned)tg);
        asm volatile("" :: "v"(ck));
        {
          const unsigned so = QK_OFF + wg * QK_REC;
#pragma unroll
          for (int cp = 0; cp < 4; ++cp) {
            stb128(xq, rws, lane * 16 + so + cp * QK_COPY);
            stb32(ck, rws, lane * 4 + so + 1024 + cp * QK_COPY);
          }
          stb32((unsigned)tg, rws, HINT_OFF + lane * 128 + (pu >> 1) * 32 + pj * 8 + (pu & 1) * 4);
          lds_put(CT_PD, ctl, 4);
        }
      }
    }
    if (pv) {
      waitvm<0>();
      lds_inc(CT_PA, ctl);
      if (w >= 4) {
        const int ro = PSCR + 128 * (8 * g + tq) + 16 * ((2 * (w - 4) + (tp >> 1)) ^ ((tq & 2) << 1)) + 8 * (tp & 1);
        const int rv = PSCR + 24576 + 128 * n + 16 * (g ^ fq);
        f32x4 d0 = {0.f, 0.f, 0.f, 0.f}, d1 = d0;
        lds_spin(CT_PA, ctl, 7);
        const unsigned org = (unsigned)tstart + (max((int)(wall_clock64() - tstart) - LAND0, 0) >> LANDS);
#pragma unroll
        for (int s = 0; s < 6; ++s) {
          const u32x4 p = vtr2(lds + ro + s * 4096, lds + (ro ^ 32) + s * 4096 + 512);
          LAS const char* pd = lds + (rv ^ ((s & 1) * 64)) + (s >> 1) * 4096;
          d0 = mfma(*(LAS const u32x4*)pd, p, d0); d1 = mfma(*(LAS const u32x4*)(pd + 2048), p, d1);
        }
        const int fi = (pj - 3) * 24 + (w - 4) * 6 + pu;
        stb128(pack8(d0, d1), rws, lane * 16 + WVO_OFF + fi * 1024);
        lds_inc(CT_PD, ctl);
        GET_TAG();
        waitvm<0>();
        if (lane < 8) stb64(u32x2{(unsigned)tg, (unsigned)(tg >> 32) | org << 8}, rws, FLAG_OFF + lane * 768 + fi * 8);
      }
    }
    const f32x4 z = {0.f, 0.f, 0.f, 0.f};
    int full = 0;
#define NEED(u_) lds_need(CT_FULL, full, ctl, (u_))
    if (w >= 4) {
      const int brg = ((g & 1) << 1) | (g >> 1);
      const int fn = (((n >> 1) & 3) << 1) | ((n >> 3) & 1);
      const int fy = ((n >> 2) & 1) * 4 + ((n >> 1) & 1) * 3;
      const int orr0 = 128 * n + 16 * (brg ^ fn), orr1 = 128 * n + 16 * ((4 + brg) ^ fn);
      const int ory0 = n < 8 ? CRS + 384 * n + 16 * (brg ^ fy) : LDS_CTL + CT_ONE, ory1 = n < 8 ? CRS + 384 * n + 16 * ((4 + brg) ^ fy) : LDS_CTL + CT_ONE;
      const int yon = n < 8 ? 1 : 0;
      const int opk = LDS_PK + 320 * g + 8 * n, ork = LDS_PK + 320 * (2 * (g >> 1) + (n >> 3)) + 256 + 8 * (n & 7) + 4 * (g & 1);
      u32x4 qk[6];
      f32x4 ref, qs;
      LAS const char *px0, *px1, *py0, *py1;
#define AT_SLOT(o_) do { px0 = lds + orr0 + (o_); px1 = lds + orr1 + (o_); py0 = lds + ory0 + __mul24(yon, (o_)); py1 = lds + ory1 + __mul24(yon, (o_)); \
      asm volatile("" : "+v"(px0), "+v"(px1), "+v"(py0), "+v"(py1)); } while (0)
#define RD_K(k_, i_) do { LAS const char* p_ = (((k_) & 1) ? px1 : px0) + ((k_) >> 1) * PART; \
    yv[i_] = *(LAS const u32x4*)((((k_) & 1) ? py1 : py0) + ((k_) >> 1) * 128); \
    xa[i_] = *(LAS const u32x4*)(p_); xb[i_] = *(LAS const u32x4*)(p_ + 2048); } while (0)
#define MF_STAT(i_) do { cy = mfma(yv[i_], yv[i_], cy); ca = mfma(yv[i_], xa[i_], ca); cb = mfma(yv[i_], xb[i_], cb); \
    ga = mfma(xa[i_], xa[i_], ga); gb = mfma(xb[i_], xb[i_], gb); } while (0)
#define MF_S(k_, i_) do { sy = mfma(qk[k_], yv[i_], sy); sa = mfma(qk[k_], xa[i_], sa); sb = mfma(qk[k_], xb[i_], sb); } while (0)
#define STATS(st, rec) do { \
      const int r = n & 3; \
      const float yy = half_copy<true>(sel4(cy, r)); \
      const f32x2 Y = swap32(yy, yy), X = swap32(sel4(ca, r), sel4(cb, r)), G = swap32(sel4(ga, r), sel4(gb, r)); \
      const float q1_ = fmaf(2.f, X[0], Y[0]) + (n < 8 ? G[0] : G[1]);       \
      const f32x2 Q = swap16(q1_, q1_); \
      const float mn_ = (X[1] + Y[1]) * (1.f / C); \
      const float var = fmaxf(fmaf(-mn_, mn_, fmaf((n & 4) ? Q[1] : Q[0], 1.f / C, EPS)), EPS), rs_ = __builtin_amdgcn_rsqf(var); \
      const f32x2 R = swap32(rs_, rs_), Mn = swap32(mn_, mn_); \
      st = f32x4{R[0], Mn[0], R[1], Mn[1]}; \
      const float v_ = (g & 1) ? var * rs_ : mn_, h_ = bfhi(__float_as_uint(v_)); \
      rec = cvtpk(h_, v_ - h_); \
    } while (0)
#define LOGITS(sv, st, base_) do { \
      _Pragma("unroll") for (int r = 0; r < 4; ++r) { \
        const float s0 = half_copy<true>(sy[r]); \
        sv[r] = fmaf(fmaf(-st[1], qs[r], sa[r] + s0), st[0], -(base_)[r]); \
        sv[4 + r] = fmaf(fmaf(-st[3], qs[r], sb[r] + s0), st[2], -(base_)[r]); \
      } \
    } while (0)
#define SV_MAX(sv) fmaxf(fmaxf(fmaxf(sv[0], sv[1]), fmaxf(sv[2], sv[3])), fmaxf(fmaxf(sv[4], sv[5]), fmaxf(sv[6], sv[7])))
#define PUBLISH(sv, st, rec, pk_, word_) do { \
      *(LAS unsigned*)(lds + ork + (pk_) * PK_B) = rec; \
      *(LAS u32x2*)(lds + opk + (pk_) * PK_B) = u32x2{cvtpk(sv[0] * st[0], sv[1] * st[0]), cvtpk(sv[2] * st[0], sv[3] * st[0])}; \
      *(LAS u32x2*)(lds + opk + (pk_) * PK_B + 128) = u32x2{cvtpk(sv[4] * st[2], sv[5] * st[2]), cvtpk(sv[6] * st[2], sv[7] * st[2])}; \
      lds_put(CT_PRDY, ctl + 4 * (pk_), (word_)); \
    } while (0)
      int s = (w + NT - 1) & 3, slot = s + 4, pk = s + 4, ncon = 0;
      {
        u32x4 yv[12], xa[12], xb[12];
        f32x4 st0, st1;
        unsigned rec0, rec1;
#pragma unroll
        for (int h = 0; h < 2; ++h) {
          f32x4 cy = z, ga = z, ca = z, gb = z, cb = z;
          AT_SLOT((s + 4 * h) * SLOT);
          lds_wait(ctl + CT_FULL, 3 * (s + 4 * h) + 3);
#pragma unroll
          for (int k = 0; k < 6; ++k) RD_K(k, 6 * h + k);
          lds_inc(CT_FREE + 16 * h, ctl + 4 * s);
          SB();
#pragma unroll
          for (int k = 0; k < 6; ++k) MF_STAT(6 * h + k);
          SB();
          if (h == 0) { STATS(st0, rec0); asm volatile("" : "+v"(st0), "+v"(rec0)); } else { STATS(st1, rec1); asm volatile("" : "+v"(st1), "+v"(rec1)); }
          SB();
        }
        lds_wait(ctl + CT_QK, 3);
#pragma unroll
        for (int k = 0; k < 6; ++k) qk[k] = *(LAS const u32x4*)(lds + LDS_QK + k * 1024 + lane * 16);
        float s0v[8], s1v[8];
        {
          f32x4 sy = z, sa = z, sb = z;
#pragma unroll
          for (int k = 0; k < 6; ++k) MF_S(k, k);
#pragma unroll
          for (int r = 0; r < 4; ++r) qs[r] = half_copy<false>(sy[r]);
          LOGITS(s0v, st0, z);
        }
        {
          f32x4 sy = z, sa = z, sb = z;
#pragma unroll
          for (int k = 0; k < 6; ++k) MF_S(k, 6 + k);
          LOGITS(s1v, st1, z);
        }
#pragma unroll
        for (int r = 0; r < 4; ++r) ref[r] = row_max(fmaxf(fmaxf(s0v[r], s0v[4 + r]), fmaxf(s1v[r], s1v[4 + r])));
        if (n < 4) __builtin_amdgcn_ds_fmaxf((LAS float*)(ctl + CT_MAX + 16 * g + 4 * n), sel4(ref, n), __ATOMIC_RELAXED, __HIP_MEMORY_SCOPE_WORKGROUP, false);
        lds_inc(CT_QR, ctl);
        lds_spin(CT_QR, ctl, 4);
        ref = *(LAS const f32x4*)(ctl + CT_MAX + 16 * g);
#pragma unroll
        for (int j = 0; j < 8; ++j) s0v[j] = __builtin_amdgcn_exp2f(s0v[j] - ref[j & 3]);
        PUBLISH(s0v, st0, rec0, s, 2 * s + 2);
#pragma unroll
        for (int j = 0; j < 8; ++j) s1v[j] = __builtin_amdgcn_exp2f(s1v[j] - ref[j & 3]);
        if (pk >= NPK) { pk -= NPK; ncon = 3; lds_spin(CT_PCON, ctl + 4 * pk, 3); }
        s += 4;
        PUBLISH(s1v, st1, rec1, pk, 2 * s + 2);
      }
      float thr = 16.f;
      f32x4 nref = -ref;
#pragma unroll 1
      for (;;) {
        s += 4; slot += 4; slot -= slot >= NS ? NS : 0; pk += 4;
        if (pk >= NPK) { pk -= NPK; ncon += 3; }
        if (s >= NT) break;
        f32x4 sy = z, cy = z, sa, ga = z, ca = z, sb, gb = z, cb = z;
        u32x4 yv[6], xa[6], xb[6];
        unsigned fl_, word = 2 * s + 2;
        AT_SLOT(slot * SLOT);
        LAS char *pw = lds + opk + pk * PK_B, *prc = lds + ork + pk * PK_B, *pfl = ctl + 4 * pk, *pfr = ctl + 4 * slot;
        asm volatile("" : "+v"(pw), "+v"(prc), "+v"(pfl), "+v"(pfr), "+v"(word));
#define RD_X "ds_read_b32 %2, %7 offset:%11\n\tds_read_b128 %3, %8 offset:%12\n\tds_read_b128 %4, %9 offset:%12\n\t" \
             "ds_read_b128 %5, %8 offset:%13\n\tds_read_b128 %6, %9 offset:%13\n"
#define RD_IN(p_) "v"(ctl), "v"(px0), "v"(px1), "s"(3 * s + (p_) + 1), "i"(CT_FULL), "i"(PART * (p_)), "i"(PART * (p_) + 2048)
#define ISSUE(p_) do { int d0_, d1_; \
        asm volatile(RD_X : "=&s"(d0_), "=&s"(d1_), "=&v"(fl_), "=&v"(xa[2 * (p_)]), "=&v"(xa[2 * (p_) + 1]), "=&v"(xb[2 * (p_)]), "=&v"(xb[2 * (p_) + 1]) \
                     : RD_IN(p_) : "memory"); } while (0)
#define CHECK(p_) do { int cnt_; \
        asm volatile("s_mov_b32 %1, 0x400000\n\ts_branch 2f\n1:\n\t" RD_X "2:\n\ts_waitcnt lgkmcnt(4)\n\tv_readfirstlane_b32 %0, %2\n\t" \
                     "s_cmp_ge_i32 %0, %10\n\ts_cbranch_scc1 3f\n\ts_sub_u32 %1, %1, 1\n\ts_cbranch_scc0 1b\n3:\n\ts_waitcnt lgkmcnt(2)" \
                     : "=&s"(full), "=&s"(cnt_), "+v"(fl_), "+v"(xa[2 * (p_)]), "+v"(xa[2 * (p_) + 1]), "+v"(xb[2 * (p_)]), "+v"(xb[2 * (p_) + 1]) \
                     : RD_IN(p_) : "memory", "scc"); \
        asm volatile("" : "+s"(full)); } while (0)
#define GOT_B(p_) do { waitlds<0>(); asm volatile("" : "+v"(xb[2 * (p_)]), "+v"(xb[2 * (p_) + 1])); } while (0)
#define MF_A(k_) do { ca = mfma(yv[k_], xa[k_], ca); ga = mfma(xa[k_], xa[k_], ga); sa = mfma(qk[k_], xa[k_], sa); } while (0)
#define MF_B(k_) do { cb = mfma(yv[k_], xb[k_], cb); gb = mfma(xb[k_], xb[k_], gb); sb = mfma(qk[k_], xb[k_], sb); } while (0)
        const unsigned pcv = *(volatile LAS unsigned*)(pfl + CT_PCON);
        NEED(3 * s);
#pragma unroll
        for (int k = 0; k < 6; ++k) yv[k] = *(LAS const u32x4*)(((k & 1) ? py1 : py0) + (k >> 1) * 128);
        int con = __builtin_amdgcn_readfirstlane(pcv);
        SB();
#pragma unroll
        for (int k = 0; k < 6; ++k) sy = mfma(qk[k], yv[k], sy);
        asm volatile("" : "+v"(sy));
        SB();
        lds_need(CT_PCON, con, pfl, ncon);
        ISSUE(0);
        SB();
#pragma unroll
        for (int k = 0; k < 6; ++k) cy = mfma(yv[k], yv[k], cy);
#pragma unroll
        for (int r = 0; r < 4; ++r) sa[r] = half_copy<true>(sy[r]);
        const float yy = half_copy<true>(sel4(cy, n & 3));
        f32x2 Y = swap32(yy, yy);
        asm volatile("" : "+v"(Y), "+v"(sa));
        SB();
        CHECK(0);
        sb = sa;
        MF_A(0);
        asm volatile("" : "+v"(ca), "+v"(ga), "+v"(sa));
        SB();
        GOT_B(0);
        ISSUE(1);
        SB();
        MF_B(0); MF_A(1); MF_B(1);
        asm volatile("" : "+v"(ca), "+v"(ga), "+v"(sa), "+v"(cb), "+v"(gb), "+v"(sb));
        SB();
        CHECK(1);
        MF_A(2);
        asm volatile("" : "+v"(ca), "+v"(ga), "+v"(sa));
        SB();
        GOT_B(1);
        ISSUE(2);
        SB();
        MF_B(2); MF_A(3); MF_B(3);
        asm volatile("" : "+v"(ca), "+v"(ga), "+v"(sa), "+v"(cb), "+v"(gb), "+v"(sb));
        SB();
        CHECK(2);
        ca = mfma(yv[5], xa[5], mfma(yv[4], xa[4], ca));
        ga = mfma(xa[5], xa[5], mfma(xa[4], xa[4], ga));
        asm volatile("" : "+v"(ca), "+v"(ga));
        SB();
        GOT_B(2);
        cb = mfma(yv[5], xb[5], mfma(yv[4], xb[4], cb));
        asm volatile("" : "+v"(cb));
        SB();
        lds_inc(CT_FREE, pfr);
        gb = mfma(xb[4], xb[4], gb);
        float ua = sel4(ca, n & 3);
        asm volatile("" : "+v"(gb), "+v"(ua));
        SB();
        gb = mfma(xb[5], xb[5], gb);
        float va = sel4(ga, n & 3);
        asm volatile("" : "+v"(gb), "+v"(va));
        SB();
        sa = mfma(qk[4], xa[4], sa);
        float ub = sel4(cb, n & 3);
        asm volatile("" : "+v"(sa), "+v"(ub));
        SB();
        sa = mfma(qk[5], xa[5], sa);
        const f32x2 X = swap32(ua, ub);
        float mn_ = (X[1] + Y[1]) * (1.f / C), q1_ = fmaf(2.f, X[0], Y[0]);
        asm volatile("" : "+v"(sa), "+v"(mn_), "+v"(q1_));
        SB();
        sb = mfma(qk[4], xb[4], sb);
        float vb = sel4(gb, n & 3);
        asm volatile("" : "+v"(sb), "+v"(vb));
        SB();
        sb = mfma(qk[5], xb[5], sb);
        const f32x2 Mn = swap32(mn_, mn_), G = swap32(va, vb);
        q1_ += n < 8 ? G[0] : G[1];
        const f32x2 Q = swap16(q1_, q1_);
        sa -= Mn[0] * qs;
        const float var = fmaxf(fmaf(-mn_, mn_, fmaf((n & 4) ? Q[1] : Q[0], 1.f / C, EPS)), EPS), rs_ = __builtin_amdgcn_rsqf(var);
        sb -= Mn[1] * qs;
        const float v_ = (g & 1) ? var * rs_ : mn_, h_ = bfhi(__float_as_uint(v_));
        *(LAS unsigned*)prc = cvtpk(h_, v_ - h_);
        const f32x2 R = swap32(rs_, rs_);
        sa = sa * R[0] + nref; sb = sb * R[1] + nref;
        const float tm = fmaxf(fmaxf(fmaxf(fmaxf(sa[0], sa[1]), sa[2]), fmaxf(fmaxf(sa[3], sb[0]), sb[1])), fmaxf(sb[2], sb[3]));
        if (__builtin_expect(__builtin_amdgcn_ballot_w64(!(tm <= thr)) != 0ull, 0)) {
#pragma unroll
          for (int r = 0; r < 4; ++r) {
            const float mx = row_max(fmaxf(sa[r], sb[r])), dm = mx <= 16.f ? 0.f : mx;
            nref[r] -= dm; sa[r] -= dm; sb[r] -= dm;
          }
          thr = __builtin_nanf("");
          word |= 1u;
          if (n == 0) *(LAS f32x4*)(lds + LDS_PK + pk * PK_B + 1280 + 16 * g) = -nref;
        }
#pragma unroll
        for (int r = 0; r < 4; ++r) { sa[r] = __builtin_amdgcn_exp2f(sa[r]); sb[r] = __builtin_amdgcn_exp2f(sb[r]); }
        float ra = R[0], rb = R[1];
        asm volatile("" : "+v"(ra), "+v"(rb));
        sa *= ra; sb *= rb;
        *(LAS u32x2*)pw = u32x2{cvtpk(sa[0], sa[1]), cvtpk(sa[2], sa[3])};
        *(LAS u32x2*)(pw + 128) = u32x2{cvtpk(sb[0], sb[1]), cvtpk(sb[2], sb[3])};
        lds_put(CT_PRDY, pfl, word);
        SB();
      }
      GET_TAG();
      LAS char* fin = lds + xslot + 8192;
      unsigned qres = (unsigned)__builtin_amdgcn_raw_buffer_load_b16(mkrs(q, NQ * C * 2), dout * 2, 0, 0) << 16;
      unsigned ao = ((tid & 255) >> 1) * 8192 + (tid & 1) * 16 + dst * 32, tw = (unsigned)(tg >> (20 * (tid & 1))), sh = 12 * (tid & 1), oo = dout * 2;
      LAS char *fw = fin + (((lane & 8) | ((g & 1) << 2) | (g & 2)) + (lane & 1)) * 16 + 4 * (w - 4), *fr = fin + (2 * (lane & 7) + ((lane >> 3) & 1)) * 16;
      float v7 = 0.f;
      unsigned fi_ = 8u;
      const unsigned t0s = lds_get(ctl + CT_T0);
      unsigned gate = (t0s ? t0s : (unsigned)tstart) + RGATE;
      const RSRC rm = rws;
      u32x4 ro = mkrs4(out, NQ * C * 2);
      unsigned long long wm = 0x0303030303030303ull;
      asm volatile("" : "+v"(ao), "+v"(tw), "+v"(sh), "+v"(oo), "+v"(fw), "+v"(fr), "+v"(fin), "+v"(v7), "+v"(qres), "+v"(fi_), "+s"(ro), "+s"(wm));
      lds_spin(CT_ST, ctl, 4);
      gate += max((int)((unsigned)wall_clock64() + OWNG - gate), 0);
      float org = *(LAS const float*)(lds + xslot + 6144 + 4 * dq);
      *(LAS float*)(fin + 256 + 4 * (w - 4)) = org;
#pragma unroll 1
      for (int spin = 0; spin < 1024; ++spin)
        if ((int)((unsigned)wall_clock64() - gate) >= 0) break;
      u32x4 A = ldb128<16>(rm, ao, 0), B = ldb128<16>(rm, ao, 128 * 8192);
#pragma unroll 1
      for (int spin = 0; spin < (1 << 18); ++spin) {
        unsigned xa_ = (A[3] ^ tw) << 12, xb_ = (B[3] ^ tw) << 12;
        if (__builtin_expect(__builtin_amdgcn_ballot_w64((xa_ | xb_) != 0u) == 0ull, 1)) break;
        xa_ |= __builtin_amdgcn_mov_dpp(xa_, 0xB1, 0xf, 0xf, true); xb_ |= __builtin_amdgcn_mov_dpp(xb_, 0xB1, 0xf, 0xf, true);
        asm volatile("" ::: "memory");
        if (xa_ != 0u) A = ldb128<16>(rm, ao, 0);
        if (xb_ != 0u) B = ldb128<16>(rm, ao, 128 * 8192);
      }
      unsigned la = (A[3] & 0xfff00000u) >> sh, lb_ = (B[3] & 0xfff00000u) >> sh;
      la |= __builtin_amdgcn_mov_dpp(la, 0xB1, 0xf, 0xf, true); lb_ |= __builtin_amdgcn_mov_dpp(lb_, 0xB1, 0xf, 0xf, true);
      float da = __uint_as_float(la) - org, db = __uint_as_float(lb_) - org;
      if (__builtin_expect(__builtin_amdgcn_ballot_w64(!(da <= 64.f) || !(db <= 64.f)) != 0ull, 0)) {
        org = xrow16_max(row_max(fmaxf(__uint_as_float(la), __uint_as_float(lb_))));
        da = __uint_as_float(la) - org; db = __uint_as_float(lb_) - org;
        *(LAS float*)(fin + 256 + 4 * (w - 4)) = org;
        fi_ = 9u;
      }
      const float wa = __builtin_amdgcn_exp2f(da), wb = __builtin_amdgcn_exp2f(db);
#define MIX(f_, i_) fmaf(f_(A[i_]), wa, f_(B[i_]) * wb)
      const f32x2 p0 = swap32(MIX(bflo, 0), MIX(bfhi, 0)), p1 = swap32(MIX(bflo, 1), MIX(bfhi, 1));
      const f32x2 p2 = swap32(MIX(bflo, 2), MIX(bfhi, 2)), p3 = swap32(wa + wb, v7);
      const f32x2 r0 = swap16(p0[0] + p0[1], p1[0] + p1[1]), r1 = swap16(p2[0] + p2[1], p3[0] + p3[1]);
      float c0 = r0[0] + r0[1], c1 = r1[0] + r1[1];
      asm volatile("s_nop 1\n\tv_add_f32_dpp %0, %0, %0 row_ror:8 row_mask:0xf bank_mask:0x3\n\tv_add_f32_dpp %0, %1, %1 row_ror:8 row_mask:0xf bank_mask:0xc\n\t"
                   "s_nop 1\n\tv_add_f32_dpp %0, %0, %0 row_shl:4 row_mask:0xf bank_mask:0x5\n\t"
                   "s_nop 1\n\tv_add_f32_dpp %0, %0, %0 quad_perm:[2,3,0,1] row_mask:0xf bank_mask:0x5\n\t"
                   "s_mov_b64 exec, %3\n\tds_write_b32 %2, %0\n\ts_mov_b64 exec, 1\n\tds_add_u32 %4, %5 offset:%6\n\ts_mov_b64 exec, -1"
                   : "+v"(c0) : "v"(c1), "v"(fw), "s"(wm), "v"(ctl), "v"(fi_), "i"(CT_FIN) : "memory");
      if (w == 4) {
        f32x4 a0, l0;
        unsigned cf;
        int cnt_, c_;
        asm volatile("s_mov_b32 %1, 0x4000000\n1:\n\tds_read_b32 %2, %5 offset:%8\n\tds_read_b128 %3, %6\n\tds_read_b128 %4, %7 offset:192\n\t"
                     "s_waitcnt lgkmcnt(2)\n\tv_readfirstlane_b32 %0, %2\n\ts_cmp_ge_i32 %0, 32\n\ts_cbranch_scc1 2f\n\t"
                     "s_sub_u32 %1, %1, 1\n\ts_cbranch_scc0 1b\n2:\n\ts_waitcnt lgkmcnt(0)"
                     : "=&s"(c_), "=&s"(cnt_), "=&v"(cf), "=&v"(a0), "=&v"(l0) : "v"(ctl), "v"(fr), "v"(fin), "i"(CT_FIN) : "memory", "scc");
        asm volatile("" : "+s"(c_));
        if (__builtin_expect(c_ != 32, 0)) {
          f32x4 m0 = *(LAS const f32x4*)(fin + 256);
          const float mm = fmaxf(fmaxf(m0[0], m0[1]), fmaxf(m0[2], m0[3]));
#pragma unroll
          for (int p = 0; p < 4; ++p) m0[p] = __builtin_amdgcn_exp2f(m0[p] - mm);
          a0 *= m0; l0 *= m0;
        }
        const float o = __uint_as_float(qres) + ((a0[0] + a0[2]) + (a0[1] + a0[3])) * __builtin_amdgcn_rcpf((l0[0] + l0[2]) + (l0[1] + l0[3]));
        const unsigned ob = cvtpk(o, __uint_as_float(__builtin_amdgcn_mov_dpp(__float_as_uint(o), 0xB1, 0xf, 0xf, true)));
        asm volatile("s_mov_b64 exec, 0x1515\n\tbuffer_store_dword %0, %1, %2, 0 offen sc1\n\ts_mov_b64 exec, -1" :: "v"(ob), "v"(oo), "s"(ro) : "memory");
        if (wg == 0 && lane == 0) stb64(u32x2{(unsigned)tg, (unsigned)(tg >> 32)}, rws, EP_OFF);
      }
    } else {
      const int ftr = ((g & 1) << 2) | ((tq >> 1) << 1) | (g >> 1);
      const int fty = (g & 1) * 4 + (tq >> 1) * 3;
      int otr[4], oty[4];
#pragma unroll
      for (int t = 0; t < 4; ++t) {
        otr[t] = (w - 1) * PART + 128 * (4 * g + tq) + 16 * ((2 * t + (tp >> 1)) ^ ftr) + 8 * (tp & 1);
        oty[t] = CRS + (w - 1) * 128 + 384 * (4 * (g & 1) + tq) + 16 * ((2 * t + (tp >> 1)) ^ fty) + 8 * (tp & 1);
      }
      int opk = LDS_PK + 320 * tp + 8 * (4 * g + tq), ork = LDS_PK + 320 * (g >> 1) + 256 + 8 * (4 * (g & 1) + tq);
      asm volatile("" : "+v"(opk), "+v"(ork), "+v"(oty[0]), "+v"(oty[1]), "+v"(oty[2]), "+v"(oty[3]));
#define RD_T(ta, ty, so_) _Pragma("unroll") for (int t_ = 0; t_ < 4; ++t_) { \
    ta[t_] = vtr2(lds + otr[t_] + (so_), lds + otr[t_] + (so_) + 2048); ty[t_] = vtr(lds + oty[t_] + (so_)); }
      f32x4 acc[4] = {z, z, z, z}, tac = z;
#define RD_P(o_) \
      pf = vtr2(lds + opk + (o_), lds + opk + (o_) + 128); \
      sf = vtr2(lds + ork + (o_), lds + ork + (o_) + 640)
#define TWICE(ty) asm volatile("s_nop 2"); _Pragma("unroll") for (int t = 0; t < 4; ++t) { u32x2 lo_, hi_; \
      asm volatile("v_mov_b64 %0, %2\n\tv_mov_b64 %1, %2" : "=&v"(lo_), "=&v"(hi_) : "v"(ty[t])); t2[t] = u32x4{lo_[0], lo_[1], hi_[0], hi_[1]}; } \
      asm volatile("s_nop 1" : "+v"(t2[0]), "+v"(t2[1]), "+v"(t2[2]), "+v"(t2[3]))
#define MF_PX(ta) \
      tac = mfma(sf, pf, tac); \
      _Pragma("unroll") for (int t = 0; t < 4; ++t) acc[t] = mfma(ta[t], pf, acc[t])
#define MF_PY() \
      _Pragma("unroll") for (int t = 0; t < 4; ++t) acc[t] = mfma(t2[t], pf, acc[t])
#define MOVE_REF(word_, o_) do { moved |= (word_) & 1; if (__builtin_expect(moved, 0)) { \
            const float m_ = *(LAS const float*)(lds + (((word_) & 1) ? LDS_PK + (o_) + 1280 : LDS_CTL + CT_MAX) + 4 * n); \
            Mtop = fmaxf(Mtop, m_); \
            const bool drop = m_ < Mtop - 64.f; \
            const float mn = drop ? M : m_, fa = __builtin_amdgcn_exp2f(M - mn); \
            M = mn; \
            _Pragma("unroll") for (int t = 0; t < 4; ++t) acc[t] *= fa; \
            tac *= fa; \
            if (drop) pf = u32x4{0u, 0u, 0u, 0u}; \
          } } while (0)
      const unsigned flo = FLAG_OFF + (wg & 7) * 768 + (w - 1) * 144 + (lane < 36 ? lane : 35) * 8;
      float M, Mtop;
      int moved = 0;
      u32x4 wvv[18], pf, sf, t2[4];
      {
        u32x4 ta[8][4], xx[6];
        u32x2 ty[8][4], fl;
        unsigned cc[6];
        const int qo = QK_OFF + (wg & 3) * QK_COPY + (2 * w - 2) * QK_REC;
#define QK_LOAD() _Pragma("unroll") for (int j = 0; j < 6; ++j) { \
          xx[j] = ldb128<16>(rws, lane * 16, qo + ((j >> 1) * 6 + (j & 1)) * QK_REC); \
          cc[j] = __builtin_amdgcn_raw_buffer_load_b32(rws, lane * 4, qo + ((j >> 1) * 6 + (j & 1)) * QK_REC + 1024, 16); }
#pragma unroll
        for (int u = 0; u < 8; ++u) {
          if (u == QKAT) {
            GET_TAG();
#pragma unroll 1
            for (int r = 0; r < 8; ++r) {
#pragma unroll 1
              for (int spin = 0; spin < 128; ++spin) {
                if ((int)(wall_clock64() - tstart) >= GATE + r * HSTEP) break;
                __builtin_amdgcn_s_sleep(1);
              }
              u32x8 hv;
              asm volatile("s_load_dwordx8 %0, %1, %2\n\ts_waitcnt lgkmcnt(0)" : "=&s"(hv) : "s"(ws), "s"(HINT_OFF + ((wg & 7) + 8 * r) * 128 + (w - 1) * 32) : "memory");
              const unsigned t_ = (unsigned)tg;
              if ((((hv[0] ^ t_) | (hv[1] ^ t_) | (hv[2] ^ t_)) | ((hv[3] ^ t_) | (hv[4] ^ t_) | (hv[5] ^ t_))) == 0u) break;
            }
            if (wg < NPR) lds_wait(ctl + CT_PD, 4);
            QK_LOAD();
#pragma unroll 1
            for (int spin = 0; spin < (1 << 18); ++spin) {
              unsigned df = 0u;
#pragma unroll
              for (int j = 0; j < 6; ++j) df |= qk_check(xx[j], (unsigned)tg) ^ cc[j];
              if (__all(df == 0u)) break;
              asm volatile("" ::: "memory");
              QK_LOAD();
            }
#pragma unroll
            for (int h = 0; h < 2; ++h) {
              u32x4 sx;
#pragma unroll
              for (int i = 0; i < 4; ++i) sx[i] = add3(xx[h][i], xx[2 + h][i], xx[4 + h][i]);
              *(LAS u32x4*)(lds + LDS_QK + (2 * w - 2 + h) * 1024 + lane * 16) = sx;
            }
            lds_inc(CT_QK, ctl);
            SB();
          }
          lds_wait(ctl + CT_FULL, 3 * u + w);
          RD_T(ta[u], ty[u], u * SLOT);
          lds_inc(CT_FREE + 4 * u, ctl);
          SB();
        }
        fl = __builtin_bit_cast(u32x2, __builtin_amdgcn_raw_buffer_load_b64(rws, flo, 0, 16));
#pragma unroll
        for (int u = 0; u < 12; ++u) {
          const int word = lds_spin(CT_PRDY + 4 * (u % NPK), ctl, 2 * u + 2);
          if (u == 0) Mtop = M = *(LAS const float*)(ctl + CT_MAX + 4 * n);
          RD_P((u % NPK) * PK_B);
          if (u >= 8) MOVE_REF(word, (u % NPK) * PK_B);
          lds_inc(CT_PCON + 4 * (u % NPK), ctl);
          SB();
          MF_PX(ta[u & 7]);
          SB();
          TWICE(ty[u & 7]);
          MF_PY();
          asm volatile("" : "+v"(acc[0]), "+v"(acc[1]), "+v"(acc[2]), "+v"(acc[3]), "+v"(tac));
          SB();
#define FLAGS_UP() __all(fl[0] == (unsigned)tg && (fl[1] & 255u) == (unsigned)(tg >> 32))
          if (u == T0AT && w == 1 && FLAGS_UP()) {
            int d = (int)(fl[1] - ((unsigned)tstart << 8)) >> 8;
            d += __builtin_amdgcn_mov_dpp(d, XOR8, 0xf, 0xf, true); d += __builtin_amdgcn_mov_dpp(d, ROR4, 0xf, 0xf, true);
            d += __builtin_amdgcn_mov_dpp(d, 0x122, 0xf, 0xf, true); d += __builtin_amdgcn_mov_dpp(d, 0x121, 0xf, 0xf, true);
            lds_put(CT_T0, ctl, ((unsigned)tstart + min(max(d >> 4, -300), 300)) | 1u);
          }
          if (u < 4) {
            NEED(3 * (u + 8) + w);
            RD_T(ta[u], ty[u], ((u + 8) % NS) * SLOT);
            lds_inc(CT_FREE + 4 * ((u + 8) % NS), ctl);
            SB();
          }
        }
#pragma unroll 1
        for (int spin = 0; spin < (1 << 18); ++spin) {
          if (FLAGS_UP()) break;
          asm volatile("" ::: "memory");
          fl = __builtin_bit_cast(u32x2, __builtin_amdgcn_raw_buffer_load_b64(rws, flo, 0, 16));
        }
        asm volatile("" ::: "memory");
        if (w == 3) lds_put(CT_WV, ctl, 1);
      }
      int slot = 3, pk = 12 % NPK;
      unsigned rdy = 0u;
      const int kro[3] = {(w - 1) * 2048, (w == 3 ? 0 : w) * 2048, (w == 1 ? 2 : w - 2) * 2048};
#define RD_PACKET() do { int cnt_; u32x2 p0_, p1_, p2_, p3_; \
        word = __builtin_amdgcn_readfirstlane(rdy); \
        asm volatile("s_mov_b32 %1, 0x400000\n1:\n\tds_read_b32 %2, %7 offset:%11\n\t" \
                     "ds_read_b64_tr_b16 %5, %9\n\tds_read_b64_tr_b16 %6, %9 offset:640\n\t" \
                     "ds_read_b64_tr_b16 %3, %8\n\tds_read_b64_tr_b16 %4, %8 offset:128\n\t" \
                     "s_cmp_ge_i32 %0, %10\n\ts_cbranch_scc1 2f\n\ts_waitcnt lgkmcnt(4)\n\tv_readfirstlane_b32 %0, %2\n\t" \
                     "s_cmp_ge_i32 %0, %10\n\ts_cbranch_scc1 2f\n\ts_sub_u32 %1, %1, 1\n\ts_cbranch_scc0 1b\n2:" \
                     : "+s"(word), "=&s"(cnt_), "=&v"(rdy), "=&v"(p0_), "=&v"(p1_), "=&v"(p2_), "=&v"(p3_) \
                     : "v"(ctl + 4 * pk), "v"(lds + opk + pk * PK_B), "v"(lds + ork + pk * PK_B), "s"(2 * s + 2), "i"(CT_PRDY) : "memory", "scc"); \
        asm volatile("" : "+s"(word)); \
        pf = u32x4{p0_[0], p0_[1], p1_[0], p1_[1]}; sf = u32x4{p2_[0], p2_[1], p3_[0], p3_[1]}; } while (0)
#define GOT_PACKET() do { waitlds<0>(); asm volatile("" : "+v"(pf), "+v"(sf) : "v"(rdy)); MOVE_REF(word, pk * PK_B); } while (0)
#define RD_Y(t_, p_) do { const u32x2 lo_ = vtr(p_); asm volatile("" ::: "memory"); const u32x2 hi_ = vtr(p_); \
        t2[t_] = u32x4{lo_[0], lo_[1], hi_[0], hi_[1]}; } while (0)
      u32x4 ta[4];
      int s = 12, word;
      NEED(36 + w);
#pragma unroll
      for (int t = 0; t < 4; ++t) { ta[t] = vtr2(lds + otr[t] + 2 * SLOT, lds + otr[t] + 2 * SLOT + 2048); RD_Y(t, lds + oty[t] + 2 * SLOT); }
      lds_inc(CT_FREE + 8, ctl);
#pragma unroll 1
      for (; s < NT - 1; ++s) {
        RD_PACKET();
        LAS const char *px[4], *py[4];
#pragma unroll
        for (int t = 0; t < 4; ++t) { px[t] = lds + otr[t] + slot * SLOT; py[t] = lds + oty[t] + slot * SLOT; asm volatile("" : "+v"(px[t]), "+v"(py[t])); }
        NEED(3 * s + 3 + w);
        GOT_PACKET();
        SB();
        tac = mfma(sf, pf, tac);
        asm volatile("" : "+v"(tac));
        SB();
        lds_inc(CT_PCON, ctl + 4 * pk);
        pk = pk == NPK - 1 ? 0 : pk + 1;
#pragma unroll
        for (int t = 0; t < 4; ++t) { acc[t] = mfma(ta[t], pf, acc[t]); SB(); ta[t] = vtr2(px[t], px[t] + 2048); SB(); }
        rdy = *(volatile LAS unsigned*)(ctl + CT_PRDY + 4 * pk);
#pragma unroll
        for (int t = 0; t < 4; ++t) { acc[t] = mfma(t2[t], pf, acc[t]); SB(); RD_Y(t, py[t]); SB(); }
        lds_inc(CT_FREE, ctl + 4 * slot);
#define WV_LOAD(j_) wvv[j_] = ldb128<16>(rws, lane * 16 + ((j_) & 1) * 1024, WVO_OFF + (w - 1) * 18432 + ((j_) / 6) * 6144 + kro[((j_) % 6) >> 1])
        if ((unsigned)(s - WVS) < WV1) {
#pragma unroll
          for (int u = 0; u < WV1; ++u)
            if (s - WVS == u) WV_LOAD(u);
        }
        if (s == NT - 3) {
#pragma unroll
          for (int j = WV1; j < 18; ++j) WV_LOAD(j);
        }
        slot = slot == NS - 1 ? 0 : slot + 1;
      }
      LAS char *xw = lds + xslot + lane * 16 + kro[0], *xl = lds + xslot + 6144 + 4 * n;
      LAS const char *xr1 = lds + xslot + lane * 16 + kro[1], *xr2 = lds + xslot + lane * 16 + kro[2];
      unsigned mo = wg * 8192 + (w - 1) * 2048 + n * 64 + g * 16, tw = (unsigned)(tg >> (20 * (g & 1))) & 0xfffffu, sh = 12 * (g & 1);
      asm volatile("" : "+v"(xw), "+v"(xl), "+v"(xr1), "+v"(xr2), "+v"(mo), "+v"(tw), "+v"(sh));
      waitvm<0>();
      RD_PACKET();
      GOT_PACKET();
      SB();
      tac = mfma(sf, pf, tac);
      asm volatile("" : "+v"(tac));
      SB();
#pragma unroll
      for (int t = 0; t < 2; ++t) acc[t] = mfma(t2[t], pf, mfma(ta[t], pf, acc[t]));
      SB();
#pragma unroll
      for (int t = 2; t < 4; ++t) acc[t] = mfma(t2[t], pf, mfma(ta[t], pf, acc[t]));
      const float L = tac[2] + tac[3], rl = __builtin_amdgcn_rcpf(L), ct = -(tac[0] + tac[1]) * rl;
      const float lse = M + __builtin_amdgcn_logf(L);
      u32x4 b[6];
      b[0] = pack8(acc[0] * rl + ct, acc[1] * rl + ct);
      *(LAS u32x4*)xw = b[0];
      *(LAS float*)xl = lse;
#pragma unroll
      for (int j = 0; j < 4; ++j) { __builtin_amdgcn_sched_group_barrier(0x008, 1, 0); __builtin_amdgcn_sched_group_barrier(0x002, 3, 0); }
      SB();
      b[1] = pack8(acc[2] * rl + ct, acc[3] * rl + ct);
      *(LAS u32x4*)(xw + 1024) = b[1];
      lds_inc(CT_XD, ctl);
      SB();
      f32x4 o[3];
      unsigned xd;
#pragma unroll
      for (int t = 0; t < 3; ++t) o[t] = mfma(wvv[6 * t], b[0], z);
      asm volatile("" : "+v"(o[0]), "+v"(o[1]), "+v"(o[2]));
      SB();
#define RD_SLICES "ds_read_b32 %0, %7 offset:%10\n\tds_read_b128 %1, %8\n\tds_read_b128 %2, %8 offset:1024\n\tds_read_b128 %3, %9\n\tds_read_b128 %4, %9 offset:1024\n"
      asm volatile(RD_SLICES : "=&v"(xd), "=&v"(b[2]), "=&v"(b[3]), "=&v"(b[4]), "=&v"(b[5]) : "i"(0), "i"(0), "v"(ctl), "v"(xr1), "v"(xr2), "i"(CT_XD) : "memory");
      SB();
#pragma unroll
      for (int t = 0; t < 3; ++t) o[t] = mfma(wvv[6 * t + 1], b[1], o[t]);
      tw |= (__float_as_uint(lse) << sh) & 0xfff00000u;
      asm volatile("" : "+v"(tw), "+v"(o[0]), "+v"(o[1]), "+v"(o[2]));
      SB();
      {
        int cnt_, c_;
        asm volatile("s_mov_b32 %6, 0x400000\n\ts_branch 2f\n1:\n\t" RD_SLICES "2:\n\ts_waitcnt lgkmcnt(4)\n\tv_readfirstlane_b32 %5, %0\n\t"
                     "s_cmp_ge_i32 %5, 3\n\ts_cbranch_scc1 3f\n\ts_sub_u32 %6, %6, 1\n\ts_cbranch_scc0 1b\n3:\n\ts_waitcnt lgkmcnt(0)"
                     : "+v"(xd), "+v"(b[2]), "+v"(b[3]), "+v"(b[4]), "+v"(b[5]), "=&s"(c_), "=&s"(cnt_)
                     : "v"(ctl), "v"(xr1), "v"(xr2), "i"(CT_XD) : "memory", "scc");
      }
      SB();
      PROJECT(2);
      lds_inc(CT_ST, ctl);
    }
  }
}

#undef rws
#define WS_AT(size_) (((size_) - (64u << 20)) & ~(size_t)0x1fffff)
extern "C" void kernel_launch(void* const* d_in, const int* in_sizes, int n_in, void* d_out, int out_size, void* d_ws, size_t ws_size,
                              hipStream_t stream) {
  dino_main<<<NWG, 512, 0, stream>>>((const u16*)d_in[0], (const u16*)d_in[1], (const u16*)d_in[2], (const u16*)d_in[3], (const u16*)d_in[4],
                                     (const u16*)d_in[5], (const u16*)d_in[6], (char*)d_ws + WS_AT(ws_size) + 4096, (u16*)d_out);
}
```

```cpp
#include <hip/hip_runtime.h>
#include <hip/hip_bf16.h>
#include <stdint.h>

typedef unsigned short u16;
using u32x2 = __attribute__((ext_vector_type(2))) unsigned;
using u32x4 = __attribute__((ext_vector_type(4))) unsigned;
using u32x8 = __attribute__((ext_vector_type(8))) unsigned;
using f32x2 = __attribute__((ext_vector_type(2))) float;
using f32x4 = __attribute__((ext_vector_type(4))) float;
using s16x8 = __attribute__((ext_vector_type(8))) short;
using bf16x2_t = __attribute__((ext_vector_type(2))) __bf16;
typedef short v4i16_t __attribute__((ext_vector_type(4)));
#define LAS __attribute__((address_space(3)))

constexpr int C = 192, NQ = 16, W0 = 512, W1 = 256, NWG = 256, NW = 8;
constexpr float EPS = 1e-5f;
constexpr float QSCALE = 0.07216878364870322f * 1.4426950408889634f;

constexpr int PART = 4096, CRS = 3 * PART;
constexpr int SLOT = CRS + 3072, NS = 10;
constexpr int LDS_QK = NS * SLOT;
constexpr int NPK = 7;
constexpr int LDS_PK = LDS_QK, PK_B = 1344;
constexpr int LDS_CTL = LDS_PK + NPK * PK_B;
constexpr int LDS_SINK = LDS_CTL + 384;
constexpr int LDS_BYTES = LDS_SINK + 256;
constexpr int PSCR = 8 * SLOT;
constexpr int CT_FULL = 8, CT_WV = 4, CT_PA = 0, CT_PB = 12, CT_QR = 16, CT_PD = 20, CT_T0 = 24, CT_XD = 28, CT_TG = 32, CT_QK = 40, CT_ST = 44,
              CT_FREE = 48, CT_FIN = 88, CT_ONE = 112, CT_PRDY = 128, CT_PCON = 160, CT_MAX = 256;

constexpr int MB_BYTES = NWG * NWG * 32;
constexpr int EP_OFF = MB_BYTES;
constexpr int FLAG_OFF = EP_OFF + 512;
constexpr int WVO_OFF = EP_OFF + 8192;
constexpr int QK_OFF = WVO_OFF + C * C * 2;
constexpr int QK_REC = 1280, QK_COPY = 18 * QK_REC;
constexpr int HINT_OFF = QK_OFF + 4 * QK_COPY;
constexpr int WS_BYTES = HINT_OFF + 64 * 128;

#define GIVE 2
constexpr int NPR = 36;
struct Tabs { unsigned seq[112]; };
constexpr Tabs make_tabs() {
  Tabs t{};
  for (int i = 0; i < 112; ++i) t.seq[i] = i + 1;
  return t;
}
__device__ const Tabs tabs = make_tabs();
#define QKAT 3
#define WV1 6
#define WMIN 24
#define WSTEP 6
#define LSTEP 20
#define OWNG 20
#define PACE0 30
#define PACE1 1720
#define RGATE 1912
#define T0AT 3
#define LAND0 200
#define TLBE 1
#define LANDS 0
#define WVS 12
#define XPAIR 1
#define CALM 2
#define HSTEP 40
#define GATE 350
#define CODE_BYTES 25792

__device__ __forceinline__ unsigned cvtpk(float lo, float hi) {
  bf16x2_t v = {(__bf16)lo, (__bf16)hi};
  return __builtin_bit_cast(unsigned, v);
}
__device__ __forceinline__ float bflo(unsigned x) { return __builtin_bit_cast(float, x << 16); }
__device__ __forceinline__ float bfhi(unsigned x) { return __builtin_bit_cast(float, x & 0xffff0000u); }
template <int CTRL> __device__ __forceinline__ float dpp(float x) {
  return __builtin_bit_cast(float, __builtin_amdgcn_mov_dpp(__builtin_bit_cast(int, x), CTRL, 0xf, 0xf, true));
}
constexpr int XOR8 = 0x128, ROR4 = 0x124;
template <bool HI8> __device__ __forceinline__ float half_copy(float x) {
  const int v = __builtin_bit_cast(int, x);
  return __builtin_bit_cast(float, __builtin_amdgcn_update_dpp(v, v, XOR8, 0xf, HI8 ? 0xc : 0x3, false));
}
__device__ __forceinline__ float row_max(float x) {
  x = fmaxf(x, dpp<XOR8>(x)); x = fmaxf(x, dpp<ROR4>(x)); x = fmaxf(x, dpp<0x122>(x));
  return fmaxf(x, dpp<0x121>(x));
}
__device__ __forceinline__ f32x2 swap32(float a, float b) {
  auto s = __builtin_amdgcn_permlane32_swap(__float_as_uint(a), __float_as_uint(b), false, false);
  return f32x2{__uint_as_float(s[0]), __uint_as_float(s[1])};
}
__device__ __forceinline__ f32x2 swap16(float a, float b) {
  auto s = __builtin_amdgcn_permlane16_swap(__float_as_uint(a), __float_as_uint(b), false, false);
  return f32x2{__uint_as_float(s[0]), __uint_as_float(s[1])};
}
__device__ __forceinline__ float xrow16_max(float x) {
  auto s = __builtin_amdgcn_permlane16_swap(__float_as_uint(x), __float_as_uint(x), false, false);
  x = fmaxf(__uint_as_float(s[0]), __uint_as_float(s[1]));
  auto t = __builtin_amdgcn_permlane32_swap(__float_as_uint(x), __float_as_uint(x), false, false);
  return fmaxf(__uint_as_float(t[0]), __uint_as_float(t[1]));
}
__device__ __forceinline__ float xrow16_sum(float x) {
  auto s = __builtin_amdgcn_permlane16_swap(__float_as_uint(x), __float_as_uint(x), false, false);
  x = __uint_as_float(s[0]) + __uint_as_float(s[1]);
  auto t = __builtin_amdgcn_permlane32_swap(__float_as_uint(x), __float_as_uint(x), false, false);
  return __uint_as_float(t[0]) + __uint_as_float(t[1]);
}
__device__ __forceinline__ f32x4 mfma(u32x4 a, u32x4 b, f32x4 c) {
  return __builtin_amdgcn_mfma_f32_16x16x32_bf16(__builtin_bit_cast(s16x8, a), __builtin_bit_cast(s16x8, b), c, 0, 0, 0);
}
__device__ __forceinline__ u32x2 vtr(LAS const char* p) {
  return __builtin_bit_cast(u32x2, __builtin_amdgcn_ds_read_tr16_b64_v4i16((LAS v4i16_t*)p));
}
__device__ __forceinline__ u32x4 vtr2(LAS const char* p, LAS const char* q_) {
  const u32x2 a = vtr(p), b = vtr(q_);
  return u32x4{a[0], a[1], b[0], b[1]};
}
__device__ __forceinline__ float sel4(f32x4 v, int r) {
  const float a = (r & 1) ? v[1] : v[0], b = (r & 1) ? v[3] : v[2];
  return (r & 2) ? b : a;
}
using RSRC = __amdgpu_buffer_rsrc_t;
__device__ __forceinline__ RSRC mkrs(const void* p, int bytes) { return __builtin_amdgcn_make_buffer_rsrc(const_cast<void*>(p), (short)0, bytes, 0x00020000); }
__device__ __forceinline__ u32x4 mkrs4(const void* p, unsigned bytes) {
  const unsigned long long b = (unsigned long long)p;
  return u32x4{(unsigned)b, (unsigned)(b >> 32) & 0xffffu, bytes, 0x00020000u};
}
template <int AUX> __device__ __forceinline__ u32x4 ldb128(RSRC r, unsigned vo, int so) { return __builtin_bit_cast(u32x4, __builtin_amdgcn_raw_buffer_load_b128(r, vo, so, AUX)); }
#define SB() __builtin_amdgcn_sched_barrier(0)
#define WAVE_SYNC() do { __builtin_amdgcn_fence(__ATOMIC_RELEASE, "wavefront"); __builtin_amdgcn_wave_barrier(); __builtin_amdgcn_fence(__ATOMIC_ACQUIRE, "wavefront"); } while (0)
#define RAW_BARRIER() asm volatile("s_waitcnt lgkmcnt(0)\n\ts_barrier" ::: "memory")
#define DMA(m0_, vo_, rs_, so_) asm volatile("s_mov_b32 m0, %0\n\ts_nop 0\n\tbuffer_load_dwordx4 %1, %2, %3 offen lds" :: "s"(m0_), "v"(vo_), "s"(rs_), "s"(so_) : "memory")
template <int N> __device__ __forceinline__ void waitlds() { __builtin_amdgcn_s_waitcnt(0xc07f | (N << 8)); }
template <int N> __device__ __forceinline__ void waitvm() { asm volatile("s_waitcnt vmcnt(%0)" :: "i"(N) : "memory"); }


__device__ __forceinline__ void lds_add(LAS char* p, int lane, unsigned v) {
  asm volatile("" ::: "memory");
  if (lane == 0) __hip_atomic_fetch_add((LAS unsigned*)p, v, __ATOMIC_RELAXED, __HIP_MEMORY_SCOPE_WORKGROUP);
  asm volatile("" ::: "memory");
}
__device__ __forceinline__ void lds_set(LAS char* p, int lane, unsigned v) {
  asm volatile("" ::: "memory");
  if (lane == 0) __hip_atomic_store((LAS unsigned*)p, v, __ATOMIC_RELAXED, __HIP_MEMORY_SCOPE_WORKGROUP);
  asm volatile("" ::: "memory");
}
__device__ __forceinline__ void lds_inc(int O, LAS char* p) {
  asm volatile("s_mov_b64 exec, 1\n\tds_add_u32 %0, %1 offset:%2\n\ts_mov_b64 exec, -1" :: "v"(p), "v"(1u), "i"(O) : "memory");
}
__device__ __forceinline__ void lds_put(int O, LAS char* p, unsigned v) {
  asm volatile("s_mov_b64 exec, 1\n\tds_write_b32 %0, %1 offset:%2\n\ts_mov_b64 exec, -1" :: "v"(p), "v"(v), "i"(O) : "memory");
}
__device__ __forceinline__ int lds_get(LAS char* p) {
  asm volatile("" ::: "memory");
  const unsigned v = __hip_atomic_load((LAS unsigned*)p, __ATOMIC_RELAXED, __HIP_MEMORY_SCOPE_WORKGROUP);
  asm volatile("" ::: "memory");
  return __builtin_amdgcn_readfirstlane(v);
}
__device__ __forceinline__ void lds_wait(LAS char* p, int target) {
#pragma unroll 1
  for (int spin = 0; spin < (1 << 22); ++spin) {
    if (lds_get(p) >= target) break;
    __builtin_amdgcn_s_sleep(1);
  }
}
__device__ __forceinline__ int lds_spin(int O, LAS char* p, int target) {
  int r, cnt = 1 << 22;
  unsigned t;
  asm volatile("1:\n\tds_read_b32 %1, %3 offset:%5\n\ts_waitcnt lgkmcnt(0)\n\tv_readfirstlane_b32 %0, %1\n\ts_cmp_ge_i32 %0, %4\n\ts_cbranch_scc1 2f\n\t"
               "s_sub_u32 %2, %2, 1\n\ts_cbranch_scc0 1b\n2:"
               : "=&s"(r), "=&v"(t), "+s"(cnt) : "v"(p), "s"(target), "i"(O) : "memory", "scc");
  return r;
}
__device__ __forceinline__ void lds_need(int O, int& have, LAS char* p, int target) {
  int cnt;
  unsigned t;
  asm volatile("s_cmp_ge_i32 %0, %4\n\ts_cbranch_scc1 2f\n\ts_mov_b32 %2, 0x400000\n"
               "1:\n\tds_read_b32 %1, %3 offset:%5\n\ts_waitcnt lgkmcnt(0)\n\tv_readfirstlane_b32 %0, %1\n\ts_cmp_ge_i32 %0, %4\n\ts_cbranch_scc1 2f\n\t"
               "s_sleep %6\n\ts_sub_u32 %2, %2, 1\n\ts_cbranch_scc0 1b\n2:"
               : "+s"(have), "=&v"(t), "=&s"(cnt) : "v"(p), "s"(target), "i"(O), "i"(CALM) : "memory", "scc");
  asm volatile("" : "+s"(have));
}
__device__ __forceinline__ u32x4 pack8(f32x4 a, f32x4 b) { return u32x4{cvtpk(a[0], a[1]), cvtpk(a[2], a[3]), cvtpk(b[0], b[1]), cvtpk(b[2], b[3])}; }

__device__ __forceinline__ void stb128(u32x4 v, RSRC r, unsigned o) { __builtin_amdgcn_raw_buffer_store_b128(v, r, o, 0, 16); }
__device__ __forceinline__ void stb64(u32x2 v, RSRC r, unsigned o) { __builtin_amdgcn_raw_buffer_store_b64(v, r, o, 0, 16); }
__device__ __forceinline__ void stb32(unsigned v, RSRC r, unsigned o) { __builtin_amdgcn_raw_buffer_store_b32(v, r, o, 0, 16); }
__device__ __forceinline__ unsigned qk_check(u32x4 x, unsigned tg) {
  const unsigned a = (x[0] ^ __builtin_amdgcn_alignbit(x[1], x[1], 11)) * 0x9e3779b1u + tg;
  const unsigned b = (x[2] + __builtin_amdgcn_alignbit(x[3], x[3], 19)) * 0x85ebca77u;
  return __builtin_amdgcn_alignbit(a, a, 15) ^ b ^ (x[1] + x[3]);
}
__device__ __forceinline__ unsigned add3(unsigned a, unsigned b, unsigned c) {
  return cvtpk((bflo(a) + bflo(b)) + bflo(c), (bfhi(a) + bfhi(b)) + bfhi(c));
}
__global__ __launch_bounds__(512, 2) void dino_main(const u16* q, const u16* f0, const u16* f1, const u16* wq, const u16* wk, const u16* wv,
                                                     const u16* wo, char* ws, u16* out) {
  __shared__ __attribute__((aligned(128))) char smem[LDS_BYTES];
  LAS char* lds = (LAS char*)smem;
  LAS char* ctl = lds + LDS_CTL;
  const unsigned lb = (unsigned)(unsigned long long)lds;
  const int w = __builtin_amdgcn_readfirstlane(threadIdx.x >> 6);
  unsigned long long pc0;
  asm volatile("s_getpc_b64 %0" : "=s"(pc0));
#define COORDS() \
  int tid = threadIdx.x, wg = blockIdx.x; \
  asm volatile("" : "+v"(tid), "+s"(wg)); \
  __builtin_assume(tid >= 0 && tid < 512); __builtin_assume(wg >= 0 && wg < NWG); \
  const int lane = tid & 63, n = lane & 15, g = lane >> 4;       \
    \
  const int dq = (wg & 7) * 2 + ((wg >> 4) & 1), dst = (wg & ~31) + (wg & 7) * 4 + ((wg >> 3) & 3); \
  const int dj = 6 * ((wg >> 5) & 1) + min(lane & 7, 5); \
  const int dout = dq * C + 48 * (wg >> 6) + 16 * (dj >> 2) + (wg & 8) + ((lane & 8) >> 1) + (dj & 3); \
  const int NT = wg < NPR ? 32 - GIVE : wg >= 40 && !(wg & XPAIR) && (wg >> 3) < 5 + ((wg & 4) ? 4 : 5) * (GIVE << XPAIR) ? 33 : 32; \
  const int xslot = (NT >= 30 ? NT - 30 : NT - 20) * SLOT;       \
  (void)xslot; (void)n; (void)g; (void)dst; (void)dout; (void)NT
#define ARGS_IN() asm volatile("s_waitcnt lgkmcnt(0)" :: "s"(q), "s"(f0), "s"(f1), "s"(wq), "s"(wk), "s"(wv), "s"(wo), "s"(ws), "s"(out) : "memory")
#define rws mkrs(ws, WS_BYTES)
#define PROJECT(k0_) do { \
      _Pragma("unroll") for (int k = k0_; k < 6; ++k) { o[0] = mfma(wvv[k], b[k], o[0]); o[1] = mfma(wvv[6 + k], b[k], o[1]); } \
      asm volatile("" : "+v"(o[0]), "+v"(o[1])); \
      SB(); \
      _Pragma("unroll") for (int k = k0_; k < 6; ++k) o[2] = mfma(wvv[12 + k], b[k], o[2]); \
      asm volatile("" : "+v"(o[2])); \
      SB(); \
      stb128(u32x4{cvtpk(o[0][0], o[0][1]), cvtpk(o[0][2], o[0][3]), cvtpk(o[1][0], o[1][1]), tw}, rws, mo); \
      SB(); \
      stb128(u32x4{cvtpk(o[1][2], o[1][3]), cvtpk(o[2][0], o[2][1]), cvtpk(o[2][2], o[2][3]), tw}, rws, mo + 1024); \
    } while (0)
  unsigned long long tg;
#define GET_TAG() tg = ((unsigned long long)((unsigned)lds_spin(CT_TG + 4, ctl, 1) & 0xffu) << 32) | (unsigned)lds_get(ctl + CT_TG)
  const unsigned long long tstart = wall_clock64();
  if (w != 0) {
    const unsigned l0 = threadIdx.x & 63;
    if (blockIdx.x < NPR) {
      const unsigned p6 = (blockIdx.x * 43) >> 8, pu = blockIdx.x - 6 * p6;
      unsigned vo = ((l0 * 16) ^ (((l0 * 4) & 0x40) | (l0 & 0x20))) + (l0 >> 3) * 256;
      const bool pv = p6 >= 3, wa = w >= 4, isq = !pv && (w & 6) == 2;
      const unsigned a6 = 6 * w - 24, pj = pv ? p6 - 3 : p6;
      unsigned so = (wa ? a6 : isq ? w - 2 : 4 * pu) * 3072 + (isq ? 0 : pv && !wa ? w - 1 : pj) * 128, ga = isq ? 128 : 3072;
      unsigned lo = lb + PSCR + (wa ? a6 : pv ? 4 * w + 20 : (0x1b181e00u >> (8 * w)) & 255u) * 1024, hi = lo + (wa ? 6144 : isq ? 3072 : 4096);
      asm volatile("" : "+v"(vo), "+s"(lo), "+s"(so), "+s"(hi), "+s"(ga));
      ARGS_IN();
      const u32x4 rw = mkrs4(wa ? (pv ? wo : wq) : pv ? wv : isq ? q : wk, isq ? NQ * C * 2 : C * C * 2);
#pragma unroll 1
      do { DMA(lo, vo, rw, so); lo += 1024; so += ga; } while (lo != hi);
    } else if (w == 7 && blockIdx.x >= 56 && blockIdx.x < 72) {
      const u32x4 rc = mkrs4((const void*)(pc0 & ~255ull), CODE_BYTES);
#pragma unroll 1
      for (int i = 0; i < CODE_BYTES; i += 4096)
        asm volatile("s_mov_b32 m0, %0\n\ts_nop 0\n\tbuffer_load_dword %1, %2, %3 offen lds" :: "s"(lb + LDS_SINK), "v"(l0 * 64), "s"(rc), "s"(i) : "memory");
    }
    if (w == 3) {
      unsigned long long ep;
      asm volatile("s_waitcnt lgkmcnt(0)\n\ts_load_dwordx2 %0, %1, %2\n\ts_barrier\n\ts_waitcnt lgkmcnt(0)" : "=&s"(ep) : "s"(ws), "s"(EP_OFF) : "memory");
      *(LAS unsigned long long*)(ctl + CT_TG) = ((ep + 0x100001ull) & 0xffffffffffull) | 0x10000000000ull;
    } else RAW_BARRIER();
  }
  SB();
  if (__builtin_expect(w == 0, 1)) {
    const unsigned l0 = threadIdx.x & 63, g0 = blockIdx.x;
    unsigned y0 = (l0 * 16) ^ (l0 >= 48 ? 48 : 0), y1 = (l0 * 16) ^ (l0 >= 32 ? 64 : 48), y2 = (l0 * 16) ^ (l0 >= 16 ? 112 : 64);
    unsigned v0 = ((l0 * 16) ^ ((l0 * 2) & 0x60)) + (l0 >> 3) * 640 + 1024, v1 = (v0 ^ 16) - 640;
    unsigned v2 = v0 + W0 * C * 2 - 2048, v3 = v1 + W0 * C * 2 - 2048;
    unsigned b0 = (g0 & 7) * (2 * W0 * C * 2) + (g0 >> 3) * 6144, b1 = (g0 & 7) * (W1 * C * 2) + (g0 >> 3) * 3072;
    {
      const unsigned iv = (l0 & 15) >= CT_ONE / 8 ? 0x3f803f80u : (l0 >> 3) == CT_MAX / 64 ? 0xff800000u : 0u;
      *(LAS u32x2*)(ctl + 8 * min(l0, 47u)) = u32x2{l0 >= 47 ? 0x3f803f80u : iv, l0 >= 47 ? 0x3f803f80u : iv};
    }
    constexpr unsigned st0 = 16 * W0 * C * 2, st1 = 8 * W1 * C * 2;
    unsigned sb = lb, fo = CT_FREE, fv = 0u;
    int lap = 0;
    u32x4 rt = mkrs4(tabs.seq, 112 * 4);
    unsigned vz = 0u;
    asm volatile("" : "+v"(v0), "+v"(v1), "+v"(v2), "+v"(v3), "+v"(y0), "+v"(y1), "+v"(y2), "+v"(vz), "+s"(b0), "+s"(b1), "+s"(rt));
    ARGS_IN();
    const u32x4 r0 = mkrs4((const char*)f0 - 1024, W0 * W0 * C * 2 + 1024), r1 = mkrs4(f1, W1 * W1 * C * 2);
    if (g0 < NPR) __builtin_amdgcn_s_barrier();
#define UNIT(p_) do { unsigned so_; \
      asm volatile("s_add_u32 m0, %1, %10\n\ts_add_u32 %0, %2, %11\n\t" \
                   "buffer_load_dwordx4 %3, %7, %0 offen nt lds\n\tbuffer_load_dwordx4 %4, %7, %0 offen offset:1024 nt lds\n\t" \
                   "buffer_load_dwordx4 %5, %7, %0 offen offset:2048 nt lds\n\tbuffer_load_dwordx4 %6, %7, %0 offen offset:3072 nt lds\n\t" \
                   "s_mov_b32 m0, %12\n\ts_mov_b64 exec, 1\n\tbuffer_load_dword %13, %8, %9 offen offset:%14 lds\n\ts_mov_b64 exec, -1" \
                   : "=&s"(so_) : "s"(sb), "s"(b0), "v"(v0), "v"(v1), "v"(v2), "v"(v3), "s"(r0), "s"(rt), "s"(12 * s), "i"((p_) * PART), "i"((p_) * 128), \
                     "s"(lb + LDS_CTL + CT_FULL - 4 * (p_)), "v"(vz), "i"(4 * (p_)) : "memory", "scc"); } while (0)
#define WINDOW() \
      asm volatile("s_waitcnt vmcnt(%1)\n\ts_cmp_ge_i32 %0, %6\n\ts_cbranch_scc1 1f\n\ts_waitcnt vmcnt(%2)\n\ts_cmp_ge_i32 %0, %7\n\ts_cbranch_scc1 1f\n\t" \
                   "s_waitcnt vmcnt(%3)\n\ts_cmp_ge_i32 %0, %8\n\ts_cbranch_scc1 1f\n\ts_waitcnt vmcnt(%4)\n\ts_cmp_ge_i32 %0, %9\n\ts_cbranch_scc1 1f\n\t" \
                   "s_waitcnt vmcnt(%5)\n1:" :: "s"(late), "i"(WMIN + 4 * WSTEP), "i"(WMIN + 3 * WSTEP), "i"(WMIN + 2 * WSTEP), "i"(WMIN + WSTEP), "i"(WMIN), \
                   "i"(4 * LSTEP * 256), "i"(3 * LSTEP * 256), "i"(2 * LSTEP * 256), "i"(LSTEP * 256) : "memory", "scc")
#define COARSE(sb_) \
      asm volatile("s_add_u32 m0, %0, %6\n\ts_nop 0\n\tbuffer_load_dwordx4 %1, %4, %5 offen nt lds\n\t" \
                   "buffer_load_dwordx4 %2, %4, %5 offen offset:1024 nt lds\n\tbuffer_load_dwordx4 %3, %4, %5 offen offset:2048 nt lds" \
                   :: "s"(sb_), "v"(y0), "v"(y1), "v"(y2), "s"(r1), "s"(b1), "i"(CRS) : "memory", "scc")
    COARSE(sb);
#pragma unroll 1
    for (int s = 0; s < 2; ++s) {
      asm volatile("s_add_u32 %0, %0, %1" : "+s"(b1) : "i"(st1) : "scc");
      UNIT(0); UNIT(1); COARSE(sb + SLOT); UNIT(2);
      sb += SLOT; b0 += st0;
    }
    if (g0 >= NPR) __builtin_amdgcn_s_barrier();
    __builtin_amdgcn_s_setprio(3);
    COORDS();
    fo += 12;
    const int tau = __builtin_amdgcn_readfirstlane((int)((PACE1 - PACE0) * 256.f * __builtin_amdgcn_rcpf((float)NT)));
    unsigned due = PACE0 * 256u + 2 * tau, t0v = 0u;
    unsigned long long now = tstart;
    const unsigned xi = (wg >> 3) - 5, xrow = 8 * (31 - xi % GIVE) + (wg & 7) + (xi / GIVE & XPAIR), xcol = xi / GIVE >> XPAIR;
    const unsigned x0 = xrow * (2 * W0 * C * 2) + xcol * 6144, x1 = xrow * (W1 * C * 2) + xcol * 3072;
#pragma unroll 1
    for (int s = 2; s < NT; ++s) {
      asm volatile("" : "+s"(now));
      const unsigned t0s = __builtin_amdgcn_readfirstlane(t0v), t0 = t0s ? t0s : (unsigned)tstart;
      if (PACE1 && (int)(((unsigned)now - t0) * 256u - due) < 0) {
#pragma unroll 1
        for (int spin = 0; spin < 256; ++spin) {
          if ((int)(((unsigned)wall_clock64() - t0) * 256u - due) >= 0) break;
          __builtin_amdgcn_s_sleep(1);
        }
      }
      fv = *(volatile LAS unsigned*)(ctl + (s == 7 ? CT_PD : fo));
      now = wall_clock64();
      UNIT(0);
      asm volatile("" : "+s"(now));
      const int late = (int)(((unsigned)now - t0) * 256u - due);
      due += tau;
      WINDOW();
      UNIT(1);
      WINDOW();
      const unsigned nsb = fo == CT_FREE ? lb : sb + SLOT;
      const unsigned n0 = s == 31 ? x0 : b0 + st0;
      b1 = s == 31 ? x1 : b1 + st1;
      if (s + 1 < NT) {
        const int need = s == 7 && wg < NPR ? 4 : lap;
        if (__builtin_expect((int)__builtin_amdgcn_readfirstlane(fv) < need, 0)) {
          lds_wait(ctl + (s == 7 ? CT_PD : fo), need);
        }
        COARSE(nsb);
      }
      now = wall_clock64();
      t0v = *(volatile LAS unsigned*)(ctl + CT_T0);
      UNIT(2);
      WINDOW();
      if (fo == CT_FREE + 4 * (NS - 1)) { fo = CT_FREE; lap += 4; } else fo += 4;
      sb = nsb; b0 = n0;
    }
    __builtin_amdgcn_s_setprio(0);
    u32x4 wvv[18], b[6];
    if (TLBE) {
      const u32x4 ra = mkrs4(ws - 4096, 4096), rb = mkrs4(out, NQ * C * 2);
      asm volatile("s_mov_b32 m0, %0\n\ts_mov_b64 exec, 1\n\tbuffer_load_dword %1, %2, 0 offen lds\n\tbuffer_load_dword %1, %3, 0 offen lds\n\ts_mov_b64 exec, -1"
                   :: "s"(lb + LDS_SINK), "v"(vz), "s"(ra), "s"(rb) : "memory");
    }
    lds_wait(ctl + CT_WV, 1);
#pragma unroll
    for (int j = 0; j < 18; ++j) wvv[j] = ldb128<16>(rws, lane * 16 + (j & 3) * 1024, WVO_OFF + 54 * 1024 + (j >> 2) * 4096);
    GET_TAG();
    LAS const char *xr = lds + xslot + lane * 16, *xl = lds + xslot + 6144 + 4 * n;
    unsigned mo = wg * 8192 + 6144 + n * 64 + g * 16, tw = (unsigned)(tg >> (20 * (g & 1))) & 0xfffffu, sh = 12 * (g & 1), lse;
    asm volatile("" : "+v"(xr), "+v"(xl), "+v"(mo), "+v"(tw), "+v"(sh));
    __builtin_amdgcn_s_waitcnt(0x0f70);
    lds_spin(CT_XD, ctl, 1);
    {
      int cnt_, c_;
      unsigned t_;
      asm volatile("s_mov_b32 %1, 0x400000\n1:\n\tds_read_b32 %2, %10 offset:%13\n\tds_read_b128 %3, %11\n\tds_read_b128 %4, %11 offset:1024\n\t"
                   "ds_read_b128 %5, %11 offset:2048\n\tds_read_b128 %6, %11 offset:3072\n\tds_read_b128 %7, %11 offset:4096\n\t"
                   "ds_read_b128 %8, %11 offset:5120\n\tds_read_b32 %9, %12\n\ts_waitcnt lgkmcnt(7)\n\tv_readfirstlane_b32 %0, %2\n\t"
                   "s_cmp_ge_i32 %0, 3\n\ts_cbranch_scc1 2f\n\ts_sub_u32 %1, %1, 1\n\ts_cbranch_scc0 1b\n2:\n\ts_waitcnt lgkmcnt(0)"
                   : "=&s"(c_), "=&s"(cnt_), "=&v"(t_), "=&v"(b[0]), "=&v"(b[1]), "=&v"(b[2]), "=&v"(b[3]), "=&v"(b[4]), "=&v"(b[5]), "=&v"(lse)
                   : "v"(ctl), "v"(xr), "v"(xl), "i"(CT_XD) : "memory", "scc");
    }
    SB();
    f32x4 o[3] = {{0.f, 0.f, 0.f, 0.f}, {0.f, 0.f, 0.f, 0.f}, {0.f, 0.f, 0.f, 0.f}};
    tw |= (lse << sh) & 0xfff00000u;
    PROJECT(0);
    lds_inc(CT_ST, ctl);
  } else {
    COORDS();
    const int tq = (lane >> 2) & 3, tp = lane & 3;
    const bool pq = wg < 18;
    const bool pv = wg >= 18 && wg < NPR;
    const int pj = wg / 6, pu = wg % 6;
    if (w >= 4 && ((w + NT) & 3) == 0 && !pq) {
      unsigned k0, k1, k2;
      asm volatile("s_load_dword %0, %3, %5\n\ts_load_dword %1, %3, %6\n\ts_load_dword %2, %4, %7\n\ts_waitcnt lgkmcnt(0)"
                   : "=&s"(k0), "=&s"(k1), "=&s"(k2) : "s"(f0), "s"(f1), "s"((wg >> 3) * 3145728 + 1572860), "s"((wg >> 3) * 3145728 + 3145724),
                     "s"((wg >> 3) * 786432 + 786428) : "memory");
    }
    const u32x4 one = {0x3f803f80u, 0x3f803f80u, 0x3f803f80u, 0x3f803f80u};
    const int fq = ((n & 2) << 1) | ((n & 4) >> 1);
    LAS const char* rq0 = lds + PSCR + 24576 + 3072 * (n >> 3) + 128 * (n & 7) + 16 * (g ^ fq);
    LAS const char* rq1 = lds + PSCR + 24576 + 3072 * (n >> 3) + 128 * (n & 7) + 16 * ((4 + g) ^ fq);
    if (pq && w >= 2) {
      {
        waitvm<0>();
        lds_inc(CT_PA, ctl);
        if (w >= 4) {
          f32x4 sq = {0.f, 0.f, 0.f, 0.f}, rr = sq, cs = sq;
          const int ro = PSCR + 128 * (8 * g + tq) + 16 * (((w & 2) * 2 + tp) ^ ((tq & 2) << 1)) + 8 * (w & 1);
          u32x4 qv[6], af[6];
          lds_spin(CT_PA, ctl, 6);
#pragma unroll
          for (int s = 0; s < 6; ++s) {
            qv[s] = *(LAS const u32x4*)(((s & 1) ? rq1 : rq0) + (s >> 1) * 1024);
            af[s] = vtr2(lds + ro + s * 4096, lds + (ro ^ 32) + s * 4096 + 512);
          }
          SB();
#pragma unroll
          for (int s = 0; s < 6; ++s) { sq = mfma(one, qv[s], sq); rr = mfma(af[s], qv[s], rr); cs = mfma(af[s], one, cs); }
          const f32x4 qq = rr - (sq[0] * (1.f / C)) * cs;
          *(LAS u32x2*)(lds + LDS_QK + 4096 + ((w >> 1) & 1) * 1024 + lane * 16 + 8 * (w & 1)) = u32x2{cvtpk(qq[0], qq[1]), cvtpk(qq[2], qq[3])};
          lds_inc(CT_PB, ctl);
        }
      }
    }
    if (pq && w == 1) {
      {
        waitvm<0>();
        u32x4 kf4[4], qv[6];
        const int ko = LDS_QK + 128 * (((n & 4) << 2) | (n & 11)) + 16 * (g ^ ((n & 2) << 1));
#pragma unroll
        for (int i = 0; i < 4; ++i) kf4[i] = *(LAS const u32x4*)(lds + (ko ^ ((i & 1) * 64 + (i >> 1) * 32)) + (i >> 1) * 512);
        lds_spin(CT_PA, ctl, 6);
#pragma unroll
        for (int s = 0; s < 6; ++s) qv[s] = *(LAS const u32x4*)(((s & 1) ? rq1 : rq0) + (s >> 1) * 1024);
        const f32x4 z = {0.f, 0.f, 0.f, 0.f};
        f32x4 gq = z, sq = z;
#pragma unroll
        for (int s = 0; s < 6; ++s) { gq = mfma(qv[s], qv[s], gq); sq = mfma(one, qv[s], sq); }
        const float mean = sq[0] * (1.f / C);
        const float s2 = xrow16_sum(g == (n >> 2) ? sel4(gq, n & 3) : 0.f);
        float sc = __builtin_amdgcn_rsqf(fmaxf(fmaf(-mean, mean, s2 * (1.f / C)), 0.f) + EPS) * QSCALE;
        asm volatile("" : "+v"(sc));
        GET_TAG();
        lds_spin(CT_PB, ctl, 4);
        const u32x4 q0 = *(LAS const u32x4*)(lds + LDS_QK + 4096 + lane * 16), q1 = *(LAS const u32x4*)(lds + LDS_QK + 5120 + lane * 16);
        const f32x4 d0 = mfma(kf4[1], q1, mfma(kf4[0], q0, z)), d1 = mfma(kf4[3], q1, mfma(kf4[2], q0, z));
        const u32x4 xq = pack8(d0 * sc, d1 * sc);
        const unsigned ck = qk_check(xq, (unsigned)tg);
        asm volatile("" :: "v"(ck));
        {
          const unsigned so = QK_OFF + wg * QK_REC;
#pragma unroll
          for (int cp = 0; cp < 4; ++cp) {
            stb128(xq, rws, lane * 16 + so + cp * QK_COPY);
            stb32(ck, rws, lane * 4 + so + 1024 + cp * QK_COPY);
          }
          stb32((unsigned)tg, rws, HINT_OFF + lane * 128 + (pu >> 1) * 32 + pj * 8 + (pu & 1) * 4);
          lds_put(CT_PD, ctl, 4);
        }
      }
    }
    if (pv) {
      waitvm<0>();
      lds_inc(CT_PA, ctl);
      if (w >= 4) {
        const int ro = PSCR + 128 * (8 * g + tq) + 16 * ((2 * (w - 4) + (tp >> 1)) ^ ((tq & 2) << 1)) + 8 * (tp & 1);
        const int rv = PSCR + 24576 + 128 * n + 16 * (g ^ fq);
        f32x4 d0 = {0.f, 0.f, 0.f, 0.f}, d1 = d0;
        lds_spin(CT_PA, ctl, 7);
        const unsigned org = (unsigned)tstart + (max((int)(wall_clock64() - tstart) - LAND0, 0) >> LANDS);
#pragma unroll
        for (int s = 0; s < 6; ++s) {
          const u32x4 p = vtr2(lds + ro + s * 4096, lds + (ro ^ 32) + s * 4096 + 512);
          LAS const char* pd = lds + (rv ^ ((s & 1) * 64)) + (s >> 1) * 4096;
          d0 = mfma(*(LAS const u32x4*)pd, p, d0); d1 = mfma(*(LAS const u32x4*)(pd + 2048), p, d1);
        }
        const int fi = (pj - 3) * 24 + (w - 4) * 6 + pu;
        stb128(pack8(d0, d1), rws, lane * 16 + WVO_OFF + fi * 1024);
        lds_inc(CT_PD, ctl);
        GET_TAG();
        waitvm<0>();
        if (lane < 8) stb64(u32x2{(unsigned)tg, (unsigned)(tg >> 32) | org << 8}, rws, FLAG_OFF + lane * 768 + fi * 8);
      }
    }
    const f32x4 z = {0.f, 0.f, 0.f, 0.f};
    int full = 0;
#define NEED(u_) lds_need(CT_FULL, full, ctl, (u_))
    if (w >= 4) {
      const int brg = ((g & 1) << 1) | (g >> 1);
      const int fn = (((n >> 1) & 3) << 1) | ((n >> 3) & 1);
      const int fy = ((n >> 2) & 1) * 4 + ((n >> 1) & 1) * 3;
      const int orr0 = 128 * n + 16 * (brg ^ fn), orr1 = 128 * n + 16 * ((4 + brg) ^ fn);
      const int ory0 = n < 8 ? CRS + 384 * n + 16 * (brg ^ fy) : LDS_CTL + CT_ONE, ory1 = n < 8 ? CRS + 384 * n + 16 * ((4 + brg) ^ fy) : LDS_CTL + CT_ONE;
      const int yon = n < 8 ? 1 : 0;
      const int opk = LDS_PK + 320 * g + 8 * n, ork = LDS_PK + 320 * (2 * (g >> 1) + (n >> 3)) + 256 + 8 * (n & 7) + 4 * (g & 1);
      u32x4 qk[6];
      f32x4 ref, qs;
      LAS const char *px0, *px1, *py0, *py1;
#define AT_SLOT(o_) do { px0 = lds + orr0 + (o_); px1 = lds + orr1 + (o_); py0 = lds + ory0 + __mul24(yon, (o_)); py1 = lds + ory1 + __mul24(yon, (o_)); \
      asm volatile("" : "+v"(px0), "+v"(px1), "+v"(py0), "+v"(py1)); } while (0)
#define RD_K(k_, i_) do { LAS const char* p_ = (((k_) & 1) ? px1 : px0) + ((k_) >> 1) * PART; \
    yv[i_] = *(LAS const u32x4*)((((k_) & 1) ? py1 : py0) + ((k_) >> 1) * 128); \
    xa[i_] = *(LAS const u32x4*)(p_); xb[i_] = *(LAS const u32x4*)(p_ + 2048); } while (0)
#define MF_STAT(i_) do { cy = mfma(yv[i_], yv[i_], cy); ca = mfma(yv[i_], xa[i_], ca); cb = mfma(yv[i_], xb[i_], cb); \
    ga = mfma(xa[i_], xa[i_], ga); gb = mfma(xb[i_], xb[i_], gb); } while (0)
#define MF_S(k_, i_) do { sy = mfma(qk[k_], yv[i_], sy); sa = mfma(qk[k_], xa[i_], sa); sb = mfma(qk[k_], xb[i_], sb); } while (0)
#define STATS(st, rec) do { \
      const int r = n & 3; \
      const float yy = half_copy<true>(sel4(cy, r)); \
      const f32x2 Y = swap32(yy, yy), X = swap32(sel4(ca, r), sel4(cb, r)), G = swap32(sel4(ga, r), sel4(gb, r)); \
      const float q1_ = fmaf(2.f, X[0], Y[0]) + (n < 8 ? G[0] : G[1]);       \
      const f32x2 Q = swap16(q1_, q1_); \
      const float mn_ = (X[1] + Y[1]) * (1.f / C); \
      const float var = fmaxf(fmaf(-mn_, mn_, fmaf((n & 4) ? Q[1] : Q[0], 1.f / C, EPS)), EPS), rs_ = __builtin_amdgcn_rsqf(var); \
      const f32x2 R = swap32(rs_, rs_), Mn = swap32(mn_, mn_); \
      st = f32x4{R[0], Mn[0], R[1], Mn[1]}; \
      const float v_ = (g & 1) ? var * rs_ : mn_, h_ = bfhi(__float_as_uint(v_)); \
      rec = cvtpk(h_, v_ - h_); \
    } while (0)
#define LOGITS(sv, st, base_) do { \
      _Pragma("unroll") for (int r = 0; r < 4; ++r) { \
        const float s0 = half_copy<true>(sy[r]); \
        sv[r] = fmaf(fmaf(-st[1], qs[r], sa[r] + s0), st[0], -(base_)[r]); \
        sv[4 + r] = fmaf(fmaf(-st[3], qs[r], sb[r] + s0), st[2], -(base_)[r]); \
      } \
    } while (0)
#define SV_MAX(sv) fmaxf(fmaxf(fmaxf(sv[0], sv[1]), fmaxf(sv[2], sv[3])), fmaxf(fmaxf(sv[4], sv[5]), fmaxf(sv[6], sv[7])))
#define PUBLISH(sv, st, rec, pk_, word_) do { \
      *(LAS unsigned*)(lds + ork + (pk_) * PK_B) = rec; \
      *(LAS u32x2*)(lds + opk + (pk_) * PK_B) = u32x2{cvtpk(sv[0] * st[0], sv[1] * st[0]), cvtpk(sv[2] * st[0], sv[3] * st[0])}; \
      *(LAS u32x2*)(lds + opk + (pk_) * PK_B + 128) = u32x2{cvtpk(sv[4] * st[2], sv[5] * st[2]), cvtpk(sv[6] * st[2], sv[7] * st[2])}; \
      lds_put(CT_PRDY, ctl + 4 * (pk_), (word_)); \
    } while (0)
      int s = (w + NT - 1) & 3, slot = s + 4, pk = s + 4, ncon = 0;
      {
        u32x4 yv[12], xa[12], xb[12];
        f32x4 st0, st1;
        unsigned rec0, rec1;
#pragma unroll
        for (int h = 0; h < 2; ++h) {
          f32x4 cy = z, ga = z, ca = z, gb = z, cb = z;
          AT_SLOT((s + 4 * h) * SLOT);
          lds_wait(ctl + CT_FULL, 3 * (s + 4 * h) + 3);
#pragma unroll
          for (int k = 0; k < 6; ++k) RD_K(k, 6 * h + k);
          lds_inc(CT_FREE + 16 * h, ctl + 4 * s);
          SB();
#pragma unroll
          for (int k = 0; k < 6; ++k) MF_STAT(6 * h + k);
          SB();
          if (h == 0) { STATS(st0, rec0); asm volatile("" : "+v"(st0), "+v"(rec0)); } else { STATS(st1, rec1); asm volatile("" : "+v"(st1), "+v"(rec1)); }
          SB();
        }
        lds_wait(ctl + CT_QK, 3);
#pragma unroll
        for (int k = 0; k < 6; ++k) qk[k] = *(LAS const u32x4*)(lds + LDS_QK + k * 1024 + lane * 16);
        float s0v[8], s1v[8];
        {
          f32x4 sy = z, sa = z, sb = z;
#pragma unroll
          for (int k = 0; k < 6; ++k) MF_S(k, k);
#pragma unroll
          for (int r = 0; r < 4; ++r) qs[r] = half_copy<false>(sy[r]);
          LOGITS(s0v, st0, z);
        }
        {
          f32x4 sy = z, sa = z, sb = z;
#pragma unroll
          for (int k = 0; k < 6; ++k) MF_S(k, 6 + k);
          LOGITS(s1v, st1, z);
        }
#pragma unroll
        for (int r = 0; r < 4; ++r) ref[r] = row_max(fmaxf(fmaxf(s0v[r], s0v[4 + r]), fmaxf(s1v[r], s1v[4 + r])));
        if (n < 4) __builtin_amdgcn_ds_fmaxf((LAS float*)(ctl + CT_MAX + 16 * g + 4 * n), sel4(ref, n), __ATOMIC_RELAXED, __HIP_MEMORY_SCOPE_WORKGROUP, false);
        lds_inc(CT_QR, ctl);
        lds_spin(CT_QR, ctl, 4);
        ref = *(LAS const f32x4*)(ctl + CT_MAX + 16 * g);
#pragma unroll
        for (int j = 0; j < 8; ++j) s0v[j] = __builtin_amdgcn_exp2f(s0v[j] - ref[j & 3]);
        PUBLISH(s0v, st0, rec0, s, 2 * s + 2);
#pragma unroll
        for (int j = 0; j < 8; ++j) s1v[j] = __builtin_amdgcn_exp2f(s1v[j] - ref[j & 3]);
        if (pk >= NPK) { pk -= NPK; ncon = 3; lds_spin(CT_PCON, ctl + 4 * pk, 3); }
        s += 4;
        PUBLISH(s1v, st1, rec1, pk, 2 * s + 2);
      }
      float thr = 16.f;
      f32x4 nref = -ref;
#pragma unroll 1
      for (;;) {
        s += 4; slot += 4; slot -= slot >= NS ? NS : 0; pk += 4;
        if (pk >= NPK) { pk -= NPK; ncon += 3; }
        if (s >= NT) break;
        f32x4 sy = z, cy = z, sa, ga = z, ca = z, sb, gb = z, cb = z;
        u32x4 yv[6], xa[6], xb[6];
        unsigned fl_, word = 2 * s + 2;
        AT_SLOT(slot * SLOT);
        LAS char *pw = lds + opk + pk * PK_B, *prc = lds + ork + pk * PK_B, *pfl = ctl + 4 * pk, *pfr = ctl + 4 * slot;
        asm volatile("" : "+v"(pw), "+v"(prc), "+v"(pfl), "+v"(pfr), "+v"(word));
#define RD_X "ds_read_b32 %2, %7 offset:%11\n\tds_read_b128 %3, %8 offset:%12\n\tds_read_b128 %4, %9 offset:%12\n\t" \
             "ds_read_b128 %5, %8 offset:%13\n\tds_read_b128 %6, %9 offset:%13\n"
#define RD_IN(p_) "v"(ctl), "v"(px0), "v"(px1), "s"(3 * s + (p_) + 1), "i"(CT_FULL), "i"(PART * (p_)), "i"(PART * (p_) + 2048)
#define ISSUE(p_) do { int d0_, d1_; \
        asm volatile(RD_X : "=&s"(d0_), "=&s"(d1_), "=&v"(fl_), "=&v"(xa[2 * (p_)]), "=&v"(xa[2 * (p_) + 1]), "=&v"(xb[2 * (p_)]), "=&v"(xb[2 * (p_) + 1]) \
                     : RD_IN(p_) : "memory"); } while (0)
#define CHECK(p_) do { int cnt_; \
        asm volatile("s_mov_b32 %1, 0x400000\n\ts_branch 2f\n1:\n\t" RD_X "2:\n\ts_waitcnt lgkmcnt(4)\n\tv_readfirstlane_b32 %0, %2\n\t" \
                     "s_cmp_ge_i32 %0, %10\n\ts_cbranch_scc1 3f\n\ts_cmp_ge_i32 %10, %14\n\ts_cbranch_scc1 4f\n\ts_sleep %15\n4:\n\t" \
                     "s_sub_u32 %1, %1, 1\n\ts_cbranch_scc0 1b\n3:\n\ts_waitcnt lgkmcnt(2)" \
                     : "=&s"(full), "=&s"(cnt_), "+v"(fl_), "+v"(xa[2 * (p_)]), "+v"(xa[2 * (p_) + 1]), "+v"(xb[2 * (p_)]), "+v"(xb[2 * (p_) + 1]) \
                     : RD_IN(p_), "s"(3 * NT - 2), "i"(CALM) : "memory", "scc"); \
        asm volatile("" : "+s"(full)); } while (0)
#define GOT_B(p_) do { waitlds<0>(); asm volatile("" : "+v"(xb[2 * (p_)]), "+v"(xb[2 * (p_) + 1])); } while (0)
#define MF_A(k_) do { ca = mfma(yv[k_], xa[k_], ca); ga = mfma(xa[k_], xa[k_], ga); sa = mfma(qk[k_], xa[k_], sa); } while (0)
#define MF_B(k_) do { cb = mfma(yv[k_], xb[k_], cb); gb = mfma(xb[k_], xb[k_], gb); sb = mfma(qk[k_], xb[k_], sb); } while (0)
        const unsigned pcv = *(volatile LAS unsigned*)(pfl + CT_PCON);
        NEED(3 * s);
#pragma unroll
        for (int k = 0; k < 6; ++k) yv[k] = *(LAS const u32x4*)(((k & 1) ? py1 : py0) + (k >> 1) * 128);
        int con = __builtin_amdgcn_readfirstlane(pcv);
        SB();
#pragma unroll
        for (int k = 0; k < 6; ++k) sy = mfma(qk[k], yv[k], sy);
        asm volatile("" : "+v"(sy));
        SB();
        lds_need(CT_PCON, con, pfl, ncon);
        ISSUE(0);
        SB();
#pragma unroll
        for (int k = 0; k < 6; ++k) cy = mfma(yv[k], yv[k], cy);
#pragma unroll
        for (int r = 0; r < 4; ++r) sa[r] = half_copy<true>(sy[r]);
        const float yy = half_copy<true>(sel4(cy, n & 3));
        f32x2 Y = swap32(yy, yy);
        asm volatile("" : "+v"(Y), "+v"(sa));
        SB();
        CHECK(0);
        sb = sa;
        MF_A(0);
        asm volatile("" : "+v"(ca), "+v"(ga), "+v"(sa));
        SB();
        GOT_B(0);
        ISSUE(1);
        SB();
        MF_B(0); MF_A(1); MF_B(1);
        asm volatile("" : "+v"(ca), "+v"(ga), "+v"(sa), "+v"(cb), "+v"(gb), "+v"(sb));
        SB();
        CHECK(1);
        MF_A(2);
        asm volatile("" : "+v"(ca), "+v"(ga), "+v"(sa));
        SB();
        GOT_B(1);
        ISSUE(2);
        SB();
        MF_B(2); MF_A(3); MF_B(3);
        asm volatile("" : "+v"(ca), "+v"(ga), "+v"(sa), "+v"(cb), "+v"(gb), "+v"(sb));
        SB();
        CHECK(2);
        ca = mfma(yv[5], xa[5], mfma(yv[4], xa[4], ca));
        ga = mfma(xa[5], xa[5], mfma(xa[4], xa[4], ga));
        asm volatile("" : "+v"(ca), "+v"(ga));
        SB();
        GOT_B(2);
        cb = mfma(yv[5], xb[5], mfma(yv[4], xb[4], cb));
        asm volatile("" : "+v"(cb));
        SB();
        lds_inc(CT_FREE, pfr);
        gb = mfma(xb[4], xb[4], gb);
        float ua = sel4(ca, n & 3);
        asm volatile("" : "+v"(gb), "+v"(ua));
        SB();
        gb = mfma(xb[5], xb[5], gb);
        float va = sel4(ga, n & 3);
        asm volatile("" : "+v"(gb), "+v"(va));
        SB();
        sa = mfma(qk[4], xa[4], sa);
        float ub = sel4(cb, n & 3);
        asm volatile("" : "+v"(sa), "+v"(ub));
        SB();
        sa = mfma(qk[5], xa[5], sa);
        const f32x2 X = swap32(ua, ub);
        float mn_ = (X[1] + Y[1]) * (1.f / C), q1_ = fmaf(2.f, X[0], Y[0]);
        asm volatile("" : "+v"(sa), "+v"(mn_), "+v"(q1_));
        SB();
        sb = mfma(qk[4], xb[4], sb);
        float vb = sel4(gb, n & 3);
        asm volatile("" : "+v"(sb), "+v"(vb));
        SB();
        sb = mfma(qk[5], xb[5], sb);
        const f32x2 Mn = swap32(mn_, mn_), G = swap32(va, vb);
        q1_ += n < 8 ? G[0] : G[1];
        const f32x2 Q = swap16(q1_, q1_);
        sa -= Mn[0] * qs;
        const float var = fmaxf(fmaf(-mn_, mn_, fmaf((n & 4) ? Q[1] : Q[0], 1.f / C, EPS)), EPS), rs_ = __builtin_amdgcn_rsqf(var);
        sb -= Mn[1] * qs;
        const float v_ = (g & 1) ? var * rs_ : mn_, h_ = bfhi(__float_as_uint(v_));
        *(LAS unsigned*)prc = cvtpk(h_, v_ - h_);
        const f32x2 R = swap32(rs_, rs_);
        sa = sa * R[0] + nref; sb = sb * R[1] + nref;
        const float tm = fmaxf(fmaxf(fmaxf(fmaxf(sa[0], sa[1]), sa[2]), fmaxf(fmaxf(sa[3], sb[0]), sb[1])), fmaxf(sb[2], sb[3]));
        if (__builtin_expect(__builtin_amdgcn_ballot_w64(!(tm <= thr)) != 0ull, 0)) {
#pragma unroll
          for (int r = 0; r < 4; ++r) {
            const float mx = row_max(fmaxf(sa[r], sb[r])), dm = mx <= 16.f ? 0.f : mx;
            nref[r] -= dm; sa[r] -= dm; sb[r] -= dm;
          }
          thr = __builtin_nanf("");
          word |= 1u;
          if (n == 0) *(LAS f32x4*)(lds + LDS_PK + pk * PK_B + 1280 + 16 * g) = -nref;
        }
#pragma unroll
        for (int r = 0; r < 4; ++r) { sa[r] = __builtin_amdgcn_exp2f(sa[r]); sb[r] = __builtin_amdgcn_exp2f(sb[r]); }
        float ra = R[0], rb = R[1];
        asm volatile("" : "+v"(ra), "+v"(rb));
        sa *= ra; sb *= rb;
        *(LAS u32x2*)pw = u32x2{cvtpk(sa[0], sa[1]), cvtpk(sa[2], sa[3])};
        *(LAS u32x2*)(pw + 128) = u32x2{cvtpk(sb[0], sb[1]), cvtpk(sb[2], sb[3])};
        lds_put(CT_PRDY, pfl, word);
        SB();
      }
      GET_TAG();
      LAS char* fin = lds + xslot + 8192;
      unsigned qres = (unsigned)__builtin_amdgcn_raw_buffer_load_b16(mkrs(q, NQ * C * 2), dout * 2, 0, 0) << 16;
      unsigned ao = ((tid & 255) >> 1) * 8192 + (tid & 1) * 16 + dst * 32, tw = (unsigned)(tg >> (20 * (tid & 1))), sh = 12 * (tid & 1), oo = dout * 2;
      LAS char *fw = fin + (((lane & 8) | ((g & 1) << 2) | (g & 2)) + (lane & 1)) * 16 + 4 * (w - 4), *fr = fin + (2 * (lane & 7) + ((lane >> 3) & 1)) * 16;
      float v7 = 0.f;
      unsigned fi_ = 8u;
      const unsigned t0s = lds_get(ctl + CT_T0);
      unsigned gate = (t0s ? t0s : (unsigned)tstart) + RGATE;
      const RSRC rm = rws;
      u32x4 ro = mkrs4(out, NQ * C * 2);
      unsigned long long wm = 0x0303030303030303ull;
      asm volatile("" : "+v"(ao), "+v"(tw), "+v"(sh), "+v"(oo), "+v"(fw), "+v"(fr), "+v"(fin), "+v"(v7), "+v"(qres), "+v"(fi_), "+s"(ro), "+s"(wm));
      lds_wait(ctl + CT_XD, 1);
      lds_spin(CT_ST, ctl, 4);
      gate += max((int)((unsigned)wall_clock64() + OWNG - gate), 0);
      float org = *(LAS const float*)(lds + xslot + 6144 + 4 * dq);
      *(LAS float*)(fin + 256 + 4 * (w - 4)) = org;
#pragma unroll 1
      for (int spin = 0; spin < 1024; ++spin)
        if ((int)((unsigned)wall_clock64() - gate) >= 0) break;
      u32x4 A = ldb128<16>(rm, ao, 0), B = ldb128<16>(rm, ao, 128 * 8192);
#pragma unroll 1
      for (int spin = 0; spin < (1 << 18); ++spin) {
        unsigned xa_ = (A[3] ^ tw) << 12, xb_ = (B[3] ^ tw) << 12;
        if (__builtin_expect(__builtin_amdgcn_ballot_w64((xa_ | xb_) != 0u) == 0ull, 1)) break;
        xa_ |= __builtin_amdgcn_mov_dpp(xa_, 0xB1, 0xf, 0xf, true); xb_ |= __builtin_amdgcn_mov_dpp(xb_, 0xB1, 0xf, 0xf, true);
        asm volatile("" ::: "memory");
        if (xa_ != 0u) A = ldb128<16>(rm, ao, 0);
        if (xb_ != 0u) B = ldb128<16>(rm, ao, 128 * 8192);
      }
      unsigned la = (A[3] & 0xfff00000u) >> sh, lb_ = (B[3] & 0xfff00000u) >> sh;
      la |= __builtin_amdgcn_mov_dpp(la, 0xB1, 0xf, 0xf, true); lb_ |= __builtin_amdgcn_mov_dpp(lb_, 0xB1, 0xf, 0xf, true);
      float da = __uint_as_float(la) - org, db = __uint_as_float(lb_) - org;
      if (__builtin_expect(__builtin_amdgcn_ballot_w64(!(da <= 64.f) || !(db <= 64.f)) != 0ull, 0)) {
        org = xrow16_max(row_max(fmaxf(__uint_as_float(la), __uint_as_float(lb_))));
        da = __uint_as_float(la) - org; db = __uint_as_float(lb_) - org;
        *(LAS float*)(fin + 256 + 4 * (w - 4)) = org;
        fi_ = 9u;
      }
      const float wa = __builtin_amdgcn_exp2f(da), wb = __builtin_amdgcn_exp2f(db);
#define MIX(f_, i_) fmaf(f_(A[i_]), wa, f_(B[i_]) * wb)
      const f32x2 p0 = swap32(MIX(bflo, 0), MIX(bfhi, 0)), p1 = swap32(MIX(bflo, 1), MIX(bfhi, 1));
      const f32x2 p2 = swap32(MIX(bflo, 2), MIX(bfhi, 2)), p3 = swap32(wa + wb, v7);
      const f32x2 r0 = swap16(p0[0] + p0[1], p1[0] + p1[1]), r1 = swap16(p2[0] + p2[1], p3[0] + p3[1]);
      float c0 = r0[0] + r0[1], c1 = r1[0] + r1[1];
      asm volatile("s_nop 1\n\tv_add_f32_dpp %0, %0, %0 row_ror:8 row_mask:0xf bank_mask:0x3\n\tv_add_f32_dpp %0, %1, %1 row_ror:8 row_mask:0xf bank_mask:0xc\n\t"
                   "s_nop 1\n\tv_add_f32_dpp %0, %0, %0 row_shl:4 row_mask:0xf bank_mask:0x5\n\t"
                   "s_nop 1\n\tv_add_f32_dpp %0, %0, %0 quad_perm:[2,3,0,1] row_mask:0xf bank_mask:0x5\n\t"
                   "s_mov_b64 exec, %3\n\tds_write_b32 %2, %0\n\ts_mov_b64 exec, 1\n\tds_add_u32 %4, %5 offset:%6\n\ts_mov_b64 exec, -1"
                   : "+v"(c0) : "v"(c1), "v"(fw), "s"(wm), "v"(ctl), "v"(fi_), "i"(CT_FIN) : "memory");
      if (w == 4) {
        f32x4 a0, l0;
        unsigned cf;
        int cnt_, c_;
        asm volatile("s_mov_b32 %1, 0x4000000\n1:\n\tds_read_b32 %2, %5 offset:%8\n\tds_read_b128 %3, %6\n\tds_read_b128 %4, %7 offset:192\n\t"
                     "s_waitcnt lgkmcnt(2)\n\tv_readfirstlane_b32 %0, %2\n\ts_cmp_ge_i32 %0, 32\n\ts_cbranch_scc1 2f\n\t"
                     "s_sub_u32 %1, %1, 1\n\ts_cbranch_scc0 1b\n2:\n\ts_waitcnt lgkmcnt(0)"
                     : "=&s"(c_), "=&s"(cnt_), "=&v"(cf), "=&v"(a0), "=&v"(l0) : "v"(ctl), "v"(fr), "v"(fin), "i"(CT_FIN) : "memory", "scc");
        asm volatile("" : "+s"(c_));
        if (__builtin_expect(c_ != 32, 0)) {
          f32x4 m0 = *(LAS const f32x4*)(fin + 256);
          const float mm = fmaxf(fmaxf(m0[0], m0[1]), fmaxf(m0[2], m0[3]));
#pragma unroll
          for (int p = 0; p < 4; ++p) m0[p] = __builtin_amdgcn_exp2f(m0[p] - mm);
          a0 *= m0; l0 *= m0;
        }
        const float o = __uint_as_float(qres) + ((a0[0] + a0[2]) + (a0[1] + a0[3])) * __builtin_amdgcn_rcpf((l0[0] + l0[2]) + (l0[1] + l0[3]));
        const unsigned ob = cvtpk(o, __uint_as_float(__builtin_amdgcn_mov_dpp(__float_as_uint(o), 0xB1, 0xf, 0xf, true)));
        asm volatile("s_mov_b64 exec, 0x1515\n\tbuffer_store_dword %0, %1, %2, 0 offen sc1\n\ts_mov_b64 exec, -1" :: "v"(ob), "v"(oo), "s"(ro) : "memory");
        if (wg == 0 && lane == 0) stb64(u32x2{(unsigned)tg, (unsigned)(tg >> 32)}, rws, EP_OFF);
      }
    } else {
      const int ftr = ((g & 1) << 2) | ((tq >> 1) << 1) | (g >> 1);
      const int fty = (g & 1) * 4 + (tq >> 1) * 3;
      int otr[4], oty[4];
#pragma unroll
      for (int t = 0; t < 4; ++t) {
        otr[t] = (w - 1) * PART + 128 * (4 * g + tq) + 16 * ((2 * t + (tp >> 1)) ^ ftr) + 8 * (tp & 1);
        oty[t] = CRS + (w - 1) * 128 + 384 * (4 * (g & 1) + tq) + 16 * ((2 * t + (tp >> 1)) ^ fty) + 8 * (tp & 1);
      }
      int opk = LDS_PK + 320 * tp + 8 * (4 * g + tq), ork = LDS_PK + 320 * (g >> 1) + 256 + 8 * (4 * (g & 1) + tq);
      asm volatile("" : "+v"(opk), "+v"(ork), "+v"(oty[0]), "+v"(oty[1]), "+v"(oty[2]), "+v"(oty[3]));
#define RD_T(ta, ty, so_) _Pragma("unroll") for (int t_ = 0; t_ < 4; ++t_) { \
    ta[t_] = vtr2(lds + otr[t_] + (so_), lds + otr[t_] + (so_) + 2048); ty[t_] = vtr(lds + oty[t_] + (so_)); }
      f32x4 acc[4] = {z, z, z, z}, tac = z;
#define RD_P(o_) \
      pf = vtr2(lds + opk + (o_), lds + opk + (o_) + 128); \
      sf = vtr2(lds + ork + (o_), lds + ork + (o_) + 640)
#define TWICE(ty) asm volatile("s_nop 2"); _Pragma("unroll") for (int t = 0; t < 4; ++t) { u32x2 lo_, hi_; \
      asm volatile("v_mov_b64 %0, %2\n\tv_mov_b64 %1, %2" : "=&v"(lo_), "=&v"(hi_) : "v"(ty[t])); t2[t] = u32x4{lo_[0], lo_[1], hi_[0], hi_[1]}; } \
      asm volatile("s_nop 1" : "+v"(t2[0]), "+v"(t2[1]), "+v"(t2[2]), "+v"(t2[3]))
#define MF_PX(ta) \
      tac = mfma(sf, pf, tac); \
      _Pragma("unroll") for (int t = 0; t < 4; ++t) acc[t] = mfma(ta[t], pf, acc[t])
#define MF_PY() \
      _Pragma("unroll") for (int t = 0; t < 4; ++t) acc[t] = mfma(t2[t], pf, acc[t])
#define MOVE_REF(word_, o_) do { moved |= (word_) & 1; if (__builtin_expect(moved, 0)) { \
            const float m_ = *(LAS const float*)(lds + (((word_) & 1) ? LDS_PK + (o_) + 1280 : LDS_CTL + CT_MAX) + 4 * n); \
            Mtop = fmaxf(Mtop, m_); \
            const bool drop = m_ < Mtop - 64.f; \
            const float mn = drop ? M : m_, fa = __builtin_amdgcn_exp2f(M - mn); \
            M = mn; \
            _Pragma("unroll") for (int t = 0; t < 4; ++t) acc[t] *= fa; \
            tac *= fa; \
            if (drop) pf = u32x4{0u, 0u, 0u, 0u}; \
          } } while (0)
      const unsigned flo = FLAG_OFF + (wg & 7) * 768 + (w - 1) * 144 + (lane < 36 ? lane : 35) * 8;
      float M, Mtop;
      int moved = 0;
      u32x4 wvv[18], pf, sf, t2[4];
      {
        u32x4 ta[8][4], xx[6];
        u32x2 ty[8][4], fl;
        unsigned cc[6];
        const int qo = QK_OFF + (wg & 3) * QK_COPY + (2 * w - 2) * QK_REC;
#define QK_LOAD() _Pragma("unroll") for (int j = 0; j < 6; ++j) { \
          xx[j] = ldb128<16>(rws, lane * 16, qo + ((j >> 1) * 6 + (j & 1)) * QK_REC); \
          cc[j] = __builtin_amdgcn_raw_buffer_load_b32(rws, lane * 4, qo + ((j >> 1) * 6 + (j & 1)) * QK_REC + 1024, 16); }
#pragma unroll
        for (int u = 0; u < 8; ++u) {
          if (u == QKAT) {
            GET_TAG();
#pragma unroll 1
            for (int r = 0; r < 8; ++r) {
#pragma unroll 1
              for (int spin = 0; spin < 128; ++spin) {
                if ((int)(wall_clock64() - tstart) >= GATE + r * HSTEP) break;
                __builtin_amdgcn_s_sleep(1);
              }
              u32x8 hv;
              asm volatile("s_load_dwordx8 %0, %1, %2\n\ts_waitcnt lgkmcnt(0)" : "=&s"(hv) : "s"(ws), "s"(HINT_OFF + ((wg & 7) + 8 * r) * 128 + (w - 1) * 32) : "memory");
              const unsigned t_ = (unsigned)tg;
              if ((((hv[0] ^ t_) | (hv[1] ^ t_) | (hv[2] ^ t_)) | ((hv[3] ^ t_) | (hv[4] ^ t_) | (hv[5] ^ t_))) == 0u) break;
            }
            if (wg < NPR) lds_wait(ctl + CT_PD, 4);
            QK_LOAD();
#pragma unroll 1
            for (int spin = 0; spin < (1 << 18); ++spin) {
              unsigned df = 0u;
#pragma unroll
              for (int j = 0; j < 6; ++j) df |= qk_check(xx[j], (unsigned)tg) ^ cc[j];
              if (__all(df == 0u)) break;
              asm volatile("" ::: "memory");
              QK_LOAD();
            }
#pragma unroll
            for (int h = 0; h < 2; ++h) {
              u32x4 sx;
#pragma unroll
              for (int i = 0; i < 4; ++i) sx[i] = add3(xx[h][i], xx[2 + h][i], xx[4 + h][i]);
              *(LAS u32x4*)(lds + LDS_QK + (2 * w - 2 + h) * 1024 + lane * 16) = sx;
            }
            lds_inc(CT_QK, ctl);
            SB();
          }
          lds_wait(ctl + CT_FULL, 3 * u + w);
          RD_T(ta[u], ty[u], u * SLOT);
          lds_inc(CT_FREE + 4 * u, ctl);
          SB();
        }
        fl = __builtin_bit_cast(u32x2, __builtin_amdgcn_raw_buffer_load_b64(rws, flo, 0, 16));
#pragma unroll
        for (int u = 0; u < 12; ++u) {
          const int word = lds_spin(CT_PRDY + 4 * (u % NPK), ctl, 2 * u + 2);
          if (u == 0) Mtop = M = *(LAS const float*)(ctl + CT_MAX + 4 * n);
          RD_P((u % NPK) * PK_B);
          if (u >= 8) MOVE_REF(word, (u % NPK) * PK_B);
          lds_inc(CT_PCON + 4 * (u % NPK), ctl);
          SB();
          MF_PX(ta[u & 7]);
          SB();
          TWICE(ty[u & 7]);
          MF_PY();
          asm volatile("" : "+v"(acc[0]), "+v"(acc[1]), "+v"(acc[2]), "+v"(acc[3]), "+v"(tac));
          SB();
#define FLAGS_UP() __all(fl[0] == (unsigned)tg && (fl[1] & 255u) == (unsigned)(tg >> 32))
          if (u == T0AT && w == 1 && FLAGS_UP()) {
            int d = (int)(fl[1] - ((unsigned)tstart << 8)) >> 8;
            d += __builtin_amdgcn_mov_dpp(d, XOR8, 0xf, 0xf, true); d += __builtin_amdgcn_mov_dpp(d, ROR4, 0xf, 0xf, true);
            d += __builtin_amdgcn_mov_dpp(d, 0x122, 0xf, 0xf, true); d += __builtin_amdgcn_mov_dpp(d, 0x121, 0xf, 0xf, true);
            lds_put(CT_T0, ctl, ((unsigned)tstart + min(max(d >> 4, -300), 300)) | 1u);
          }
          if (u < 4) {
            NEED(3 * (u + 8) + w);
            RD_T(ta[u], ty[u], ((u + 8) % NS) * SLOT);
            lds_inc(CT_FREE + 4 * ((u + 8) % NS), ctl);
            SB();
          }
        }
#pragma unroll 1
        for (int spin = 0; spin < (1 << 18); ++spin) {
          if (FLAGS_UP()) break;
          asm volatile("" ::: "memory");
          fl = __builtin_bit_cast(u32x2, __builtin_amdgcn_raw_buffer_load_b64(rws, flo, 0, 16));
        }
        asm volatile("" ::: "memory");
        if (w == 3) lds_put(CT_WV, ctl, 1);
      }
      int slot = 3, pk = 12 % NPK;
      unsigned rdy = 0u;
      const int kro[3] = {(w - 1) * 2048, (w == 3 ? 0 : w) * 2048, (w == 1 ? 2 : w - 2) * 2048};
#define RD_PACKET(z_) do { int cnt_; u32x2 p0_, p1_, p2_, p3_; \
        word = __builtin_amdgcn_readfirstlane(rdy); \
        asm volatile("s_mov_b32 %1, 0x400000\n1:\n\tds_read_b32 %2, %7 offset:%11\n\t" \
                     "ds_read_b64_tr_b16 %5, %9\n\tds_read_b64_tr_b16 %6, %9 offset:640\n\t" \
                     "ds_read_b64_tr_b16 %3, %8\n\tds_read_b64_tr_b16 %4, %8 offset:128\n\t" \
                     "s_cmp_ge_i32 %0, %10\n\ts_cbranch_scc1 2f\n\ts_waitcnt lgkmcnt(4)\n\tv_readfirstlane_b32 %0, %2\n\t" \
                     "s_cmp_ge_i32 %0, %10\n\ts_cbranch_scc1 2f\n\t" z_ "s_sub_u32 %1, %1, 1\n\ts_cbranch_scc0 1b\n2:" \
                     : "+s"(word), "=&s"(cnt_), "=&v"(rdy), "=&v"(p0_), "=&v"(p1_), "=&v"(p2_), "=&v"(p3_) \
                     : "v"(ctl + 4 * pk), "v"(lds + opk + pk * PK_B), "v"(lds + ork + pk * PK_B), "s"(2 * s + 2), "i"(CT_PRDY), "i"(CALM) : "memory", "scc"); \
        asm volatile("" : "+s"(word)); \
        pf = u32x4{p0_[0], p0_[1], p1_[0], p1_[1]}; sf = u32x4{p2_[0], p2_[1], p3_[0], p3_[1]}; } while (0)
#define GOT_PACKET() do { waitlds<0>(); asm volatile("" : "+v"(pf), "+v"(sf) : "v"(rdy)); MOVE_REF(word, pk * PK_B); } while (0)
#define RD_Y(t_, p_) do { const u32x2 lo_ = vtr(p_); asm volatile("" ::: "memory"); const u32x2 hi_ = vtr(p_); \
        t2[t_] = u32x4{lo_[0], lo_[1], hi_[0], hi_[1]}; } while (0)
      u32x4 ta[4];
      int s = 12, word;
      NEED(36 + w);
#pragma unroll
      for (int t = 0; t < 4; ++t) { ta[t] = vtr2(lds + otr[t] + 2 * SLOT, lds + otr[t] + 2 * SLOT + 2048); RD_Y(t, lds + oty[t] + 2 * SLOT); }
      lds_inc(CT_FREE + 8, ctl);
#pragma unroll 1
      for (; s < NT - 1; ++s) {
        RD_PACKET("s_sleep %12\n\t");
        LAS const char *px[4], *py[4];
#pragma unroll
        for (int t = 0; t < 4; ++t) { px[t] = lds + otr[t] + slot * SLOT; py[t] = lds + oty[t] + slot * SLOT; asm volatile("" : "+v"(px[t]), "+v"(py[t])); }
        NEED(3 * s + 3 + w);
        GOT_PACKET();
        SB();
        tac = mfma(sf, pf, tac);
        asm volatile("" : "+v"(tac));
        SB();
        lds_inc(CT_PCON, ctl + 4 * pk);
        pk = pk == NPK - 1 ? 0 : pk + 1;
#pragma unroll
        for (int t = 0; t < 4; ++t) { acc[t] = mfma(ta[t], pf, acc[t]); SB(); ta[t] = vtr2(px[t], px[t] + 2048); SB(); }
        rdy = *(volatile LAS unsigned*)(ctl + CT_PRDY + 4 * pk);
#pragma unroll
        for (int t = 0; t < 4; ++t) { acc[t] = mfma(t2[t], pf, acc[t]); SB(); RD_Y(t, py[t]); SB(); }
        lds_inc(CT_FREE, ctl + 4 * slot);
#define WV_LOAD(j_) wvv[j_] = ldb128<16>(rws, lane * 16 + ((j_) & 1) * 1024, WVO_OFF + (w - 1) * 18432 + ((j_) / 6) * 6144 + kro[((j_) % 6) >> 1])
        if ((unsigned)(s - WVS) < WV1) {
#pragma unroll
          for (int u = 0; u < WV1; ++u)
            if (s - WVS == u) WV_LOAD(u);
        }
        if (s == NT - 3) {
#pragma unroll
          for (int j = WV1; j < 18; ++j) WV_LOAD(j);
        }
        slot = slot == NS - 1 ? 0 : slot + 1;
      }
      LAS char *xw = lds + xslot + lane * 16 + kro[0], *xl = lds + xslot + 6144 + 4 * n;
      LAS const char *xr1 = lds + xslot + lane * 16 + kro[1], *xr2 = lds + xslot + lane * 16 + kro[2];
      unsigned mo = wg * 8192 + (w - 1) * 2048 + n * 64 + g * 16, tw = (unsigned)(tg >> (20 * (g & 1))) & 0xfffffu, sh = 12 * (g & 1);
      asm volatile("" : "+v"(xw), "+v"(xl), "+v"(xr1), "+v"(xr2), "+v"(mo), "+v"(tw), "+v"(sh));
      waitvm<0>();
      RD_PACKET("");
      GOT_PACKET();
      SB();
      tac = mfma(sf, pf, tac);
      asm volatile("" : "+v"(tac));
      SB();
#pragma unroll
      for (int t = 0; t < 2; ++t) acc[t] = mfma(t2[t], pf, mfma(ta[t], pf, acc[t]));
      SB();
#pragma unroll
      for (int t = 2; t < 4; ++t) acc[t] = mfma(t2[t], pf, mfma(ta[t], pf, acc[t]));
      const float L = tac[2] + tac[3], rl = __builtin_amdgcn_rcpf(L), ct = -(tac[0] + tac[1]) * rl;
      const float lse = M + __builtin_amdgcn_logf(L);
      u32x4 b[6];
      b[0] = pack8(acc[0] * rl + ct, acc[1] * rl + ct);
      *(LAS u32x4*)xw = b[0];
      *(LAS float*)xl = lse;
#pragma unroll
      for (int j = 0; j < 4; ++j) { __builtin_amdgcn_sched_group_barrier(0x008, 1, 0); __builtin_amdgcn_sched_group_barrier(0x002, 3, 0); }
      SB();
      b[1] = pack8(acc[2] * rl + ct, acc[3] * rl + ct);
      *(LAS u32x4*)(xw + 1024) = b[1];
      lds_inc(CT_XD, ctl);
      SB();
      f32x4 o[3];
      unsigned xd;
#pragma unroll
      for (int t = 0; t < 3; ++t) o[t] = mfma(wvv[6 * t], b[0], z);
      asm volatile("" : "+v"(o[0]), "+v"(o[1]), "+v"(o[2]));
      SB();
#define RD_SLICES "ds_read_b32 %0, %7 offset:%10\n\tds_read_b128 %1, %8\n\tds_read_b128 %2, %8 offset:1024\n\tds_read_b128 %3, %9\n\tds_read_b128 %4, %9 offset:1024\n"
      asm volatile(RD_SLICES : "=&v"(xd), "=&v"(b[2]), "=&v"(b[3]), "=&v"(b[4]), "=&v"(b[5]) : "i"(0), "i"(0), "v"(ctl), "v"(xr1), "v"(xr2), "i"(CT_XD) : "memory");
      SB();
#pragma unroll
      for (int t = 0; t < 3; ++t) o[t] = mfma(wvv[6 * t + 1], b[1], o[t]);
      tw |= (__float_as_uint(lse) << sh) & 0xfff00000u;
      asm volatile("" : "+v"(tw), "+v"(o[0]), "+v"(o[1]), "+v"(o[2]));
      SB();
      {
        int cnt_, c_;
        asm volatile("s_mov_b32 %6, 0x400000\n\ts_branch 2f\n1:\n\t" RD_SLICES "2:\n\ts_waitcnt lgkmcnt(4)\n\tv_readfirstlane_b32 %5, %0\n\t"
                     "s_cmp_ge_i32 %5, 3\n\ts_cbranch_scc1 3f\n\ts_sub_u32 %6, %6, 1\n\ts_cbranch_scc0 1b\n3:\n\ts_waitcnt lgkmcnt(0)"
                     : "+v"(xd), "+v"(b[2]), "+v"(b[3]), "+v"(b[4]), "+v"(b[5]), "=&s"(c_), "=&s"(cnt_)
                     : "v"(ctl), "v"(xr1), "v"(xr2), "i"(CT_XD) : "memory", "scc");
      }
      SB();
      PROJECT(2);
      lds_inc(CT_ST, ctl);
    }
  }
}

#undef rws
#define WS_AT(size_) (((size_) - (64u << 20)) & ~(size_t)0x1fffff)
extern "C" void kernel_launch(void* const* d_in, const int* in_sizes, int n_in, void* d_out, int out_size, void* d_ws, size_t ws_size,
                              hipStream_t stream) {
  dino_main<<<NWG, 512, 0, stream>>>((const u16*)d_in[0], (const u16*)d_in[1], (const u16*)d_in[2], (const u16*)d_in[3], (const u16*)d_in[4],
                                     (const u16*)d_in[5], (const u16*)d_in[6], (char*)d_ws + WS_AT(ws_size) + 4096, (u16*)d_out);
}
```

```cpp
#include <hip/hip_runtime.h>
#include <hip/hip_bf16.h>
#include <stdint.h>

typedef unsigned short u16;
using u32x2 = __attribute__((ext_vector_type(2))) unsigned;
using u32x4 = __attribute__((ext_vector_type(4))) unsigned;
using u32x8 = __attribute__((ext_vector_type(8))) unsigned;
using f32x2 = __attribute__((ext_vector_type(2))) float;
using f32x4 = __attribute__((ext_vector_type(4))) float;
using s16x8 = __attribute__((ext_vector_type(8))) short;
using bf16x2_t = __attribute__((ext_vector_type(2))) __bf16;
typedef short v4i16_t __attribute__((ext_vector_type(4)));
#define LAS __attribute__((address_space(3)))

constexpr int C = 192, NQ = 16, W0 = 512, W1 = 256, NWG = 256, NW = 8;
constexpr float EPS = 1e-5f;
constexpr float QSCALE = 0.07216878364870322f * 1.4426950408889634f;

constexpr int PART = 4096, CRS = 3 * PART;
constexpr int SLOT = CRS + 3072, NS = 10;
constexpr int LDS_QK = NS * SLOT;
constexpr int NPK = 7;
constexpr int LDS_PK = LDS_QK, PK_B = 1344;
constexpr int LDS_CTL = LDS_PK + NPK * PK_B;
constexpr int LDS_SINK = LDS_CTL + 384;
constexpr int LDS_BYTES = LDS_SINK + 256;
constexpr int PSCR = 8 * SLOT;
constexpr int CT_FULL = 8, CT_WV = 4, CT_PA = 0, CT_PB = 12, CT_QR = 16, CT_PD = 20, CT_T0 = 24, CT_XD = 28, CT_TG = 32, CT_QK = 40, CT_ST = 44,
              CT_FREE = 48, CT_FIN = 88, CT_ONE = 112, CT_PRDY = 128, CT_PCON = 160, CT_MAX = 256;

constexpr int MB_BYTES = NWG * NWG * 32;
constexpr int EP_OFF = MB_BYTES;
constexpr int FLAG_OFF = EP_OFF + 512;
constexpr int WVO_OFF = EP_OFF + 8192;
constexpr int QK_OFF = WVO_OFF + C * C * 2;
constexpr int QK_REC = 1280, QK_COPY = 18 * QK_REC;
constexpr int HINT_OFF = QK_OFF + 4 * QK_COPY;
constexpr int WS_BYTES = HINT_OFF + 64 * 128;

#define GIVE 2
constexpr int NPR = 36;
struct Tabs { unsigned seq[112]; };
constexpr Tabs make_tabs() {
  Tabs t{};
  for (int i = 0; i < 112; ++i) t.seq[i] = i + 1;
  return t;
}
__device__ const Tabs tabs = make_tabs();
#define QKAT 3
#define WV1 6
#define WMIN 27
#define WSTEP 5
#define LSTEP 20
#define OWNG 20
#define PACE0 30
#define PACE1 1735
#define RGATE 1908
#define T0AT 3
#define LAND0 210
#define WVS 12
#define CALM 2
#define HSTEP 40
#define GATE 350
#define CODE_BYTES 25792

__device__ __forceinline__ unsigned cvtpk(float lo, float hi) {
  bf16x2_t v = {(__bf16)lo, (__bf16)hi};
  return __builtin_bit_cast(unsigned, v);
}
__device__ __forceinline__ float bflo(unsigned x) { return __builtin_bit_cast(float, x << 16); }
__device__ __forceinline__ float bfhi(unsigned x) { return __builtin_bit_cast(float, x & 0xffff0000u); }
template <int CTRL> __device__ __forceinline__ float dpp(float x) {
  return __builtin_bit_cast(float, __builtin_amdgcn_mov_dpp(__builtin_bit_cast(int, x), CTRL, 0xf, 0xf, true));
}
constexpr int XOR8 = 0x128, ROR4 = 0x124;
template <bool HI8> __device__ __forceinline__ float half_copy(float x) {
  const int v = __builtin_bit_cast(int, x);
  return __builtin_bit_cast(float, __builtin_amdgcn_update_dpp(v, v, XOR8, 0xf, HI8 ? 0xc : 0x3, false));
}
__device__ __forceinline__ float row_max(float x) {
  x = fmaxf(x, dpp<XOR8>(x)); x = fmaxf(x, dpp<ROR4>(x)); x = fmaxf(x, dpp<0x122>(x));
  return fmaxf(x, dpp<0x121>(x));
}
__device__ __forceinline__ f32x2 swap32(float a, float b) {
  auto s = __builtin_amdgcn_permlane32_swap(__float_as_uint(a), __float_as_uint(b), false, false);
  return f32x2{__uint_as_float(s[0]), __uint_as_float(s[1])};
}
__device__ __forceinline__ f32x2 swap16(float a, float b) {
  auto s = __builtin_amdgcn_permlane16_swap(__float_as_uint(a), __float_as_uint(b), false, false);
  return f32x2{__uint_as_float(s[0]), __uint_as_float(s[1])};
}
__device__ __forceinline__ float xrow16_max(float x) {
  auto s = __builtin_amdgcn_permlane16_swap(__float_as_uint(x), __float_as_uint(x), false, false);
  x = fmaxf(__uint_as_float(s[0]), __uint_as_float(s[1]));
  auto t = __builtin_amdgcn_permlane32_swap(__float_as_uint(x), __float_as_uint(x), false, false);
  return fmaxf(__uint_as_float(t[0]), __uint_as_float(t[1]));
}
__device__ __forceinline__ float xrow16_sum(float x) {
  auto s = __builtin_amdgcn_permlane16_swap(__float_as_uint(x), __float_as_uint(x), false, false);
  x = __uint_as_float(s[0]) + __uint_as_float(s[1]);
  auto t = __builtin_amdgcn_permlane32_swap(__float_as_uint(x), __float_as_uint(x), false, false);
  return __uint_as_float(t[0]) + __uint_as_float(t[1]);
}
__device__ __forceinline__ f32x4 mfma(u32x4 a, u32x4 b, f32x4 c) {
  return __builtin_amdgcn_mfma_f32_16x16x32_bf16(__builtin_bit_cast(s16x8, a), __builtin_bit_cast(s16x8, b), c, 0, 0, 0);
}
__device__ __forceinline__ u32x2 vtr(LAS const char* p) {
  return __builtin_bit_cast(u32x2, __builtin_amdgcn_ds_read_tr16_b64_v4i16((LAS v4i16_t*)p));
}
__device__ __forceinline__ u32x4 vtr2(LAS const char* p, LAS const char* q_) {
  const u32x2 a = vtr(p), b = vtr(q_);
  return u32x4{a[0], a[1], b[0], b[1]};
}
__device__ __forceinline__ float sel4(f32x4 v, int r) {
  const float a = (r & 1) ? v[1] : v[0], b = (r & 1) ? v[3] : v[2];
  return (r & 2) ? b : a;
}
using RSRC = __amdgpu_buffer_rsrc_t;
__device__ __forceinline__ RSRC mkrs(const void* p, int bytes) { return __builtin_amdgcn_make_buffer_rsrc(const_cast<void*>(p), (short)0, bytes, 0x00020000); }
__device__ __forceinline__ u32x4 mkrs4(const void* p, unsigned bytes) {
  const unsigned long long b = (unsigned long long)p;
  return u32x4{(unsigned)b, (unsigned)(b >> 32) & 0xffffu, bytes, 0x00020000u};
}
template <int AUX> __device__ __forceinline__ u32x4 ldb128(RSRC r, unsigned vo, int so) { return __builtin_bit_cast(u32x4, __builtin_amdgcn_raw_buffer_load_b128(r, vo, so, AUX)); }
#define SB() __builtin_amdgcn_sched_barrier(0)
#define RAW_BARRIER() asm volatile("s_waitcnt lgkmcnt(0)\n\ts_barrier" ::: "memory")
#define DMA(m0_, vo_, rs_, so_) asm volatile("s_mov_b32 m0, %0\n\ts_nop 0\n\tbuffer_load_dwordx4 %1, %2, %3 offen lds" :: "s"(m0_), "v"(vo_), "s"(rs_), "s"(so_) : "memory")
template <int N> __device__ __forceinline__ void waitlds() { __builtin_amdgcn_s_waitcnt(0xc07f | (N << 8)); }
template <int N> __device__ __forceinline__ void waitvm() { asm volatile("s_waitcnt vmcnt(%0)" :: "i"(N) : "memory"); }


__device__ __forceinline__ void lds_inc(int O, LAS char* p) {
  asm volatile("s_mov_b64 exec, 1\n\tds_add_u32 %0, %1 offset:%2\n\ts_mov_b64 exec, -1" :: "v"(p), "v"(1u), "i"(O) : "memory");
}
__device__ __forceinline__ void lds_put(int O, LAS char* p, unsigned v) {
  asm volatile("s_mov_b64 exec, 1\n\tds_write_b32 %0, %1 offset:%2\n\ts_mov_b64 exec, -1" :: "v"(p), "v"(v), "i"(O) : "memory");
}
__device__ __forceinline__ int lds_get(LAS char* p) {
  asm volatile("" ::: "memory");
  const unsigned v = __hip_atomic_load((LAS unsigned*)p, __ATOMIC_RELAXED, __HIP_MEMORY_SCOPE_WORKGROUP);
  asm volatile("" ::: "memory");
  return __builtin_amdgcn_readfirstlane(v);
}
__device__ __forceinline__ void lds_wait(LAS char* p, int target) {
#pragma unroll 1
  for (int spin = 0; spin < (1 << 22); ++spin) {
    if (lds_get(p) >= target) break;
    __builtin_amdgcn_s_sleep(1);
  }
}
__device__ __forceinline__ int lds_spin(int O, LAS char* p, int target) {
  int r, cnt = 1 << 22;
  unsigned t;
  asm volatile("1:\n\tds_read_b32 %1, %3 offset:%5\n\ts_waitcnt lgkmcnt(0)\n\tv_readfirstlane_b32 %0, %1\n\ts_cmp_ge_i32 %0, %4\n\ts_cbranch_scc1 2f\n\t"
               "s_sub_u32 %2, %2, 1\n\ts_cbranch_scc0 1b\n2:"
               : "=&s"(r), "=&v"(t), "+s"(cnt) : "v"(p), "s"(target), "i"(O) : "memory", "scc");
  return r;
}
__device__ __forceinline__ void lds_need(int O, int& have, LAS char* p, int target) {
  int cnt;
  unsigned t;
  asm volatile("s_cmp_ge_i32 %0, %4\n\ts_cbranch_scc1 2f\n\ts_mov_b32 %2, 0x400000\n"
               "1:\n\tds_read_b32 %1, %3 offset:%5\n\ts_waitcnt lgkmcnt(0)\n\tv_readfirstlane_b32 %0, %1\n\ts_cmp_ge_i32 %0, %4\n\ts_cbranch_scc1 2f\n\t"
               "s_sleep %6\n\ts_sub_u32 %2, %2, 1\n\ts_cbranch_scc0 1b\n2:"
               : "+s"(have), "=&v"(t), "=&s"(cnt) : "v"(p), "s"(target), "i"(O), "i"(CALM) : "memory", "scc");
  asm volatile("" : "+s"(have));
}
__device__ __forceinline__ u32x4 pack8(f32x4 a, f32x4 b) { return u32x4{cvtpk(a[0], a[1]), cvtpk(a[2], a[3]), cvtpk(b[0], b[1]), cvtpk(b[2], b[3])}; }

__device__ __forceinline__ void stb128(u32x4 v, RSRC r, unsigned o) { __builtin_amdgcn_raw_buffer_store_b128(v, r, o, 0, 16); }
__device__ __forceinline__ void stb64(u32x2 v, RSRC r, unsigned o) { __builtin_amdgcn_raw_buffer_store_b64(v, r, o, 0, 16); }
__device__ __forceinline__ void stb32(unsigned v, RSRC r, unsigned o) { __builtin_amdgcn_raw_buffer_store_b32(v, r, o, 0, 16); }
__device__ __forceinline__ unsigned qk_check(u32x4 x, unsigned tg) {
  const unsigned a = (x[0] ^ __builtin_amdgcn_alignbit(x[1], x[1], 11)) * 0x9e3779b1u + tg;
  const unsigned b = (x[2] + __builtin_amdgcn_alignbit(x[3], x[3], 19)) * 0x85ebca77u;
  return __builtin_amdgcn_alignbit(a, a, 15) ^ b ^ (x[1] + x[3]);
}
__device__ __forceinline__ unsigned add3(unsigned a, unsigned b, unsigned c) {
  return cvtpk((bflo(a) + bflo(b)) + bflo(c), (bfhi(a) + bfhi(b)) + bfhi(c));
}
__global__ __launch_bounds__(512, 2) void dino_main(const u16* q, const u16* f0, const u16* f1, const u16* wq, const u16* wk, const u16* wv,
                                                     const u16* wo, char* ws, u16* out) {
  __shared__ __attribute__((aligned(128))) char smem[LDS_BYTES];
  LAS char* lds = (LAS char*)smem;
  LAS char* ctl = lds + LDS_CTL;
  const unsigned lb = (unsigned)(unsigned long long)lds;
  const int w = __builtin_amdgcn_readfirstlane(threadIdx.x >> 6);
  unsigned long long pc0;
  asm volatile("s_getpc_b64 %0" : "=s"(pc0));
#define COORDS() \
  int tid = threadIdx.x, wg = blockIdx.x; \
  asm volatile("" : "+v"(tid), "+s"(wg)); \
  __builtin_assume(tid >= 0 && tid < 512); __builtin_assume(wg >= 0 && wg < NWG); \
  const int lane = tid & 63, n = lane & 15, g = lane >> 4;       \
    \
  const int dq = (wg & 7) * 2 + ((wg >> 4) & 1), dst = (wg & ~31) + (wg & 7) * 4 + ((wg >> 3) & 3); \
  const int dj = 6 * ((wg >> 5) & 1) + min(lane & 7, 5); \
  const int dout = dq * C + 48 * (wg >> 6) + 16 * (dj >> 2) + (wg & 8) + ((lane & 8) >> 1) + (dj & 3); \
  const int NT = wg < NPR ? 32 - GIVE : wg >= 40 && !(wg & 1) && (wg >> 3) < 5 + ((wg & 4) ? 8 : 10) * GIVE ? 33 : 32; \
  const int xslot = (NT >= 30 ? NT - 30 : NT - 20) * SLOT;       \
  (void)xslot; (void)n; (void)g; (void)dst; (void)dout; (void)NT
#define ARGS_IN() asm volatile("s_waitcnt lgkmcnt(0)" :: "s"(q), "s"(f0), "s"(f1), "s"(wq), "s"(wk), "s"(wv), "s"(wo), "s"(ws), "s"(out) : "memory")
#define rws mkrs(ws, WS_BYTES)
#define PROJECT(k0_) do { \
      _Pragma("unroll") for (int k = k0_; k < 6; ++k) { o[0] = mfma(wvv[k], b[k], o[0]); o[1] = mfma(wvv[6 + k], b[k], o[1]); } \
      asm volatile("" : "+v"(o[0]), "+v"(o[1])); \
      SB(); \
      _Pragma("unroll") for (int k = k0_; k < 6; ++k) o[2] = mfma(wvv[12 + k], b[k], o[2]); \
      asm volatile("" : "+v"(o[2])); \
      SB(); \
      stb128(u32x4{cvtpk(o[0][0], o[0][1]), cvtpk(o[0][2], o[0][3]), cvtpk(o[1][0], o[1][1]), tw}, rws, mo); \
      SB(); \
      stb128(u32x4{cvtpk(o[1][2], o[1][3]), cvtpk(o[2][0], o[2][1]), cvtpk(o[2][2], o[2][3]), tw}, rws, mo + 1024); \
    } while (0)
  unsigned long long tg;
#define GET_TAG() tg = ((unsigned long long)((unsigned)lds_spin(CT_TG + 4, ctl, 1) & 0xffu) << 32) | (unsigned)lds_get(ctl + CT_TG)
  const unsigned long long tstart = wall_clock64();
  if (w != 0) {
    const unsigned l0 = threadIdx.x & 63;
    if (blockIdx.x < NPR) {
      const unsigned p6 = (blockIdx.x * 43) >> 8, pu = blockIdx.x - 6 * p6;
      unsigned vo = ((l0 * 16) ^ (((l0 * 4) & 0x40) | (l0 & 0x20))) + (l0 >> 3) * 256;
      const bool pv = p6 >= 3, wa = w >= 4, isq = !pv && (w & 6) == 2;
      const unsigned a6 = 6 * w - 24, pj = pv ? p6 - 3 : p6;
      unsigned so = (wa ? a6 : isq ? w - 2 : 4 * pu) * 3072 + (isq ? 0 : pv && !wa ? w - 1 : pj) * 128, ga = isq ? 128 : 3072;
      unsigned lo = lb + PSCR + (wa ? a6 : pv ? 4 * w + 20 : (0x1b181e00u >> (8 * w)) & 255u) * 1024, hi = lo + (wa ? 6144 : isq ? 3072 : 4096);
      asm volatile("" : "+v"(vo), "+s"(lo), "+s"(so), "+s"(hi), "+s"(ga));
      ARGS_IN();
      const u32x4 rw = mkrs4(wa ? (pv ? wo : wq) : pv ? wv : isq ? q : wk, isq ? NQ * C * 2 : C * C * 2);
#pragma unroll 1
      do { DMA(lo, vo, rw, so); lo += 1024; so += ga; } while (lo != hi);
    } else if (w == 7 && blockIdx.x >= 56 && blockIdx.x < 72) {
      const u32x4 rc = mkrs4((const void*)(pc0 & ~255ull), CODE_BYTES);
#pragma unroll 1
      for (int i = 0; i < CODE_BYTES; i += 4096)
        asm volatile("s_mov_b32 m0, %0\n\ts_nop 0\n\tbuffer_load_dword %1, %2, %3 offen lds" :: "s"(lb + LDS_SINK), "v"(l0 * 64), "s"(rc), "s"(i) : "memory");
    }
    if (w == 3) {
      unsigned long long ep;
      asm volatile("s_waitcnt lgkmcnt(0)\n\ts_load_dwordx2 %0, %1, %2\n\ts_barrier\n\ts_waitcnt lgkmcnt(0)" : "=&s"(ep) : "s"(ws), "s"(EP_OFF) : "memory");
      *(LAS unsigned long long*)(ctl + CT_TG) = ((ep + 0x100001ull) & 0xffffffffffull) | 0x10000000000ull;
    } else RAW_BARRIER();
  }
  SB();
  if (__builtin_expect(w == 0, 1)) {
    const unsigned l0 = threadIdx.x & 63, g0 = blockIdx.x;
    unsigned y0 = (l0 * 16) ^ (l0 >= 48 ? 48 : 0), y1 = (l0 * 16) ^ (l0 >= 32 ? 64 : 48), y2 = (l0 * 16) ^ (l0 >= 16 ? 112 : 64);
    unsigned v0 = ((l0 * 16) ^ ((l0 * 2) & 0x60)) + (l0 >> 3) * 640 + 1024, v1 = (v0 ^ 16) - 640;
    unsigned v2 = v0 + W0 * C * 2 - 2048, v3 = v1 + W0 * C * 2 - 2048;
    unsigned b0 = (g0 & 7) * (2 * W0 * C * 2) + (g0 >> 3) * 6144, b1 = (g0 & 7) * (W1 * C * 2) + (g0 >> 3) * 3072;
    {
      const unsigned iv = (l0 & 15) >= CT_ONE / 8 ? 0x3f803f80u : (l0 >> 3) == CT_MAX / 64 ? 0xff800000u : 0u;
      *(LAS u32x2*)(ctl + 8 * min(l0, 47u)) = u32x2{l0 >= 47 ? 0x3f803f80u : iv, l0 >= 47 ? 0x3f803f80u : iv};
    }
    constexpr unsigned st0 = 16 * W0 * C * 2, st1 = 8 * W1 * C * 2;
    unsigned sb = lb, fo = CT_FREE, fv = 0u;
    int lap = 0;
    u32x4 rt = mkrs4(tabs.seq, 112 * 4);
    unsigned vz = 0u;
    asm volatile("" : "+v"(v0), "+v"(v1), "+v"(v2), "+v"(v3), "+v"(y0), "+v"(y1), "+v"(y2), "+v"(vz), "+s"(b0), "+s"(b1), "+s"(rt));
    ARGS_IN();
    const u32x4 r0 = mkrs4((const char*)f0 - 1024, W0 * W0 * C * 2 + 1024), r1 = mkrs4(f1, W1 * W1 * C * 2);
    if (g0 < NPR) __builtin_amdgcn_s_barrier();
#define UNIT(p_) do { unsigned so_; \
      asm volatile("s_add_u32 m0, %1, %10\n\ts_add_u32 %0, %2, %11\n\t" \
                   "buffer_load_dwordx4 %3, %7, %0 offen nt lds\n\tbuffer_load_dwordx4 %4, %7, %0 offen offset:1024 nt lds\n\t" \
                   "buffer_load_dwordx4 %5, %7, %0 offen offset:2048 nt lds\n\tbuffer_load_dwordx4 %6, %7, %0 offen offset:3072 nt lds\n\t" \
                   "s_mov_b32 m0, %12\n\ts_mov_b64 exec, 1\n\tbuffer_load_dword %13, %8, %9 offen offset:%14 lds\n\ts_mov_b64 exec, -1" \
                   : "=&s"(so_) : "s"(sb), "s"(b0), "v"(v0), "v"(v1), "v"(v2), "v"(v3), "s"(r0), "s"(rt), "s"(12 * s), "i"((p_) * PART), "i"((p_) * 128), \
                     "s"(lb + LDS_CTL + CT_FULL - 4 * (p_)), "v"(vz), "i"(4 * (p_)) : "memory", "scc"); } while (0)
#define WINDOW() \
      asm volatile("s_waitcnt vmcnt(%1)\n\ts_cmp_ge_i32 %0, %6\n\ts_cbranch_scc1 1f\n\ts_waitcnt vmcnt(%2)\n\ts_cmp_ge_i32 %0, %7\n\ts_cbranch_scc1 1f\n\t" \
                   "s_waitcnt vmcnt(%3)\n\ts_cmp_ge_i32 %0, %8\n\ts_cbranch_scc1 1f\n\ts_waitcnt vmcnt(%4)\n\ts_cmp_ge_i32 %0, %9\n\ts_cbranch_scc1 1f\n\t" \
                   "s_waitcnt vmcnt(%5)\n1:" :: "s"(late), "i"(WMIN + 4 * WSTEP), "i"(WMIN + 3 * WSTEP), "i"(WMIN + 2 * WSTEP), "i"(WMIN + WSTEP), "i"(WMIN), \
                   "i"(4 * LSTEP * 256), "i"(3 * LSTEP * 256), "i"(2 * LSTEP * 256), "i"(LSTEP * 256) : "memory", "scc")
#define COARSE(sb_) \
      asm volatile("s_add_u32 m0, %0, %6\n\ts_nop 0\n\tbuffer_load_dwordx4 %1, %4, %5 offen nt lds\n\t" \
                   "buffer_load_dwordx4 %2, %4, %5 offen offset:1024 nt lds\n\tbuffer_load_dwordx4 %3, %4, %5 offen offset:2048 nt lds" \
                   :: "s"(sb_), "v"(y0), "v"(y1), "v"(y2), "s"(r1), "s"(b1), "i"(CRS) : "memory", "scc")
    COARSE(sb);
#pragma unroll 1
    for (int s = 0; s < 2; ++s) {
      asm volatile("s_add_u32 %0, %0, %1" : "+s"(b1) : "i"(st1) : "scc");
      UNIT(0); UNIT(1); COARSE(sb + SLOT); UNIT(2);
      sb += SLOT; b0 += st0;
    }
    if (g0 >= NPR) __builtin_amdgcn_s_barrier();
    __builtin_amdgcn_s_setprio(3);
    COORDS();
    fo += 12;
    const int tau = __builtin_amdgcn_readfirstlane((int)((PACE1 - PACE0) * 256.f * __builtin_amdgcn_rcpf((float)NT)));
    unsigned due = PACE0 * 256u + 2 * tau, t0v = 0u;
    unsigned long long now = tstart;
    const unsigned xi = (wg >> 3) - 5, xrow = 8 * (31 - xi % GIVE) + (wg & 7) + (xi / GIVE & 1), xcol = xi / GIVE >> 1;
    const unsigned x0 = xrow * (2 * W0 * C * 2) + xcol * 6144, x1 = xrow * (W1 * C * 2) + xcol * 3072;
#pragma unroll 1
    for (int s = 2; s < NT; ++s) {
      asm volatile("" : "+s"(now));
      const unsigned t0s = __builtin_amdgcn_readfirstlane(t0v), t0 = t0s ? t0s : (unsigned)tstart;
      if ((int)(((unsigned)now - t0) * 256u - due) < 0) {
#pragma unroll 1
        for (int spin = 0; spin < 256; ++spin) {
          if ((int)(((unsigned)wall_clock64() - t0) * 256u - due) >= 0) break;
          __builtin_amdgcn_s_sleep(1);
        }
      }
      fv = *(volatile LAS unsigned*)(ctl + (s == 7 ? CT_PD : fo));
      now = wall_clock64();
      UNIT(0);
      asm volatile("" : "+s"(now));
      const int late = (int)(((unsigned)now - t0) * 256u - due);
      due += tau;
      WINDOW();
      UNIT(1);
      WINDOW();
      const unsigned nsb = fo == CT_FREE ? lb : sb + SLOT;
      const unsigned n0 = s == 31 ? x0 : b0 + st0;
      b1 = s == 31 ? x1 : b1 + st1;
      if (s + 1 < NT) {
        const int need = s == 7 && wg < NPR ? 4 : lap;
        if (__builtin_expect((int)__builtin_amdgcn_readfirstlane(fv) < need, 0)) {
          lds_wait(ctl + (s == 7 ? CT_PD : fo), need);
        }
        COARSE(nsb);
      }
      now = wall_clock64();
      t0v = *(volatile LAS unsigned*)(ctl + CT_T0);
      UNIT(2);
      WINDOW();
      if (fo == CT_FREE + 4 * (NS - 1)) { fo = CT_FREE; lap += 4; } else fo += 4;
      sb = nsb; b0 = n0;
    }
    __builtin_amdgcn_s_setprio(0);
    u32x4 wvv[18], b[6];
    asm volatile("s_mov_b32 m0, %0\n\ts_mov_b64 exec, 1\n\tbuffer_load_dword %1, %2, 0 offen lds\n\ts_mov_b64 exec, -1"
                 :: "s"(lb + LDS_SINK), "v"(vz), "s"(mkrs4(ws - 4096, 4096)) : "memory");
    lds_wait(ctl + CT_WV, 1);
#pragma unroll
    for (int j = 0; j < 18; ++j) wvv[j] = ldb128<16>(rws, lane * 16 + (j & 3) * 1024, WVO_OFF + 54 * 1024 + (j >> 2) * 4096);
    GET_TAG();
    LAS const char *xr = lds + xslot + lane * 16, *xl = lds + xslot + 6144 + 4 * n;
    unsigned mo = wg * 8192 + 6144 + n * 64 + g * 16, tw = (unsigned)(tg >> (20 * (g & 1))) & 0xfffffu, sh = 12 * (g & 1), lse;
    asm volatile("" : "+v"(xr), "+v"(xl), "+v"(mo), "+v"(tw), "+v"(sh));
    __builtin_amdgcn_s_waitcnt(0x0f70);
    lds_spin(CT_XD, ctl, 1);
    {
      int cnt_, c_;
      unsigned t_;
      asm volatile("s_mov_b32 %1, 0x400000\n1:\n\tds_read_b32 %2, %10 offset:%13\n\tds_read_b128 %3, %11\n\tds_read_b128 %4, %11 offset:1024\n\t"
                   "ds_read_b128 %5, %11 offset:2048\n\tds_read_b128 %6, %11 offset:3072\n\tds_read_b128 %7, %11 offset:4096\n\t"
                   "ds_read_b128 %8, %11 offset:5120\n\tds_read_b32 %9, %12\n\ts_waitcnt lgkmcnt(7)\n\tv_readfirstlane_b32 %0, %2\n\t"
                   "s_cmp_ge_i32 %0, 3\n\ts_cbranch_scc1 2f\n\ts_sub_u32 %1, %1, 1\n\ts_cbranch_scc0 1b\n2:\n\ts_waitcnt lgkmcnt(0)"
                   : "=&s"(c_), "=&s"(cnt_), "=&v"(t_), "=&v"(b[0]), "=&v"(b[1]), "=&v"(b[2]), "=&v"(b[3]), "=&v"(b[4]), "=&v"(b[5]), "=&v"(lse)
                   : "v"(ctl), "v"(xr), "v"(xl), "i"(CT_XD) : "memory", "scc");
    }
    SB();
    f32x4 o[3] = {{0.f, 0.f, 0.f, 0.f}, {0.f, 0.f, 0.f, 0.f}, {0.f, 0.f, 0.f, 0.f}};
    tw |= (lse << sh) & 0xfff00000u;
    PROJECT(0);
    lds_inc(CT_ST, ctl);
  } else {
    COORDS();
    const int tq = (lane >> 2) & 3, tp = lane & 3;
    const bool pq = wg < 18;
    const bool pv = wg >= 18 && wg < NPR;
    const int pj = wg / 6, pu = wg % 6;
    if (w >= 4 && ((w + NT) & 3) == 0 && !pq) {
      unsigned k0, k1, k2;
      asm volatile("s_load_dword %0, %3, %5\n\ts_load_dword %1, %3, %6\n\ts_load_dword %2, %4, %7\n\ts_waitcnt lgkmcnt(0)"
                   : "=&s"(k0), "=&s"(k1), "=&s"(k2) : "s"(f0), "s"(f1), "s"((wg >> 3) * 3145728 + 1572860), "s"((wg >> 3) * 3145728 + 3145724),
                     "s"((wg >> 3) * 786432 + 786428) : "memory");
    }
    const u32x4 one = {0x3f803f80u, 0x3f803f80u, 0x3f803f80u, 0x3f803f80u};
    const int fq = ((n & 2) << 1) | ((n & 4) >> 1);
    LAS const char* rq0 = lds + PSCR + 24576 + 3072 * (n >> 3) + 128 * (n & 7) + 16 * (g ^ fq);
    LAS const char* rq1 = lds + PSCR + 24576 + 3072 * (n >> 3) + 128 * (n & 7) + 16 * ((4 + g) ^ fq);
    if (pq && w >= 2) {
      {
        waitvm<0>();
        lds_inc(CT_PA, ctl);
        if (w >= 4) {
          f32x4 sq = {0.f, 0.f, 0.f, 0.f}, rr = sq, cs = sq;
          const int ro = PSCR + 128 * (8 * g + tq) + 16 * (((w & 2) * 2 + tp) ^ ((tq & 2) << 1)) + 8 * (w & 1);
          u32x4 qv[6], af[6];
          lds_spin(CT_PA, ctl, 6);
#pragma unroll
          for (int s = 0; s < 6; ++s) {
            qv[s] = *(LAS const u32x4*)(((s & 1) ? rq1 : rq0) + (s >> 1) * 1024);
            af[s] = vtr2(lds + ro + s * 4096, lds + (ro ^ 32) + s * 4096 + 512);
          }
          SB();
#pragma unroll
          for (int s = 0; s < 6; ++s) { sq = mfma(one, qv[s], sq); rr = mfma(af[s], qv[s], rr); cs = mfma(af[s], one, cs); }
          const f32x4 qq = rr - (sq[0] * (1.f / C)) * cs;
          *(LAS u32x2*)(lds + LDS_QK + 4096 + ((w >> 1) & 1) * 1024 + lane * 16 + 8 * (w & 1)) = u32x2{cvtpk(qq[0], qq[1]), cvtpk(qq[2], qq[3])};
          lds_inc(CT_PB, ctl);
        }
      }
    }
    if (pq && w == 1) {
      {
        waitvm<0>();
        u32x4 kf4[4], qv[6];
        const int ko = LDS_QK + 128 * (((n & 4) << 2) | (n & 11)) + 16 * (g ^ ((n & 2) << 1));
#pragma unroll
        for (int i = 0; i < 4; ++i) kf4[i] = *(LAS const u32x4*)(lds + (ko ^ ((i & 1) * 64 + (i >> 1) * 32)) + (i >> 1) * 512);
        lds_spin(CT_PA, ctl, 6);
#pragma unroll
        for (int s = 0; s < 6; ++s) qv[s] = *(LAS const u32x4*)(((s & 1) ? rq1 : rq0) + (s >> 1) * 1024);
        const f32x4 z = {0.f, 0.f, 0.f, 0.f};
        f32x4 gq = z, sq = z;
#pragma unroll
        for (int s = 0; s < 6; ++s) { gq = mfma(qv[s], qv[s], gq); sq = mfma(one, qv[s], sq); }
        const float mean = sq[0] * (1.f / C);
        const float s2 = xrow16_sum(g == (n >> 2) ? sel4(gq, n & 3) : 0.f);
        float sc = __builtin_amdgcn_rsqf(fmaxf(fmaf(-mean, mean, s2 * (1.f / C)), 0.f) + EPS) * QSCALE;
        asm volatile("" : "+v"(sc));
        GET_TAG();
        lds_spin(CT_PB, ctl, 4);
        const u32x4 q0 = *(LAS const u32x4*)(lds + LDS_QK + 4096 + lane * 16), q1 = *(LAS const u32x4*)(lds + LDS_QK + 5120 + lane * 16);
        const f32x4 d0 = mfma(kf4[1], q1, mfma(kf4[0], q0, z)), d1 = mfma(kf4[3], q1, mfma(kf4[2], q0, z));
        const u32x4 xq = pack8(d0 * sc, d1 * sc);
        const unsigned ck = qk_check(xq, (unsigned)tg);
        asm volatile("" :: "v"(ck));
        {
          const unsigned so = QK_OFF + wg * QK_REC;
#pragma unroll
          for (int cp = 0; cp < 4; ++cp) {
            stb128(xq, rws, lane * 16 + so + cp * QK_COPY);
            stb32(ck, rws, lane * 4 + so + 1024 + cp * QK_COPY);
          }
          stb32((unsigned)tg, rws, HINT_OFF + lane * 128 + (pu >> 1) * 32 + pj * 8 + (pu & 1) * 4);
          lds_put(CT_PD, ctl, 4);
        }
      }
    }
    if (pv) {
      waitvm<0>();
      lds_inc(CT_PA, ctl);
      if (w >= 4) {
        const int ro = PSCR + 128 * (8 * g + tq) + 16 * ((2 * (w - 4) + (tp >> 1)) ^ ((tq & 2) << 1)) + 8 * (tp & 1);
        const int rv = PSCR + 24576 + 128 * n + 16 * (g ^ fq);
        f32x4 d0 = {0.f, 0.f, 0.f, 0.f}, d1 = d0;
        lds_spin(CT_PA, ctl, 7);
        const unsigned org = (unsigned)tstart + max((int)(wall_clock64() - tstart) - LAND0, 0);
#pragma unroll
        for (int s = 0; s < 6; ++s) {
          const u32x4 p = vtr2(lds + ro + s * 4096, lds + (ro ^ 32) + s * 4096 + 512);
          LAS const char* pd = lds + (rv ^ ((s & 1) * 64)) + (s >> 1) * 4096;
          d0 = mfma(*(LAS const u32x4*)pd, p, d0); d1 = mfma(*(LAS const u32x4*)(pd + 2048), p, d1);
        }
        const int fi = (pj - 3) * 24 + (w - 4) * 6 + pu;
        stb128(pack8(d0, d1), rws, lane * 16 + WVO_OFF + fi * 1024);
        lds_inc(CT_PD, ctl);
        GET_TAG();
        waitvm<0>();
        if (lane < 8) stb64(u32x2{(unsigned)tg, (unsigned)(tg >> 32) | org << 8}, rws, FLAG_OFF + lane * 768 + fi * 8);
      }
    }
    const f32x4 z = {0.f, 0.f, 0.f, 0.f};
    int full = 0;
#define NEED(u_) lds_need(CT_FULL, full, ctl, (u_))
    if (w >= 4) {
      const int brg = ((g & 1) << 1) | (g >> 1);
      const int fn = (((n >> 1) & 3) << 1) | ((n >> 3) & 1);
      const int fy = ((n >> 2) & 1) * 4 + ((n >> 1) & 1) * 3;
      const int orr0 = 128 * n + 16 * (brg ^ fn), orr1 = 128 * n + 16 * ((4 + brg) ^ fn);
      const int ory0 = n < 8 ? CRS + 384 * n + 16 * (brg ^ fy) : LDS_CTL + CT_ONE, ory1 = n < 8 ? CRS + 384 * n + 16 * ((4 + brg) ^ fy) : LDS_CTL + CT_ONE;
      const int yon = n < 8 ? 1 : 0;
      const int opk = LDS_PK + 320 * g + 8 * n, ork = LDS_PK + 320 * (2 * (g >> 1) + (n >> 3)) + 256 + 8 * (n & 7) + 4 * (g & 1);
      u32x4 qk[6];
      f32x4 ref, qs;
      LAS const char *px0, *px1, *py0, *py1;
#define AT_SLOT(o_) do { px0 = lds + orr0 + (o_); px1 = lds + orr1 + (o_); py0 = lds + ory0 + __mul24(yon, (o_)); py1 = lds + ory1 + __mul24(yon, (o_)); \
      asm volatile("" : "+v"(px0), "+v"(px1), "+v"(py0), "+v"(py1)); } while (0)
#define RD_K(k_, i_) do { LAS const char* p_ = (((k_) & 1) ? px1 : px0) + ((k_) >> 1) * PART; \
    yv[i_] = *(LAS const u32x4*)((((k_) & 1) ? py1 : py0) + ((k_) >> 1) * 128); \
    xa[i_] = *(LAS const u32x4*)(p_); xb[i_] = *(LAS const u32x4*)(p_ + 2048); } while (0)
#define MF_STAT(i_) do { cy = mfma(yv[i_], yv[i_], cy); ca = mfma(yv[i_], xa[i_], ca); cb = mfma(yv[i_], xb[i_], cb); \
    ga = mfma(xa[i_], xa[i_], ga); gb = mfma(xb[i_], xb[i_], gb); } while (0)
#define MF_S(k_, i_) do { sy = mfma(qk[k_], yv[i_], sy); sa = mfma(qk[k_], xa[i_], sa); sb = mfma(qk[k_], xb[i_], sb); } while (0)
#define STATS(st, rec) do { \
      const int r = n & 3; \
      const float yy = half_copy<true>(sel4(cy, r)); \
      const f32x2 Y = swap32(yy, yy), X = swap32(sel4(ca, r), sel4(cb, r)), G = swap32(sel4(ga, r), sel4(gb, r)); \
      const float q1_ = fmaf(2.f, X[0], Y[0]) + (n < 8 ? G[0] : G[1]);       \
      const f32x2 Q = swap16(q1_, q1_); \
      const float mn_ = (X[1] + Y[1]) * (1.f / C); \
      const float var = fmaxf(fmaf(-mn_, mn_, fmaf((n & 4) ? Q[1] : Q[0], 1.f / C, EPS)), EPS), rs_ = __builtin_amdgcn_rsqf(var); \
      const f32x2 R = swap32(rs_, rs_), Mn = swap32(mn_, mn_); \
      st = f32x4{R[0], Mn[0], R[1], Mn[1]}; \
      const float v_ = (g & 1) ? var * rs_ : mn_, h_ = bfhi(__float_as_uint(v_)); \
      rec = cvtpk(h_, v_ - h_); \
    } while (0)
#define LOGITS(sv, st, base_) do { \
      _Pragma("unroll") for (int r = 0; r < 4; ++r) { \
        const float s0 = half_copy<true>(sy[r]); \
        sv[r] = fmaf(fmaf(-st[1], qs[r], sa[r] + s0), st[0], -(base_)[r]); \
        sv[4 + r] = fmaf(fmaf(-st[3], qs[r], sb[r] + s0), st[2], -(base_)[r]); \
      } \
    } while (0)
#define PUBLISH(sv, st, rec, pk_, word_) do { \
      *(LAS unsigned*)(lds + ork + (pk_) * PK_B) = rec; \
      *(LAS u32x2*)(lds + opk + (pk_) * PK_B) = u32x2{cvtpk(sv[0] * st[0], sv[1] * st[0]), cvtpk(sv[2] * st[0], sv[3] * st[0])}; \
      *(LAS u32x2*)(lds + opk + (pk_) * PK_B + 128) = u32x2{cvtpk(sv[4] * st[2], sv[5] * st[2]), cvtpk(sv[6] * st[2], sv[7] * st[2])}; \
      lds_put(CT_PRDY, ctl + 4 * (pk_), (word_)); \
    } while (0)
      int s = (w + NT - 1) & 3, slot = s + 4, pk = s + 4, ncon = 0;
      {
        u32x4 yv[12], xa[12], xb[12];
        f32x4 st0, st1;
        unsigned rec0, rec1;
#pragma unroll
        for (int h = 0; h < 2; ++h) {
          f32x4 cy = z, ga = z, ca = z, gb = z, cb = z;
          AT_SLOT((s + 4 * h) * SLOT);
          lds_wait(ctl + CT_FULL, 3 * (s + 4 * h) + 3);
#pragma unroll
          for (int k = 0; k < 6; ++k) RD_K(k, 6 * h + k);
          lds_inc(CT_FREE + 16 * h, ctl + 4 * s);
          SB();
#pragma unroll
          for (int k = 0; k < 6; ++k) MF_STAT(6 * h + k);
          SB();
          if (h == 0) { STATS(st0, rec0); asm volatile("" : "+v"(st0), "+v"(rec0)); } else { STATS(st1, rec1); asm volatile("" : "+v"(st1), "+v"(rec1)); }
          SB();
        }
        lds_wait(ctl + CT_QK, 3);
#pragma unroll
        for (int k = 0; k < 6; ++k) qk[k] = *(LAS const u32x4*)(lds + LDS_QK + k * 1024 + lane * 16);
        float s0v[8], s1v[8];
        {
          f32x4 sy = z, sa = z, sb = z;
#pragma unroll
          for (int k = 0; k < 6; ++k) MF_S(k, k);
#pragma unroll
          for (int r = 0; r < 4; ++r) qs[r] = half_copy<false>(sy[r]);
          LOGITS(s0v, st0, z);
        }
        {
          f32x4 sy = z, sa = z, sb = z;
#pragma unroll
          for (int k = 0; k < 6; ++k) MF_S(k, 6 + k);
          LOGITS(s1v, st1, z);
        }
#pragma unroll
        for (int r = 0; r < 4; ++r) ref[r] = row_max(fmaxf(fmaxf(s0v[r], s0v[4 + r]), fmaxf(s1v[r], s1v[4 + r])));
        if (n < 4) __builtin_amdgcn_ds_fmaxf((LAS float*)(ctl + CT_MAX + 16 * g + 4 * n), sel4(ref, n), __ATOMIC_RELAXED, __HIP_MEMORY_SCOPE_WORKGROUP, false);
        lds_inc(CT_QR, ctl);
        lds_spin(CT_QR, ctl, 4);
        ref = *(LAS const f32x4*)(ctl + CT_MAX + 16 * g);
#pragma unroll
        for (int j = 0; j < 8; ++j) s0v[j] = __builtin_amdgcn_exp2f(s0v[j] - ref[j & 3]);
        PUBLISH(s0v, st0, rec0, s, 2 * s + 2);
#pragma unroll
        for (int j = 0; j < 8; ++j) s1v[j] = __builtin_amdgcn_exp2f(s1v[j] - ref[j & 3]);
        if (pk >= NPK) { pk -= NPK; ncon = 3; lds_spin(CT_PCON, ctl + 4 * pk, 3); }
        s += 4;
        PUBLISH(s1v, st1, rec1, pk, 2 * s + 2);
      }
      float thr = 16.f;
      f32x4 nref = -ref;
#pragma unroll 1
      for (;;) {
        s += 4; slot += 4; slot -= slot >= NS ? NS : 0; pk += 4;
        if (pk >= NPK) { pk -= NPK; ncon += 3; }
        if (s >= NT) break;
        f32x4 sy = z, cy = z, sa, ga = z, ca = z, sb, gb = z, cb = z;
        u32x4 yv[6], xa[6], xb[6];
        unsigned fl_, word = 2 * s + 2;
        AT_SLOT(slot * SLOT);
        LAS char *pw = lds + opk + pk * PK_B, *prc = lds + ork + pk * PK_B, *pfl = ctl + 4 * pk, *pfr = ctl + 4 * slot;
        asm volatile("" : "+v"(pw), "+v"(prc), "+v"(pfl), "+v"(pfr), "+v"(word));
#define RD_X "ds_read_b32 %2, %7 offset:%11\n\tds_read_b128 %3, %8 offset:%12\n\tds_read_b128 %4, %9 offset:%12\n\t" \
             "ds_read_b128 %5, %8 offset:%13\n\tds_read_b128 %6, %9 offset:%13\n"
#define RD_IN(p_) "v"(ctl), "v"(px0), "v"(px1), "s"(3 * s + (p_) + 1), "i"(CT_FULL), "i"(PART * (p_)), "i"(PART * (p_) + 2048)
#define ISSUE(p_) do { int d0_, d1_; \
        asm volatile(RD_X : "=&s"(d0_), "=&s"(d1_), "=&v"(fl_), "=&v"(xa[2 * (p_)]), "=&v"(xa[2 * (p_) + 1]), "=&v"(xb[2 * (p_)]), "=&v"(xb[2 * (p_) + 1]) \
                     : RD_IN(p_) : "memory"); } while (0)
#define CHECK(p_) do { int cnt_; \
        asm volatile("s_mov_b32 %1, 0x400000\n\ts_branch 2f\n1:\n\t" RD_X "2:\n\ts_waitcnt lgkmcnt(4)\n\tv_readfirstlane_b32 %0, %2\n\t" \
                     "s_cmp_ge_i32 %0, %10\n\ts_cbranch_scc1 3f\n\ts_cmp_ge_i32 %10, %14\n\ts_cbranch_scc1 4f\n\ts_sleep %15\n4:\n\t" \
                     "s_sub_u32 %1, %1, 1\n\ts_cbranch_scc0 1b\n3:\n\ts_waitcnt lgkmcnt(2)" \
                     : "=&s"(full), "=&s"(cnt_), "+v"(fl_), "+v"(xa[2 * (p_)]), "+v"(xa[2 * (p_) + 1]), "+v"(xb[2 * (p_)]), "+v"(xb[2 * (p_) + 1]) \
                     : RD_IN(p_), "s"(3 * NT - 2), "i"(CALM) : "memory", "scc"); \
        asm volatile("" : "+s"(full)); } while (0)
#define GOT_B(p_) do { waitlds<0>(); asm volatile("" : "+v"(xb[2 * (p_)]), "+v"(xb[2 * (p_) + 1])); } while (0)
#define MF_A(k_) do { ca = mfma(yv[k_], xa[k_], ca); ga = mfma(xa[k_], xa[k_], ga); sa = mfma(qk[k_], xa[k_], sa); } while (0)
#define MF_B(k_) do { cb = mfma(yv[k_], xb[k_], cb); gb = mfma(xb[k_], xb[k_], gb); sb = mfma(qk[k_], xb[k_], sb); } while (0)
        const unsigned pcv = *(volatile LAS unsigned*)(pfl + CT_PCON);
        NEED(3 * s);
#pragma unroll
        for (int k = 0; k < 6; ++k) yv[k] = *(LAS const u32x4*)(((k & 1) ? py1 : py0) + (k >> 1) * 128);
        int con = __builtin_amdgcn_readfirstlane(pcv);
        SB();
#pragma unroll
        for (int k = 0; k < 6; ++k) sy = mfma(qk[k], yv[k], sy);
        asm volatile("" : "+v"(sy));
        SB();
        lds_need(CT_PCON, con, pfl, ncon);
        ISSUE(0);
        SB();
#pragma unroll
        for (int k = 0; k < 6; ++k) cy = mfma(yv[k], yv[k], cy);
#pragma unroll
        for (int r = 0; r < 4; ++r) sa[r] = half_copy<true>(sy[r]);
        const float yy = half_copy<true>(sel4(cy, n & 3));
        f32x2 Y = swap32(yy, yy);
        asm volatile("" : "+v"(Y), "+v"(sa));
        SB();
        CHECK(0);
        sb = sa;
        MF_A(0);
        asm volatile("" : "+v"(ca), "+v"(ga), "+v"(sa));
        SB();
        GOT_B(0);
        ISSUE(1);
        SB();
        MF_B(0); MF_A(1); MF_B(1);
        asm volatile("" : "+v"(ca), "+v"(ga), "+v"(sa), "+v"(cb), "+v"(gb), "+v"(sb));
        SB();
        CHECK(1);
        MF_A(2);
        asm volatile("" : "+v"(ca), "+v"(ga), "+v"(sa));
        SB();
        GOT_B(1);
        ISSUE(2);
        SB();
        MF_B(2); MF_A(3); MF_B(3);
        asm volatile("" : "+v"(ca), "+v"(ga), "+v"(sa), "+v"(cb), "+v"(gb), "+v"(sb));
        SB();
        CHECK(2);
        ca = mfma(yv[5], xa[5], mfma(yv[4], xa[4], ca));
        ga = mfma(xa[5], xa[5], mfma(xa[4], xa[4], ga));
        asm volatile("" : "+v"(ca), "+v"(ga));
        SB();
        GOT_B(2);
        cb = mfma(yv[5], xb[5], mfma(yv[4], xb[4], cb));
        asm volatile("" : "+v"(cb));
        SB();
        lds_inc(CT_FREE, pfr);
        gb = mfma(xb[4], xb[4], gb);
        float ua = sel4(ca, n & 3);
        asm volatile("" : "+v"(gb), "+v"(ua));
        SB();
        gb = mfma(xb[5], xb[5], gb);
        float va = sel4(ga, n & 3);
        asm volatile("" : "+v"(gb), "+v"(va));
        SB();
        sa = mfma(qk[4], xa[4], sa);
        float ub = sel4(cb, n & 3);
        asm volatile("" : "+v"(sa), "+v"(ub));
        SB();
        sa = mfma(qk[5], xa[5], sa);
        const f32x2 X = swap32(ua, ub);
        float mn_ = (X[1] + Y[1]) * (1.f / C), q1_ = fmaf(2.f, X[0], Y[0]);
        asm volatile("" : "+v"(sa), "+v"(mn_), "+v"(q1_));
        SB();
        sb = mfma(qk[4], xb[4], sb);
        float vb = sel4(gb, n & 3);
        asm volatile("" : "+v"(sb), "+v"(vb));
        SB();
        sb = mfma(qk[5], xb[5], sb);
        const f32x2 Mn = swap32(mn_, mn_), G = swap32(va, vb);
        q1_ += n < 8 ? G[0] : G[1];
        const f32x2 Q = swap16(q1_, q1_);
        sa -= Mn[0] * qs;
        const float var = fmaxf(fmaf(-mn_, mn_, fmaf((n & 4) ? Q[1] : Q[0], 1.f / C, EPS)), EPS), rs_ = __builtin_amdgcn_rsqf(var);
        sb -= Mn[1] * qs;
        const float v_ = (g & 1) ? var * rs_ : mn_, h_ = bfhi(__float_as_uint(v_));
        *(LAS unsigned*)prc = cvtpk(h_, v_ - h_);
        const f32x2 R = swap32(rs_, rs_);
        sa = sa * R[0] + nref; sb = sb * R[1] + nref;
        const float tm = fmaxf(fmaxf(fmaxf(fmaxf(sa[0], sa[1]), sa[2]), fmaxf(fmaxf(sa[3], sb[0]), sb[1])), fmaxf(sb[2], sb[3]));
        if (__builtin_expect(__builtin_amdgcn_ballot_w64(!(tm <= thr)) != 0ull, 0)) {
#pragma unroll
          for (int r = 0; r < 4; ++r) {
            const float mx = row_max(fmaxf(sa[r], sb[r])), dm = mx <= 16.f ? 0.f : mx;
            nref[r] -= dm; sa[r] -= dm; sb[r] -= dm;
          }
          thr = __builtin_nanf("");
          word |= 1u;
          if (n == 0) *(LAS f32x4*)(lds + LDS_PK + pk * PK_B + 1280 + 16 * g) = -nref;
        }
#pragma unroll
        for (int r = 0; r < 4; ++r) { sa[r] = __builtin_amdgcn_exp2f(sa[r]); sb[r] = __builtin_amdgcn_exp2f(sb[r]); }
        float ra = R[0], rb = R[1];
        asm volatile("" : "+v"(ra), "+v"(rb));
        sa *= ra; sb *= rb;
        *(LAS u32x2*)pw = u32x2{cvtpk(sa[0], sa[1]), cvtpk(sa[2], sa[3])};
        *(LAS u32x2*)(pw + 128) = u32x2{cvtpk(sb[0], sb[1]), cvtpk(sb[2], sb[3])};
        lds_put(CT_PRDY, pfl, word);
        SB();
      }
      GET_TAG();
      LAS char* fin = lds + xslot + 8192;
      unsigned qres = (unsigned)__builtin_amdgcn_raw_buffer_load_b16(mkrs(q, NQ * C * 2), dout * 2, 0, 0) << 16;
      unsigned ao = ((tid & 255) >> 1) * 8192 + (tid & 1) * 16 + dst * 32, tw = (unsigned)(tg >> (20 * (tid & 1))), sh = 12 * (tid & 1), oo = dout * 2;
      LAS char *fw = fin + (((lane & 8) | ((g & 1) << 2) | (g & 2)) + (lane & 1)) * 16 + 4 * (w - 4), *fr = fin + (2 * (lane & 7) + ((lane >> 3) & 1)) * 16;
      float v7 = 0.f;
      unsigned fi_ = 8u;
      const unsigned t0s = lds_get(ctl + CT_T0);
      unsigned gate = (t0s ? t0s : (unsigned)tstart) + RGATE;
      const RSRC rm = rws;
      u32x4 ro = mkrs4(out, NQ * C * 2);
      unsigned long long wm = 0x0303030303030303ull;
      asm volatile("" : "+v"(ao), "+v"(tw), "+v"(sh), "+v"(oo), "+v"(fw), "+v"(fr), "+v"(fin), "+v"(v7), "+v"(qres), "+v"(fi_), "+s"(ro), "+s"(wm));
      if (w == 4) asm volatile("s_mov_b64 exec, 0x1515\n\tbuffer_store_dword %0, %1, %2, 0 offen sc1\n\ts_mov_b64 exec, -1" :: "v"(qres), "v"(oo), "s"(ro) : "memory");
      lds_wait(ctl + CT_XD, 1);
      lds_spin(CT_ST, ctl, 4);
      gate += max((int)((unsigned)wall_clock64() + OWNG - gate), 0);
      float org = *(LAS const float*)(lds + xslot + 6144 + 4 * dq);
      *(LAS float*)(fin + 256 + 4 * (w - 4)) = org;
#pragma unroll 1
      for (int spin = 0; spin < 1024; ++spin)
        if ((int)((unsigned)wall_clock64() - gate) >= 0) break;
      u32x4 A = ldb128<16>(rm, ao, 0), B = ldb128<16>(rm, ao, 128 * 8192);
#pragma unroll 1
      for (int spin = 0; spin < (1 << 18); ++spin) {
        unsigned xa_ = (A[3] ^ tw) << 12, xb_ = (B[3] ^ tw) << 12;
        if (__builtin_expect(__builtin_amdgcn_ballot_w64((xa_ | xb_) != 0u) == 0ull, 1)) break;
        xa_ |= __builtin_amdgcn_mov_dpp(xa_, 0xB1, 0xf, 0xf, true); xb_ |= __builtin_amdgcn_mov_dpp(xb_, 0xB1, 0xf, 0xf, true);
        asm volatile("" ::: "memory");
        if (xa_ != 0u) A = ldb128<16>(rm, ao, 0);
        if (xb_ != 0u) B = ldb128<16>(rm, ao, 128 * 8192);
      }
      unsigned la = (A[3] & 0xfff00000u) >> sh, lb_ = (B[3] & 0xfff00000u) >> sh;
      la |= __builtin_amdgcn_mov_dpp(la, 0xB1, 0xf, 0xf, true); lb_ |= __builtin_amdgcn_mov_dpp(lb_, 0xB1, 0xf, 0xf, true);
      float da = __uint_as_float(la) - org, db = __uint_as_float(lb_) - org;
      if (__builtin_expect(__builtin_amdgcn_ballot_w64(!(da <= 64.f) || !(db <= 64.f)) != 0ull, 0)) {
        org = xrow16_max(row_max(fmaxf(__uint_as_float(la), __uint_as_float(lb_))));
        da = __uint_as_float(la) - org; db = __uint_as_float(lb_) - org;
        *(LAS float*)(fin + 256 + 4 * (w - 4)) = org;
        fi_ = 9u;
      }
      const float wa = __builtin_amdgcn_exp2f(da), wb = __builtin_amdgcn_exp2f(db);
#define MIX(f_, i_) fmaf(f_(A[i_]), wa, f_(B[i_]) * wb)
      const f32x2 p0 = swap32(MIX(bflo, 0), MIX(bfhi, 0)), p1 = swap32(MIX(bflo, 1), MIX(bfhi, 1));
      const f32x2 p2 = swap32(MIX(bflo, 2), MIX(bfhi, 2)), p3 = swap32(wa + wb, v7);
      const f32x2 r0 = swap16(p0[0] + p0[1], p1[0] + p1[1]), r1 = swap16(p2[0] + p2[1], p3[0] + p3[1]);
      float c0 = r0[0] + r0[1], c1 = r1[0] + r1[1];
      asm volatile("s_nop 1\n\tv_add_f32_dpp %0, %0, %0 row_ror:8 row_mask:0xf bank_mask:0x3\n\tv_add_f32_dpp %0, %1, %1 row_ror:8 row_mask:0xf bank_mask:0xc\n\t"
                   "s_nop 1\n\tv_add_f32_dpp %0, %0, %0 row_shl:4 row_mask:0xf bank_mask:0x5\n\t"
                   "s_nop 1\n\tv_add_f32_dpp %0, %0, %0 quad_perm:[2,3,0,1] row_mask:0xf bank_mask:0x5\n\t"
                   "s_mov_b64 exec, %3\n\tds_write_b32 %2, %0\n\ts_mov_b64 exec, 1\n\tds_add_u32 %4, %5 offset:%6\n\ts_mov_b64 exec, -1"
                   : "+v"(c0) : "v"(c1), "v"(fw), "s"(wm), "v"(ctl), "v"(fi_), "i"(CT_FIN) : "memory");
      if (w == 4) {
        f32x4 a0, l0;
        unsigned cf;
        int cnt_, c_;
        asm volatile("s_mov_b32 %1, 0x4000000\n1:\n\tds_read_b32 %2, %5 offset:%8\n\tds_read_b128 %3, %6\n\tds_read_b128 %4, %7 offset:192\n\t"
                     "s_waitcnt lgkmcnt(2)\n\tv_readfirstlane_b32 %0, %2\n\ts_cmp_ge_i32 %0, 32\n\ts_cbranch_scc1 2f\n\t"
                     "s_sub_u32 %1, %1, 1\n\ts_cbranch_scc0 1b\n2:\n\ts_waitcnt lgkmcnt(0)"
                     : "=&s"(c_), "=&s"(cnt_), "=&v"(cf), "=&v"(a0), "=&v"(l0) : "v"(ctl), "v"(fr), "v"(fin), "i"(CT_FIN) : "memory", "scc");
        asm volatile("" : "+s"(c_));
        if (__builtin_expect(c_ != 32, 0)) {
          f32x4 m0 = *(LAS const f32x4*)(fin + 256);
          const float mm = fmaxf(fmaxf(m0[0], m0[1]), fmaxf(m0[2], m0[3]));
#pragma unroll
          for (int p = 0; p < 4; ++p) m0[p] = __builtin_amdgcn_exp2f(m0[p] - mm);
          a0 *= m0; l0 *= m0;
        }
        const float o = __uint_as_float(qres) + ((a0[0] + a0[2]) + (a0[1] + a0[3])) * __builtin_amdgcn_rcpf((l0[0] + l0[2]) + (l0[1] + l0[3]));
        const unsigned ob = cvtpk(o, __uint_as_float(__builtin_amdgcn_mov_dpp(__float_as_uint(o), 0xB1, 0xf, 0xf, true)));
        asm volatile("s_mov_b64 exec, 0x1515\n\tbuffer_store_dword %0, %1, %2, 0 offen sc1\n\ts_mov_b64 exec, -1" :: "v"(ob), "v"(oo), "s"(ro) : "memory");
        if (wg == 0 && lane == 0) stb64(u32x2{(unsigned)tg, (unsigned)(tg >> 32)}, rws, EP_OFF);
      }
    } else {
      const int ftr = ((g & 1) << 2) | ((tq >> 1) << 1) | (g >> 1);
      const int fty = (g & 1) * 4 + (tq >> 1) * 3;
      int otr[4], oty[4];
#pragma unroll
      for (int t = 0; t < 4; ++t) {
        otr[t] = (w - 1) * PART + 128 * (4 * g + tq) + 16 * ((2 * t + (tp >> 1)) ^ ftr) + 8 * (tp & 1);
        oty[t] = CRS + (w - 1) * 128 + 384 * (4 * (g & 1) + tq) + 16 * ((2 * t + (tp >> 1)) ^ fty) + 8 * (tp & 1);
      }
      int opk = LDS_PK + 320 * tp + 8 * (4 * g + tq), ork = LDS_PK + 320 * (g >> 1) + 256 + 8 * (4 * (g & 1) + tq);
      asm volatile("" : "+v"(opk), "+v"(ork), "+v"(oty[0]), "+v"(oty[1]), "+v"(oty[2]), "+v"(oty[3]));
#define RD_T(ta, ty, so_) _Pragma("unroll") for (int t_ = 0; t_ < 4; ++t_) { \
    ta[t_] = vtr2(lds + otr[t_] + (so_), lds + otr[t_] + (so_) + 2048); ty[t_] = vtr(lds + oty[t_] + (so_)); }
      f32x4 acc[4] = {z, z, z, z}, tac = z;
#define RD_P(o_) \
      pf = vtr2(lds + opk + (o_), lds + opk + (o_) + 128); \
      sf = vtr2(lds + ork + (o_), lds + ork + (o_) + 640)
#define TWICE(ty) asm volatile("s_nop 2"); _Pragma("unroll") for (int t = 0; t < 4; ++t) { u32x2 lo_, hi_; \
      asm volatile("v_mov_b64 %0, %2\n\tv_mov_b64 %1, %2" : "=&v"(lo_), "=&v"(hi_) : "v"(ty[t])); t2[t] = u32x4{lo_[0], lo_[1], hi_[0], hi_[1]}; } \
      asm volatile("s_nop 1" : "+v"(t2[0]), "+v"(t2[1]), "+v"(t2[2]), "+v"(t2[3]))
#define MF_PX(ta) \
      tac = mfma(sf, pf, tac); \
      _Pragma("unroll") for (int t = 0; t < 4; ++t) acc[t] = mfma(ta[t], pf, acc[t])
#define MF_PY() \
      _Pragma("unroll") for (int t = 0; t < 4; ++t) acc[t] = mfma(t2[t], pf, acc[t])
#define MOVE_REF(word_, o_) do { moved |= (word_) & 1; if (__builtin_expect(moved, 0)) { \
            const float m_ = *(LAS const float*)(lds + (((word_) & 1) ? LDS_PK + (o_) + 1280 : LDS_CTL + CT_MAX) + 4 * n); \
            Mtop = fmaxf(Mtop, m_); \
            const bool drop = m_ < Mtop - 64.f; \
            const float mn = drop ? M : m_, fa = __builtin_amdgcn_exp2f(M - mn); \
            M = mn; \
            _Pragma("unroll") for (int t = 0; t < 4; ++t) acc[t] *= fa; \
            tac *= fa; \
            if (drop) pf = u32x4{0u, 0u, 0u, 0u}; \
          } } while (0)
      const unsigned flo = FLAG_OFF + (wg & 7) * 768 + (w - 1) * 144 + (lane < 36 ? lane : 35) * 8;
      float M, Mtop;
      int moved = 0;
      u32x4 wvv[18], pf, sf, t2[4];
      {
        u32x4 ta[8][4], xx[6];
        u32x2 ty[8][4], fl;
        unsigned cc[6];
        const int qo = QK_OFF + (wg & 3) * QK_COPY + (2 * w - 2) * QK_REC;
#define QK_LOAD() _Pragma("unroll") for (int j = 0; j < 6; ++j) { \
          xx[j] = ldb128<16>(rws, lane * 16, qo + ((j >> 1) * 6 + (j & 1)) * QK_REC); \
          cc[j] = __builtin_amdgcn_raw_buffer_load_b32(rws, lane * 4, qo + ((j >> 1) * 6 + (j & 1)) * QK_REC + 1024, 16); }
#pragma unroll
        for (int u = 0; u < 8; ++u) {
          if (u == QKAT) {
            GET_TAG();
#pragma unroll 1
            for (int r = 0; r < 8; ++r) {
#pragma unroll 1
              for (int spin = 0; spin < 128; ++spin) {
                if ((int)(wall_clock64() - tstart) >= GATE + r * HSTEP) break;
                __builtin_amdgcn_s_sleep(1);
              }
              u32x8 hv;
              asm volatile("s_load_dwordx8 %0, %1, %2\n\ts_waitcnt lgkmcnt(0)" : "=&s"(hv) : "s"(ws), "s"(HINT_OFF + ((wg & 7) + 8 * r) * 128 + (w - 1) * 32) : "memory");
              const unsigned t_ = (unsigned)tg;
              if ((((hv[0] ^ t_) | (hv[1] ^ t_) | (hv[2] ^ t_)) | ((hv[3] ^ t_) | (hv[4] ^ t_) | (hv[5] ^ t_))) == 0u) break;
            }
            if (wg < NPR) lds_wait(ctl + CT_PD, 4);
            QK_LOAD();
#pragma unroll 1
            for (int spin = 0; spin < (1 << 18); ++spin) {
              unsigned df = 0u;
#pragma unroll
              for (int j = 0; j < 6; ++j) df |= qk_check(xx[j], (unsigned)tg) ^ cc[j];
              if (__all(df == 0u)) break;
              asm volatile("" ::: "memory");
              QK_LOAD();
            }
#pragma unroll
            for (int h = 0; h < 2; ++h) {
              u32x4 sx;
#pragma unroll
              for (int i = 0; i < 4; ++i) sx[i] = add3(xx[h][i], xx[2 + h][i], xx[4 + h][i]);
              *(LAS u32x4*)(lds + LDS_QK + (2 * w - 2 + h) * 1024 + lane * 16) = sx;
            }
            lds_inc(CT_QK, ctl);
            SB();
          }
          lds_wait(ctl + CT_FULL, 3 * u + w);
          RD_T(ta[u], ty[u], u * SLOT);
          lds_inc(CT_FREE + 4 * u, ctl);
          SB();
        }
        fl = __builtin_bit_cast(u32x2, __builtin_amdgcn_raw_buffer_load_b64(rws, flo, 0, 16));
#pragma unroll
        for (int u = 0; u < 12; ++u) {
          const int word = lds_spin(CT_PRDY + 4 * (u % NPK), ctl, 2 * u + 2);
          if (u == 0) Mtop = M = *(LAS const float*)(ctl + CT_MAX + 4 * n);
          RD_P((u % NPK) * PK_B);
          if (u >= 8) MOVE_REF(word, (u % NPK) * PK_B);
          lds_inc(CT_PCON + 4 * (u % NPK), ctl);
          SB();
          MF_PX(ta[u & 7]);
          SB();
          TWICE(ty[u & 7]);
          MF_PY();
          asm volatile("" : "+v"(acc[0]), "+v"(acc[1]), "+v"(acc[2]), "+v"(acc[3]), "+v"(tac));
          SB();
#define FLAGS_UP() __all(fl[0] == (unsigned)tg && (fl[1] & 255u) == (unsigned)(tg >> 32))
          if (u == T0AT && w == 1 && FLAGS_UP()) {
            int d = (int)(fl[1] - ((unsigned)tstart << 8)) >> 8;
            d += __builtin_amdgcn_mov_dpp(d, XOR8, 0xf, 0xf, true); d += __builtin_amdgcn_mov_dpp(d, ROR4, 0xf, 0xf, true);
            d += __builtin_amdgcn_mov_dpp(d, 0x122, 0xf, 0xf, true); d += __builtin_amdgcn_mov_dpp(d, 0x121, 0xf, 0xf, true);
            lds_put(CT_T0, ctl, ((unsigned)tstart + min(max(d >> 4, -300), 300)) | 1u);
          }
          if (u < 4) {
            NEED(3 * (u + 8) + w);
            RD_T(ta[u], ty[u], ((u + 8) % NS) * SLOT);
            lds_inc(CT_FREE + 4 * ((u + 8) % NS), ctl);
            SB();
          }
        }
#pragma unroll 1
        for (int spin = 0; spin < (1 << 18); ++spin) {
          if (FLAGS_UP()) break;
          asm volatile("" ::: "memory");
          fl = __builtin_bit_cast(u32x2, __builtin_amdgcn_raw_buffer_load_b64(rws, flo, 0, 16));
        }
        asm volatile("" ::: "memory");
        if (w == 3) lds_put(CT_WV, ctl, 1);
      }
      int slot = 3, pk = 12 % NPK;
      unsigned rdy = 0u;
      const int kro[3] = {(w - 1) * 2048, (w == 3 ? 0 : w) * 2048, (w == 1 ? 2 : w - 2) * 2048};
#define RD_PACKET(z_) do { int cnt_; u32x2 p0_, p1_, p2_, p3_; \
        word = __builtin_amdgcn_readfirstlane(rdy); \
        asm volatile("s_mov_b32 %1, 0x400000\n1:\n\tds_read_b32 %2, %7 offset:%11\n\t" \
                     "ds_read_b64_tr_b16 %5, %9\n\tds_read_b64_tr_b16 %6, %9 offset:640\n\t" \
                     "ds_read_b64_tr_b16 %3, %8\n\tds_read_b64_tr_b16 %4, %8 offset:128\n\t" \
                     "s_cmp_ge_i32 %0, %10\n\ts_cbranch_scc1 2f\n\ts_waitcnt lgkmcnt(4)\n\tv_readfirstlane_b32 %0, %2\n\t" \
                     "s_cmp_ge_i32 %0, %10\n\ts_cbranch_scc1 2f\n\t" z_ "s_sub_u32 %1, %1, 1\n\ts_cbranch_scc0 1b\n2:" \
                     : "+s"(word), "=&s"(cnt_), "=&v"(rdy), "=&v"(p0_), "=&v"(p1_), "=&v"(p2_), "=&v"(p3_) \
                     : "v"(ctl + 4 * pk), "v"(lds + opk + pk * PK_B), "v"(lds + ork + pk * PK_B), "s"(2 * s + 2), "i"(CT_PRDY), "i"(CALM) : "memory", "scc"); \
        asm volatile("" : "+s"(word)); \
        pf = u32x4{p0_[0], p0_[1], p1_[0], p1_[1]}; sf = u32x4{p2_[0], p2_[1], p3_[0], p3_[1]}; } while (0)
#define GOT_PACKET() do { waitlds<0>(); asm volatile("" : "+v"(pf), "+v"(sf) : "v"(rdy)); MOVE_REF(word, pk * PK_B); } while (0)
#define RD_Y(t_, p_) do { const u32x2 lo_ = vtr(p_); asm volatile("" ::: "memory"); const u32x2 hi_ = vtr(p_); \
        t2[t_] = u32x4{lo_[0], lo_[1], hi_[0], hi_[1]}; } while (0)
      u32x4 ta[4];
      int s = 12, word;
      NEED(36 + w);
#pragma unroll
      for (int t = 0; t < 4; ++t) { ta[t] = vtr2(lds + otr[t] + 2 * SLOT, lds + otr[t] + 2 * SLOT + 2048); RD_Y(t, lds + oty[t] + 2 * SLOT); }
      lds_inc(CT_FREE + 8, ctl);
#pragma unroll 1
      for (; s < NT - 1; ++s) {
        RD_PACKET("s_sleep %12\n\t");
        LAS const char *px[4], *py[4];
#pragma unroll
        for (int t = 0; t < 4; ++t) { px[t] = lds + otr[t] + slot * SLOT; py[t] = lds + oty[t] + slot * SLOT; asm volatile("" : "+v"(px[t]), "+v"(py[t])); }
        NEED(3 * s + 3 + w);
        GOT_PACKET();
        SB();
        tac = mfma(sf, pf, tac);
        asm volatile("" : "+v"(tac));
        SB();
        lds_inc(CT_PCON, ctl + 4 * pk);
        pk = pk == NPK - 1 ? 0 : pk + 1;
#pragma unroll
        for (int t = 0; t < 4; ++t) { acc[t] = mfma(ta[t], pf, acc[t]); SB(); ta[t] = vtr2(px[t], px[t] + 2048); SB(); }
        rdy = *(volatile LAS unsigned*)(ctl + CT_PRDY + 4 * pk);
#pragma unroll
        for (int t = 0; t < 4; ++t) { acc[t] = mfma(t2[t], pf, acc[t]); SB(); RD_Y(t, py[t]); SB(); }
        lds_inc(CT_FREE, ctl + 4 * slot);
#define WV_LOAD(j_) wvv[j_] = ldb128<16>(rws, lane * 16 + ((j_) & 1) * 1024, WVO_OFF + (w - 1) * 18432 + ((j_) / 6) * 6144 + kro[((j_) % 6) >> 1])
        if ((unsigned)(s - WVS) < WV1) {
#pragma unroll
          for (int u = 0; u < WV1; ++u)
            if (s - WVS == u) WV_LOAD(u);
        }
        if (s == NT - 3) {
#pragma unroll
          for (int j = WV1; j < 18; ++j) WV_LOAD(j);
        }
        slot = slot == NS - 1 ? 0 : slot + 1;
      }
      LAS char *xw = lds + xslot + lane * 16 + kro[0], *xl = lds + xslot + 6144 + 4 * n;
      LAS const char *xr1 = lds + xslot + lane * 16 + kro[1], *xr2 = lds + xslot + lane * 16 + kro[2];
      unsigned mo = wg * 8192 + (w - 1) * 2048 + n * 64 + g * 16, tw = (unsigned)(tg >> (20 * (g & 1))) & 0xfffffu, sh = 12 * (g & 1);
      asm volatile("" : "+v"(xw), "+v"(xl), "+v"(xr1), "+v"(xr2), "+v"(mo), "+v"(tw), "+v"(sh));
      waitvm<0>();
      RD_PACKET("");
      GOT_PACKET();
      SB();
      tac = mfma(sf, pf, tac);
      asm volatile("" : "+v"(tac));
      SB();
#pragma unroll
      for (int t = 0; t < 2; ++t) acc[t] = mfma(t2[t], pf, mfma(ta[t], pf, acc[t]));
      SB();
#pragma unroll
      for (int t = 2; t < 4; ++t) acc[t] = mfma(t2[t], pf, mfma(ta[t], pf, acc[t]));
      const float L = tac[2] + tac[3], rl = __builtin_amdgcn_rcpf(L), ct = -(tac[0] + tac[1]) * rl;
      const float lse = M + __builtin_amdgcn_logf(L);
      u32x4 b[6];
      b[0] = pack8(acc[0] * rl + ct, acc[1] * rl + ct);
      *(LAS u32x4*)xw = b[0];
      *(LAS float*)xl = lse;
#pragma unroll
      for (int j = 0; j < 4; ++j) { __builtin_amdgcn_sched_group_barrier(0x008, 1, 0); __builtin_amdgcn_sched_group_barrier(0x002, 3, 0); }
      SB();
      b[1] = pack8(acc[2] * rl + ct, acc[3] * rl + ct);
      *(LAS u32x4*)(xw + 1024) = b[1];
      lds_inc(CT_XD, ctl);
      SB();
      f32x4 o[3];
      unsigned xd;
#pragma unroll
      for (int t = 0; t < 3; ++t) o[t] = mfma(wvv[6 * t], b[0], z);
      asm volatile("" : "+v"(o[0]), "+v"(o[1]), "+v"(o[2]));
      SB();
#define RD_SLICES "ds_read_b32 %0, %7 offset:%10\n\tds_read_b128 %1, %8\n\tds_read_b128 %2, %8 offset:1024\n\tds_read_b128 %3, %9\n\tds_read_b128 %4, %9 offset:1024\n"
      asm volatile(RD_SLICES : "=&v"(xd), "=&v"(b[2]), "=&v"(b[3]), "=&v"(b[4]), "=&v"(b[5]) : "i"(0), "i"(0), "v"(ctl), "v"(xr1), "v"(xr2), "i"(CT_XD) : "memory");
      SB();
#pragma unroll
      for (int t = 0; t < 3; ++t) o[t] = mfma(wvv[6 * t + 1], b[1], o[t]);
      tw |= (__float_as_uint(lse) << sh) & 0xfff00000u;
      asm volatile("" : "+v"(tw), "+v"(o[0]), "+v"(o[1]), "+v"(o[2]));
      SB();
      {
        int cnt_, c_;
        asm volatile("s_mov_b32 %6, 0x400000\n\ts_branch 2f\n1:\n\t" RD_SLICES "2:\n\ts_waitcnt lgkmcnt(4)\n\tv_readfirstlane_b32 %5, %0\n\t"
                     "s_cmp_ge_i32 %5, 3\n\ts_cbranch_scc1 3f\n\ts_sub_u32 %6, %6, 1\n\ts_cbranch_scc0 1b\n3:\n\ts_waitcnt lgkmcnt(0)"
                     : "+v"(xd), "+v"(b[2]), "+v"(b[3]), "+v"(b[4]), "+v"(b[5]), "=&s"(c_), "=&s"(cnt_)
                     : "v"(ctl), "v"(xr1), "v"(xr2), "i"(CT_XD) : "memory", "scc");
      }
      SB();
      PROJECT(2);
      lds_inc(CT_ST, ctl);
    }
  }
}

#undef rws
#define WS_AT(size_) ((size_) > (64u << 20) ? ((size_) - (64u << 20)) & ~(size_t)0x1fffff : 0)
extern "C" void kernel_launch(void* const* d_in, const int* in_sizes, int n_in, void* d_out, int out_size, void* d_ws, size_t ws_size,
                              hipStream_t stream) {
  dino_main<<<NWG, 512, 0, stream>>>((const u16*)d_in[0], (const u16*)d_in[1], (const u16*)d_in[2], (const u16*)d_in[3], (const u16*)d_in[4],
                                     (const u16*)d_in[5], (const u16*)d_in[6], (char*)d_ws + WS_AT(ws_size) + 4096, (u16*)d_out);
}
```

```cpp
#include <hip/hip_runtime.h>
#include <hip/hip_bf16.h>
#include <stdint.h>

typedef unsigned short u16;
using u32x2 = __attribute__((ext_vector_type(2))) unsigned;
using u32x4 = __attribute__((ext_vector_type(4))) unsigned;
using u32x8 = __attribute__((ext_vector_type(8))) unsigned;
using f32x2 = __attribute__((ext_vector_type(2))) float;
using f32x4 = __attribute__((ext_vector_type(4))) float;
using s16x8 = __attribute__((ext_vector_type(8))) short;
using bf16x2_t = __attribute__((ext_vector_type(2))) __bf16;
typedef short v4i16_t __attribute__((ext_vector_type(4)));
#define LAS __attribute__((address_space(3)))

constexpr int C = 192, NQ = 16, W0 = 512, W1 = 256, NWG = 256;
constexpr float EPS = 1e-5f;
constexpr float QSCALE = 0.07216878364870322f * 1.4426950408889634f;

constexpr int PART = 4096, CRS = 3 * PART;
constexpr int SLOT = CRS + 3072, NS = 10;
constexpr int LDS_QK = NS * SLOT;
constexpr int NPK = 7;
constexpr int LDS_PK = LDS_QK, PK_B = 1344;
constexpr int LDS_CTL = LDS_PK + NPK * PK_B;
constexpr int LDS_SINK = LDS_CTL + 384;
constexpr int LDS_BYTES = LDS_SINK + 256;
constexpr int PSCR = 8 * SLOT;
constexpr int CT_FULL = 8, CT_WV = 4, CT_PA = 0, CT_PB = 12, CT_QR = 16, CT_PD = 20, CT_T0 = 24, CT_XD = 28, CT_TG = 32, CT_QK = 40, CT_ST = 44,
              CT_FREE = 48, CT_FIN = 88, CT_ONE = 112, CT_PRDY = 128, CT_PCON = 160, CT_MAX = 256;

constexpr int MB_BYTES = NWG * NWG * 32;
constexpr int EP_OFF = MB_BYTES;
constexpr int FLAG_OFF = EP_OFF + 512;
constexpr int WVO_OFF = EP_OFF + 8192;
constexpr int QK_OFF = WVO_OFF + C * C * 2;
constexpr int QK_REC = 1280, QK_COPY = 18 * QK_REC;
constexpr int HINT_OFF = QK_OFF + 4 * QK_COPY;
constexpr int WS_BYTES = HINT_OFF + 64 * 128;

#define GIVE 2
constexpr int NPR = 36;
struct Tabs { unsigned seq[112]; };
constexpr Tabs make_tabs() {
  Tabs t{};
  for (int i = 0; i < 112; ++i) t.seq[i] = i + 1;
  return t;
}
__device__ const Tabs tabs = make_tabs();
#define QKAT 3
#define WV1 6
#define WMIN 27
#define WSTEP 5
#define LSTEP 35
#define OWNG 28
#define PACE0 30
#define PACE1 1740
#define RGATE 1910
#define T0AT 3
#define WVS 12
#define CALM 2
#define HSTEP 40
#define GATE 350
#define CODE_BYTES 25856

__device__ __forceinline__ unsigned cvtpk(float lo, float hi) {
  bf16x2_t v = {(__bf16)lo, (__bf16)hi};
  return __builtin_bit_cast(unsigned, v);
}
__device__ __forceinline__ float bflo(unsigned x) { return __builtin_bit_cast(float, x << 16); }
__device__ __forceinline__ float bfhi(unsigned x) { return __builtin_bit_cast(float, x & 0xffff0000u); }
template <int CTRL> __device__ __forceinline__ float dpp(float x) {
  return __builtin_bit_cast(float, __builtin_amdgcn_mov_dpp(__builtin_bit_cast(int, x), CTRL, 0xf, 0xf, true));
}
constexpr int XOR8 = 0x128, ROR4 = 0x124;
template <bool HI8> __device__ __forceinline__ float half_copy(float x) {
  const int v = __builtin_bit_cast(int, x);
  return __builtin_bit_cast(float, __builtin_amdgcn_update_dpp(v, v, XOR8, 0xf, HI8 ? 0xc : 0x3, false));
}
__device__ __forceinline__ float row_max(float x) {
  x = fmaxf(x, dpp<XOR8>(x)); x = fmaxf(x, dpp<ROR4>(x)); x = fmaxf(x, dpp<0x122>(x));
  return fmaxf(x, dpp<0x121>(x));
}
__device__ __forceinline__ f32x2 swap32(float a, float b) {
  auto s = __builtin_amdgcn_permlane32_swap(__float_as_uint(a), __float_as_uint(b), false, false);
  return f32x2{__uint_as_float(s[0]), __uint_as_float(s[1])};
}
__device__ __forceinline__ f32x2 swap16(float a, float b) {
  auto s = __builtin_amdgcn_permlane16_swap(__float_as_uint(a), __float_as_uint(b), false, false);
  return f32x2{__uint_as_float(s[0]), __uint_as_float(s[1])};
}
__device__ __forceinline__ float xrow16_max(float x) {
  auto s = __builtin_amdgcn_permlane16_swap(__float_as_uint(x), __float_as_uint(x), false, false);
  x = fmaxf(__uint_as_float(s[0]), __uint_as_float(s[1]));
  auto t = __builtin_amdgcn_permlane32_swap(__float_as_uint(x), __float_as_uint(x), false, false);
  return fmaxf(__uint_as_float(t[0]), __uint_as_float(t[1]));
}
__device__ __forceinline__ float xrow16_sum(float x) {
  auto s = __builtin_amdgcn_permlane16_swap(__float_as_uint(x), __float_as_uint(x), false, false);
  x = __uint_as_float(s[0]) + __uint_as_float(s[1]);
  auto t = __builtin_amdgcn_permlane32_swap(__float_as_uint(x), __float_as_uint(x), false, false);
  return __uint_as_float(t[0]) + __uint_as_float(t[1]);
}
__device__ __forceinline__ f32x4 mfma(u32x4 a, u32x4 b, f32x4 c) {
  return __builtin_amdgcn_mfma_f32_16x16x32_bf16(__builtin_bit_cast(s16x8, a), __builtin_bit_cast(s16x8, b), c, 0, 0, 0);
}
__device__ __forceinline__ u32x2 vtr(LAS const char* p) {
  return __builtin_bit_cast(u32x2, __builtin_amdgcn_ds_read_tr16_b64_v4i16((LAS v4i16_t*)p));
}
__device__ __forceinline__ u32x4 vtr2(LAS const char* p, LAS const char* q_) {
  const u32x2 a = vtr(p), b = vtr(q_);
  return u32x4{a[0], a[1], b[0], b[1]};
}
__device__ __forceinline__ float sel4(f32x4 v, int r) {
  const float a = (r & 1) ? v[1] : v[0], b = (r & 1) ? v[3] : v[2];
  return (r & 2) ? b : a;
}
using RSRC = __amdgpu_buffer_rsrc_t;
__device__ __forceinline__ RSRC mkrs(const void* p, int bytes) { return __builtin_amdgcn_make_buffer_rsrc(const_cast<void*>(p), (short)0, bytes, 0x00020000); }
__device__ __forceinline__ u32x4 mkrs4(const void* p, unsigned bytes) {
  const unsigned long long b = (unsigned long long)p;
  return u32x4{(unsigned)b, (unsigned)(b >> 32) & 0xffffu, bytes, 0x00020000u};
}
template <int AUX> __device__ __forceinline__ u32x4 ldb128(RSRC r, unsigned vo, int so) { return __builtin_bit_cast(u32x4, __builtin_amdgcn_raw_buffer_load_b128(r, vo, so, AUX)); }
#define SB() __builtin_amdgcn_sched_barrier(0)
#define RAW_BARRIER() asm volatile("s_waitcnt lgkmcnt(0)\n\ts_barrier" ::: "memory")
#define DMA(m0_, vo_, rs_, so_) asm volatile("s_mov_b32 m0, %0\n\ts_nop 0\n\tbuffer_load_dwordx4 %1, %2, %3 offen lds" :: "s"(m0_), "v"(vo_), "s"(rs_), "s"(so_) : "memory")
template <int N> __device__ __forceinline__ void waitlds() { __builtin_amdgcn_s_waitcnt(0xc07f | (N << 8)); }
template <int N> __device__ __forceinline__ void waitvm() { asm volatile("s_waitcnt vmcnt(%0)" :: "i"(N) : "memory"); }


__device__ __forceinline__ void lds_inc(int O, LAS char* p) {
  asm volatile("s_mov_b64 exec, 1\n\tds_add_u32 %0, %1 offset:%2\n\ts_mov_b64 exec, -1" :: "v"(p), "v"(1u), "i"(O) : "memory");
}
__device__ __forceinline__ void lds_put(int O, LAS char* p, unsigned v) {
  asm volatile("s_mov_b64 exec, 1\n\tds_write_b32 %0, %1 offset:%2\n\ts_mov_b64 exec, -1" :: "v"(p), "v"(v), "i"(O) : "memory");
}
__device__ __forceinline__ int lds_get(LAS char* p) {
  asm volatile("" ::: "memory");
  const unsigned v = __hip_atomic_load((LAS unsigned*)p, __ATOMIC_RELAXED, __HIP_MEMORY_SCOPE_WORKGROUP);
  asm volatile("" ::: "memory");
  return __builtin_amdgcn_readfirstlane(v);
}
__device__ __forceinline__ void lds_wait(LAS char* p, int target) {
#pragma unroll 1
  for (int spin = 0; spin < (1 << 22); ++spin) {
    if (lds_get(p) >= target) break;
    __builtin_amdgcn_s_sleep(1);
  }
}
__device__ __forceinline__ int lds_spin(int O, LAS char* p, int target) {
  int r, cnt = 1 << 22;
  unsigned t;
  asm volatile("1:\n\tds_read_b32 %1, %3 offset:%5\n\ts_waitcnt lgkmcnt(0)\n\tv_readfirstlane_b32 %0, %1\n\ts_cmp_ge_i32 %0, %4\n\ts_cbranch_scc1 2f\n\t"
               "s_sub_u32 %2, %2, 1\n\ts_cbranch_scc0 1b\n2:"
               : "=&s"(r), "=&v"(t), "+s"(cnt) : "v"(p), "s"(target), "i"(O) : "memory", "scc");
  return r;
}
__device__ __forceinline__ void lds_need(int O, int& have, LAS char* p, int target) {
  int cnt;
  unsigned t;
  asm volatile("s_cmp_ge_i32 %0, %4\n\ts_cbranch_scc1 2f\n\ts_mov_b32 %2, 0x400000\n"
               "1:\n\tds_read_b32 %1, %3 offset:%5\n\ts_waitcnt lgkmcnt(0)\n\tv_readfirstlane_b32 %0, %1\n\ts_cmp_ge_i32 %0, %4\n\ts_cbranch_scc1 2f\n\t"
               "s_sleep %6\n\ts_sub_u32 %2, %2, 1\n\ts_cbranch_scc0 1b\n2:"
               : "+s"(have), "=&v"(t), "=&s"(cnt) : "v"(p), "s"(target), "i"(O), "i"(CALM) : "memory", "scc");
  asm volatile("" : "+s"(have));
}
__device__ __forceinline__ u32x4 pack8(f32x4 a, f32x4 b) { return u32x4{cvtpk(a[0], a[1]), cvtpk(a[2], a[3]), cvtpk(b[0], b[1]), cvtpk(b[2], b[3])}; }

__device__ __forceinline__ void stb128(u32x4 v, RSRC r, unsigned o) { __builtin_amdgcn_raw_buffer_store_b128(v, r, o, 0, 16); }
__device__ __forceinline__ void stb64(u32x2 v, RSRC r, unsigned o) { __builtin_amdgcn_raw_buffer_store_b64(v, r, o, 0, 16); }
__device__ __forceinline__ void stb32(unsigned v, RSRC r, unsigned o) { __builtin_amdgcn_raw_buffer_store_b32(v, r, o, 0, 16); }
__device__ __forceinline__ unsigned qk_check(u32x4 x, unsigned tg) {
  const unsigned a = (x[0] ^ __builtin_amdgcn_alignbit(x[1], x[1], 11)) * 0x9e3779b1u + tg;
  const unsigned b = (x[2] + __builtin_amdgcn_alignbit(x[3], x[3], 19)) * 0x85ebca77u;
  return __builtin_amdgcn_alignbit(a, a, 15) ^ b ^ (x[1] + x[3]);
}
__device__ __forceinline__ unsigned add3(unsigned a, unsigned b, unsigned c) {
  return cvtpk((bflo(a) + bflo(b)) + bflo(c), (bfhi(a) + bfhi(b)) + bfhi(c));
}
__global__ __launch_bounds__(512, 2) void dino_main(const u16* q, const u16* f0, const u16* f1, const u16* wq, const u16* wk, const u16* wv,
                                                     const u16* wo, char* ws, u16* out) {
  __shared__ __attribute__((aligned(128))) char smem[LDS_BYTES];
  LAS char* lds = (LAS char*)smem;
  LAS char* ctl = lds + LDS_CTL;
  const unsigned lb = (unsigned)(unsigned long long)lds;
  const int w = __builtin_amdgcn_readfirstlane(threadIdx.x >> 6);
  unsigned long long pc0;
  asm volatile("s_getpc_b64 %0" : "=s"(pc0));
#define COORDS() \
  int tid = threadIdx.x, wg = blockIdx.x; \
  asm volatile("" : "+v"(tid), "+s"(wg)); \
  __builtin_assume(tid >= 0 && tid < 512); __builtin_assume(wg >= 0 && wg < NWG); \
  const int lane = tid & 63, n = lane & 15, g = lane >> 4;       \
    \
  const int dq = (wg & 7) * 2 + ((wg >> 4) & 1), dst = (wg & ~31) + (wg & 7) * 4 + ((wg >> 3) & 3); \
  const int dj = 6 * ((wg >> 5) & 1) + min(lane & 7, 5); \
  const int dout = dq * C + 48 * (wg >> 6) + 16 * (dj >> 2) + (wg & 8) + ((lane & 8) >> 1) + (dj & 3); \
  const int NT = wg < NPR ? 32 - GIVE : wg >= 40 && !(wg & 1) && (wg >> 3) < 5 + ((wg & 4) ? 8 : 10) * GIVE ? 33 : 32; \
  const int xslot = (NT >= 30 ? NT - 30 : NT - 20) * SLOT;       \
  (void)xslot; (void)n; (void)g; (void)dst; (void)dout; (void)NT
#define ARGS_IN() asm volatile("s_waitcnt lgkmcnt(0)" :: "s"(q), "s"(f0), "s"(f1), "s"(wq), "s"(wk), "s"(wv), "s"(wo), "s"(ws), "s"(out) : "memory")
#define rws mkrs(ws, WS_BYTES)
#define PROJECT(k0_) do { \
      _Pragma("unroll") for (int k = k0_; k < 6; ++k) { o[0] = mfma(wvv[k], b[k], o[0]); o[1] = mfma(wvv[6 + k], b[k], o[1]); } \
      asm volatile("" : "+v"(o[0]), "+v"(o[1])); \
      SB(); \
      _Pragma("unroll") for (int k = k0_; k < 6; ++k) o[2] = mfma(wvv[12 + k], b[k], o[2]); \
      asm volatile("" : "+v"(o[2])); \
      SB(); \
      stb128(u32x4{cvtpk(o[0][0], o[0][1]), cvtpk(o[0][2], o[0][3]), cvtpk(o[1][0], o[1][1]), tw}, rws, mo); \
      SB(); \
      stb128(u32x4{cvtpk(o[1][2], o[1][3]), cvtpk(o[2][0], o[2][1]), cvtpk(o[2][2], o[2][3]), tw}, rws, mo + 1024); \
    } while (0)
  unsigned long long tg;
#define GET_TAG() tg = ((unsigned long long)((unsigned)lds_spin(CT_TG + 4, ctl, 1) & 0xffu) << 32) | (unsigned)lds_get(ctl + CT_TG)
  const unsigned long long tstart = wall_clock64();
  if (w != 0) {
    const unsigned l0 = threadIdx.x & 63;
    if (blockIdx.x < NPR) {
      const unsigned p6 = (blockIdx.x * 43) >> 8, pu = blockIdx.x - 6 * p6;
      unsigned vo = ((l0 * 16) ^ (((l0 * 4) & 0x40) | (l0 & 0x20))) + (l0 >> 3) * 256;
      const bool pv = p6 >= 3, wa = w >= 4, isq = !pv && (w & 6) == 2;
      const unsigned a6 = 6 * w - 24, pj = pv ? p6 - 3 : p6;
      unsigned so = (wa ? a6 : isq ? w - 2 : 4 * pu) * 3072 + (isq ? 0 : pv && !wa ? w - 1 : pj) * 128, ga = isq ? 128 : 3072;
      unsigned lo = lb + PSCR + (wa ? a6 : pv ? 4 * w + 20 : (0x1b181e00u >> (8 * w)) & 255u) * 1024, hi = lo + (wa ? 6144 : isq ? 3072 : 4096);
      asm volatile("" : "+v"(vo), "+s"(lo), "+s"(so), "+s"(hi), "+s"(ga));
      ARGS_IN();
      const u32x4 rw = mkrs4(wa ? (pv ? wo : wq) : pv ? wv : isq ? q : wk, isq ? NQ * C * 2 : C * C * 2);
#pragma unroll 1
      do { DMA(lo, vo, rw, so); lo += 1024; so += ga; } while (lo != hi);
    } else if (w == 7 && blockIdx.x >= 56 && blockIdx.x < 72) {
      const u32x4 rc = mkrs4((const void*)(pc0 & ~255ull), CODE_BYTES);
#pragma unroll 1
      for (int i = 0; i < CODE_BYTES; i += 4096)
        asm volatile("s_mov_b32 m0, %0\n\ts_nop 0\n\tbuffer_load_dword %1, %2, %3 offen lds" :: "s"(lb + LDS_SINK), "v"(l0 * 64), "s"(rc), "s"(i) : "memory");
    }
    if (w == 3) {
      unsigned long long ep;
      asm volatile("s_waitcnt lgkmcnt(0)\n\ts_load_dwordx2 %0, %1, %2\n\ts_barrier\n\ts_waitcnt lgkmcnt(0)" : "=&s"(ep) : "s"(ws), "s"(EP_OFF) : "memory");
      *(LAS unsigned long long*)(ctl + CT_TG) = ((ep + 0x100001ull) & 0xffffffffffull) | 0x10000000000ull;
    } else RAW_BARRIER();
  }
  SB();
  if (__builtin_expect(w == 0, 1)) {
    const unsigned l0 = threadIdx.x & 63, g0 = blockIdx.x;
    unsigned y0 = (l0 * 16) ^ (l0 >= 48 ? 48 : 0), y1 = (l0 * 16) ^ (l0 >= 32 ? 64 : 48), y2 = (l0 * 16) ^ (l0 >= 16 ? 112 : 64);
    unsigned v0 = ((l0 * 16) ^ ((l0 * 2) & 0x60)) + (l0 >> 3) * 640 + 1024, v1 = (v0 ^ 16) - 640;
    unsigned v2 = v0 + W0 * C * 2 - 2048, v3 = v1 + W0 * C * 2 - 2048;
    unsigned b0 = (g0 & 7) * (2 * W0 * C * 2) + (g0 >> 3) * 6144, b1 = (g0 & 7) * (W1 * C * 2) + (g0 >> 3) * 3072;
    {
      const unsigned iv = (l0 & 15) >= CT_ONE / 8 ? 0x3f803f80u : (l0 >> 3) == CT_MAX / 64 ? 0xff800000u : 0u;
      *(LAS u32x2*)(ctl + 8 * min(l0, 47u)) = u32x2{l0 >= 47 ? 0x3f803f80u : iv, l0 >= 47 ? 0x3f803f80u : iv};
    }
    constexpr unsigned st0 = 16 * W0 * C * 2, st1 = 8 * W1 * C * 2;
    unsigned sb = lb, fo = CT_FREE, fv = 0u;
    int lap = 0;
    u32x4 rt = mkrs4(tabs.seq, 112 * 4);
    unsigned vz = 0u;
    asm volatile("" : "+v"(v0), "+v"(v1), "+v"(v2), "+v"(v3), "+v"(y0), "+v"(y1), "+v"(y2), "+v"(vz), "+s"(b0), "+s"(b1), "+s"(rt));
    ARGS_IN();
    const u32x4 r0 = mkrs4((const char*)f0 - 1024, W0 * W0 * C * 2 + 1024), r1 = mkrs4(f1, W1 * W1 * C * 2);
    if (g0 < NPR) __builtin_amdgcn_s_barrier();
#define UNIT(p_) do { unsigned so_; \
      asm volatile("s_add_u32 m0, %1, %10\n\ts_add_u32 %0, %2, %11\n\t" \
                   "buffer_load_dwordx4 %3, %7, %0 offen nt lds\n\tbuffer_load_dwordx4 %4, %7, %0 offen offset:1024 nt lds\n\t" \
                   "buffer_load_dwordx4 %5, %7, %0 offen offset:2048 nt lds\n\tbuffer_load_dwordx4 %6, %7, %0 offen offset:3072 nt lds\n\t" \
                   "s_mov_b32 m0, %12\n\ts_mov_b64 exec, 1\n\tbuffer_load_dword %13, %8, %9 offen offset:%14 lds\n\ts_mov_b64 exec, -1" \
                   : "=&s"(so_) : "s"(sb), "s"(b0), "v"(v0), "v"(v1), "v"(v2), "v"(v3), "s"(r0), "s"(rt), "s"(12 * s), "i"((p_) * PART), "i"((p_) * 128), \
                     "s"(lb + LDS_CTL + CT_FULL - 4 * (p_)), "v"(vz), "i"(4 * (p_)) : "memory", "scc"); } while (0)
#define WINDOW() \
      asm volatile("s_waitcnt vmcnt(%1)\n\ts_cmp_ge_i32 %0, %6\n\ts_cbranch_scc1 1f\n\ts_waitcnt vmcnt(%2)\n\ts_cmp_ge_i32 %0, %7\n\ts_cbranch_scc1 1f\n\t" \
                   "s_waitcnt vmcnt(%3)\n\ts_cmp_ge_i32 %0, %8\n\ts_cbranch_scc1 1f\n\ts_waitcnt vmcnt(%4)\n\ts_cmp_ge_i32 %0, %9\n\ts_cbranch_scc1 1f\n\t" \
                   "s_waitcnt vmcnt(%5)\n1:" :: "s"(late), "i"(WMIN + 4 * WSTEP), "i"(WMIN + 3 * WSTEP), "i"(WMIN + 2 * WSTEP), "i"(WMIN + WSTEP), "i"(WMIN), \
                   "i"(4 * LSTEP * 256), "i"(3 * LSTEP * 256), "i"(2 * LSTEP * 256), "i"(LSTEP * 256) : "memory", "scc")
#define COARSE(sb_) \
      asm volatile("s_add_u32 m0, %0, %6\n\ts_nop 0\n\tbuffer_load_dwordx4 %1, %4, %5 offen nt lds\n\t" \
                   "buffer_load_dwordx4 %2, %4, %5 offen offset:1024 nt lds\n\tbuffer_load_dwordx4 %3, %4, %5 offen offset:2048 nt lds" \
                   :: "s"(sb_), "v"(y0), "v"(y1), "v"(y2), "s"(r1), "s"(b1), "i"(CRS) : "memory", "scc")
    COARSE(sb);
#pragma unroll 1
    for (int s = 0; s < 2; ++s) {
      asm volatile("s_add_u32 %0, %0, %1" : "+s"(b1) : "i"(st1) : "scc");
      UNIT(0); UNIT(1); COARSE(sb + SLOT); UNIT(2);
      sb += SLOT; b0 += st0;
    }
    if (g0 >= NPR) __builtin_amdgcn_s_barrier();
    __builtin_amdgcn_s_setprio(3);
    COORDS();
    fo += 12;
    const int tau = __builtin_amdgcn_readfirstlane((int)((PACE1 - PACE0) * 256.f * __builtin_amdgcn_rcpf((float)NT)));
    unsigned due = PACE0 * 256u + 2 * tau, t0v = 0u;
    unsigned long long now = tstart;
    const unsigned xi = (wg >> 3) - 5, xrow = 8 * (31 - xi % GIVE) + (wg & 7) + (xi / GIVE & 1), xcol = xi / GIVE >> 1;
    const unsigned x0 = xrow * (2 * W0 * C * 2) + xcol * 6144, x1 = xrow * (W1 * C * 2) + xcol * 3072;
#pragma unroll 1
    for (int s = 2; s < NT; ++s) {
      asm volatile("" : "+s"(now));
      const unsigned t0s = __builtin_amdgcn_readfirstlane(t0v), t0 = t0s ? t0s : (unsigned)tstart;
      if ((int)(((unsigned)now - t0) * 256u - due) < 0) {
#pragma unroll 1
        for (int spin = 0; spin < 256; ++spin) {
          if ((int)(((unsigned)wall_clock64() - t0) * 256u - due) >= 0) break;
          __builtin_amdgcn_s_sleep(1);
        }
      }
      fv = *(volatile LAS unsigned*)(ctl + (s == 7 ? CT_PD : fo));
      now = wall_clock64();
      UNIT(0);
      asm volatile("" : "+s"(now));
      const int late = (int)(((unsigned)now - t0) * 256u - due);
      due += tau;
      WINDOW();
      UNIT(1);
      WINDOW();
      const unsigned nsb = fo == CT_FREE ? lb : sb + SLOT;
      const unsigned n0 = s == 31 ? x0 : b0 + st0;
      b1 = s == 31 ? x1 : b1 + st1;
      if (s + 1 < NT) {
        const int need = s == 7 && wg < NPR ? 4 : lap;
        if (__builtin_expect((int)__builtin_amdgcn_readfirstlane(fv) < need, 0)) {
          lds_wait(ctl + (s == 7 ? CT_PD : fo), need);
        }
        COARSE(nsb);
      }
      now = wall_clock64();
      t0v = *(volatile LAS unsigned*)(ctl + CT_T0);
      UNIT(2);
      WINDOW();
      if (fo == CT_FREE + 4 * (NS - 1)) { fo = CT_FREE; lap += 4; } else fo += 4;
      sb = nsb; b0 = n0;
    }
    __builtin_amdgcn_s_setprio(0);
    u32x4 wvv[18], b[6];
    asm volatile("s_mov_b32 m0, %0\n\ts_mov_b64 exec, 1\n\tbuffer_load_dword %1, %2, 0 offen lds\n\ts_mov_b64 exec, -1"
                 :: "s"(lb + LDS_SINK), "v"(vz), "s"(mkrs4(ws - 4096, 4096)) : "memory");
    lds_wait(ctl + CT_WV, 1);
#pragma unroll
    for (int j = 0; j < 18; ++j) wvv[j] = ldb128<16>(rws, lane * 16 + (j & 3) * 1024, WVO_OFF + 54 * 1024 + (j >> 2) * 4096);
    GET_TAG();
    LAS const char *xr = lds + xslot + lane * 16, *xl = lds + xslot + 6144 + 4 * n;
    unsigned mo = wg * 8192 + 6144 + n * 64 + g * 16, tw = (unsigned)(tg >> (20 * (g & 1))) & 0xfffffu, sh = 12 * (g & 1), lse;
    asm volatile("" : "+v"(xr), "+v"(xl), "+v"(mo), "+v"(tw), "+v"(sh));
    __builtin_amdgcn_s_waitcnt(0x0f70);
    lds_spin(CT_XD, ctl, 1);
    {
      int cnt_, c_;
      unsigned t_;
      asm volatile("s_mov_b32 %1, 0x400000\n1:\n\tds_read_b32 %2, %10 offset:%13\n\tds_read_b128 %3, %11\n\tds_read_b128 %4, %11 offset:1024\n\t"
                   "ds_read_b128 %5, %11 offset:2048\n\tds_read_b128 %6, %11 offset:3072\n\tds_read_b128 %7, %11 offset:4096\n\t"
                   "ds_read_b128 %8, %11 offset:5120\n\tds_read_b32 %9, %12\n\ts_waitcnt lgkmcnt(7)\n\tv_readfirstlane_b32 %0, %2\n\t"
                   "s_cmp_ge_i32 %0, 3\n\ts_cbranch_scc1 2f\n\ts_sub_u32 %1, %1, 1\n\ts_cbranch_scc0 1b\n2:\n\ts_waitcnt lgkmcnt(0)"
                   : "=&s"(c_), "=&s"(cnt_), "=&v"(t_), "=&v"(b[0]), "=&v"(b[1]), "=&v"(b[2]), "=&v"(b[3]), "=&v"(b[4]), "=&v"(b[5]), "=&v"(lse)
                   : "v"(ctl), "v"(xr), "v"(xl), "i"(CT_XD) : "memory", "scc");
    }
    SB();
    f32x4 o[3] = {{0.f, 0.f, 0.f, 0.f}, {0.f, 0.f, 0.f, 0.f}, {0.f, 0.f, 0.f, 0.f}};
    tw |= (lse << sh) & 0xfff00000u;
    PROJECT(0);
    lds_inc(CT_ST, ctl);
  } else {
    COORDS();
    const int tq = (lane >> 2) & 3, tp = lane & 3;
    const bool pq = wg < 18;
    const bool pv = wg >= 18 && wg < NPR;
    const int pj = wg / 6, pu = wg % 6;
    if (w >= 4 && ((w + NT) & 3) == 0 && !pq) {
      unsigned k0, k1, k2;
      asm volatile("s_load_dword %0, %3, %5\n\ts_load_dword %1, %3, %6\n\ts_load_dword %2, %4, %7\n\ts_waitcnt lgkmcnt(0)"
                   : "=&s"(k0), "=&s"(k1), "=&s"(k2) : "s"(f0), "s"(f1), "s"((wg >> 3) * 3145728 + 1572860), "s"((wg >> 3) * 3145728 + 3145724),
                     "s"((wg >> 3) * 786432 + 786428) : "memory");
    }
    const u32x4 one = {0x3f803f80u, 0x3f803f80u, 0x3f803f80u, 0x3f803f80u};
    const int fq = ((n & 2) << 1) | ((n & 4) >> 1);
    LAS const char* rq0 = lds + PSCR + 24576 + 3072 * (n >> 3) + 128 * (n & 7) + 16 * (g ^ fq);
    LAS const char* rq1 = lds + PSCR + 24576 + 3072 * (n >> 3) + 128 * (n & 7) + 16 * ((4 + g) ^ fq);
    if (pq && w >= 2) {
      {
        waitvm<0>();
        lds_inc(CT_PA, ctl);
        if (w >= 4) {
          f32x4 sq = {0.f, 0.f, 0.f, 0.f}, rr = sq, cs = sq;
          const int ro = PSCR + 128 * (8 * g + tq) + 16 * (((w & 2) * 2 + tp) ^ ((tq & 2) << 1)) + 8 * (w & 1);
          u32x4 qv[6], af[6];
          lds_spin(CT_PA, ctl, 6);
#pragma unroll
          for (int s = 0; s < 6; ++s) {
            qv[s] = *(LAS const u32x4*)(((s & 1) ? rq1 : rq0) + (s >> 1) * 1024);
            af[s] = vtr2(lds + ro + s * 4096, lds + (ro ^ 32) + s * 4096 + 512);
          }
          SB();
#pragma unroll
          for (int s = 0; s < 6; ++s) { sq = mfma(one, qv[s], sq); rr = mfma(af[s], qv[s], rr); cs = mfma(af[s], one, cs); }
          const f32x4 qq = rr - (sq[0] * (1.f / C)) * cs;
          *(LAS u32x2*)(lds + LDS_QK + 4096 + ((w >> 1) & 1) * 1024 + lane * 16 + 8 * (w & 1)) = u32x2{cvtpk(qq[0], qq[1]), cvtpk(qq[2], qq[3])};
          lds_inc(CT_PB, ctl);
        }
      }
    }
    if (pq && w == 1) {
      {
        waitvm<0>();
        u32x4 kf4[4], qv[6];
        const int ko = LDS_QK + 128 * (((n & 4) << 2) | (n & 11)) + 16 * (g ^ ((n & 2) << 1));
#pragma unroll
        for (int i = 0; i < 4; ++i) kf4[i] = *(LAS const u32x4*)(lds + (ko ^ ((i & 1) * 64 + (i >> 1) * 32)) + (i >> 1) * 512);
        lds_spin(CT_PA, ctl, 6);
#pragma unroll
        for (int s = 0; s < 6; ++s) qv[s] = *(LAS const u32x4*)(((s & 1) ? rq1 : rq0) + (s >> 1) * 1024);
        const f32x4 z = {0.f, 0.f, 0.f, 0.f};
        f32x4 gq = z, sq = z;
#pragma unroll
        for (int s = 0; s < 6; ++s) { gq = mfma(qv[s], qv[s], gq); sq = mfma(one, qv[s], sq); }
        const float mean = sq[0] * (1.f / C);
        const float s2 = xrow16_sum(g == (n >> 2) ? sel4(gq, n & 3) : 0.f);
        float sc = __builtin_amdgcn_rsqf(fmaxf(fmaf(-mean, mean, s2 * (1.f / C)), 0.f) + EPS) * QSCALE;
        asm volatile("" : "+v"(sc));
        GET_TAG();
        lds_spin(CT_PB, ctl, 4);
        const u32x4 q0 = *(LAS const u32x4*)(lds + LDS_QK + 4096 + lane * 16), q1 = *(LAS const u32x4*)(lds + LDS_QK + 5120 + lane * 16);
        const f32x4 d0 = mfma(kf4[1], q1, mfma(kf4[0], q0, z)), d1 = mfma(kf4[3], q1, mfma(kf4[2], q0, z));
        const u32x4 xq = pack8(d0 * sc, d1 * sc);
        const unsigned ck = qk_check(xq, (unsigned)tg);
        asm volatile("" :: "v"(ck));
        {
          const unsigned so = QK_OFF + wg * QK_REC;
#pragma unroll
          for (int cp = 0; cp < 4; ++cp) {
            stb128(xq, rws, lane * 16 + so + cp * QK_COPY);
            stb32(ck, rws, lane * 4 + so + 1024 + cp * QK_COPY);
          }
          stb32((unsigned)tg, rws, HINT_OFF + lane * 128 + (pu >> 1) * 32 + pj * 8 + (pu & 1) * 4);
          lds_put(CT_PD, ctl, 4);
        }
      }
    }
    if (pv) {
      waitvm<0>();
      lds_inc(CT_PA, ctl);
      if (w >= 4) {
        const int ro = PSCR + 128 * (8 * g + tq) + 16 * ((2 * (w - 4) + (tp >> 1)) ^ ((tq & 2) << 1)) + 8 * (tp & 1);
        const int rv = PSCR + 24576 + 128 * n + 16 * (g ^ fq);
        f32x4 d0 = {0.f, 0.f, 0.f, 0.f}, d1 = d0;
        lds_spin(CT_PA, ctl, 7);
#pragma unroll
        for (int s = 0; s < 6; ++s) {
          const u32x4 p = vtr2(lds + ro + s * 4096, lds + (ro ^ 32) + s * 4096 + 512);
          LAS const char* pd = lds + (rv ^ ((s & 1) * 64)) + (s >> 1) * 4096;
          d0 = mfma(*(LAS const u32x4*)pd, p, d0); d1 = mfma(*(LAS const u32x4*)(pd + 2048), p, d1);
        }
        const int fi = (pj - 3) * 24 + (w - 4) * 6 + pu;
        stb128(pack8(d0, d1), rws, lane * 16 + WVO_OFF + fi * 1024);
        lds_inc(CT_PD, ctl);
        GET_TAG();
        waitvm<0>();
        if (lane < 8) stb64(u32x2{(unsigned)tg, (unsigned)(tg >> 32) | (unsigned)tstart << 8}, rws, FLAG_OFF + lane * 768 + fi * 8);
      }
    }
    const f32x4 z = {0.f, 0.f, 0.f, 0.f};
    int full = 0;
#define NEED(u_) lds_need(CT_FULL, full, ctl, (u_))
    if (w >= 4) {
      const int brg = ((g & 1) << 1) | (g >> 1);
      const int fn = (((n >> 1) & 3) << 1) | ((n >> 3) & 1);
      const int fy = ((n >> 2) & 1) * 4 + ((n >> 1) & 1) * 3;
      const int orr0 = 128 * n + 16 * (brg ^ fn), orr1 = 128 * n + 16 * ((4 + brg) ^ fn);
      const int ory0 = n < 8 ? CRS + 384 * n + 16 * (brg ^ fy) : LDS_CTL + CT_ONE, ory1 = n < 8 ? CRS + 384 * n + 16 * ((4 + brg) ^ fy) : LDS_CTL + CT_ONE;
      const int yon = n < 8 ? 1 : 0;
      const int opk = LDS_PK + 320 * g + 8 * n, ork = LDS_PK + 320 * (2 * (g >> 1) + (n >> 3)) + 256 + 8 * (n & 7) + 4 * (g & 1);
      u32x4 qk[6];
      f32x4 ref, qs;
      LAS const char *px0, *px1, *py0, *py1;
#define AT_SLOT(o_) do { px0 = lds + orr0 + (o_); px1 = lds + orr1 + (o_); py0 = lds + ory0 + __mul24(yon, (o_)); py1 = lds + ory1 + __mul24(yon, (o_)); \
      asm volatile("" : "+v"(px0), "+v"(px1), "+v"(py0), "+v"(py1)); } while (0)
#define RD_K(k_, i_) do { LAS const char* p_ = (((k_) & 1) ? px1 : px0) + ((k_) >> 1) * PART; \
    yv[i_] = *(LAS const u32x4*)((((k_) & 1) ? py1 : py0) + ((k_) >> 1) * 128); \
    xa[i_] = *(LAS const u32x4*)(p_); xb[i_] = *(LAS const u32x4*)(p_ + 2048); } while (0)
#define MF_STAT(i_) do { cy = mfma(yv[i_], yv[i_], cy); ca = mfma(yv[i_], xa[i_], ca); cb = mfma(yv[i_], xb[i_], cb); \
    ga = mfma(xa[i_], xa[i_], ga); gb = mfma(xb[i_], xb[i_], gb); } while (0)
#define MF_S(k_, i_) do { sy = mfma(qk[k_], yv[i_], sy); sa = mfma(qk[k_], xa[i_], sa); sb = mfma(qk[k_], xb[i_], sb); } while (0)
#define STATS(st, rec) do { \
      const int r = n & 3; \
      const float yy = half_copy<true>(sel4(cy, r)); \
      const f32x2 Y = swap32(yy, yy), X = swap32(sel4(ca, r), sel4(cb, r)), G = swap32(sel4(ga, r), sel4(gb, r)); \
      const float q1_ = fmaf(2.f, X[0], Y[0]) + (n < 8 ? G[0] : G[1]);       \
      const f32x2 Q = swap16(q1_, q1_); \
      const float mn_ = (X[1] + Y[1]) * (1.f / C); \
      const float var = fmaxf(fmaf(-mn_, mn_, fmaf((n & 4) ? Q[1] : Q[0], 1.f / C, EPS)), EPS), rs_ = __builtin_amdgcn_rsqf(var); \
      const f32x2 R = swap32(rs_, rs_), Mn = swap32(mn_, mn_); \
      st = f32x4{R[0], Mn[0], R[1], Mn[1]}; \
      const float v_ = (g & 1) ? var * rs_ : mn_, h_ = bfhi(__float_as_uint(v_)); \
      rec = cvtpk(h_, v_ - h_); \
    } while (0)
#define LOGITS(sv, st, base_) do { \
      _Pragma("unroll") for (int r = 0; r < 4; ++r) { \
        const float s0 = half_copy<true>(sy[r]); \
        sv[r] = fmaf(fmaf(-st[1], qs[r], sa[r] + s0), st[0], -(base_)[r]); \
        sv[4 + r] = fmaf(fmaf(-st[3], qs[r], sb[r] + s0), st[2], -(base_)[r]); \
      } \
    } while (0)
#define PUBLISH(sv, st, rec, pk_, word_) do { \
      *(LAS unsigned*)(lds + ork + (pk_) * PK_B) = rec; \
      *(LAS u32x2*)(lds + opk + (pk_) * PK_B) = u32x2{cvtpk(sv[0] * st[0], sv[1] * st[0]), cvtpk(sv[2] * st[0], sv[3] * st[0])}; \
      *(LAS u32x2*)(lds + opk + (pk_) * PK_B + 128) = u32x2{cvtpk(sv[4] * st[2], sv[5] * st[2]), cvtpk(sv[6] * st[2], sv[7] * st[2])}; \
      lds_put(CT_PRDY, ctl + 4 * (pk_), (word_)); \
    } while (0)
      int s = (w + NT - 1) & 3, slot = s + 4, pk = s + 4, ncon = 0;
      {
        u32x4 yv[12], xa[12], xb[12];
        f32x4 st0, st1;
        unsigned rec0, rec1;
#pragma unroll
        for (int h = 0; h < 2; ++h) {
          f32x4 cy = z, ga = z, ca = z, gb = z, cb = z;
          AT_SLOT((s + 4 * h) * SLOT);
          lds_wait(ctl + CT_FULL, 3 * (s + 4 * h) + 3);
#pragma unroll
          for (int k = 0; k < 6; ++k) RD_K(k, 6 * h + k);
          lds_inc(CT_FREE + 16 * h, ctl + 4 * s);
          SB();
#pragma unroll
          for (int k = 0; k < 6; ++k) MF_STAT(6 * h + k);
          SB();
          if (h == 0) { STATS(st0, rec0); asm volatile("" : "+v"(st0), "+v"(rec0)); } else { STATS(st1, rec1); asm volatile("" : "+v"(st1), "+v"(rec1)); }
          SB();
        }
        lds_wait(ctl + CT_QK, 3);
#pragma unroll
        for (int k = 0; k < 6; ++k) qk[k] = *(LAS const u32x4*)(lds + LDS_QK + k * 1024 + lane * 16);
        float s0v[8], s1v[8];
        {
          f32x4 sy = z, sa = z, sb = z;
#pragma unroll
          for (int k = 0; k < 6; ++k) MF_S(k, k);
#pragma unroll
          for (int r = 0; r < 4; ++r) qs[r] = half_copy<false>(sy[r]);
          LOGITS(s0v, st0, z);
        }
        {
          f32x4 sy = z, sa = z, sb = z;
#pragma unroll
          for (int k = 0; k < 6; ++k) MF_S(k, 6 + k);
          LOGITS(s1v, st1, z);
        }
#pragma unroll
        for (int r = 0; r < 4; ++r) ref[r] = row_max(fmaxf(fmaxf(s0v[r], s0v[4 + r]), fmaxf(s1v[r], s1v[4 + r])));
        if (n < 4) __builtin_amdgcn_ds_fmaxf((LAS float*)(ctl + CT_MAX + 16 * g + 4 * n), sel4(ref, n), __ATOMIC_RELAXED, __HIP_MEMORY_SCOPE_WORKGROUP, false);
        lds_inc(CT_QR, ctl);
        lds_spin(CT_QR, ctl, 4);
        ref = *(LAS const f32x4*)(ctl + CT_MAX + 16 * g);
#pragma unroll
        for (int j = 0; j < 8; ++j) s0v[j] = __builtin_amdgcn_exp2f(s0v[j] - ref[j & 3]);
        PUBLISH(s0v, st0, rec0, s, 2 * s + 2);
#pragma unroll
        for (int j = 0; j < 8; ++j) s1v[j] = __builtin_amdgcn_exp2f(s1v[j] - ref[j & 3]);
        if (pk >= NPK) { pk -= NPK; ncon = 3; lds_spin(CT_PCON, ctl + 4 * pk, 3); }
        s += 4;
        PUBLISH(s1v, st1, rec1, pk, 2 * s + 2);
      }
      float thr = 16.f;
      f32x4 nref = -ref;
#pragma unroll 1
      for (;;) {
        s += 4; slot += 4; slot -= slot >= NS ? NS : 0; pk += 4;
        if (pk >= NPK) { pk -= NPK; ncon += 3; }
        if (s >= NT) break;
        f32x4 sy = z, cy = z, sa, ga = z, ca = z, sb, gb = z, cb = z;
        u32x4 yv[6], xa[6], xb[6];
        unsigned fl_, word = 2 * s + 2;
        AT_SLOT(slot * SLOT);
        LAS char *pw = lds + opk + pk * PK_B, *prc = lds + ork + pk * PK_B, *pfl = ctl + 4 * pk, *pfr = ctl + 4 * slot;
        asm volatile("" : "+v"(pw), "+v"(prc), "+v"(pfl), "+v"(pfr), "+v"(word));
#define RD_X "ds_read_b32 %2, %7 offset:%11\n\tds_read_b128 %3, %8 offset:%12\n\tds_read_b128 %4, %9 offset:%12\n\t" \
             "ds_read_b128 %5, %8 offset:%13\n\tds_read_b128 %6, %9 offset:%13\n"
#define RD_IN(p_) "v"(ctl), "v"(px0), "v"(px1), "s"(3 * s + (p_) + 1), "i"(CT_FULL), "i"(PART * (p_)), "i"(PART * (p_) + 2048)
#define ISSUE(p_) do { int d0_, d1_; \
        asm volatile(RD_X : "=&s"(d0_), "=&s"(d1_), "=&v"(fl_), "=&v"(xa[2 * (p_)]), "=&v"(xa[2 * (p_) + 1]), "=&v"(xb[2 * (p_)]), "=&v"(xb[2 * (p_) + 1]) \
                     : RD_IN(p_) : "memory"); } while (0)
#define CHECK(p_) do { int cnt_; \
        asm volatile("s_mov_b32 %1, 0x400000\n\ts_branch 2f\n1:\n\t" RD_X "2:\n\ts_waitcnt lgkmcnt(4)\n\tv_readfirstlane_b32 %0, %2\n\t" \
                     "s_cmp_ge_i32 %0, %10\n\ts_cbranch_scc1 3f\n\ts_cmp_ge_i32 %10, %14\n\ts_cbranch_scc1 4f\n\ts_sleep %15\n4:\n\t" \
                     "s_sub_u32 %1, %1, 1\n\ts_cbranch_scc0 1b\n3:\n\ts_waitcnt lgkmcnt(2)" \
                     : "=&s"(full), "=&s"(cnt_), "+v"(fl_), "+v"(xa[2 * (p_)]), "+v"(xa[2 * (p_) + 1]), "+v"(xb[2 * (p_)]), "+v"(xb[2 * (p_) + 1]) \
                     : RD_IN(p_), "s"(3 * NT - 2), "i"(CALM) : "memory", "scc"); \
        asm volatile("" : "+s"(full)); } while (0)
#define GOT_B(p_) do { waitlds<0>(); asm volatile("" : "+v"(xb[2 * (p_)]), "+v"(xb[2 * (p_) + 1])); } while (0)
#define MF_A(k_) do { ca = mfma(yv[k_], xa[k_], ca); ga = mfma(xa[k_], xa[k_], ga); sa = mfma(qk[k_], xa[k_], sa); } while (0)
#define MF_B(k_) do { cb = mfma(yv[k_], xb[k_], cb); gb = mfma(xb[k_], xb[k_], gb); sb = mfma(qk[k_], xb[k_], sb); } while (0)
        const unsigned pcv = *(volatile LAS unsigned*)(pfl + CT_PCON);
        NEED(3 * s);
#pragma unroll
        for (int k = 0; k < 6; ++k) yv[k] = *(LAS const u32x4*)(((k & 1) ? py1 : py0) + (k >> 1) * 128);
        int con = __builtin_amdgcn_readfirstlane(pcv);
        SB();
#pragma unroll
        for (int k = 0; k < 6; ++k) sy = mfma(qk[k], yv[k], sy);
        asm volatile("" : "+v"(sy));
        SB();
        lds_need(CT_PCON, con, pfl, ncon);
        ISSUE(0);
        SB();
#pragma unroll
        for (int k = 0; k < 6; ++k) cy = mfma(yv[k], yv[k], cy);
#pragma unroll
        for (int r = 0; r < 4; ++r) sa[r] = half_copy<true>(sy[r]);
        const float yy = half_copy<true>(sel4(cy, n & 3));
        f32x2 Y = swap32(yy, yy);
        asm volatile("" : "+v"(Y), "+v"(sa));
        SB();
        CHECK(0);
        sb = sa;
        MF_A(0);
        asm volatile("" : "+v"(ca), "+v"(ga), "+v"(sa));
        SB();
        GOT_B(0);
        ISSUE(1);
        SB();
        MF_B(0); MF_A(1); MF_B(1);
        asm volatile("" : "+v"(ca), "+v"(ga), "+v"(sa), "+v"(cb), "+v"(gb), "+v"(sb));
        SB();
        CHECK(1);
        MF_A(2);
        asm volatile("" : "+v"(ca), "+v"(ga), "+v"(sa));
        SB();
        GOT_B(1);
        ISSUE(2);
        SB();
        MF_B(2); MF_A(3); MF_B(3);
        asm volatile("" : "+v"(ca), "+v"(ga), "+v"(sa), "+v"(cb), "+v"(gb), "+v"(sb));
        SB();
        CHECK(2);
        ca = mfma(yv[5], xa[5], mfma(yv[4], xa[4], ca));
        ga = mfma(xa[5], xa[5], mfma(xa[4], xa[4], ga));
        asm volatile("" : "+v"(ca), "+v"(ga));
        SB();
        GOT_B(2);
        cb = mfma(yv[5], xb[5], mfma(yv[4], xb[4], cb));
        asm volatile("" : "+v"(cb));
        SB();
        lds_inc(CT_FREE, pfr);
        gb = mfma(xb[4], xb[4], gb);
        float ua = sel4(ca, n & 3);
        asm volatile("" : "+v"(gb), "+v"(ua));
        SB();
        gb = mfma(xb[5], xb[5], gb);
        float va = sel4(ga, n & 3);
        asm volatile("" : "+v"(gb), "+v"(va));
        SB();
        sa = mfma(qk[4], xa[4], sa);
        float ub = sel4(cb, n & 3);
        asm volatile("" : "+v"(sa), "+v"(ub));
        SB();
        sa = mfma(qk[5], xa[5], sa);
        const f32x2 X = swap32(ua, ub);
        float mn_ = (X[1] + Y[1]) * (1.f / C), q1_ = fmaf(2.f, X[0], Y[0]);
        asm volatile("" : "+v"(sa), "+v"(mn_), "+v"(q1_));
        SB();
        sb = mfma(qk[4], xb[4], sb);
        float vb = sel4(gb, n & 3);
        asm volatile("" : "+v"(sb), "+v"(vb));
        SB();
        sb = mfma(qk[5], xb[5], sb);
        const f32x2 Mn = swap32(mn_, mn_), G = swap32(va, vb);
        q1_ += n < 8 ? G[0] : G[1];
        const f32x2 Q = swap16(q1_, q1_);
        sa -= Mn[0] * qs;
        const float var = fmaxf(fmaf(-mn_, mn_, fmaf((n & 4) ? Q[1] : Q[0], 1.f / C, EPS)), EPS), rs_ = __builtin_amdgcn_rsqf(var);
        sb -= Mn[1] * qs;
        const float v_ = (g & 1) ? var * rs_ : mn_, h_ = bfhi(__float_as_uint(v_));
        *(LAS unsigned*)prc = cvtpk(h_, v_ - h_);
        const f32x2 R = swap32(rs_, rs_);
        sa = sa * R[0] + nref; sb = sb * R[1] + nref;
        const float tm = fmaxf(fmaxf(fmaxf(fmaxf(sa[0], sa[1]), sa[2]), fmaxf(fmaxf(sa[3], sb[0]), sb[1])), fmaxf(sb[2], sb[3]));
        if (__builtin_expect(__builtin_amdgcn_ballot_w64(!(tm <= thr)) != 0ull, 0)) {
#pragma unroll
          for (int r = 0; r < 4; ++r) {
            const float mx = row_max(fmaxf(sa[r], sb[r])), dm = mx <= 16.f ? 0.f : mx;
            nref[r] -= dm; sa[r] -= dm; sb[r] -= dm;
          }
          thr = __builtin_nanf("");
          word |= 1u;
          if (n == 0) *(LAS f32x4*)(lds + LDS_PK + pk * PK_B + 1280 + 16 * g) = -nref;
        }
#pragma unroll
        for (int r = 0; r < 4; ++r) { sa[r] = __builtin_amdgcn_exp2f(sa[r]); sb[r] = __builtin_amdgcn_exp2f(sb[r]); }
        float ra = R[0], rb = R[1];
        asm volatile("" : "+v"(ra), "+v"(rb));
        sa *= ra; sb *= rb;
        *(LAS u32x2*)pw = u32x2{cvtpk(sa[0], sa[1]), cvtpk(sa[2], sa[3])};
        *(LAS u32x2*)(pw + 128) = u32x2{cvtpk(sb[0], sb[1]), cvtpk(sb[2], sb[3])};
        lds_put(CT_PRDY, pfl, word);
        SB();
      }
      GET_TAG();
      LAS char* fin = lds + xslot + 8192;
      unsigned qres = (unsigned)__builtin_amdgcn_raw_buffer_load_b16(mkrs(q, NQ * C * 2), dout * 2, 0, 0) << 16;
      unsigned ao = ((tid & 255) >> 1) * 8192 + (tid & 1) * 16 + dst * 32, tw = (unsigned)(tg >> (20 * (tid & 1))), sh = 12 * (tid & 1), oo = dout * 2;
      LAS char *fw = fin + (((lane & 8) | ((g & 1) << 2) | (g & 2)) + (lane & 1)) * 16 + 4 * (w - 4), *fr = fin + (2 * (lane & 7) + ((lane >> 3) & 1)) * 16;
      float v7 = 0.f;
      unsigned fi_ = 8u;
      const unsigned t0s = lds_get(ctl + CT_T0);
      unsigned gate = (t0s ? t0s : (unsigned)tstart) + RGATE;
      const RSRC rm = rws;
      u32x4 ro = mkrs4(out, NQ * C * 2);
      unsigned long long wm = 0x0303030303030303ull;
      asm volatile("" : "+v"(ao), "+v"(tw), "+v"(sh), "+v"(oo), "+v"(fw), "+v"(fr), "+v"(fin), "+v"(v7), "+v"(qres), "+v"(fi_), "+s"(ro), "+s"(wm));
      if (w == 4) asm volatile("s_mov_b64 exec, 0x1515\n\tbuffer_store_dword %0, %1, %2, 0 offen sc1\n\ts_mov_b64 exec, -1" :: "v"(qres), "v"(oo), "s"(ro) : "memory");
      lds_wait(ctl + CT_XD, 1);
      lds_spin(CT_ST, ctl, 4);
      gate += max((int)((unsigned)wall_clock64() + OWNG - gate), 0);
      float org = *(LAS const float*)(lds + xslot + 6144 + 4 * dq);
      *(LAS float*)(fin + 256 + 4 * (w - 4)) = org;
#pragma unroll 1
      for (int spin = 0; spin < 1024; ++spin)
        if ((int)((unsigned)wall_clock64() - gate) >= 0) break;
      u32x4 A = ldb128<16>(rm, ao, 0), B = ldb128<16>(rm, ao, 128 * 8192);
#pragma unroll 1
      for (int spin = 0; spin < (1 << 18); ++spin) {
        unsigned xa_ = (A[3] ^ tw) << 12, xb_ = (B[3] ^ tw) << 12;
        if (__builtin_expect(__builtin_amdgcn_ballot_w64((xa_ | xb_) != 0u) == 0ull, 1)) break;
        xa_ |= __builtin_amdgcn_mov_dpp(xa_, 0xB1, 0xf, 0xf, true); xb_ |= __builtin_amdgcn_mov_dpp(xb_, 0xB1, 0xf, 0xf, true);
        asm volatile("" ::: "memory");
        if (xa_ != 0u) A = ldb128<16>(rm, ao, 0);
        if (xb_ != 0u) B = ldb128<16>(rm, ao, 128 * 8192);
      }
      unsigned la = (A[3] & 0xfff00000u) >> sh, lb_ = (B[3] & 0xfff00000u) >> sh;
      la |= __builtin_amdgcn_mov_dpp(la, 0xB1, 0xf, 0xf, true); lb_ |= __builtin_amdgcn_mov_dpp(lb_, 0xB1, 0xf, 0xf, true);
      float da = __uint_as_float(la) - org, db = __uint_as_float(lb_) - org;
      if (__builtin_expect(__builtin_amdgcn_ballot_w64(!(da <= 64.f) || !(db <= 64.f)) != 0ull, 0)) {
        org = xrow16_max(row_max(fmaxf(__uint_as_float(la), __uint_as_float(lb_))));
        da = __uint_as_float(la) - org; db = __uint_as_float(lb_) - org;
        *(LAS float*)(fin + 256 + 4 * (w - 4)) = org;
        fi_ = 9u;
      }
      const float wa = __builtin_amdgcn_exp2f(da), wb = __builtin_amdgcn_exp2f(db);
#define MIX(f_, i_) fmaf(f_(A[i_]), wa, f_(B[i_]) * wb)
      const f32x2 p0 = swap32(MIX(bflo, 0), MIX(bfhi, 0)), p1 = swap32(MIX(bflo, 1), MIX(bfhi, 1));
      const f32x2 p2 = swap32(MIX(bflo, 2), MIX(bfhi, 2)), p3 = swap32(wa + wb, v7);
      const f32x2 r0 = swap16(p0[0] + p0[1], p1[0] + p1[1]), r1 = swap16(p2[0] + p2[1], p3[0] + p3[1]);
      float c0 = r0[0] + r0[1], c1 = r1[0] + r1[1];
      asm volatile("s_nop 1\n\tv_add_f32_dpp %0, %0, %0 row_ror:8 row_mask:0xf bank_mask:0x3\n\tv_add_f32_dpp %0, %1, %1 row_ror:8 row_mask:0xf bank_mask:0xc\n\t"
                   "s_nop 1\n\tv_add_f32_dpp %0, %0, %0 row_shl:4 row_mask:0xf bank_mask:0x5\n\t"
                   "s_nop 1\n\tv_add_f32_dpp %0, %0, %0 quad_perm:[2,3,0,1] row_mask:0xf bank_mask:0x5\n\t"
                   "s_mov_b64 exec, %3\n\tds_write_b32 %2, %0\n\ts_mov_b64 exec, 1\n\tds_add_u32 %4, %5 offset:%6\n\ts_mov_b64 exec, -1"
                   : "+v"(c0) : "v"(c1), "v"(fw), "s"(wm), "v"(ctl), "v"(fi_), "i"(CT_FIN) : "memory");
      if (w == 4) {
        f32x4 a0, l0;
        unsigned cf;
        int cnt_, c_;
        asm volatile("s_mov_b32 %1, 0x4000000\n1:\n\tds_read_b32 %2, %5 offset:%8\n\tds_read_b128 %3, %6\n\tds_read_b128 %4, %7 offset:192\n\t"
                     "s_waitcnt lgkmcnt(2)\n\tv_readfirstlane_b32 %0, %2\n\ts_cmp_ge_i32 %0, 32\n\ts_cbranch_scc1 2f\n\t"
                     "s_sub_u32 %1, %1, 1\n\ts_cbranch_scc0 1b\n2:\n\ts_waitcnt lgkmcnt(0)"
                     : "=&s"(c_), "=&s"(cnt_), "=&v"(cf), "=&v"(a0), "=&v"(l0) : "v"(ctl), "v"(fr), "v"(fin), "i"(CT_FIN) : "memory", "scc");
        asm volatile("" : "+s"(c_));
        if (__builtin_expect(c_ != 32, 0)) {
          f32x4 m0 = *(LAS const f32x4*)(fin + 256);
          const float mm = fmaxf(fmaxf(m0[0], m0[1]), fmaxf(m0[2], m0[3]));
#pragma unroll
          for (int p = 0; p < 4; ++p) m0[p] = __builtin_amdgcn_exp2f(m0[p] - mm);
          a0 *= m0; l0 *= m0;
        }
        const float o = __uint_as_float(qres) + ((a0[0] + a0[2]) + (a0[1] + a0[3])) * __builtin_amdgcn_rcpf((l0[0] + l0[2]) + (l0[1] + l0[3]));
        const unsigned ob = cvtpk(o, __uint_as_float(__builtin_amdgcn_mov_dpp(__float_as_uint(o), 0xB1, 0xf, 0xf, true)));
        asm volatile("s_mov_b64 exec, 0x1515\n\tbuffer_store_dword %0, %1, %2, 0 offen sc1\n\ts_mov_b64 exec, -1" :: "v"(ob), "v"(oo), "s"(ro) : "memory");
        if (wg == 0 && lane == 0) stb64(u32x2{(unsigned)tg, (unsigned)(tg >> 32)}, rws, EP_OFF);
      }
    } else {
      const int ftr = ((g & 1) << 2) | ((tq >> 1) << 1) | (g >> 1);
      const int fty = (g & 1) * 4 + (tq >> 1) * 3;
      int otr[4], oty[4];
#pragma unroll
      for (int t = 0; t < 4; ++t) {
        otr[t] = (w - 1) * PART + 128 * (4 * g + tq) + 16 * ((2 * t + (tp >> 1)) ^ ftr) + 8 * (tp & 1);
        oty[t] = CRS + (w - 1) * 128 + 384 * (4 * (g & 1) + tq) + 16 * ((2 * t + (tp >> 1)) ^ fty) + 8 * (tp & 1);
      }
      int opk = LDS_PK + 320 * tp + 8 * (4 * g + tq), ork = LDS_PK + 320 * (g >> 1) + 256 + 8 * (4 * (g & 1) + tq);
      asm volatile("" : "+v"(opk), "+v"(ork), "+v"(oty[0]), "+v"(oty[1]), "+v"(oty[2]), "+v"(oty[3]));
#define RD_T(ta, ty, so_) _Pragma("unroll") for (int t_ = 0; t_ < 4; ++t_) { \
    ta[t_] = vtr2(lds + otr[t_] + (so_), lds + otr[t_] + (so_) + 2048); ty[t_] = vtr(lds + oty[t_] + (so_)); }
      f32x4 acc[4] = {z, z, z, z}, tac = z;
#define RD_P(o_) \
      pf = vtr2(lds + opk + (o_), lds + opk + (o_) + 128); \
      sf = vtr2(lds + ork + (o_), lds + ork + (o_) + 640)
#define TWICE(ty) asm volatile("s_nop 2"); _Pragma("unroll") for (int t = 0; t < 4; ++t) { u32x2 lo_, hi_; \
      asm volatile("v_mov_b64 %0, %2\n\tv_mov_b64 %1, %2" : "=&v"(lo_), "=&v"(hi_) : "v"(ty[t])); t2[t] = u32x4{lo_[0], lo_[1], hi_[0], hi_[1]}; } \
      asm volatile("s_nop 1" : "+v"(t2[0]), "+v"(t2[1]), "+v"(t2[2]), "+v"(t2[3]))
#define MF_PX(ta) \
      tac = mfma(sf, pf, tac); \
      _Pragma("unroll") for (int t = 0; t < 4; ++t) acc[t] = mfma(ta[t], pf, acc[t])
#define MF_PY() \
      _Pragma("unroll") for (int t = 0; t < 4; ++t) acc[t] = mfma(t2[t], pf, acc[t])
#define MOVE_REF(word_, o_) do { moved |= (word_) & 1; if (__builtin_expect(moved, 0)) { \
            const float m_ = *(LAS const float*)(lds + (((word_) & 1) ? LDS_PK + (o_) + 1280 : LDS_CTL + CT_MAX) + 4 * n); \
            Mtop = fmaxf(Mtop, m_); \
            const bool drop = m_ < Mtop - 64.f; \
            const float mn = drop ? M : m_, fa = __builtin_amdgcn_exp2f(M - mn); \
            M = mn; \
            _Pragma("unroll") for (int t = 0; t < 4; ++t) acc[t] *= fa; \
            tac *= fa; \
            if (drop) pf = u32x4{0u, 0u, 0u, 0u}; \
          } } while (0)
      const unsigned flo = FLAG_OFF + (wg & 7) * 768 + (w - 1) * 144 + (lane >= 16 ? min(lane - 16, 35) : (lane >> 3) * 6 + (lane & 7) + ((lane & 7) < 6 ? 0 : 18)) * 8;
      float M, Mtop;
      int moved = 0;
      u32x4 wvv[18], pf, sf, t2[4];
      {
        u32x4 ta[8][4], xx[6];
        u32x2 ty[8][4], fl;
        unsigned cc[6];
        const int qo = QK_OFF + (wg & 3) * QK_COPY + (2 * w - 2) * QK_REC;
#define QK_LOAD() _Pragma("unroll") for (int j = 0; j < 6; ++j) { \
          xx[j] = ldb128<16>(rws, lane * 16, qo + ((j >> 1) * 6 + (j & 1)) * QK_REC); \
          cc[j] = __builtin_amdgcn_raw_buffer_load_b32(rws, lane * 4, qo + ((j >> 1) * 6 + (j & 1)) * QK_REC + 1024, 16); }
#pragma unroll
        for (int u = 0; u < 8; ++u) {
          if (u == QKAT) {
            GET_TAG();
#pragma unroll 1
            for (int r = 0; r < 8; ++r) {
#pragma unroll 1
              for (int spin = 0; spin < 128; ++spin) {
                if ((int)(wall_clock64() - tstart) >= GATE + r * HSTEP) break;
                __builtin_amdgcn_s_sleep(1);
              }
              u32x8 hv;
              asm volatile("s_load_dwordx8 %0, %1, %2\n\ts_waitcnt lgkmcnt(0)" : "=&s"(hv) : "s"(ws), "s"(HINT_OFF + ((wg & 7) + 8 * r) * 128 + (w - 1) * 32) : "memory");
              const unsigned t_ = (unsigned)tg;
              if ((((hv[0] ^ t_) | (hv[1] ^ t_) | (hv[2] ^ t_)) | ((hv[3] ^ t_) | (hv[4] ^ t_) | (hv[5] ^ t_))) == 0u) break;
            }
            if (wg < NPR) lds_wait(ctl + CT_PD, 4);
            QK_LOAD();
#pragma unroll 1
            for (int spin = 0; spin < (1 << 18); ++spin) {
              unsigned df = 0u;
#pragma unroll
              for (int j = 0; j < 6; ++j) df |= qk_check(xx[j], (unsigned)tg) ^ cc[j];
              if (__all(df == 0u)) break;
              asm volatile("" ::: "memory");
              QK_LOAD();
            }
#pragma unroll
            for (int h = 0; h < 2; ++h) {
              u32x4 sx;
#pragma unroll
              for (int i = 0; i < 4; ++i) sx[i] = add3(xx[h][i], xx[2 + h][i], xx[4 + h][i]);
              *(LAS u32x4*)(lds + LDS_QK + (2 * w - 2 + h) * 1024 + lane * 16) = sx;
            }
            lds_inc(CT_QK, ctl);
            SB();
          }
          lds_wait(ctl + CT_FULL, 3 * u + w);
          RD_T(ta[u], ty[u], u * SLOT);
          lds_inc(CT_FREE + 4 * u, ctl);
          SB();
        }
        fl = __builtin_bit_cast(u32x2, __builtin_amdgcn_raw_buffer_load_b64(rws, flo, 0, 16));
#pragma unroll
        for (int u = 0; u < 12; ++u) {
          const int word = lds_spin(CT_PRDY + 4 * (u % NPK), ctl, 2 * u + 2);
          if (u == 0) Mtop = M = *(LAS const float*)(ctl + CT_MAX + 4 * n);
          RD_P((u % NPK) * PK_B);
          if (u >= 8) MOVE_REF(word, (u % NPK) * PK_B);
          lds_inc(CT_PCON + 4 * (u % NPK), ctl);
          SB();
          MF_PX(ta[u & 7]);
          SB();
          TWICE(ty[u & 7]);
          MF_PY();
          asm volatile("" : "+v"(acc[0]), "+v"(acc[1]), "+v"(acc[2]), "+v"(acc[3]), "+v"(tac));
          SB();
#define FLAGS_UP() __all(fl[0] == (unsigned)tg && (fl[1] & 255u) == (unsigned)(tg >> 32))
          if (u == T0AT && w == 1 && FLAGS_UP()) {
            int d = (int)(fl[1] - ((unsigned)tstart << 8)) >> 8;
            d += __builtin_amdgcn_mov_dpp(d, XOR8, 0xf, 0xf, true); d += __builtin_amdgcn_mov_dpp(d, ROR4, 0xf, 0xf, true);
            d += __builtin_amdgcn_mov_dpp(d, 0x122, 0xf, 0xf, true); d += __builtin_amdgcn_mov_dpp(d, 0x121, 0xf, 0xf, true);
            lds_put(CT_T0, ctl, ((unsigned)tstart + min(max(d >> 4, -300), 300)) | 1u);
          }
          if (u < 4) {
            NEED(3 * (u + 8) + w);
            RD_T(ta[u], ty[u], ((u + 8) % NS) * SLOT);
            lds_inc(CT_FREE + 4 * ((u + 8) % NS), ctl);
            SB();
          }
        }
#pragma unroll 1
        for (int spin = 0; spin < (1 << 18); ++spin) {
          if (FLAGS_UP()) break;
          asm volatile("" ::: "memory");
          fl = __builtin_bit_cast(u32x2, __builtin_amdgcn_raw_buffer_load_b64(rws, flo, 0, 16));
        }
        asm volatile("" ::: "memory");
        if (w == 3) lds_put(CT_WV, ctl, 1);
      }
      int slot = 3, pk = 12 % NPK;
      unsigned rdy = 0u;
      const int kro[3] = {(w - 1) * 2048, (w == 3 ? 0 : w) * 2048, (w == 1 ? 2 : w - 2) * 2048};
#define RD_PACKET(z_) do { int cnt_; u32x2 p0_, p1_, p2_, p3_; \
        word = __builtin_amdgcn_readfirstlane(rdy); \
        asm volatile("s_mov_b32 %1, 0x400000\n1:\n\tds_read_b32 %2, %7 offset:%11\n\t" \
                     "ds_read_b64_tr_b16 %5, %9\n\tds_read_b64_tr_b16 %6, %9 offset:640\n\t" \
                     "ds_read_b64_tr_b16 %3, %8\n\tds_read_b64_tr_b16 %4, %8 offset:128\n\t" \
                     "s_cmp_ge_i32 %0, %10\n\ts_cbranch_scc1 2f\n\ts_waitcnt lgkmcnt(4)\n\tv_readfirstlane_b32 %0, %2\n\t" \
                     "s_cmp_ge_i32 %0, %10\n\ts_cbranch_scc1 2f\n\t" z_ "s_sub_u32 %1, %1, 1\n\ts_cbranch_scc0 1b\n2:" \
                     : "+s"(word), "=&s"(cnt_), "=&v"(rdy), "=&v"(p0_), "=&v"(p1_), "=&v"(p2_), "=&v"(p3_) \
                     : "v"(ctl + 4 * pk), "v"(lds + opk + pk * PK_B), "v"(lds + ork + pk * PK_B), "s"(2 * s + 2), "i"(CT_PRDY), "i"(CALM) : "memory", "scc"); \
        asm volatile("" : "+s"(word)); \
        pf = u32x4{p0_[0], p0_[1], p1_[0], p1_[1]}; sf = u32x4{p2_[0], p2_[1], p3_[0], p3_[1]}; } while (0)
#define GOT_PACKET() do { waitlds<0>(); asm volatile("" : "+v"(pf), "+v"(sf) : "v"(rdy)); MOVE_REF(word, pk * PK_B); } while (0)
#define RD_Y(t_, p_) do { const u32x2 lo_ = vtr(p_); asm volatile("" ::: "memory"); const u32x2 hi_ = vtr(p_); \
        t2[t_] = u32x4{lo_[0], lo_[1], hi_[0], hi_[1]}; } while (0)
      u32x4 ta[4];
      int s = 12, word;
      NEED(36 + w);
#pragma unroll
      for (int t = 0; t < 4; ++t) { ta[t] = vtr2(lds + otr[t] + 2 * SLOT, lds + otr[t] + 2 * SLOT + 2048); RD_Y(t, lds + oty[t] + 2 * SLOT); }
      lds_inc(CT_FREE + 8, ctl);
#pragma unroll 1
      for (; s < NT - 1; ++s) {
        RD_PACKET("s_sleep %12\n\t");
        LAS const char *px[4], *py[4];
#pragma unroll
        for (int t = 0; t < 4; ++t) { px[t] = lds + otr[t] + slot * SLOT; py[t] = lds + oty[t] + slot * SLOT; asm volatile("" : "+v"(px[t]), "+v"(py[t])); }
        NEED(3 * s + 3 + w);
        GOT_PACKET();
        SB();
        tac = mfma(sf, pf, tac);
        asm volatile("" : "+v"(tac));
        SB();
        lds_inc(CT_PCON, ctl + 4 * pk);
        pk = pk == NPK - 1 ? 0 : pk + 1;
#pragma unroll
        for (int t = 0; t < 4; ++t) { acc[t] = mfma(ta[t], pf, acc[t]); SB(); ta[t] = vtr2(px[t], px[t] + 2048); SB(); }
        rdy = *(volatile LAS unsigned*)(ctl + CT_PRDY + 4 * pk);
#pragma unroll
        for (int t = 0; t < 4; ++t) { acc[t] = mfma(t2[t], pf, acc[t]); SB(); RD_Y(t, py[t]); SB(); }
        lds_inc(CT_FREE, ctl + 4 * slot);
#define WV_LOAD(j_) wvv[j_] = ldb128<16>(rws, lane * 16 + ((j_) & 1) * 1024, WVO_OFF + (w - 1) * 18432 + ((j_) / 6) * 6144 + kro[((j_) % 6) >> 1])
        if ((unsigned)(s - WVS) < WV1) {
#pragma unroll
          for (int u = 0; u < WV1; ++u)
            if (s - WVS == u) WV_LOAD(u);
        }
        if (s == NT - 3) {
#pragma unroll
          for (int j = WV1; j < 18; ++j) WV_LOAD(j);
        }
        slot = slot == NS - 1 ? 0 : slot + 1;
      }
      LAS char *xw = lds + xslot + lane * 16 + kro[0], *xl = lds + xslot + 6144 + 4 * n;
      LAS const char *xr1 = lds + xslot + lane * 16 + kro[1], *xr2 = lds + xslot + lane * 16 + kro[2];
      unsigned mo = wg * 8192 + (w - 1) * 2048 + n * 64 + g * 16, tw = (unsigned)(tg >> (20 * (g & 1))) & 0xfffffu, sh = 12 * (g & 1);
      asm volatile("" : "+v"(xw), "+v"(xl), "+v"(xr1), "+v"(xr2), "+v"(mo), "+v"(tw), "+v"(sh));
      waitvm<0>();
      RD_PACKET("");
      GOT_PACKET();
      SB();
      tac = mfma(sf, pf, tac);
      asm volatile("" : "+v"(tac));
      SB();
#pragma unroll
      for (int t = 0; t < 2; ++t) acc[t] = mfma(t2[t], pf, mfma(ta[t], pf, acc[t]));
      SB();
#pragma unroll
      for (int t = 2; t < 4; ++t) acc[t] = mfma(t2[t], pf, mfma(ta[t], pf, acc[t]));
      const float L = tac[2] + tac[3], rl = __builtin_amdgcn_rcpf(L), ct = -(tac[0] + tac[1]) * rl;
      const float lse = M + __builtin_amdgcn_logf(L);
      u32x4 b[6];
      b[0] = pack8(acc[0] * rl + ct, acc[1] * rl + ct);
      *(LAS u32x4*)xw = b[0];
      *(LAS float*)xl = lse;
#pragma unroll
      for (int j = 0; j < 4; ++j) { __builtin_amdgcn_sched_group_barrier(0x008, 1, 0); __builtin_amdgcn_sched_group_barrier(0x002, 3, 0); }
      SB();
      b[1] = pack8(acc[2] * rl + ct, acc[3] * rl + ct);
      *(LAS u32x4*)(xw + 1024) = b[1];
      lds_inc(CT_XD, ctl);
      SB();
      f32x4 o[3];
      unsigned xd;
#pragma unroll
      for (int t = 0; t < 3; ++t) o[t] = mfma(wvv[6 * t], b[0], z);
      asm volatile("" : "+v"(o[0]), "+v"(o[1]), "+v"(o[2]));
      SB();
#define RD_SLICES "ds_read_b32 %0, %7 offset:%10\n\tds_read_b128 %1, %8\n\tds_read_b128 %2, %8 offset:1024\n\tds_read_b128 %3, %9\n\tds_read_b128 %4, %9 offset:1024\n"
      asm volatile(RD_SLICES : "=&v"(xd), "=&v"(b[2]), "=&v"(b[3]), "=&v"(b[4]), "=&v"(b[5]) : "i"(0), "i"(0), "v"(ctl), "v"(xr1), "v"(xr2), "i"(CT_XD) : "memory");
      SB();
#pragma unroll
      for (int t = 0; t < 3; ++t) o[t] = mfma(wvv[6 * t + 1], b[1], o[t]);
      tw |= (__float_as_uint(lse) << sh) & 0xfff00000u;
      asm volatile("" : "+v"(tw), "+v"(o[0]), "+v"(o[1]), "+v"(o[2]));
      SB();
      {
        int cnt_, c_;
        asm volatile("s_mov_b32 %6, 0x400000\n\ts_branch 2f\n1:\n\t" RD_SLICES "2:\n\ts_waitcnt lgkmcnt(4)\n\tv_readfirstlane_b32 %5, %0\n\t"
                     "s_cmp_ge_i32 %5, 3\n\ts_cbranch_scc1 3f\n\ts_sub_u32 %6, %6, 1\n\ts_cbranch_scc0 1b\n3:\n\ts_waitcnt lgkmcnt(0)"
                     : "+v"(xd), "+v"(b[2]), "+v"(b[3]), "+v"(b[4]), "+v"(b[5]), "=&s"(c_), "=&s"(cnt_)
                     : "v"(ctl), "v"(xr1), "v"(xr2), "i"(CT_XD) : "memory", "scc");
      }
      SB();
      PROJECT(2);
      lds_inc(CT_ST, ctl);
    }
  }
}

#undef rws
#define WS_AT(size_) ((size_) > (64u << 20) ? ((size_) - (64u << 20)) & ~(size_t)0x1fffff : 0)
extern "C" void kernel_launch(void* const* d_in, const int* in_sizes, int n_in, void* d_out, int out_size, void* d_ws, size_t ws_size,
                              hipStream_t stream) {
  dino_main<<<NWG, 512, 0, stream>>>((const u16*)d_in[0], (const u16*)d_in[1], (const u16*)d_in[2], (const u16*)d_in[3], (const u16*)d_in[4],
                                     (const u16*)d_in[5], (const u16*)d_in[6], (char*)d_ws + WS_AT(ws_size) + 4096, (u16*)d_out);
}
```

```cpp
#include <hip/hip_runtime.h>
#include <hip/hip_bf16.h>
#include <stdint.h>

typedef unsigned short u16;
using u32x2 = __attribute__((ext_vector_type(2))) unsigned;
using u32x4 = __attribute__((ext_vector_type(4))) unsigned;
using u32x8 = __attribute__((ext_vector_type(8))) unsigned;
using f32x2 = __attribute__((ext_vector_type(2))) float;
using f32x4 = __attribute__((ext_vector_type(4))) float;
using s16x8 = __attribute__((ext_vector_type(8))) short;
using bf16x2_t = __attribute__((ext_vector_type(2))) __bf16;
typedef short v4i16_t __attribute__((ext_vector_type(4)));
#define LAS __attribute__((address_space(3)))

constexpr int C = 192, NQ = 16, W0 = 512, W1 = 256, NWG = 256;
constexpr float EPS = 1e-5f;
constexpr float QSCALE = 0.07216878364870322f * 1.4426950408889634f;

constexpr int PART = 4096, CRS = 3 * PART;
constexpr int SLOT = CRS + 3072, NS = 10;
constexpr int LDS_QK = NS * SLOT;
constexpr int NPK = 7;
constexpr int LDS_PK = LDS_QK, PK_B = 1344;
constexpr int LDS_CTL = LDS_PK + NPK * PK_B;
constexpr int LDS_SINK = LDS_CTL + 384;
constexpr int LDS_BYTES = LDS_SINK + 256;
constexpr int PSCR = 8 * SLOT;
constexpr int CT_FULL = 8, CT_WV = 4, CT_PA = 0, CT_PB = 12, CT_QR = 16, CT_PD = 20, CT_T0 = 24, CT_XD = 28, CT_TG = 32, CT_QK = 40, CT_ST = 44,
              CT_FREE = 48, CT_FIN = 88, CT_ONE = 112, CT_PRDY = 128, CT_PCON = 160, CT_MAX = 256;

constexpr int MB_BYTES = NWG * NWG * 32;
constexpr int EP_OFF = MB_BYTES;
constexpr int FLAG_OFF = EP_OFF + 512;
constexpr int WVO_OFF = EP_OFF + 8192;
constexpr int QK_OFF = WVO_OFF + C * C * 2;
constexpr int QK_REC = 1280, QK_COPY = 18 * QK_REC;
constexpr int HINT_OFF = QK_OFF + 4 * QK_COPY;
constexpr int WS_BYTES = HINT_OFF + 64 * 128;

#define GIVE 2
constexpr int NPR = 36;
struct Tabs { unsigned seq[112]; };
constexpr Tabs make_tabs() {
  Tabs t{};
  for (int i = 0; i < 112; ++i) t.seq[i] = i + 1;
  return t;
}
__device__ const Tabs tabs = make_tabs();
#define QKAT 3
#define WV1 6
#define WMIN 28
#define WSTEP 5
#define LSTEP 40
#define OWNG 30
#define PACE0 30
#define PACE1 1740
#define RGATE 1922
#define T0AT 3
#define WVS 12
#define CALM 2
#define HSTEP 40
#define GATE 350
#define CODE_BYTES 25984

__device__ __forceinline__ unsigned cvtpk(float lo, float hi) {
  bf16x2_t v = {(__bf16)lo, (__bf16)hi};
  return __builtin_bit_cast(unsigned, v);
}
__device__ __forceinline__ float bflo(unsigned x) { return __builtin_bit_cast(float, x << 16); }
__device__ __forceinline__ float bfhi(unsigned x) { return __builtin_bit_cast(float, x & 0xffff0000u); }
template <int CTRL> __device__ __forceinline__ float dpp(float x) {
  return __builtin_bit_cast(float, __builtin_amdgcn_mov_dpp(__builtin_bit_cast(int, x), CTRL, 0xf, 0xf, true));
}
constexpr int XOR8 = 0x128, ROR4 = 0x124;
template <bool HI8> __device__ __forceinline__ float half_copy(float x) {
  const int v = __builtin_bit_cast(int, x);
  return __builtin_bit_cast(float, __builtin_amdgcn_update_dpp(v, v, XOR8, 0xf, HI8 ? 0xc : 0x3, false));
}
__device__ __forceinline__ float row_max(float x) {
  x = fmaxf(x, dpp<XOR8>(x)); x = fmaxf(x, dpp<ROR4>(x)); x = fmaxf(x, dpp<0x122>(x));
  return fmaxf(x, dpp<0x121>(x));
}
__device__ __forceinline__ f32x2 swap32(float a, float b) {
  auto s = __builtin_amdgcn_permlane32_swap(__float_as_uint(a), __float_as_uint(b), false, false);
  return f32x2{__uint_as_float(s[0]), __uint_as_float(s[1])};
}
__device__ __forceinline__ f32x2 swap16(float a, float b) {
  auto s = __builtin_amdgcn_permlane16_swap(__float_as_uint(a), __float_as_uint(b), false, false);
  return f32x2{__uint_as_float(s[0]), __uint_as_float(s[1])};
}
__device__ __forceinline__ float xrow16_max(float x) {
  auto s = __builtin_amdgcn_permlane16_swap(__float_as_uint(x), __float_as_uint(x), false, false);
  x = fmaxf(__uint_as_float(s[0]), __uint_as_float(s[1]));
  auto t = __builtin_amdgcn_permlane32_swap(__float_as_uint(x), __float_as_uint(x), false, false);
  return fmaxf(__uint_as_float(t[0]), __uint_as_float(t[1]));
}
__device__ __forceinline__ float xrow16_sum(float x) {
  auto s = __builtin_amdgcn_permlane16_swap(__float_as_uint(x), __float_as_uint(x), false, false);
  x = __uint_as_float(s[0]) + __uint_as_float(s[1]);
  auto t = __builtin_amdgcn_permlane32_swap(__float_as_uint(x), __float_as_uint(x), false, false);
  return __uint_as_float(t[0]) + __uint_as_float(t[1]);
}
__device__ __forceinline__ f32x4 mfma(u32x4 a, u32x4 b, f32x4 c) {
  return __builtin_amdgcn_mfma_f32_16x16x32_bf16(__builtin_bit_cast(s16x8, a), __builtin_bit_cast(s16x8, b), c, 0, 0, 0);
}
__device__ __forceinline__ u32x2 vtr(LAS const char* p) {
  return __builtin_bit_cast(u32x2, __builtin_amdgcn_ds_read_tr16_b64_v4i16((LAS v4i16_t*)p));
}
__device__ __forceinline__ u32x4 vtr2(LAS const char* p, LAS const char* q_) {
  const u32x2 a = vtr(p), b = vtr(q_);
  return u32x4{a[0], a[1], b[0], b[1]};
}
__device__ __forceinline__ float sel4(f32x4 v, int r) {
  const float a = (r & 1) ? v[1] : v[0], b = (r & 1) ? v[3] : v[2];
  return (r & 2) ? b : a;
}
using RSRC = __amdgpu_buffer_rsrc_t;
__device__ __forceinline__ RSRC mkrs(const void* p, int bytes) { return __builtin_amdgcn_make_buffer_rsrc(const_cast<void*>(p), (short)0, bytes, 0x00020000); }
__device__ __forceinline__ u32x4 mkrs4(const void* p, unsigned bytes) {
  const unsigned long long b = (unsigned long long)p;
  return u32x4{(unsigned)b, (unsigned)(b >> 32) & 0xffffu, bytes, 0x00020000u};
}
template <int AUX> __device__ __forceinline__ u32x4 ldb128(RSRC r, unsigned vo, int so) { return __builtin_bit_cast(u32x4, __builtin_amdgcn_raw_buffer_load_b128(r, vo, so, AUX)); }
#define SB() __builtin_amdgcn_sched_barrier(0)
#define RAW_BARRIER() asm volatile("s_waitcnt lgkmcnt(0)\n\ts_barrier" ::: "memory")
#define DMA(m0_, vo_, rs_, so_) asm volatile("s_mov_b32 m0, %0\n\ts_nop 0\n\tbuffer_load_dwordx4 %1, %2, %3 offen lds" :: "s"(m0_), "v"(vo_), "s"(rs_), "s"(so_) : "memory")
template <int N> __device__ __forceinline__ void waitlds() { __builtin_amdgcn_s_waitcnt(0xc07f | (N << 8)); }
template <int N> __device__ __forceinline__ void waitvm() { asm volatile("s_waitcnt vmcnt(%0)" :: "i"(N) : "memory"); }


__device__ __forceinline__ void lds_inc(int O, LAS char* p) {
  asm volatile("s_mov_b64 exec, 1\n\tds_add_u32 %0, %1 offset:%2\n\ts_mov_b64 exec, -1" :: "v"(p), "v"(1u), "i"(O) : "memory");
}
__device__ __forceinline__ void lds_put(int O, LAS char* p, unsigned v) {
  asm volatile("s_mov_b64 exec, 1\n\tds_write_b32 %0, %1 offset:%2\n\ts_mov_b64 exec, -1" :: "v"(p), "v"(v), "i"(O) : "memory");
}
__device__ __forceinline__ int lds_get(LAS char* p) {
  asm volatile("" ::: "memory");
  const unsigned v = __hip_atomic_load((LAS unsigned*)p, __ATOMIC_RELAXED, __HIP_MEMORY_SCOPE_WORKGROUP);
  asm volatile("" ::: "memory");
  return __builtin_amdgcn_readfirstlane(v);
}
__device__ __forceinline__ void lds_wait(LAS char* p, int target) {
#pragma unroll 1
  for (int spin = 0; spin < (1 << 22); ++spin) {
    if (lds_get(p) >= target) break;
    __builtin_amdgcn_s_sleep(1);
  }
}
__device__ __forceinline__ int lds_spin(int O, LAS char* p, int target) {
  int r, cnt = 1 << 22;
  unsigned t;
  asm volatile("1:\n\tds_read_b32 %1, %3 offset:%5\n\ts_waitcnt lgkmcnt(0)\n\tv_readfirstlane_b32 %0, %1\n\ts_cmp_ge_i32 %0, %4\n\ts_cbranch_scc1 2f\n\t"
               "s_sub_u32 %2, %2, 1\n\ts_cbranch_scc0 1b\n2:"
               : "=&s"(r), "=&v"(t), "+s"(cnt) : "v"(p), "s"(target), "i"(O) : "memory", "scc");
  return r;
}
__device__ __forceinline__ void lds_need(int O, int& have, LAS char* p, int target) {
  int cnt;
  unsigned t;
  asm volatile("s_cmp_ge_i32 %0, %4\n\ts_cbranch_scc1 2f\n\ts_mov_b32 %2, 0x400000\n"
               "1:\n\tds_read_b32 %1, %3 offset:%5\n\ts_waitcnt lgkmcnt(0)\n\tv_readfirstlane_b32 %0, %1\n\ts_cmp_ge_i32 %0, %4\n\ts_cbranch_scc1 2f\n\t"
               "s_sleep %6\n\ts_sub_u32 %2, %2, 1\n\ts_cbranch_scc0 1b\n2:"
               : "+s"(have), "=&v"(t), "=&s"(cnt) : "v"(p), "s"(target), "i"(O), "i"(CALM) : "memory", "scc");
  asm volatile("" : "+s"(have));
}
__device__ __forceinline__ u32x4 pack8(f32x4 a, f32x4 b) { return u32x4{cvtpk(a[0], a[1]), cvtpk(a[2], a[3]), cvtpk(b[0], b[1]), cvtpk(b[2], b[3])}; }

__device__ __forceinline__ void stb128(u32x4 v, RSRC r, unsigned o) { __builtin_amdgcn_raw_buffer_store_b128(v, r, o, 0, 16); }
__device__ __forceinline__ void stb64(u32x2 v, RSRC r, unsigned o) { __builtin_amdgcn_raw_buffer_store_b64(v, r, o, 0, 16); }
__device__ __forceinline__ void stb32(unsigned v, RSRC r, unsigned o) { __builtin_amdgcn_raw_buffer_store_b32(v, r, o, 0, 16); }
__device__ __forceinline__ unsigned qk_check(u32x4 x, unsigned tg) {
  const unsigned a = (x[0] ^ __builtin_amdgcn_alignbit(x[1], x[1], 11)) * 0x9e3779b1u + tg;
  const unsigned b = (x[2] + __builtin_amdgcn_alignbit(x[3], x[3], 19)) * 0x85ebca77u;
  return __builtin_amdgcn_alignbit(a, a, 15) ^ b ^ (x[1] + x[3]);
}
__device__ __forceinline__ unsigned add3(unsigned a, unsigned b, unsigned c) {
  return cvtpk((bflo(a) + bflo(b)) + bflo(c), (bfhi(a) + bfhi(b)) + bfhi(c));
}
__global__ __launch_bounds__(512, 2) void dino_main(const u16* q, const u16* f0, const u16* f1, const u16* wq, const u16* wk, const u16* wv,
                                                     const u16* wo, char* ws, u16* out) {
  __shared__ __attribute__((aligned(128))) char smem[LDS_BYTES];
  LAS char* lds = (LAS char*)smem;
  LAS char* ctl = lds + LDS_CTL;
  const unsigned lb = (unsigned)(unsigned long long)lds;
  const int w = __builtin_amdgcn_readfirstlane(threadIdx.x >> 6);
  unsigned long long pc0;
  asm volatile("s_getpc_b64 %0" : "=s"(pc0));
#define COORDS() \
  int tid = threadIdx.x, wg = blockIdx.x; \
  asm volatile("" : "+v"(tid), "+s"(wg)); \
  __builtin_assume(tid >= 0 && tid < 512); __builtin_assume(wg >= 0 && wg < NWG); \
  const int lane = tid & 63, n = lane & 15, g = lane >> 4;       \
    \
  const int dq = (wg & 7) * 2 + ((wg >> 4) & 1), dst = (wg & ~31) + (wg & 7) * 4 + ((wg >> 3) & 3); \
  const int dj = 6 * ((wg >> 5) & 1) + min(lane & 7, 5); \
  const int dout = dq * C + 48 * (wg >> 6) + 16 * (dj >> 2) + (wg & 8) + ((lane & 8) >> 1) + (dj & 3); \
  const int NT = wg < NPR ? 32 - GIVE : wg >= 40 && !(wg & 1) && (wg >> 3) < 5 + ((wg & 4) ? 8 : 10) * GIVE ? 33 : 32; \
  const int xslot = (NT >= 30 ? NT - 30 : NT - 20) * SLOT;       \
  (void)xslot; (void)n; (void)g; (void)dst; (void)dout; (void)NT
#define ARGS_IN() asm volatile("s_waitcnt lgkmcnt(0)" :: "s"(q), "s"(f0), "s"(f1), "s"(wq), "s"(wk), "s"(wv), "s"(wo), "s"(ws), "s"(out) : "memory")
#define rws mkrs(ws, WS_BYTES)
#define PROJECT(k0_) do { \
      _Pragma("unroll") for (int k = k0_; k < 6; ++k) { o[0] = mfma(wvv[k], b[k], o[0]); o[1] = mfma(wvv[6 + k], b[k], o[1]); } \
      asm volatile("" : "+v"(o[0]), "+v"(o[1])); \
      SB(); \
      _Pragma("unroll") for (int k = k0_; k < 6; ++k) o[2] = mfma(wvv[12 + k], b[k], o[2]); \
      asm volatile("" : "+v"(o[2])); \
      SB(); \
      stb128(u32x4{cvtpk(o[0][0], o[0][1]), cvtpk(o[0][2], o[0][3]), cvtpk(o[1][0], o[1][1]), tw}, rws, mo); \
      SB(); \
      stb128(u32x4{cvtpk(o[1][2], o[1][3]), cvtpk(o[2][0], o[2][1]), cvtpk(o[2][2], o[2][3]), tw}, rws, mo + 1024); \
    } while (0)
  unsigned long long tg;
#define GET_TAG() tg = ((unsigned long long)((unsigned)lds_spin(CT_TG + 4, ctl, 1) & 0xffu) << 32) | (unsigned)lds_get(ctl + CT_TG)
  const unsigned long long tstart = wall_clock64();
  if (w != 0) {
    const unsigned l0 = threadIdx.x & 63;
    if (blockIdx.x < NPR) {
      const unsigned p6 = (blockIdx.x * 43) >> 8, pu = blockIdx.x - 6 * p6;
      unsigned vo = ((l0 * 16) ^ (((l0 * 4) & 0x40) | (l0 & 0x20))) + (l0 >> 3) * 256;
      const bool pv = p6 >= 3, wa = w >= 4, isq = !pv && (w & 6) == 2;
      const unsigned a6 = 6 * w - 24, pj = pv ? p6 - 3 : p6;
      unsigned so = (wa ? a6 : isq ? w - 2 : 4 * pu) * 3072 + (isq ? 0 : pv && !wa ? w - 1 : pj) * 128, ga = isq ? 128 : 3072;
      unsigned lo = lb + PSCR + (wa ? a6 : pv ? 4 * w + 20 : (0x1b181e00u >> (8 * w)) & 255u) * 1024, hi = lo + (wa ? 6144 : isq ? 3072 : 4096);
      asm volatile("" : "+v"(vo), "+s"(lo), "+s"(so), "+s"(hi), "+s"(ga));
      ARGS_IN();
      const u32x4 rw = mkrs4(wa ? (pv ? wo : wq) : pv ? wv : isq ? q : wk, isq ? NQ * C * 2 : C * C * 2);
#pragma unroll 1
      do { DMA(lo, vo, rw, so); lo += 1024; so += ga; } while (lo != hi);
    } else if (w == 7 && blockIdx.x >= 56 && blockIdx.x < 72) {
      const u32x4 rc = mkrs4((const void*)(pc0 & ~255ull), CODE_BYTES);
#pragma unroll 1
      for (int i = 0; i < CODE_BYTES; i += 4096)
        asm volatile("s_mov_b32 m0, %0\n\ts_nop 0\n\tbuffer_load_dword %1, %2, %3 offen lds" :: "s"(lb + LDS_SINK), "v"(l0 * 64), "s"(rc), "s"(i) : "memory");
    }
    if (w == 3) {
      unsigned long long ep;
      asm volatile("s_waitcnt lgkmcnt(0)\n\ts_load_dwordx2 %0, %1, %2\n\ts_barrier\n\ts_waitcnt lgkmcnt(0)" : "=&s"(ep) : "s"(ws), "s"(EP_OFF) : "memory");
      *(LAS unsigned long long*)(ctl + CT_TG) = ((ep + 0x100001ull) & 0xffffffffffull) | 0x10000000000ull;
    } else RAW_BARRIER();
  }
  SB();
  if (__builtin_expect(w == 0, 1)) {
    const unsigned l0 = threadIdx.x & 63, g0 = blockIdx.x;
    unsigned y0 = (l0 * 16) ^ (l0 >= 48 ? 48 : 0), y1 = (l0 * 16) ^ (l0 >= 32 ? 64 : 48), y2 = (l0 * 16) ^ (l0 >= 16 ? 112 : 64);
    unsigned v0 = ((l0 * 16) ^ ((l0 * 2) & 0x60)) + (l0 >> 3) * 640 + 1024, v1 = (v0 ^ 16) - 640;
    unsigned v2 = v0 + W0 * C * 2 - 2048, v3 = v1 + W0 * C * 2 - 2048;
    unsigned b0 = (g0 & 7) * (2 * W0 * C * 2) + (g0 >> 3) * 6144, b1 = (g0 & 7) * (W1 * C * 2) + (g0 >> 3) * 3072;
    {
      const unsigned iv = (l0 & 15) >= CT_ONE / 8 ? 0x3f803f80u : (l0 >> 3) == CT_MAX / 64 ? 0xff800000u : 0u;
      *(LAS u32x2*)(ctl + 8 * min(l0, 47u)) = u32x2{l0 >= 47 ? 0x3f803f80u : iv, l0 >= 47 ? 0x3f803f80u : iv};
    }
    constexpr unsigned st0 = 16 * W0 * C * 2, st1 = 8 * W1 * C * 2;
    unsigned sb = lb, fo = CT_FREE, fv = 0u;
    int lap = 0;
    u32x4 rt = mkrs4(tabs.seq, 112 * 4);
    unsigned vz = 0u;
    asm volatile("" : "+v"(v0), "+v"(v1), "+v"(v2), "+v"(v3), "+v"(y0), "+v"(y1), "+v"(y2), "+v"(vz), "+s"(b0), "+s"(b1), "+s"(rt));
    ARGS_IN();
    const u32x4 r0 = mkrs4((const char*)f0 - 1024, W0 * W0 * C * 2 + 1024), r1 = mkrs4(f1, W1 * W1 * C * 2);
    if (g0 < NPR) __builtin_amdgcn_s_barrier();
#define UNIT(p_) do { unsigned so_; \
      asm volatile("s_add_u32 m0, %1, %10\n\ts_add_u32 %0, %2, %11\n\t" \
                   "buffer_load_dwordx4 %3, %7, %0 offen nt lds\n\tbuffer_load_dwordx4 %4, %7, %0 offen offset:1024 nt lds\n\t" \
                   "buffer_load_dwordx4 %5, %7, %0 offen offset:2048 nt lds\n\tbuffer_load_dwordx4 %6, %7, %0 offen offset:3072 nt lds\n\t" \
                   "s_mov_b32 m0, %12\n\ts_mov_b64 exec, 1\n\tbuffer_load_dword %13, %8, %9 offen offset:%14 lds\n\ts_mov_b64 exec, -1" \
                   : "=&s"(so_) : "s"(sb), "s"(b0), "v"(v0), "v"(v1), "v"(v2), "v"(v3), "s"(r0), "s"(rt), "s"(12 * s), "i"((p_) * PART), "i"((p_) * 128), \
                     "s"(lb + LDS_CTL + CT_FULL - 4 * (p_)), "v"(vz), "i"(4 * (p_)) : "memory", "scc"); } while (0)
#define WINDOW() \
      asm volatile("s_waitcnt vmcnt(%1)\n\ts_cmp_ge_i32 %0, %6\n\ts_cbranch_scc1 1f\n\ts_waitcnt vmcnt(%2)\n\ts_cmp_ge_i32 %0, %7\n\ts_cbranch_scc1 1f\n\t" \
                   "s_waitcnt vmcnt(%3)\n\ts_cmp_ge_i32 %0, %8\n\ts_cbranch_scc1 1f\n\ts_waitcnt vmcnt(%4)\n\ts_cmp_ge_i32 %0, %9\n\ts_cbranch_scc1 1f\n\t" \
                   "s_waitcnt vmcnt(%5)\n1:" :: "s"(late), "i"(WMIN + 4 * WSTEP), "i"(WMIN + 3 * WSTEP), "i"(WMIN + 2 * WSTEP), "i"(WMIN + WSTEP), "i"(WMIN), \
                   "i"(4 * LSTEP * 256), "i"(3 * LSTEP * 256), "i"(2 * LSTEP * 256), "i"(LSTEP * 256) : "memory", "scc")
#define COARSE(sb_) \
      asm volatile("s_add_u32 m0, %0, %6\n\ts_nop 0\n\tbuffer_load_dwordx4 %1, %4, %5 offen nt lds\n\t" \
                   "buffer_load_dwordx4 %2, %4, %5 offen offset:1024 nt lds\n\tbuffer_load_dwordx4 %3, %4, %5 offen offset:2048 nt lds" \
                   :: "s"(sb_), "v"(y0), "v"(y1), "v"(y2), "s"(r1), "s"(b1), "i"(CRS) : "memory", "scc")
    COARSE(sb);
#pragma unroll 1
    for (int s = 0; s < 2; ++s) {
      asm volatile("s_add_u32 %0, %0, %1" : "+s"(b1) : "i"(st1) : "scc");
      UNIT(0); UNIT(1); COARSE(sb + SLOT); UNIT(2);
      sb += SLOT; b0 += st0;
    }
    if (g0 >= NPR) __builtin_amdgcn_s_barrier();
    __builtin_amdgcn_s_setprio(3);
    COORDS();
    fo += 12;
    const int tau = __builtin_amdgcn_readfirstlane((int)((PACE1 - PACE0) * 256.f * __builtin_amdgcn_rcpf((float)NT)));
    unsigned due = PACE0 * 256u + 2 * tau, t0v = 0u;
    unsigned long long now = tstart;
    const unsigned xi = (wg >> 3) - 5, xrow = 8 * (31 - xi % GIVE) + (wg & 7) + (xi / GIVE & 1), xcol = xi / GIVE >> 1;
    const unsigned x0 = xrow * (2 * W0 * C * 2) + xcol * 6144, x1 = xrow * (W1 * C * 2) + xcol * 3072;
#pragma unroll 1
    for (int s = 2; s < NT; ++s) {
      asm volatile("" : "+s"(now));
      const unsigned t0s = __builtin_amdgcn_readfirstlane(t0v), t0 = t0s ? t0s : (unsigned)tstart;
      if ((int)(((unsigned)now - t0) * 256u - due) < 0) {
#pragma unroll 1
        for (int spin = 0; spin < 256; ++spin) {
          if ((int)(((unsigned)wall_clock64() - t0) * 256u - due) >= 0) break;
          __builtin_amdgcn_s_sleep(1);
        }
      }
      fv = *(volatile LAS unsigned*)(ctl + (s == 7 ? CT_PD : fo));
      now = wall_clock64();
      UNIT(0);
      asm volatile("" : "+s"(now));
      const int late = (int)(((unsigned)now - t0) * 256u - due);
      due += tau;
      WINDOW();
      UNIT(1);
      WINDOW();
      const unsigned nsb = fo == CT_FREE ? lb : sb + SLOT;
      const unsigned n0 = s == 31 ? x0 : b0 + st0;
      b1 = s == 31 ? x1 : b1 + st1;
      if (s + 1 < NT) {
        const int need = s == 7 && wg < NPR ? 4 : lap;
        if (__builtin_expect((int)__builtin_amdgcn_readfirstlane(fv) < need, 0)) {
          lds_wait(ctl + (s == 7 ? CT_PD : fo), need);
        }
        COARSE(nsb);
      }
      now = wall_clock64();
      t0v = *(volatile LAS unsigned*)(ctl + CT_T0);
      UNIT(2);
      WINDOW();
      if (fo == CT_FREE + 4 * (NS - 1)) { fo = CT_FREE; lap += 4; } else fo += 4;
      sb = nsb; b0 = n0;
    }
    __builtin_amdgcn_s_setprio(0);
    u32x4 wvv[18], b[6];
    asm volatile("s_mov_b32 m0, %0\n\ts_mov_b64 exec, 1\n\tbuffer_load_dword %1, %2, 0 offen lds\n\ts_mov_b64 exec, -1"
                 :: "s"(lb + LDS_SINK), "v"(vz), "s"(mkrs4(ws - 4096, 4096)) : "memory");
    lds_wait(ctl + CT_WV, 1);
#pragma unroll
    for (int j = 0; j < 18; ++j) wvv[j] = ldb128<16>(rws, lane * 16 + (j & 3) * 1024, WVO_OFF + 54 * 1024 + (j >> 2) * 4096);
    GET_TAG();
    LAS const char *xr = lds + xslot + lane * 16, *xl = lds + xslot + 6144 + 4 * n;
    unsigned mo = wg * 8192 + 6144 + n * 64 + g * 16, tw = (unsigned)(tg >> (20 * (g & 1))) & 0xfffffu, sh = 12 * (g & 1), lse;
    asm volatile("" : "+v"(xr), "+v"(xl), "+v"(mo), "+v"(tw), "+v"(sh));
    __builtin_amdgcn_s_waitcnt(0x0f70);
    lds_spin(CT_PRDY, ctl + 4 * ((NT - 1) % NPK), 2 * NT);
    __builtin_amdgcn_s_sleep(2);
    __builtin_amdgcn_s_setprio(2);
    {
      int cnt_, c_;
      unsigned t_;
      asm volatile("s_mov_b32 %1, 0x400000\n1:\n\tds_read_b32 %2, %10 offset:%13\n\tds_read_b128 %3, %11\n\tds_read_b128 %4, %11 offset:1024\n\t"
                   "ds_read_b128 %5, %11 offset:2048\n\tds_read_b128 %6, %11 offset:3072\n\tds_read_b128 %7, %11 offset:4096\n\t"
                   "ds_read_b128 %8, %11 offset:5120\n\tds_read_b32 %9, %12\n\ts_waitcnt lgkmcnt(7)\n\tv_readfirstlane_b32 %0, %2\n\t"
                   "s_cmp_ge_i32 %0, 3\n\ts_cbranch_scc1 2f\n\ts_sub_u32 %1, %1, 1\n\ts_cbranch_scc0 1b\n2:\n\ts_waitcnt lgkmcnt(5)"
                   : "=&s"(c_), "=&s"(cnt_), "=&v"(t_), "=&v"(b[0]), "=&v"(b[1]), "=&v"(b[2]), "=&v"(b[3]), "=&v"(b[4]), "=&v"(b[5]), "=&v"(lse)
                   : "v"(ctl), "v"(xr), "v"(xl), "i"(CT_XD) : "memory", "scc");
    }
    SB();
    f32x4 o[3] = {{0.f, 0.f, 0.f, 0.f}, {0.f, 0.f, 0.f, 0.f}, {0.f, 0.f, 0.f, 0.f}};
#pragma unroll
    for (int k = 0; k < 2; ++k)
#pragma unroll
      for (int t = 0; t < 3; ++t) o[t] = mfma(wvv[6 * t + k], b[k], o[t]);
    asm volatile("s_waitcnt lgkmcnt(0)" : "+v"(o[0]), "+v"(o[1]), "+v"(o[2]), "+v"(b[2]), "+v"(b[3]), "+v"(b[4]), "+v"(b[5]), "+v"(lse));
    SB();
    tw |= (lse << sh) & 0xfff00000u;
    PROJECT(2);
    lds_inc(CT_ST, ctl);
  } else {
    COORDS();
    const int tq = (lane >> 2) & 3, tp = lane & 3;
    const bool pq = wg < 18;
    const bool pv = wg >= 18 && wg < NPR;
    const int pj = wg / 6, pu = wg % 6;
    if (w >= 4 && ((w + NT) & 3) == 0 && !pq) {
      unsigned k0, k1, k2;
      asm volatile("s_load_dword %0, %3, %5\n\ts_load_dword %1, %3, %6\n\ts_load_dword %2, %4, %7\n\ts_waitcnt lgkmcnt(0)"
                   : "=&s"(k0), "=&s"(k1), "=&s"(k2) : "s"(f0), "s"(f1), "s"((wg >> 3) * 3145728 + 1572860), "s"((wg >> 3) * 3145728 + 3145724),
                     "s"((wg >> 3) * 786432 + 786428) : "memory");
    }
    const u32x4 one = {0x3f803f80u, 0x3f803f80u, 0x3f803f80u, 0x3f803f80u};
    const int fq = ((n & 2) << 1) | ((n & 4) >> 1);
    LAS const char* rq0 = lds + PSCR + 24576 + 3072 * (n >> 3) + 128 * (n & 7) + 16 * (g ^ fq);
    LAS const char* rq1 = lds + PSCR + 24576 + 3072 * (n >> 3) + 128 * (n & 7) + 16 * ((4 + g) ^ fq);
    if (pq && w >= 2) {
      {
        waitvm<0>();
        lds_inc(CT_PA, ctl);
        if (w >= 4) {
          f32x4 sq = {0.f, 0.f, 0.f, 0.f}, rr = sq, cs = sq;
          const int ro = PSCR + 128 * (8 * g + tq) + 16 * (((w & 2) * 2 + tp) ^ ((tq & 2) << 1)) + 8 * (w & 1);
          u32x4 qv[6], af[6];
          lds_spin(CT_PA, ctl, 6);
#pragma unroll
          for (int s = 0; s < 6; ++s) {
            qv[s] = *(LAS const u32x4*)(((s & 1) ? rq1 : rq0) + (s >> 1) * 1024);
            af[s] = vtr2(lds + ro + s * 4096, lds + (ro ^ 32) + s * 4096 + 512);
          }
          SB();
#pragma unroll
          for (int s = 0; s < 6; ++s) { sq = mfma(one, qv[s], sq); rr = mfma(af[s], qv[s], rr); cs = mfma(af[s], one, cs); }
          const f32x4 qq = rr - (sq[0] * (1.f / C)) * cs;
          *(LAS u32x2*)(lds + LDS_QK + 4096 + ((w >> 1) & 1) * 1024 + lane * 16 + 8 * (w & 1)) = u32x2{cvtpk(qq[0], qq[1]), cvtpk(qq[2], qq[3])};
          lds_inc(CT_PB, ctl);
        }
      }
    }
    if (pq && w == 1) {
      {
        waitvm<0>();
        u32x4 kf4[4], qv[6];
        const int ko = LDS_QK + 128 * (((n & 4) << 2) | (n & 11)) + 16 * (g ^ ((n & 2) << 1));
#pragma unroll
        for (int i = 0; i < 4; ++i) kf4[i] = *(LAS const u32x4*)(lds + (ko ^ ((i & 1) * 64 + (i >> 1) * 32)) + (i >> 1) * 512);
        lds_spin(CT_PA, ctl, 6);
#pragma unroll
        for (int s = 0; s < 6; ++s) qv[s] = *(LAS const u32x4*)(((s & 1) ? rq1 : rq0) + (s >> 1) * 1024);
        const f32x4 z = {0.f, 0.f, 0.f, 0.f};
        f32x4 gq = z, sq = z;
#pragma unroll
        for (int s = 0; s < 6; ++s) { gq = mfma(qv[s], qv[s], gq); sq = mfma(one, qv[s], sq); }
        const float mean = sq[0] * (1.f / C);
        const float s2 = xrow16_sum(g == (n >> 2) ? sel4(gq, n & 3) : 0.f);
        float sc = __builtin_amdgcn_rsqf(fmaxf(fmaf(-mean, mean, s2 * (1.f / C)), 0.f) + EPS) * QSCALE;
        asm volatile("" : "+v"(sc));
        GET_TAG();
        lds_spin(CT_PB, ctl, 4);
        const u32x4 q0 = *(LAS const u32x4*)(lds + LDS_QK + 4096 + lane * 16), q1 = *(LAS const u32x4*)(lds + LDS_QK + 5120 + lane * 16);
        const f32x4 d0 = mfma(kf4[1], q1, mfma(kf4[0], q0, z)), d1 = mfma(kf4[3], q1, mfma(kf4[2], q0, z));
        const u32x4 xq = pack8(d0 * sc, d1 * sc);
        const unsigned ck = qk_check(xq, (unsigned)tg);
        asm volatile("" :: "v"(ck));
        {
          const unsigned so = QK_OFF + wg * QK_REC;
#pragma unroll
          for (int cp = 0; cp < 4; ++cp) {
            stb128(xq, rws, lane * 16 + so + cp * QK_COPY);
            stb32(ck, rws, lane * 4 + so + 1024 + cp * QK_COPY);
          }
          stb32((unsigned)tg, rws, HINT_OFF + lane * 128 + (pu >> 1) * 32 + pj * 8 + (pu & 1) * 4);
          lds_put(CT_PD, ctl, 4);
        }
      }
    }
    if (pv) {
      waitvm<0>();
      lds_inc(CT_PA, ctl);
      if (w >= 4) {
        const int ro = PSCR + 128 * (8 * g + tq) + 16 * ((2 * (w - 4) + (tp >> 1)) ^ ((tq & 2) << 1)) + 8 * (tp & 1);
        const int rv = PSCR + 24576 + 128 * n + 16 * (g ^ fq);
        f32x4 d0 = {0.f, 0.f, 0.f, 0.f}, d1 = d0;
        lds_spin(CT_PA, ctl, 7);
#pragma unroll
        for (int s = 0; s < 6; ++s) {
          const u32x4 p = vtr2(lds + ro + s * 4096, lds + (ro ^ 32) + s * 4096 + 512);
          LAS const char* pd = lds + (rv ^ ((s & 1) * 64)) + (s >> 1) * 4096;
          d0 = mfma(*(LAS const u32x4*)pd, p, d0); d1 = mfma(*(LAS const u32x4*)(pd + 2048), p, d1);
        }
        const int fi = (pj - 3) * 24 + (w - 4) * 6 + pu;
        stb128(pack8(d0, d1), rws, lane * 16 + WVO_OFF + fi * 1024);
        lds_inc(CT_PD, ctl);
        GET_TAG();
        waitvm<0>();
        if (lane < 8) stb64(u32x2{(unsigned)tg, (unsigned)(tg >> 32) | (unsigned)tstart << 8}, rws, FLAG_OFF + lane * 768 + fi * 8);
      }
    }
    const f32x4 z = {0.f, 0.f, 0.f, 0.f};
    int full = 0;
#define NEED(u_) lds_need(CT_FULL, full, ctl, (u_))
    if (w >= 4) {
      const int brg = ((g & 1) << 1) | (g >> 1);
      const int fn = (((n >> 1) & 3) << 1) | ((n >> 3) & 1);
      const int fy = ((n >> 2) & 1) * 4 + ((n >> 1) & 1) * 3;
      const int orr0 = 128 * n + 16 * (brg ^ fn), orr1 = 128 * n + 16 * ((4 + brg) ^ fn);
      const int ory0 = n < 8 ? CRS + 384 * n + 16 * (brg ^ fy) : LDS_CTL + CT_ONE, ory1 = n < 8 ? CRS + 384 * n + 16 * ((4 + brg) ^ fy) : LDS_CTL + CT_ONE;
      const int yon = n < 8 ? 1 : 0;
      const int opk = LDS_PK + 320 * g + 8 * n, ork = LDS_PK + 320 * (2 * (g >> 1) + (n >> 3)) + 256 + 8 * (n & 7) + 4 * (g & 1);
      u32x4 qk[6];
      f32x4 ref, qs;
      LAS const char *px0, *px1, *py0, *py1;
#define AT_SLOT(o_) do { px0 = lds + orr0 + (o_); px1 = lds + orr1 + (o_); py0 = lds + ory0 + __mul24(yon, (o_)); py1 = lds + ory1 + __mul24(yon, (o_)); \
      asm volatile("" : "+v"(px0), "+v"(px1), "+v"(py0), "+v"(py1)); } while (0)
#define RD_K(k_, i_) do { LAS const char* p_ = (((k_) & 1) ? px1 : px0) + ((k_) >> 1) * PART; \
    yv[i_] = *(LAS const u32x4*)((((k_) & 1) ? py1 : py0) + ((k_) >> 1) * 128); \
    xa[i_] = *(LAS const u32x4*)(p_); xb[i_] = *(LAS const u32x4*)(p_ + 2048); } while (0)
#define MF_STAT(i_) do { cy = mfma(yv[i_], yv[i_], cy); ca = mfma(yv[i_], xa[i_], ca); cb = mfma(yv[i_], xb[i_], cb); \
    ga = mfma(xa[i_], xa[i_], ga); gb = mfma(xb[i_], xb[i_], gb); } while (0)
#define MF_S(k_, i_) do { sy = mfma(qk[k_], yv[i_], sy); sa = mfma(qk[k_], xa[i_], sa); sb = mfma(qk[k_], xb[i_], sb); } while (0)
#define STATS(st, rec) do { \
      const int r = n & 3; \
      const float yy = half_copy<true>(sel4(cy, r)); \
      const f32x2 Y = swap32(yy, yy), X = swap32(sel4(ca, r), sel4(cb, r)), G = swap32(sel4(ga, r), sel4(gb, r)); \
      const float q1_ = fmaf(2.f, X[0], Y[0]) + (n < 8 ? G[0] : G[1]);       \
      const f32x2 Q = swap16(q1_, q1_); \
      const float mn_ = (X[1] + Y[1]) * (1.f / C); \
      const float var = fmaxf(fmaf(-mn_, mn_, fmaf((n & 4) ? Q[1] : Q[0], 1.f / C, EPS)), EPS), rs_ = __builtin_amdgcn_rsqf(var); \
      const f32x2 R = swap32(rs_, rs_), Mn = swap32(mn_, mn_); \
      st = f32x4{R[0], Mn[0], R[1], Mn[1]}; \
      const float v_ = (g & 1) ? var * rs_ : mn_, h_ = bfhi(__float_as_uint(v_)); \
      rec = cvtpk(h_, v_ - h_); \
    } while (0)
#define LOGITS(sv, st, base_) do { \
      _Pragma("unroll") for (int r = 0; r < 4; ++r) { \
        const float s0 = half_copy<true>(sy[r]); \
        sv[r] = fmaf(fmaf(-st[1], qs[r], sa[r] + s0), st[0], -(base_)[r]); \
        sv[4 + r] = fmaf(fmaf(-st[3], qs[r], sb[r] + s0), st[2], -(base_)[r]); \
      } \
    } while (0)
#define PUBLISH(sv, st, rec, pk_, word_) do { \
      *(LAS unsigned*)(lds + ork + (pk_) * PK_B) = rec; \
      *(LAS u32x2*)(lds + opk + (pk_) * PK_B) = u32x2{cvtpk(sv[0] * st[0], sv[1] * st[0]), cvtpk(sv[2] * st[0], sv[3] * st[0])}; \
      *(LAS u32x2*)(lds + opk + (pk_) * PK_B + 128) = u32x2{cvtpk(sv[4] * st[2], sv[5] * st[2]), cvtpk(sv[6] * st[2], sv[7] * st[2])}; \
      lds_put(CT_PRDY, ctl + 4 * (pk_), (word_)); \
    } while (0)
      int s = (w + NT - 1) & 3, slot = s + 4, pk = s + 4, ncon = 0;
      {
        u32x4 yv[12], xa[12], xb[12];
        f32x4 st0, st1;
        unsigned rec0, rec1;
#pragma unroll
        for (int h = 0; h < 2; ++h) {
          f32x4 cy = z, ga = z, ca = z, gb = z, cb = z;
          AT_SLOT((s + 4 * h) * SLOT);
          lds_wait(ctl + CT_FULL, 3 * (s + 4 * h) + 3);
#pragma unroll
          for (int k = 0; k < 6; ++k) RD_K(k, 6 * h + k);
          lds_inc(CT_FREE + 16 * h, ctl + 4 * s);
          SB();
#pragma unroll
          for (int k = 0; k < 6; ++k) MF_STAT(6 * h + k);
          SB();
          if (h == 0) { STATS(st0, rec0); asm volatile("" : "+v"(st0), "+v"(rec0)); } else { STATS(st1, rec1); asm volatile("" : "+v"(st1), "+v"(rec1)); }
          SB();
        }
        lds_wait(ctl + CT_QK, 3);
#pragma unroll
        for (int k = 0; k < 6; ++k) qk[k] = *(LAS const u32x4*)(lds + LDS_QK + k * 1024 + lane * 16);
        float s0v[8], s1v[8];
        {
          f32x4 sy = z, sa = z, sb = z;
#pragma unroll
          for (int k = 0; k < 6; ++k) MF_S(k, k);
#pragma unroll
          for (int r = 0; r < 4; ++r) qs[r] = half_copy<false>(sy[r]);
          LOGITS(s0v, st0, z);
        }
        {
          f32x4 sy = z, sa = z, sb = z;
#pragma unroll
          for (int k = 0; k < 6; ++k) MF_S(k, 6 + k);
          LOGITS(s1v, st1, z);
        }
#pragma unroll
        for (int r = 0; r < 4; ++r) ref[r] = row_max(fmaxf(fmaxf(s0v[r], s0v[4 + r]), fmaxf(s1v[r], s1v[4 + r])));
        if (n < 4) __builtin_amdgcn_ds_fmaxf((LAS float*)(ctl + CT_MAX + 16 * g + 4 * n), sel4(ref, n), __ATOMIC_RELAXED, __HIP_MEMORY_SCOPE_WORKGROUP, false);
        lds_inc(CT_QR, ctl);
        lds_spin(CT_QR, ctl, 4);
        ref = *(LAS const f32x4*)(ctl + CT_MAX + 16 * g);
#pragma unroll
        for (int j = 0; j < 8; ++j) s0v[j] = __builtin_amdgcn_exp2f(s0v[j] - ref[j & 3]);
        PUBLISH(s0v, st0, rec0, s, 2 * s + 2);
#pragma unroll
        for (int j = 0; j < 8; ++j) s1v[j] = __builtin_amdgcn_exp2f(s1v[j] - ref[j & 3]);
        if (pk >= NPK) { pk -= NPK; ncon = 3; lds_spin(CT_PCON, ctl + 4 * pk, 3); }
        s += 4;
        PUBLISH(s1v, st1, rec1, pk, 2 * s + 2);
      }
      float thr = 16.f;
      f32x4 nref = -ref;
#pragma unroll 1
      for (;;) {
        s += 4; slot += 4; slot -= slot >= NS ? NS : 0; pk += 4;
        if (pk >= NPK) { pk -= NPK; ncon += 3; }
        if (s >= NT) break;
        f32x4 sy = z, cy = z, sa, ga = z, ca = z, sb, gb = z, cb = z;
        u32x4 yv[6], xa[6], xb[6];
        unsigned fl_, word = 2 * s + 2;
        AT_SLOT(slot * SLOT);
        LAS char *pw = lds + opk + pk * PK_B, *prc = lds + ork + pk * PK_B, *pfl = ctl + 4 * pk, *pfr = ctl + 4 * slot;
        asm volatile("" : "+v"(pw), "+v"(prc), "+v"(pfl), "+v"(pfr), "+v"(word));
#define RD_X "ds_read_b32 %2, %7 offset:%11\n\tds_read_b128 %3, %8 offset:%12\n\tds_read_b128 %4, %9 offset:%12\n\t" \
             "ds_read_b128 %5, %8 offset:%13\n\tds_read_b128 %6, %9 offset:%13\n"
#define RD_IN(p_) "v"(ctl), "v"(px0), "v"(px1), "s"(3 * s + (p_) + 1), "i"(CT_FULL), "i"(PART * (p_)), "i"(PART * (p_) + 2048)
#define ISSUE(p_) do { int d0_, d1_; \
        asm volatile(RD_X : "=&s"(d0_), "=&s"(d1_), "=&v"(fl_), "=&v"(xa[2 * (p_)]), "=&v"(xa[2 * (p_) + 1]), "=&v"(xb[2 * (p_)]), "=&v"(xb[2 * (p_) + 1]) \
                     : RD_IN(p_) : "memory"); } while (0)
#define CHECK(p_) do { int cnt_; \
        asm volatile("s_mov_b32 %1, 0x400000\n\ts_branch 2f\n1:\n\t" RD_X "2:\n\ts_waitcnt lgkmcnt(4)\n\tv_readfirstlane_b32 %0, %2\n\t" \
                     "s_cmp_ge_i32 %0, %10\n\ts_cbranch_scc1 3f\n\ts_cmp_ge_i32 %10, %14\n\ts_cbranch_scc1 4f\n\ts_sleep %15\n4:\n\t" \
                     "s_sub_u32 %1, %1, 1\n\ts_cbranch_scc0 1b\n3:\n\ts_waitcnt lgkmcnt(2)" \
                     : "=&s"(full), "=&s"(cnt_), "+v"(fl_), "+v"(xa[2 * (p_)]), "+v"(xa[2 * (p_) + 1]), "+v"(xb[2 * (p_)]), "+v"(xb[2 * (p_) + 1]) \
                     : RD_IN(p_), "s"(3 * NT - 2), "i"(CALM) : "memory", "scc"); \
        asm volatile("" : "+s"(full)); } while (0)
#define GOT_B(p_) do { waitlds<0>(); asm volatile("" : "+v"(xb[2 * (p_)]), "+v"(xb[2 * (p_) + 1])); } while (0)
#define MF_A(k_) do { ca = mfma(yv[k_], xa[k_], ca); ga = mfma(xa[k_], xa[k_], ga); sa = mfma(qk[k_], xa[k_], sa); } while (0)
#define MF_B(k_) do { cb = mfma(yv[k_], xb[k_], cb); gb = mfma(xb[k_], xb[k_], gb); sb = mfma(qk[k_], xb[k_], sb); } while (0)
        const unsigned pcv = *(volatile LAS unsigned*)(pfl + CT_PCON);
        NEED(3 * s);
#pragma unroll
        for (int k = 0; k < 6; ++k) yv[k] = *(LAS const u32x4*)(((k & 1) ? py1 : py0) + (k >> 1) * 128);
        int con = __builtin_amdgcn_readfirstlane(pcv);
        SB();
#pragma unroll
        for (int k = 0; k < 6; ++k) sy = mfma(qk[k], yv[k], sy);
        asm volatile("" : "+v"(sy));
        SB();
        lds_need(CT_PCON, con, pfl, ncon);
        ISSUE(0);
        SB();
#pragma unroll
        for (int k = 0; k < 6; ++k) cy = mfma(yv[k], yv[k], cy);
#pragma unroll
        for (int r = 0; r < 4; ++r) sa[r] = half_copy<true>(sy[r]);
        const float yy = half_copy<true>(sel4(cy, n & 3));
        f32x2 Y = swap32(yy, yy);
        asm volatile("" : "+v"(Y), "+v"(sa));
        SB();
        CHECK(0);
        sb = sa;
        MF_A(0);
        asm volatile("" : "+v"(ca), "+v"(ga), "+v"(sa));
        SB();
        GOT_B(0);
        ISSUE(1);
        SB();
        MF_B(0); MF_A(1); MF_B(1);
        asm volatile("" : "+v"(ca), "+v"(ga), "+v"(sa), "+v"(cb), "+v"(gb), "+v"(sb));
        SB();
        CHECK(1);
        MF_A(2);
        asm volatile("" : "+v"(ca), "+v"(ga), "+v"(sa));
        SB();
        GOT_B(1);
        ISSUE(2);
        SB();
        MF_B(2); MF_A(3); MF_B(3);
        asm volatile("" : "+v"(ca), "+v"(ga), "+v"(sa), "+v"(cb), "+v"(gb), "+v"(sb));
        SB();
        CHECK(2);
        ca = mfma(yv[5], xa[5], mfma(yv[4], xa[4], ca));
        ga = mfma(xa[5], xa[5], mfma(xa[4], xa[4], ga));
        asm volatile("" : "+v"(ca), "+v"(ga));
        SB();
        GOT_B(2);
        cb = mfma(yv[5], xb[5], mfma(yv[4], xb[4], cb));
        asm volatile("" : "+v"(cb));
        SB();
        lds_inc(CT_FREE, pfr);
        gb = mfma(xb[4], xb[4], gb);
        float ua = sel4(ca, n & 3);
        asm volatile("" : "+v"(gb), "+v"(ua));
        SB();
        gb = mfma(xb[5], xb[5], gb);
        float va = sel4(ga, n & 3);
        asm volatile("" : "+v"(gb), "+v"(va));
        SB();
        sa = mfma(qk[4], xa[4], sa);
        float ub = sel4(cb, n & 3);
        asm volatile("" : "+v"(sa), "+v"(ub));
        SB();
        sa = mfma(qk[5], xa[5], sa);
        const f32x2 X = swap32(ua, ub);
        float mn_ = (X[1] + Y[1]) * (1.f / C), q1_ = fmaf(2.f, X[0], Y[0]);
        asm volatile("" : "+v"(sa), "+v"(mn_), "+v"(q1_));
        SB();
        sb = mfma(qk[4], xb[4], sb);
        float vb = sel4(gb, n & 3);
        asm volatile("" : "+v"(sb), "+v"(vb));
        SB();
        sb = mfma(qk[5], xb[5], sb);
        const f32x2 Mn = swap32(mn_, mn_), G = swap32(va, vb);
        q1_ += n < 8 ? G[0] : G[1];
        const f32x2 Q = swap16(q1_, q1_);
        sa -= Mn[0] * qs;
        const float var = fmaxf(fmaf(-mn_, mn_, fmaf((n & 4) ? Q[1] : Q[0], 1.f / C, EPS)), EPS), rs_ = __builtin_amdgcn_rsqf(var);
        sb -= Mn[1] * qs;
        const float v_ = (g & 1) ? var * rs_ : mn_, h_ = bfhi(__float_as_uint(v_));
        *(LAS unsigned*)prc = cvtpk(h_, v_ - h_);
        const f32x2 R = swap32(rs_, rs_);
        sa = sa * R[0] + nref; sb = sb * R[1] + nref;
        const float tm = fmaxf(fmaxf(fmaxf(fmaxf(sa[0], sa[1]), sa[2]), fmaxf(fmaxf(sa[3], sb[0]), sb[1])), fmaxf(sb[2], sb[3]));
        if (__builtin_expect(__builtin_amdgcn_ballot_w64(!(tm <= thr)) != 0ull, 0)) {
#pragma unroll
          for (int r = 0; r < 4; ++r) {
            const float mx = row_max(fmaxf(sa[r], sb[r])), dm = mx <= 16.f ? 0.f : mx;
            nref[r] -= dm; sa[r] -= dm; sb[r] -= dm;
          }
          thr = __builtin_nanf("");
          word |= 1u;
          if (n == 0) *(LAS f32x4*)(lds + LDS_PK + pk * PK_B + 1280 + 16 * g) = -nref;
        }
#pragma unroll
        for (int r = 0; r < 4; ++r) { sa[r] = __builtin_amdgcn_exp2f(sa[r]); sb[r] = __builtin_amdgcn_exp2f(sb[r]); }
        float ra = R[0], rb = R[1];
        asm volatile("" : "+v"(ra), "+v"(rb));
        sa *= ra; sb *= rb;
        *(LAS u32x2*)pw = u32x2{cvtpk(sa[0], sa[1]), cvtpk(sa[2], sa[3])};
        *(LAS u32x2*)(pw + 128) = u32x2{cvtpk(sb[0], sb[1]), cvtpk(sb[2], sb[3])};
        lds_put(CT_PRDY, pfl, word);
        SB();
      }
      GET_TAG();
      LAS char* fin = lds + xslot + 8192;
      unsigned qres = (unsigned)__builtin_amdgcn_raw_buffer_load_b16(mkrs(q, NQ * C * 2), dout * 2, 0, 0) << 16;
      unsigned ao = ((tid & 255) >> 1) * 8192 + (tid & 1) * 16 + dst * 32, tw = (unsigned)(tg >> (20 * (tid & 1))), sh = 12 * (tid & 1), oo = dout * 2;
      LAS char *fw = fin + (((lane & 8) | ((g & 1) << 2) | (g & 2)) + (lane & 1)) * 16 + 4 * (w - 4), *fr = fin + (2 * (lane & 7) + ((lane >> 3) & 1)) * 16;
      float v7 = 0.f;
      unsigned fi_ = 8u;
      const unsigned t0s = lds_get(ctl + CT_T0);
      unsigned gate = (t0s ? t0s : (unsigned)tstart) + RGATE;
      u32x4 ro = mkrs4(out, NQ * C * 2), rm4 = mkrs4(ws, WS_BYTES);
      unsigned half = 128 * 8192;
      unsigned long long wm = 0x0303030303030303ull;
      asm volatile("" : "+v"(ao), "+v"(tw), "+v"(sh), "+v"(oo), "+v"(fw), "+v"(fr), "+v"(fin), "+v"(v7), "+v"(qres), "+v"(fi_), "+s"(ro), "+s"(wm), "+s"(rm4), "+s"(half));
      if (w == 4) asm volatile("s_mov_b64 exec, 0x1515\n\tbuffer_store_dword %0, %1, %2, 0 offen sc1\n\ts_mov_b64 exec, -1" :: "v"(qres), "v"(oo), "s"(ro) : "memory");
      lds_wait(ctl + CT_XD, 1);
      lds_spin(CT_ST, ctl, 4);
      gate += max((int)((unsigned)wall_clock64() + OWNG - gate), 0);
      float org = *(LAS const float*)(lds + xslot + 6144 + 4 * dq);
      *(LAS float*)(fin + 256 + 4 * (w - 4)) = org;
#pragma unroll 1
      for (int spin = 0; spin < 1024; ++spin)
        if ((int)((unsigned)wall_clock64() - gate) >= 0) break;
      u32x4 A, B;
      asm volatile("buffer_load_dwordx4 %0, %2, %3, 0 offen sc1\n\tbuffer_load_dwordx4 %1, %2, %3, %4 offen sc1\n\ts_waitcnt vmcnt(0)"
                   : "=&v"(A), "=&v"(B) : "v"(ao), "s"(rm4), "s"(half) : "memory");
      if (__builtin_expect(__builtin_amdgcn_ballot_w64((((A[3] ^ tw) | (B[3] ^ tw)) & 0xfffffu) != 0u) != 0ull, 1)) {
#pragma unroll 1
        for (int spin = 0; spin < (1 << 18); ++spin) {
          unsigned xa_ = (A[3] ^ tw) << 12, xb_ = (B[3] ^ tw) << 12;
          if (__builtin_amdgcn_ballot_w64((xa_ | xb_) != 0u) == 0ull) break;
          xa_ |= __builtin_amdgcn_mov_dpp(xa_, 0xB1, 0xf, 0xf, true); xb_ |= __builtin_amdgcn_mov_dpp(xb_, 0xB1, 0xf, 0xf, true);
          asm volatile("" ::: "memory");
          if (xa_ != 0u) A = ldb128<16>(rws, ao, 0);
          if (xb_ != 0u) B = ldb128<16>(rws, ao, 128 * 8192);
        }
      }
      unsigned la = (A[3] & 0xfff00000u) >> sh, lb_ = (B[3] & 0xfff00000u) >> sh;
      la |= __builtin_amdgcn_mov_dpp(la, 0xB1, 0xf, 0xf, true); lb_ |= __builtin_amdgcn_mov_dpp(lb_, 0xB1, 0xf, 0xf, true);
      float da = __uint_as_float(la) - org, db = __uint_as_float(lb_) - org;
      f32x2 ua_[3], ub_[3];
#pragma unroll
      for (int i = 0; i < 3; ++i) { ua_[i] = f32x2{bflo(A[i]), bfhi(A[i])}; ub_[i] = f32x2{bflo(B[i]), bfhi(B[i])}; asm volatile("" : "+v"(ua_[i]), "+v"(ub_[i])); }
      if (__builtin_expect(__builtin_amdgcn_ballot_w64(!(fmaxf(da, db) <= 64.f)) != 0ull, 0)) {
        org = xrow16_max(row_max(fmaxf(__uint_as_float(la), __uint_as_float(lb_))));
        da = __uint_as_float(la) - org; db = __uint_as_float(lb_) - org;
        *(LAS float*)(fin + 256 + 4 * (w - 4)) = org;
        fi_ = 9u;
      }
      const float wa = __builtin_amdgcn_exp2f(da), wb = __builtin_amdgcn_exp2f(db);
#pragma unroll
      for (int i = 0; i < 3; ++i) ua_[i] = ua_[i] * wa + ub_[i] * wb;
      const f32x2 p0 = swap32(ua_[0][0], ua_[0][1]), p1 = swap32(ua_[1][0], ua_[1][1]), p2 = swap32(ua_[2][0], ua_[2][1]), p3 = swap32(wa + wb, v7);
      const f32x2 r0 = swap16(p0[0] + p0[1], p1[0] + p1[1]), r1 = swap16(p2[0] + p2[1], p3[0] + p3[1]);
      float c0 = r0[0] + r0[1], c1 = r1[0] + r1[1];
      asm volatile("s_nop 1\n\tv_add_f32_dpp %0, %0, %0 row_ror:8 row_mask:0xf bank_mask:0x3\n\tv_add_f32_dpp %0, %1, %1 row_ror:8 row_mask:0xf bank_mask:0xc\n\t"
                   "s_nop 1\n\tv_add_f32_dpp %0, %0, %0 row_shl:4 row_mask:0xf bank_mask:0x5\n\t"
                   "s_nop 1\n\tv_add_f32_dpp %0, %0, %0 quad_perm:[2,3,0,1] row_mask:0xf bank_mask:0x5\n\t"
                   "s_mov_b64 exec, %3\n\tds_write_b32 %2, %0\n\ts_mov_b64 exec, 1\n\tds_add_u32 %4, %5 offset:%6\n\ts_mov_b64 exec, -1"
                   : "+v"(c0) : "v"(c1), "v"(fw), "s"(wm), "v"(ctl), "v"(fi_), "i"(CT_FIN) : "memory");
      if (w == 4) {
        f32x4 a0, l0;
        unsigned cf;
        int cnt_, c_;
        asm volatile("s_mov_b32 %1, 0x4000000\n1:\n\tds_read_b32 %2, %5 offset:%8\n\tds_read_b128 %3, %6\n\tds_read_b128 %4, %7 offset:192\n\t"
                     "s_waitcnt lgkmcnt(2)\n\tv_readfirstlane_b32 %0, %2\n\ts_cmp_ge_i32 %0, 32\n\ts_cbranch_scc1 2f\n\t"
                     "s_sub_u32 %1, %1, 1\n\ts_cbranch_scc0 1b\n2:\n\ts_waitcnt lgkmcnt(0)"
                     : "=&s"(c_), "=&s"(cnt_), "=&v"(cf), "=&v"(a0), "=&v"(l0) : "v"(ctl), "v"(fr), "v"(fin), "i"(CT_FIN) : "memory", "scc");
        asm volatile("" : "+s"(c_));
        if (__builtin_expect(c_ != 32, 0)) {
          f32x4 m0 = *(LAS const f32x4*)(fin + 256);
          const float mm = fmaxf(fmaxf(m0[0], m0[1]), fmaxf(m0[2], m0[3]));
#pragma unroll
          for (int p = 0; p < 4; ++p) m0[p] = __builtin_amdgcn_exp2f(m0[p] - mm);
          a0 *= m0; l0 *= m0;
        }
        const float o = __uint_as_float(qres) + ((a0[0] + a0[2]) + (a0[1] + a0[3])) * __builtin_amdgcn_rcpf((l0[0] + l0[2]) + (l0[1] + l0[3]));
        const unsigned ob = cvtpk(o, __uint_as_float(__builtin_amdgcn_mov_dpp(__float_as_uint(o), 0xB1, 0xf, 0xf, true)));
        asm volatile("s_mov_b64 exec, 0x1515\n\tbuffer_store_dword %0, %1, %2, 0 offen sc1\n\ts_mov_b64 exec, -1" :: "v"(ob), "v"(oo), "s"(ro) : "memory");
        if (wg == 0 && lane == 0) stb64(u32x2{(unsigned)tg, (unsigned)(tg >> 32)}, rws, EP_OFF);
      }
    } else {
      const int ftr = ((g & 1) << 2) | ((tq >> 1) << 1) | (g >> 1);
      const int fty = (g & 1) * 4 + (tq >> 1) * 3;
      int otr[4], oty[4];
#pragma unroll
      for (int t = 0; t < 4; ++t) {
        otr[t] = (w - 1) * PART + 128 * (4 * g + tq) + 16 * ((2 * t + (tp >> 1)) ^ ftr) + 8 * (tp & 1);
        oty[t] = CRS + (w - 1) * 128 + 384 * (4 * (g & 1) + tq) + 16 * ((2 * t + (tp >> 1)) ^ fty) + 8 * (tp & 1);
      }
      int opk = LDS_PK + 320 * tp + 8 * (4 * g + tq), ork = LDS_PK + 320 * (g >> 1) + 256 + 8 * (4 * (g & 1) + tq);
      asm volatile("" : "+v"(opk), "+v"(ork), "+v"(oty[0]), "+v"(oty[1]), "+v"(oty[2]), "+v"(oty[3]));
#define RD_T(ta, ty, so_) _Pragma("unroll") for (int t_ = 0; t_ < 4; ++t_) { \
    ta[t_] = vtr2(lds + otr[t_] + (so_), lds + otr[t_] + (so_) + 2048); ty[t_] = vtr(lds + oty[t_] + (so_)); }
      f32x4 acc[4] = {z, z, z, z}, tac = z;
#define RD_P(o_) \
      pf = vtr2(lds + opk + (o_), lds + opk + (o_) + 128); \
      sf = vtr2(lds + ork + (o_), lds + ork + (o_) + 640)
#define TWICE(ty) asm volatile("s_nop 2"); _Pragma("unroll") for (int t = 0; t < 4; ++t) { u32x2 lo_, hi_; \
      asm volatile("v_mov_b64 %0, %2\n\tv_mov_b64 %1, %2" : "=&v"(lo_), "=&v"(hi_) : "v"(ty[t])); t2[t] = u32x4{lo_[0], lo_[1], hi_[0], hi_[1]}; } \
      asm volatile("s_nop 1" : "+v"(t2[0]), "+v"(t2[1]), "+v"(t2[2]), "+v"(t2[3]))
#define MF_PX(ta) \
      tac = mfma(sf, pf, tac); \
      _Pragma("unroll") for (int t = 0; t < 4; ++t) acc[t] = mfma(ta[t], pf, acc[t])
#define MF_PY() \
      _Pragma("unroll") for (int t = 0; t < 4; ++t) acc[t] = mfma(t2[t], pf, acc[t])
#define MOVE_REF(word_, o_) do { moved |= (word_) & 1; if (__builtin_expect(moved, 0)) { \
            const float m_ = *(LAS const float*)(lds + (((word_) & 1) ? LDS_PK + (o_) + 1280 : LDS_CTL + CT_MAX) + 4 * n); \
            Mtop = fmaxf(Mtop, m_); \
            const bool drop = m_ < Mtop - 64.f; \
            const float mn = drop ? M : m_, fa = __builtin_amdgcn_exp2f(M - mn); \
            M = mn; \
            _Pragma("unroll") for (int t = 0; t < 4; ++t) acc[t] *= fa; \
            tac *= fa; \
            if (drop) pf = u32x4{0u, 0u, 0u, 0u}; \
          } } while (0)
      const unsigned flo = FLAG_OFF + (wg & 7) * 768 + (w - 1) * 144 + (lane >= 16 ? min(lane - 16, 35) : (lane >> 3) * 6 + (lane & 7) + ((lane & 7) < 6 ? 0 : 18)) * 8;
      float M, Mtop;
      int moved = 0;
      u32x4 wvv[18], pf, sf, t2[4];
      {
        u32x4 ta[8][4], xx[6];
        u32x2 ty[8][4], fl;
        unsigned cc[6];
        const int qo = QK_OFF + (wg & 3) * QK_COPY + (2 * w - 2) * QK_REC;
#define QK_LOAD() _Pragma("unroll") for (int j = 0; j < 6; ++j) { \
          xx[j] = ldb128<16>(rws, lane * 16, qo + ((j >> 1) * 6 + (j & 1)) * QK_REC); \
          cc[j] = __builtin_amdgcn_raw_buffer_load_b32(rws, lane * 4, qo + ((j >> 1) * 6 + (j & 1)) * QK_REC + 1024, 16); }
#pragma unroll
        for (int u = 0; u < 8; ++u) {
          if (u == QKAT) {
            GET_TAG();
#pragma unroll 1
            for (int r = 0; r < 8; ++r) {
#pragma unroll 1
              for (int spin = 0; spin < 128; ++spin) {
                if ((int)(wall_clock64() - tstart) >= GATE + r * HSTEP) break;
                __builtin_amdgcn_s_sleep(1);
              }
              u32x8 hv;
              asm volatile("s_load_dwordx8 %0, %1, %2\n\ts_waitcnt lgkmcnt(0)" : "=&s"(hv) : "s"(ws), "s"(HINT_OFF + ((wg & 7) + 8 * r) * 128 + (w - 1) * 32) : "memory");
              const unsigned t_ = (unsigned)tg;
              if ((((hv[0] ^ t_) | (hv[1] ^ t_) | (hv[2] ^ t_)) | ((hv[3] ^ t_) | (hv[4] ^ t_) | (hv[5] ^ t_))) == 0u) break;
            }
            if (wg < NPR) lds_wait(ctl + CT_PD, 4);
            QK_LOAD();
#pragma unroll 1
            for (int spin = 0; spin < (1 << 18); ++spin) {
              unsigned df = 0u;
#pragma unroll
              for (int j = 0; j < 6; ++j) df |= qk_check(xx[j], (unsigned)tg) ^ cc[j];
              if (__all(df == 0u)) break;
              asm volatile("" ::: "memory");
              QK_LOAD();
            }
#pragma unroll
            for (int h = 0; h < 2; ++h) {
              u32x4 sx;
#pragma unroll
              for (int i = 0; i < 4; ++i) sx[i] = add3(xx[h][i], xx[2 + h][i], xx[4 + h][i]);
              *(LAS u32x4*)(lds + LDS_QK + (2 * w - 2 + h) * 1024 + lane * 16) = sx;
            }
            lds_inc(CT_QK, ctl);
            SB();
          }
          lds_wait(ctl + CT_FULL, 3 * u + w);
          RD_T(ta[u], ty[u], u * SLOT);
          lds_inc(CT_FREE + 4 * u, ctl);
          SB();
        }
        fl = __builtin_bit_cast(u32x2, __builtin_amdgcn_raw_buffer_load_b64(rws, flo, 0, 16));
#pragma unroll
        for (int u = 0; u < 12; ++u) {
          const int word = lds_spin(CT_PRDY + 4 * (u % NPK), ctl, 2 * u + 2);
          if (u == 0) Mtop = M = *(LAS const float*)(ctl + CT_MAX + 4 * n);
          RD_P((u % NPK) * PK_B);
          if (u >= 8) MOVE_REF(word, (u % NPK) * PK_B);
          lds_inc(CT_PCON + 4 * (u % NPK), ctl);
          SB();
          MF_PX(ta[u & 7]);
          SB();
          TWICE(ty[u & 7]);
          MF_PY();
          asm volatile("" : "+v"(acc[0]), "+v"(acc[1]), "+v"(acc[2]), "+v"(acc[3]), "+v"(tac));
          SB();
#define FLAGS_UP() __all(fl[0] == (unsigned)tg && (fl[1] & 255u) == (unsigned)(tg >> 32))
          if (u == T0AT && w == 1 && FLAGS_UP()) {
            int d = (int)(fl[1] - ((unsigned)tstart << 8)) >> 8;
            d += __builtin_amdgcn_mov_dpp(d, XOR8, 0xf, 0xf, true); d += __builtin_amdgcn_mov_dpp(d, ROR4, 0xf, 0xf, true);
            d += __builtin_amdgcn_mov_dpp(d, 0x122, 0xf, 0xf, true); d += __builtin_amdgcn_mov_dpp(d, 0x121, 0xf, 0xf, true);
            lds_put(CT_T0, ctl, ((unsigned)tstart + min(max(d >> 4, -300), 300)) | 1u);
          }
          if (u < 4) {
            NEED(3 * (u + 8) + w);
            RD_T(ta[u], ty[u], ((u + 8) % NS) * SLOT);
            lds_inc(CT_FREE + 4 * ((u + 8) % NS), ctl);
            SB();
          }
        }
#pragma unroll 1
        for (int spin = 0; spin < (1 << 18); ++spin) {
          if (FLAGS_UP()) break;
          asm volatile("" ::: "memory");
          fl = __builtin_bit_cast(u32x2, __builtin_amdgcn_raw_buffer_load_b64(rws, flo, 0, 16));
        }
        asm volatile("" ::: "memory");
        if (w == 3) lds_put(CT_WV, ctl, 1);
      }
      int slot = 3, pk = 12 % NPK;
      unsigned rdy = 0u;
      const int kro[3] = {(w - 1) * 2048, (w == 3 ? 0 : w) * 2048, (w == 1 ? 2 : w - 2) * 2048};
#define RD_PACKET(z_) do { int cnt_; u32x2 p0_, p1_, p2_, p3_; \
        word = __builtin_amdgcn_readfirstlane(rdy); \
        asm volatile("s_mov_b32 %1, 0x400000\n1:\n\tds_read_b32 %2, %7 offset:%11\n\t" \
                     "ds_read_b64_tr_b16 %5, %9\n\tds_read_b64_tr_b16 %6, %9 offset:640\n\t" \
                     "ds_read_b64_tr_b16 %3, %8\n\tds_read_b64_tr_b16 %4, %8 offset:128\n\t" \
                     "s_cmp_ge_i32 %0, %10\n\ts_cbranch_scc1 2f\n\ts_waitcnt lgkmcnt(4)\n\tv_readfirstlane_b32 %0, %2\n\t" \
                     "s_cmp_ge_i32 %0, %10\n\ts_cbranch_scc1 2f\n\t" z_ "s_sub_u32 %1, %1, 1\n\ts_cbranch_scc0 1b\n2:" \
                     : "+s"(word), "=&s"(cnt_), "=&v"(rdy), "=&v"(p0_), "=&v"(p1_), "=&v"(p2_), "=&v"(p3_) \
                     : "v"(ctl + 4 * pk), "v"(lds + opk + pk * PK_B), "v"(lds + ork + pk * PK_B), "s"(2 * s + 2), "i"(CT_PRDY), "i"(CALM) : "memory", "scc"); \
        asm volatile("" : "+s"(word)); \
        pf = u32x4{p0_[0], p0_[1], p1_[0], p1_[1]}; sf = u32x4{p2_[0], p2_[1], p3_[0], p3_[1]}; } while (0)
#define GOT_PACKET() do { waitlds<0>(); asm volatile("" : "+v"(pf), "+v"(sf) : "v"(rdy)); MOVE_REF(word, pk * PK_B); } while (0)
#define RD_Y(t_, p_) do { const u32x2 lo_ = vtr(p_); asm volatile("" ::: "memory"); const u32x2 hi_ = vtr(p_); \
        t2[t_] = u32x4{lo_[0], lo_[1], hi_[0], hi_[1]}; } while (0)
      u32x4 ta[4];
      int s = 12, word;
      NEED(36 + w);
#pragma unroll
      for (int t = 0; t < 4; ++t) { ta[t] = vtr2(lds + otr[t] + 2 * SLOT, lds + otr[t] + 2 * SLOT + 2048); RD_Y(t, lds + oty[t] + 2 * SLOT); }
      lds_inc(CT_FREE + 8, ctl);
#pragma unroll 1
      for (; s < NT - 1; ++s) {
        RD_PACKET("s_sleep %12\n\t");
        LAS const char *px[4], *py[4];
#pragma unroll
        for (int t = 0; t < 4; ++t) { px[t] = lds + otr[t] + slot * SLOT; py[t] = lds + oty[t] + slot * SLOT; asm volatile("" : "+v"(px[t]), "+v"(py[t])); }
        NEED(3 * s + 3 + w);
        GOT_PACKET();
        SB();
        tac = mfma(sf, pf, tac);
        asm volatile("" : "+v"(tac));
        SB();
        lds_inc(CT_PCON, ctl + 4 * pk);
        pk = pk == NPK - 1 ? 0 : pk + 1;
#pragma unroll
        for (int t = 0; t < 4; ++t) { acc[t] = mfma(ta[t], pf, acc[t]); SB(); ta[t] = vtr2(px[t], px[t] + 2048); SB(); }
        rdy = *(volatile LAS unsigned*)(ctl + CT_PRDY + 4 * pk);
#pragma unroll
        for (int t = 0; t < 4; ++t) { acc[t] = mfma(t2[t], pf, acc[t]); SB(); RD_Y(t, py[t]); SB(); }
        lds_inc(CT_FREE, ctl + 4 * slot);
#define WV_LOAD(j_) wvv[j_] = ldb128<16>(rws, lane * 16 + ((j_) & 1) * 1024, WVO_OFF + (w - 1) * 18432 + ((j_) / 6) * 6144 + kro[((j_) % 6) >> 1])
        if ((unsigned)(s - WVS) < WV1) {
#pragma unroll
          for (int u = 0; u < WV1; ++u)
            if (s - WVS == u) WV_LOAD(u);
        }
        if (s == NT - 3) {
#pragma unroll
          for (int j = WV1; j < 18; ++j) WV_LOAD(j);
        }
        slot = slot == NS - 1 ? 0 : slot + 1;
      }
      LAS char *xw = lds + xslot + lane * 16 + kro[0], *xl = lds + xslot + 6144 + 4 * n;
      LAS const char *xr1 = lds + xslot + lane * 16 + kro[1], *xr2 = lds + xslot + lane * 16 + kro[2];
      unsigned mo = wg * 8192 + (w - 1) * 2048 + n * 64 + g * 16, tw = (unsigned)(tg >> (20 * (g & 1))) & 0xfffffu, sh = 12 * (g & 1);
      asm volatile("" : "+v"(xw), "+v"(xl), "+v"(xr1), "+v"(xr2), "+v"(mo), "+v"(tw), "+v"(sh));
      __builtin_amdgcn_s_setprio(2);
      RD_PACKET("");
      GOT_PACKET();
      SB();
      tac = mfma(sf, pf, tac);
      asm volatile("" : "+v"(tac));
      SB();
#pragma unroll
      for (int t = 0; t < 2; ++t) acc[t] = mfma(t2[t], pf, mfma(ta[t], pf, acc[t]));
      SB();
#pragma unroll
      for (int t = 2; t < 4; ++t) acc[t] = mfma(t2[t], pf, mfma(ta[t], pf, acc[t]));
      const float L = tac[2] + tac[3], rl = __builtin_amdgcn_rcpf(L), ct = -(tac[0] + tac[1]) * rl;
      const float lse = M + __builtin_amdgcn_logf(L);
      u32x4 b[6];
      b[0] = pack8(acc[0] * rl + ct, acc[1] * rl + ct);
      *(LAS u32x4*)xw = b[0];
      *(LAS float*)xl = lse;
#pragma unroll
      for (int j = 0; j < 4; ++j) { __builtin_amdgcn_sched_group_barrier(0x008, 1, 0); __builtin_amdgcn_sched_group_barrier(0x002, 3, 0); }
      SB();
      b[1] = pack8(acc[2] * rl + ct, acc[3] * rl + ct);
      *(LAS u32x4*)(xw + 1024) = b[1];
      lds_inc(CT_XD, ctl);
      SB();
      f32x4 o[3];
      unsigned xd;
#pragma unroll
      for (int t = 0; t < 3; ++t) o[t] = mfma(wvv[6 * t], b[0], z);
      asm volatile("" : "+v"(o[0]), "+v"(o[1]), "+v"(o[2]));
      SB();
#define RD_SLICES "ds_read_b32 %0, %7 offset:%10\n\tds_read_b128 %1, %8\n\tds_read_b128 %2, %8 offset:1024\n\tds_read_b128 %3, %9\n\tds_read_b128 %4, %9 offset:1024\n"
      asm volatile(RD_SLICES : "=&v"(xd), "=&v"(b[2]), "=&v"(b[3]), "=&v"(b[4]), "=&v"(b[5]) : "i"(0), "i"(0), "v"(ctl), "v"(xr1), "v"(xr2), "i"(CT_XD) : "memory");
      SB();
#pragma unroll
      for (int t = 0; t < 3; ++t) o[t] = mfma(wvv[6 * t + 1], b[1], o[t]);
      tw |= (__float_as_uint(lse) << sh) & 0xfff00000u;
      asm volatile("" : "+v"(tw), "+v"(o[0]), "+v"(o[1]), "+v"(o[2]));
      SB();
      {
        int cnt_, c_;
        asm volatile("s_mov_b32 %6, 0x400000\n\ts_branch 2f\n1:\n\t" RD_SLICES "2:\n\ts_waitcnt lgkmcnt(4)\n\tv_readfirstlane_b32 %5, %0\n\t"
                     "s_cmp_ge_i32 %5, 3\n\ts_cbranch_scc1 3f\n\ts_sub_u32 %6, %6, 1\n\ts_cbranch_scc0 1b\n3:\n\ts_waitcnt lgkmcnt(0)"
                     : "+v"(xd), "+v"(b[2]), "+v"(b[3]), "+v"(b[4]), "+v"(b[5]), "=&s"(c_), "=&s"(cnt_)
                     : "v"(ctl), "v"(xr1), "v"(xr2), "i"(CT_XD) : "memory", "scc");
      }
      SB();
      PROJECT(2);
      lds_inc(CT_ST, ctl);
    }
  }
}

#undef rws
#define WS_AT(size_) ((size_) > (64u << 20) ? ((size_) - (64u << 20)) & ~(size_t)0x1fffff : 0)
extern "C" void kernel_launch(void* const* d_in, const int* in_sizes, int n_in, void* d_out, int out_size, void* d_ws, size_t ws_size,
                              hipStream_t stream) {
  dino_main<<<NWG, 512, 0, stream>>>((const u16*)d_in[0], (const u16*)d_in[1], (const u16*)d_in[2], (const u16*)d_in[3], (const u16*)d_in[4],
                                     (const u16*)d_in[5], (const u16*)d_in[6], (char*)d_ws + WS_AT(ws_size) + 4096, (u16*)d_out);
}
```

```cpp
#include <hip/hip_runtime.h>
#include <hip/hip_bf16.h>
#include <stdint.h>

typedef unsigned short u16;
using u32x2 = __attribute__((ext_vector_type(2))) unsigned;
using u32x4 = __attribute__((ext_vector_type(4))) unsigned;
using u32x8 = __attribute__((ext_vector_type(8))) unsigned;
using f32x2 = __attribute__((ext_vector_type(2))) float;
using f32x4 = __attribute__((ext_vector_type(4))) float;
using s16x8 = __attribute__((ext_vector_type(8))) short;
using bf16x2_t = __attribute__((ext_vector_type(2))) __bf16;
typedef short v4i16_t __attribute__((ext_vector_type(4)));
#define LAS __attribute__((address_space(3)))

constexpr int C = 192, NQ = 16, W0 = 512, W1 = 256, NWG = 256;
constexpr float EPS = 1e-5f;
constexpr float QSCALE = 0.07216878364870322f * 1.4426950408889634f;

constexpr int PART = 4096, CRS = 3 * PART;
constexpr int SLOT = CRS + 3072, NS = 10;
constexpr int LDS_QK = NS * SLOT;
constexpr int NPK = 7;
constexpr int LDS_PK = LDS_QK, PK_B = 1344;
constexpr int LDS_CTL = LDS_PK + NPK * PK_B;
constexpr int LDS_SINK = LDS_CTL + 384;
constexpr int LDS_BYTES = LDS_SINK + 256;
constexpr int PSCR = 8 * SLOT;
constexpr int CT_FULL = 8, CT_WV = 4, CT_PA = 0, CT_PB = 12, CT_QR = 16, CT_PD = 20, CT_T0 = 24, CT_XD = 28, CT_TG = 32, CT_QK = 40, CT_ST = 44,
              CT_FREE = 48, CT_FIN = 88, CT_ONE = 112, CT_PRDY = 128, CT_PCON = 160, CT_MAX = 256;

constexpr int MB_BYTES = NWG * NWG * 32;
constexpr int EP_OFF = MB_BYTES;
constexpr int FLAG_OFF = EP_OFF + 512;
constexpr int WVO_OFF = EP_OFF + 8192;
constexpr int QK_OFF = WVO_OFF + C * C * 2;
constexpr int QK_REC = 1280, QK_COPY = 18 * QK_REC;
constexpr int HINT_OFF = QK_OFF + 4 * QK_COPY;
constexpr int WS_BYTES = HINT_OFF + 64 * 128;

#define GIVE 2
constexpr int NPR = 36;
struct Tabs { unsigned seq[112]; };
constexpr Tabs make_tabs() {
  Tabs t{};
  for (int i = 0; i < 112; ++i) t.seq[i] = i + 1;
  return t;
}
__device__ const Tabs tabs = make_tabs();
#define QKAT 3
#define WV1 6
#define WMIN 28
#define WSTEP 5
#define LSTEP 40
#define OWNG 30
#define PACE0 30
#define PACE1 1740
#define RGATE 1922
#define T0AT 3
#define WVS 12
#define CALM 2
#define LAND0 210
#define LANDG 2
#define HSTEP 40
#define GATE 350
#define CODE_BYTES 25984

__device__ __forceinline__ unsigned cvtpk(float lo, float hi) {
  bf16x2_t v = {(__bf16)lo, (__bf16)hi};
  return __builtin_bit_cast(unsigned, v);
}
__device__ __forceinline__ float bflo(unsigned x) { return __builtin_bit_cast(float, x << 16); }
__device__ __forceinline__ float bfhi(unsigned x) { return __builtin_bit_cast(float, x & 0xffff0000u); }
template <int CTRL> __device__ __forceinline__ float dpp(float x) {
  return __builtin_bit_cast(float, __builtin_amdgcn_mov_dpp(__builtin_bit_cast(int, x), CTRL, 0xf, 0xf, true));
}
constexpr int XOR8 = 0x128, ROR4 = 0x124;
template <bool HI8> __device__ __forceinline__ float half_copy(float x) {
  const int v = __builtin_bit_cast(int, x);
  return __builtin_bit_cast(float, __builtin_amdgcn_update_dpp(v, v, XOR8, 0xf, HI8 ? 0xc : 0x3, false));
}
__device__ __forceinline__ float row_max(float x) {
  x = fmaxf(x, dpp<XOR8>(x)); x = fmaxf(x, dpp<ROR4>(x)); x = fmaxf(x, dpp<0x122>(x));
  return fmaxf(x, dpp<0x121>(x));
}
__device__ __forceinline__ f32x2 swap32(float a, float b) {
  auto s = __builtin_amdgcn_permlane32_swap(__float_as_uint(a), __float_as_uint(b), false, false);
  return f32x2{__uint_as_float(s[0]), __uint_as_float(s[1])};
}
__device__ __forceinline__ f32x2 swap16(float a, float b) {
  auto s = __builtin_amdgcn_permlane16_swap(__float_as_uint(a), __float_as_uint(b), false, false);
  return f32x2{__uint_as_float(s[0]), __uint_as_float(s[1])};
}
__device__ __forceinline__ float xrow16_max(float x) {
  auto s = __builtin_amdgcn_permlane16_swap(__float_as_uint(x), __float_as_uint(x), false, false);
  x = fmaxf(__uint_as_float(s[0]), __uint_as_float(s[1]));
  auto t = __builtin_amdgcn_permlane32_swap(__float_as_uint(x), __float_as_uint(x), false, false);
  return fmaxf(__uint_as_float(t[0]), __uint_as_float(t[1]));
}
__device__ __forceinline__ float xrow16_sum(float x) {
  auto s = __builtin_amdgcn_permlane16_swap(__float_as_uint(x), __float_as_uint(x), false, false);
  x = __uint_as_float(s[0]) + __uint_as_float(s[1]);
  auto t = __builtin_amdgcn_permlane32_swap(__float_as_uint(x), __float_as_uint(x), false, false);
  return __uint_as_float(t[0]) + __uint_as_float(t[1]);
}
__device__ __forceinline__ f32x4 mfma(u32x4 a, u32x4 b, f32x4 c) {
  return __builtin_amdgcn_mfma_f32_16x16x32_bf16(__builtin_bit_cast(s16x8, a), __builtin_bit_cast(s16x8, b), c, 0, 0, 0);
}
__device__ __forceinline__ u32x2 vtr(LAS const char* p) {
  return __builtin_bit_cast(u32x2, __builtin_amdgcn_ds_read_tr16_b64_v4i16((LAS v4i16_t*)p));
}
__device__ __forceinline__ u32x4 vtr2(LAS const char* p, LAS const char* q_) {
  const u32x2 a = vtr(p), b = vtr(q_);
  return u32x4{a[0], a[1], b[0], b[1]};
}
__device__ __forceinline__ float sel4(f32x4 v, int r) {
  const float a = (r & 1) ? v[1] : v[0], b = (r & 1) ? v[3] : v[2];
  return (r & 2) ? b : a;
}
using RSRC = __amdgpu_buffer_rsrc_t;
__device__ __forceinline__ RSRC mkrs(const void* p, int bytes) { return __builtin_amdgcn_make_buffer_rsrc(const_cast<void*>(p), (short)0, bytes, 0x00020000); }
__device__ __forceinline__ u32x4 mkrs4(const void* p, unsigned bytes) {
  const unsigned long long b = (unsigned long long)p;
  return u32x4{(unsigned)b, (unsigned)(b >> 32) & 0xffffu, bytes, 0x00020000u};
}
template <int AUX> __device__ __forceinline__ u32x4 ldb128(RSRC r, unsigned vo, int so) { return __builtin_bit_cast(u32x4, __builtin_amdgcn_raw_buffer_load_b128(r, vo, so, AUX)); }
#define SB() __builtin_amdgcn_sched_barrier(0)
#define RAW_BARRIER() asm volatile("s_waitcnt lgkmcnt(0)\n\ts_barrier" ::: "memory")
#define DMA(m0_, vo_, rs_, so_) asm volatile("s_mov_b32 m0, %0\n\ts_nop 0\n\tbuffer_load_dwordx4 %1, %2, %3 offen lds" :: "s"(m0_), "v"(vo_), "s"(rs_), "s"(so_) : "memory")
template <int N> __device__ __forceinline__ void waitlds() { __builtin_amdgcn_s_waitcnt(0xc07f | (N << 8)); }
template <int N> __device__ __forceinline__ void waitvm() { asm volatile("s_waitcnt vmcnt(%0)" :: "i"(N) : "memory"); }


__device__ __forceinline__ void lds_inc(int O, LAS char* p) {
  asm volatile("s_mov_b64 exec, 1\n\tds_add_u32 %0, %1 offset:%2\n\ts_mov_b64 exec, -1" :: "v"(p), "v"(1u), "i"(O) : "memory");
}
__device__ __forceinline__ void lds_put(int O, LAS char* p, unsigned v) {
  asm volatile("s_mov_b64 exec, 1\n\tds_write_b32 %0, %1 offset:%2\n\ts_mov_b64 exec, -1" :: "v"(p), "v"(v), "i"(O) : "memory");
}
__device__ __forceinline__ int lds_get(LAS char* p) {
  asm volatile("" ::: "memory");
  const unsigned v = __hip_atomic_load((LAS unsigned*)p, __ATOMIC_RELAXED, __HIP_MEMORY_SCOPE_WORKGROUP);
  asm volatile("" ::: "memory");
  return __builtin_amdgcn_readfirstlane(v);
}
__device__ __forceinline__ void lds_wait(LAS char* p, int target) {
#pragma unroll 1
  for (int spin = 0; spin < (1 << 22); ++spin) {
    if (lds_get(p) >= target) break;
    __builtin_amdgcn_s_sleep(1);
  }
}
__device__ __forceinline__ int lds_spin(int O, LAS char* p, int target) {
  int r, cnt = 1 << 22;
  unsigned t;
  asm volatile("1:\n\tds_read_b32 %1, %3 offset:%5\n\ts_waitcnt lgkmcnt(0)\n\tv_readfirstlane_b32 %0, %1\n\ts_cmp_ge_i32 %0, %4\n\ts_cbranch_scc1 2f\n\t"
               "s_sub_u32 %2, %2, 1\n\ts_cbranch_scc0 1b\n2:"
               : "=&s"(r), "=&v"(t), "+s"(cnt) : "v"(p), "s"(target), "i"(O) : "memory", "scc");
  return r;
}
__device__ __forceinline__ void lds_need(int O, int& have, LAS char* p, int target) {
  int cnt;
  unsigned t;
  asm volatile("s_cmp_ge_i32 %0, %4\n\ts_cbranch_scc1 2f\n\ts_mov_b32 %2, 0x400000\n"
               "1:\n\tds_read_b32 %1, %3 offset:%5\n\ts_waitcnt lgkmcnt(0)\n\tv_readfirstlane_b32 %0, %1\n\ts_cmp_ge_i32 %0, %4\n\ts_cbranch_scc1 2f\n\t"
               "s_sleep %6\n\ts_sub_u32 %2, %2, 1\n\ts_cbranch_scc0 1b\n2:"
               : "+s"(have), "=&v"(t), "=&s"(cnt) : "v"(p), "s"(target), "i"(O), "i"(CALM) : "memory", "scc");
  asm volatile("" : "+s"(have));
}
__device__ __forceinline__ u32x4 pack8(f32x4 a, f32x4 b) { return u32x4{cvtpk(a[0], a[1]), cvtpk(a[2], a[3]), cvtpk(b[0], b[1]), cvtpk(b[2], b[3])}; }

__device__ __forceinline__ void stb128(u32x4 v, RSRC r, unsigned o) { __builtin_amdgcn_raw_buffer_store_b128(v, r, o, 0, 16); }
__device__ __forceinline__ void stb64(u32x2 v, RSRC r, unsigned o) { __builtin_amdgcn_raw_buffer_store_b64(v, r, o, 0, 16); }
__device__ __forceinline__ void stb32(unsigned v, RSRC r, unsigned o) { __builtin_amdgcn_raw_buffer_store_b32(v, r, o, 0, 16); }
__device__ __forceinline__ unsigned qk_check(u32x4 x, unsigned tg) {
  const unsigned a = (x[0] ^ __builtin_amdgcn_alignbit(x[1], x[1], 11)) * 0x9e3779b1u + tg;
  const unsigned b = (x[2] + __builtin_amdgcn_alignbit(x[3], x[3], 19)) * 0x85ebca77u;
  return __builtin_amdgcn_alignbit(a, a, 15) ^ b ^ (x[1] + x[3]);
}
__device__ __forceinline__ unsigned add3(unsigned a, unsigned b, unsigned c) {
  return cvtpk((bflo(a) + bflo(b)) + bflo(c), (bfhi(a) + bfhi(b)) + bfhi(c));
}
__global__ __launch_bounds__(512, 2) void dino_main(const u16* q, const u16* f0, const u16* f1, const u16* wq, const u16* wk, const u16* wv,
                                                     const u16* wo, char* ws, u16* out) {
  __shared__ __attribute__((aligned(128))) char smem[LDS_BYTES];
  LAS char* lds = (LAS char*)smem;
  LAS char* ctl = lds + LDS_CTL;
  const unsigned lb = (unsigned)(unsigned long long)lds;
  const int w = __builtin_amdgcn_readfirstlane(threadIdx.x >> 6);
  unsigned long long pc0;
  asm volatile("s_getpc_b64 %0" : "=s"(pc0));
#define COORDS() \
  int tid = threadIdx.x, wg = blockIdx.x; \
  asm volatile("" : "+v"(tid), "+s"(wg)); \
  __builtin_assume(tid >= 0 && tid < 512); __builtin_assume(wg >= 0 && wg < NWG); \
  const int lane = tid & 63, n = lane & 15, g = lane >> 4;       \
    \
  const int dq = (wg & 7) * 2 + ((wg >> 4) & 1), dst = (wg & ~31) + (wg & 7) * 4 + ((wg >> 3) & 3); \
  const int dj = 6 * ((wg >> 5) & 1) + min(lane & 7, 5); \
  const int dout = dq * C + 48 * (wg >> 6) + 16 * (dj >> 2) + (wg & 8) + ((lane & 8) >> 1) + (dj & 3); \
  const int NT = wg < NPR ? 32 - GIVE : wg >= 40 && !(wg & 1) && (wg >> 3) < 5 + ((wg & 4) ? 8 : 10) * GIVE ? 33 : 32; \
  const int xslot = (NT >= 30 ? NT - 30 : NT - 20) * SLOT;       \
  (void)xslot; (void)n; (void)g; (void)dst; (void)dout; (void)NT
#define ARGS_IN() asm volatile("s_waitcnt lgkmcnt(0)" :: "s"(q), "s"(f0), "s"(f1), "s"(wq), "s"(wk), "s"(wv), "s"(wo), "s"(ws), "s"(out) : "memory")
#define rws mkrs(ws, WS_BYTES)
#define PROJECT(k0_) do { \
      _Pragma("unroll") for (int k = k0_; k < 6; ++k) { o[0] = mfma(wvv[k], b[k], o[0]); o[1] = mfma(wvv[6 + k], b[k], o[1]); } \
      asm volatile("" : "+v"(o[0]), "+v"(o[1])); \
      SB(); \
      _Pragma("unroll") for (int k = k0_; k < 6; ++k) o[2] = mfma(wvv[12 + k], b[k], o[2]); \
      asm volatile("" : "+v"(o[2])); \
      SB(); \
      stb128(u32x4{cvtpk(o[0][0], o[0][1]), cvtpk(o[0][2], o[0][3]), cvtpk(o[1][0], o[1][1]), tw}, rws, mo); \
      SB(); \
      stb128(u32x4{cvtpk(o[1][2], o[1][3]), cvtpk(o[2][0], o[2][1]), cvtpk(o[2][2], o[2][3]), tw}, rws, mo + 1024); \
    } while (0)
  unsigned long long tg;
#define GET_TAG() tg = ((unsigned long long)((unsigned)lds_spin(CT_TG + 4, ctl, 1) & 0xffu) << 32) | (unsigned)lds_get(ctl + CT_TG)
  const unsigned long long tstart = wall_clock64();
  if (w != 0) {
    const unsigned l0 = threadIdx.x & 63;
    if (blockIdx.x < NPR) {
      const unsigned p6 = (blockIdx.x * 43) >> 8, pu = blockIdx.x - 6 * p6;
      unsigned vo = ((l0 * 16) ^ (((l0 * 4) & 0x40) | (l0 & 0x20))) + (l0 >> 3) * 256;
      const bool pv = p6 >= 3, wa = w >= 4, isq = !pv && (w & 6) == 2;
      const unsigned a6 = 6 * w - 24, pj = pv ? p6 - 3 : p6;
      unsigned so = (wa ? a6 : isq ? w - 2 : 4 * pu) * 3072 + (isq ? 0 : pv && !wa ? w - 1 : pj) * 128, ga = isq ? 128 : 3072;
      unsigned lo = lb + PSCR + (wa ? a6 : pv ? 4 * w + 20 : (0x1b181e00u >> (8 * w)) & 255u) * 1024, hi = lo + (wa ? 6144 : isq ? 3072 : 4096);
      asm volatile("" : "+v"(vo), "+s"(lo), "+s"(so), "+s"(hi), "+s"(ga));
      ARGS_IN();
      const u32x4 rw = mkrs4(wa ? (pv ? wo : wq) : pv ? wv : isq ? q : wk, isq ? NQ * C * 2 : C * C * 2);
#pragma unroll 1
      do { DMA(lo, vo, rw, so); lo += 1024; so += ga; } while (lo != hi);
    } else if (w == 7 && blockIdx.x >= 56 && blockIdx.x < 72) {
      const u32x4 rc = mkrs4((const void*)(pc0 & ~255ull), CODE_BYTES);
#pragma unroll 1
      for (int i = 0; i < CODE_BYTES; i += 4096)
        asm volatile("s_mov_b32 m0, %0\n\ts_nop 0\n\tbuffer_load_dword %1, %2, %3 offen lds" :: "s"(lb + LDS_SINK), "v"(l0 * 64), "s"(rc), "s"(i) : "memory");
    }
    if (w == 3) {
      unsigned long long ep;
      asm volatile("s_waitcnt lgkmcnt(0)\n\ts_load_dwordx2 %0, %1, %2\n\ts_barrier\n\ts_waitcnt lgkmcnt(0)" : "=&s"(ep) : "s"(ws), "s"(EP_OFF) : "memory");
      *(LAS unsigned long long*)(ctl + CT_TG) = ((ep + 0x100001ull) & 0xffffffffffull) | 0x10000000000ull;
    } else RAW_BARRIER();
  }
  SB();
  if (__builtin_expect(w == 0, 1)) {
    const unsigned l0 = threadIdx.x & 63, g0 = blockIdx.x;
    unsigned y0 = (l0 * 16) ^ (l0 >= 48 ? 48 : 0), y1 = (l0 * 16) ^ (l0 >= 32 ? 64 : 48), y2 = (l0 * 16) ^ (l0 >= 16 ? 112 : 64);
    unsigned v0 = ((l0 * 16) ^ ((l0 * 2) & 0x60)) + (l0 >> 3) * 640 + 1024, v1 = (v0 ^ 16) - 640;
    unsigned v2 = v0 + W0 * C * 2 - 2048, v3 = v1 + W0 * C * 2 - 2048;
    unsigned b0 = (g0 & 7) * (2 * W0 * C * 2) + (g0 >> 3) * 6144, b1 = (g0 & 7) * (W1 * C * 2) + (g0 >> 3) * 3072;
    {
      const unsigned iv = (l0 & 15) >= CT_ONE / 8 ? 0x3f803f80u : (l0 >> 3) == CT_MAX / 64 ? 0xff800000u : 0u;
      *(LAS u32x2*)(ctl + 8 * min(l0, 47u)) = u32x2{l0 >= 47 ? 0x3f803f80u : iv, l0 >= 47 ? 0x3f803f80u : iv};
    }
    constexpr unsigned st0 = 16 * W0 * C * 2, st1 = 8 * W1 * C * 2;
    unsigned sb = lb, fo = CT_FREE, fv = 0u;
    int lap = 0;
    u32x4 rt = mkrs4(tabs.seq, 112 * 4);
    unsigned vz = 0u;
    asm volatile("" : "+v"(v0), "+v"(v1), "+v"(v2), "+v"(v3), "+v"(y0), "+v"(y1), "+v"(y2), "+v"(vz), "+s"(b0), "+s"(b1), "+s"(rt));
    ARGS_IN();
    const u32x4 r0 = mkrs4((const char*)f0 - 1024, W0 * W0 * C * 2 + 1024), r1 = mkrs4(f1, W1 * W1 * C * 2);
    if (g0 < NPR) __builtin_amdgcn_s_barrier();
#define UNIT(p_) do { unsigned so_; \
      asm volatile("s_add_u32 m0, %1, %10\n\ts_add_u32 %0, %2, %11\n\t" \
                   "buffer_load_dwordx4 %3, %7, %0 offen nt lds\n\tbuffer_load_dwordx4 %4, %7, %0 offen offset:1024 nt lds\n\t" \
                   "buffer_load_dwordx4 %5, %7, %0 offen offset:2048 nt lds\n\tbuffer_load_dwordx4 %6, %7, %0 offen offset:3072 nt lds\n\t" \
                   "s_mov_b32 m0, %12\n\ts_mov_b64 exec, 1\n\tbuffer_load_dword %13, %8, %9 offen offset:%14 lds\n\ts_mov_b64 exec, -1" \
                   : "=&s"(so_) : "s"(sb), "s"(b0), "v"(v0), "v"(v1), "v"(v2), "v"(v3), "s"(r0), "s"(rt), "s"(12 * s), "i"((p_) * PART), "i"((p_) * 128), \
                     "s"(lb + LDS_CTL + CT_FULL - 4 * (p_)), "v"(vz), "i"(4 * (p_)) : "memory", "scc"); } while (0)
#define WINDOW() \
      asm volatile("s_waitcnt vmcnt(%1)\n\ts_cmp_ge_i32 %0, %6\n\ts_cbranch_scc1 1f\n\ts_waitcnt vmcnt(%2)\n\ts_cmp_ge_i32 %0, %7\n\ts_cbranch_scc1 1f\n\t" \
                   "s_waitcnt vmcnt(%3)\n\ts_cmp_ge_i32 %0, %8\n\ts_cbranch_scc1 1f\n\ts_waitcnt vmcnt(%4)\n\ts_cmp_ge_i32 %0, %9\n\ts_cbranch_scc1 1f\n\t" \
                   "s_waitcnt vmcnt(%5)\n1:" :: "s"(late), "i"(WMIN + 4 * WSTEP), "i"(WMIN + 3 * WSTEP), "i"(WMIN + 2 * WSTEP), "i"(WMIN + WSTEP), "i"(WMIN), \
                   "i"(4 * LSTEP * 256), "i"(3 * LSTEP * 256), "i"(2 * LSTEP * 256), "i"(LSTEP * 256) : "memory", "scc")
#define COARSE(sb_) \
      asm volatile("s_add_u32 m0, %0, %6\n\ts_nop 0\n\tbuffer_load_dwordx4 %1, %4, %5 offen nt lds\n\t" \
                   "buffer_load_dwordx4 %2, %4, %5 offen offset:1024 nt lds\n\tbuffer_load_dwordx4 %3, %4, %5 offen offset:2048 nt lds" \
                   :: "s"(sb_), "v"(y0), "v"(y1), "v"(y2), "s"(r1), "s"(b1), "i"(CRS) : "memory", "scc")
    COARSE(sb);
#pragma unroll 1
    for (int s = 0; s < 2; ++s) {
      asm volatile("s_add_u32 %0, %0, %1" : "+s"(b1) : "i"(st1) : "scc");
      UNIT(0); UNIT(1); COARSE(sb + SLOT); UNIT(2);
      sb += SLOT; b0 += st0;
    }
    if (g0 >= NPR) __builtin_amdgcn_s_barrier();
    __builtin_amdgcn_s_setprio(3);
    COORDS();
    fo += 12;
    const int tau = __builtin_amdgcn_readfirstlane((int)((PACE1 - PACE0) * 256.f * __builtin_amdgcn_rcpf((float)NT)));
    unsigned due = PACE0 * 256u + 2 * tau, t0v = 0u;
    unsigned long long now = tstart;
    const unsigned xi = (wg >> 3) - 5, xrow = 8 * (31 - xi % GIVE) + (wg & 7) + (xi / GIVE & 1), xcol = xi / GIVE >> 1;
    const unsigned x0 = xrow * (2 * W0 * C * 2) + xcol * 6144, x1 = xrow * (W1 * C * 2) + xcol * 3072;
#pragma unroll 1
    for (int s = 2; s < NT; ++s) {
      asm volatile("" : "+s"(now));
      const unsigned t0s = __builtin_amdgcn_readfirstlane(t0v), t0 = t0s ? t0s : (unsigned)tstart;
      if ((int)(((unsigned)now - t0) * 256u - due) < 0) {
#pragma unroll 1
        for (int spin = 0; spin < 256; ++spin) {
          if ((int)(((unsigned)wall_clock64() - t0) * 256u - due) >= 0) break;
          __builtin_amdgcn_s_sleep(1);
        }
      }
      fv = *(volatile LAS unsigned*)(ctl + (s == 7 ? CT_PD : fo));
      now = wall_clock64();
      UNIT(0);
      asm volatile("" : "+s"(now));
      const int late = (int)(((unsigned)now - t0) * 256u - due);
      due += tau;
      WINDOW();
      UNIT(1);
      WINDOW();
      const unsigned nsb = fo == CT_FREE ? lb : sb + SLOT;
      const unsigned n0 = s == 31 ? x0 : b0 + st0;
      b1 = s == 31 ? x1 : b1 + st1;
      if (s + 1 < NT) {
        const int need = s == 7 && wg < NPR ? 4 : lap;
        if (__builtin_expect((int)__builtin_amdgcn_readfirstlane(fv) < need, 0)) {
          lds_wait(ctl + (s == 7 ? CT_PD : fo), need);
        }
        COARSE(nsb);
      }
      now = wall_clock64();
      t0v = *(volatile LAS unsigned*)(ctl + CT_T0);
      UNIT(2);
      WINDOW();
      if (fo == CT_FREE + 4 * (NS - 1)) { fo = CT_FREE; lap += 4; } else fo += 4;
      sb = nsb; b0 = n0;
    }
    __builtin_amdgcn_s_setprio(0);
    u32x4 wvv[18], b[6];
    asm volatile("s_mov_b32 m0, %0\n\ts_mov_b64 exec, 1\n\tbuffer_load_dword %1, %2, 0 offen lds\n\ts_mov_b64 exec, -1"
                 :: "s"(lb + LDS_SINK), "v"(vz), "s"(mkrs4(ws - 4096, 4096)) : "memory");
    lds_wait(ctl + CT_WV, 1);
#pragma unroll
    for (int j = 0; j < 18; ++j) wvv[j] = ldb128<16>(rws, lane * 16 + (j & 3) * 1024, WVO_OFF + 54 * 1024 + (j >> 2) * 4096);
    GET_TAG();
    LAS const char *xr = lds + xslot + lane * 16, *xl = lds + xslot + 6144 + 4 * n;
    unsigned mo = wg * 8192 + 6144 + n * 64 + g * 16, tw = (unsigned)(tg >> (20 * (g & 1))) & 0xfffffu, sh = 12 * (g & 1), lse;
    asm volatile("" : "+v"(xr), "+v"(xl), "+v"(mo), "+v"(tw), "+v"(sh));
    __builtin_amdgcn_s_waitcnt(0x0f70);
    lds_spin(CT_PRDY, ctl + 4 * ((NT - 1) % NPK), 2 * NT);
    __builtin_amdgcn_s_sleep(2);
    __builtin_amdgcn_s_setprio(2);
    {
      int cnt_, c_;
      unsigned t_;
      asm volatile("s_mov_b32 %1, 0x400000\n1:\n\tds_read_b32 %2, %10 offset:%13\n\tds_read_b128 %3, %11\n\tds_read_b128 %4, %11 offset:1024\n\t"
                   "ds_read_b128 %5, %11 offset:2048\n\tds_read_b128 %6, %11 offset:3072\n\tds_read_b128 %7, %11 offset:4096\n\t"
                   "ds_read_b128 %8, %11 offset:5120\n\tds_read_b32 %9, %12\n\ts_waitcnt lgkmcnt(7)\n\tv_readfirstlane_b32 %0, %2\n\t"
                   "s_cmp_ge_i32 %0, 3\n\ts_cbranch_scc1 2f\n\ts_sub_u32 %1, %1, 1\n\ts_cbranch_scc0 1b\n2:\n\ts_waitcnt lgkmcnt(5)"
                   : "=&s"(c_), "=&s"(cnt_), "=&v"(t_), "=&v"(b[0]), "=&v"(b[1]), "=&v"(b[2]), "=&v"(b[3]), "=&v"(b[4]), "=&v"(b[5]), "=&v"(lse)
                   : "v"(ctl), "v"(xr), "v"(xl), "i"(CT_XD) : "memory", "scc");
    }
    SB();
    f32x4 o[3] = {{0.f, 0.f, 0.f, 0.f}, {0.f, 0.f, 0.f, 0.f}, {0.f, 0.f, 0.f, 0.f}};
#pragma unroll
    for (int k = 0; k < 2; ++k)
#pragma unroll
      for (int t = 0; t < 3; ++t) o[t] = mfma(wvv[6 * t + k], b[k], o[t]);
    asm volatile("s_waitcnt lgkmcnt(0)" : "+v"(o[0]), "+v"(o[1]), "+v"(o[2]), "+v"(b[2]), "+v"(b[3]), "+v"(b[4]), "+v"(b[5]), "+v"(lse));
    SB();
    tw |= (lse << sh) & 0xfff00000u;
    PROJECT(2);
    lds_inc(CT_ST, ctl);
  } else {
    COORDS();
    const int tq = (lane >> 2) & 3, tp = lane & 3;
    const bool pq = wg < 18;
    const bool pv = wg >= 18 && wg < NPR;
    const int pj = wg / 6, pu = wg % 6;
    if (w >= 4 && ((w + NT) & 3) == 0 && !pq) {
      unsigned k0, k1, k2;
      asm volatile("s_load_dword %0, %3, %5\n\ts_load_dword %1, %3, %6\n\ts_load_dword %2, %4, %7\n\ts_waitcnt lgkmcnt(0)"
                   : "=&s"(k0), "=&s"(k1), "=&s"(k2) : "s"(f0), "s"(f1), "s"((wg >> 3) * 3145728 + 1572860), "s"((wg >> 3) * 3145728 + 3145724),
                     "s"((wg >> 3) * 786432 + 786428) : "memory");
    }
    const u32x4 one = {0x3f803f80u, 0x3f803f80u, 0x3f803f80u, 0x3f803f80u};
    const int fq = ((n & 2) << 1) | ((n & 4) >> 1);
    LAS const char* rq0 = lds + PSCR + 24576 + 3072 * (n >> 3) + 128 * (n & 7) + 16 * (g ^ fq);
    LAS const char* rq1 = lds + PSCR + 24576 + 3072 * (n >> 3) + 128 * (n & 7) + 16 * ((4 + g) ^ fq);
    if (pq && w >= 2) {
      {
        waitvm<0>();
        lds_inc(CT_PA, ctl);
        if (w >= 4) {
          f32x4 sq = {0.f, 0.f, 0.f, 0.f}, rr = sq, cs = sq;
          const int ro = PSCR + 128 * (8 * g + tq) + 16 * (((w & 2) * 2 + tp) ^ ((tq & 2) << 1)) + 8 * (w & 1);
          u32x4 qv[6], af[6];
          lds_spin(CT_PA, ctl, 6);
#pragma unroll
          for (int s = 0; s < 6; ++s) {
            qv[s] = *(LAS const u32x4*)(((s & 1) ? rq1 : rq0) + (s >> 1) * 1024);
            af[s] = vtr2(lds + ro + s * 4096, lds + (ro ^ 32) + s * 4096 + 512);
          }
          SB();
#pragma unroll
          for (int s = 0; s < 6; ++s) { sq = mfma(one, qv[s], sq); rr = mfma(af[s], qv[s], rr); cs = mfma(af[s], one, cs); }
          const f32x4 qq = rr - (sq[0] * (1.f / C)) * cs;
          *(LAS u32x2*)(lds + LDS_QK + 4096 + ((w >> 1) & 1) * 1024 + lane * 16 + 8 * (w & 1)) = u32x2{cvtpk(qq[0], qq[1]), cvtpk(qq[2], qq[3])};
          lds_inc(CT_PB, ctl);
        }
      }
    }
    if (pq && w == 1) {
      {
        waitvm<0>();
        u32x4 kf4[4], qv[6];
        const int ko = LDS_QK + 128 * (((n & 4) << 2) | (n & 11)) + 16 * (g ^ ((n & 2) << 1));
#pragma unroll
        for (int i = 0; i < 4; ++i) kf4[i] = *(LAS const u32x4*)(lds + (ko ^ ((i & 1) * 64 + (i >> 1) * 32)) + (i >> 1) * 512);
        lds_spin(CT_PA, ctl, 6);
#pragma unroll
        for (int s = 0; s < 6; ++s) qv[s] = *(LAS const u32x4*)(((s & 1) ? rq1 : rq0) + (s >> 1) * 1024);
        const f32x4 z = {0.f, 0.f, 0.f, 0.f};
        f32x4 gq = z, sq = z;
#pragma unroll
        for (int s = 0; s < 6; ++s) { gq = mfma(qv[s], qv[s], gq); sq = mfma(one, qv[s], sq); }
        const float mean = sq[0] * (1.f / C);
        const float s2 = xrow16_sum(g == (n >> 2) ? sel4(gq, n & 3) : 0.f);
        float sc = __builtin_amdgcn_rsqf(fmaxf(fmaf(-mean, mean, s2 * (1.f / C)), 0.f) + EPS) * QSCALE;
        asm volatile("" : "+v"(sc));
        GET_TAG();
        lds_spin(CT_PB, ctl, 4);
        const u32x4 q0 = *(LAS const u32x4*)(lds + LDS_QK + 4096 + lane * 16), q1 = *(LAS const u32x4*)(lds + LDS_QK + 5120 + lane * 16);
        const f32x4 d0 = mfma(kf4[1], q1, mfma(kf4[0], q0, z)), d1 = mfma(kf4[3], q1, mfma(kf4[2], q0, z));
        const u32x4 xq = pack8(d0 * sc, d1 * sc);
        const unsigned ck = qk_check(xq, (unsigned)tg);
        asm volatile("" :: "v"(ck));
        {
          const unsigned so = QK_OFF + wg * QK_REC;
#pragma unroll
          for (int cp = 0; cp < 4; ++cp) {
            stb128(xq, rws, lane * 16 + so + cp * QK_COPY);
            stb32(ck, rws, lane * 4 + so + 1024 + cp * QK_COPY);
          }
          stb32((unsigned)tg, rws, HINT_OFF + lane * 128 + (pu >> 1) * 32 + pj * 8 + (pu & 1) * 4);
          lds_put(CT_PD, ctl, 4);
        }
      }
    }
    if (pv) {
      waitvm<0>();
      lds_inc(CT_PA, ctl);
      if (w >= 4) {
        const int ro = PSCR + 128 * (8 * g + tq) + 16 * ((2 * (w - 4) + (tp >> 1)) ^ ((tq & 2) << 1)) + 8 * (tp & 1);
        const int rv = PSCR + 24576 + 128 * n + 16 * (g ^ fq);
        f32x4 d0 = {0.f, 0.f, 0.f, 0.f}, d1 = d0;
        lds_spin(CT_PA, ctl, 7);
        const unsigned org = (unsigned)tstart + (max((int)(wall_clock64() - tstart) - LAND0, 0) * LANDG >> 2);
#pragma unroll
        for (int s = 0; s < 6; ++s) {
          const u32x4 p = vtr2(lds + ro + s * 4096, lds + (ro ^ 32) + s * 4096 + 512);
          LAS const char* pd = lds + (rv ^ ((s & 1) * 64)) + (s >> 1) * 4096;
          d0 = mfma(*(LAS const u32x4*)pd, p, d0); d1 = mfma(*(LAS const u32x4*)(pd + 2048), p, d1);
        }
        const int fi = (pj - 3) * 24 + (w - 4) * 6 + pu;
        stb128(pack8(d0, d1), rws, lane * 16 + WVO_OFF + fi * 1024);
        lds_inc(CT_PD, ctl);
        GET_TAG();
        waitvm<0>();
        if (lane < 8) stb64(u32x2{(unsigned)tg, (unsigned)(tg >> 32) | org << 8}, rws, FLAG_OFF + lane * 768 + fi * 8);
      }
    }
    const f32x4 z = {0.f, 0.f, 0.f, 0.f};
    int full = 0;
#define NEED(u_) lds_need(CT_FULL, full, ctl, (u_))
    if (w >= 4) {
      const int brg = ((g & 1) << 1) | (g >> 1);
      const int fn = (((n >> 1) & 3) << 1) | ((n >> 3) & 1);
      const int fy = ((n >> 2) & 1) * 4 + ((n >> 1) & 1) * 3;
      const int orr0 = 128 * n + 16 * (brg ^ fn), orr1 = 128 * n + 16 * ((4 + brg) ^ fn);
      const int ory0 = n < 8 ? CRS + 384 * n + 16 * (brg ^ fy) : LDS_CTL + CT_ONE, ory1 = n < 8 ? CRS + 384 * n + 16 * ((4 + brg) ^ fy) : LDS_CTL + CT_ONE;
      const int yon = n < 8 ? 1 : 0;
      const int opk = LDS_PK + 320 * g + 8 * n, ork = LDS_PK + 320 * (2 * (g >> 1) + (n >> 3)) + 256 + 8 * (n & 7) + 4 * (g & 1);
      u32x4 qk[6];
      f32x4 ref, qs;
      LAS const char *px0, *px1, *py0, *py1;
#define AT_SLOT(o_) do { px0 = lds + orr0 + (o_); px1 = lds + orr1 + (o_); py0 = lds + ory0 + __mul24(yon, (o_)); py1 = lds + ory1 + __mul24(yon, (o_)); \
      asm volatile("" : "+v"(px0), "+v"(px1), "+v"(py0), "+v"(py1)); } while (0)
#define RD_K(k_, i_) do { LAS const char* p_ = (((k_) & 1) ? px1 : px0) + ((k_) >> 1) * PART; \
    yv[i_] = *(LAS const u32x4*)((((k_) & 1) ? py1 : py0) + ((k_) >> 1) * 128); \
    xa[i_] = *(LAS const u32x4*)(p_); xb[i_] = *(LAS const u32x4*)(p_ + 2048); } while (0)
#define MF_STAT(i_) do { cy = mfma(yv[i_], yv[i_], cy); ca = mfma(yv[i_], xa[i_], ca); cb = mfma(yv[i_], xb[i_], cb); \
    ga = mfma(xa[i_], xa[i_], ga); gb = mfma(xb[i_], xb[i_], gb); } while (0)
#define MF_S(k_, i_) do { sy = mfma(qk[k_], yv[i_], sy); sa = mfma(qk[k_], xa[i_], sa); sb = mfma(qk[k_], xb[i_], sb); } while (0)
#define STATS(st, rec) do { \
      const int r = n & 3; \
      const float yy = half_copy<true>(sel4(cy, r)); \
      const f32x2 Y = swap32(yy, yy), X = swap32(sel4(ca, r), sel4(cb, r)), G = swap32(sel4(ga, r), sel4(gb, r)); \
      const float q1_ = fmaf(2.f, X[0], Y[0]) + (n < 8 ? G[0] : G[1]);       \
      const f32x2 Q = swap16(q1_, q1_); \
      const float mn_ = (X[1] + Y[1]) * (1.f / C); \
      const float var = fmaxf(fmaf(-mn_, mn_, fmaf((n & 4) ? Q[1] : Q[0], 1.f / C, EPS)), EPS), rs_ = __builtin_amdgcn_rsqf(var); \
      const f32x2 R = swap32(rs_, rs_), Mn = swap32(mn_, mn_); \
      st = f32x4{R[0], Mn[0], R[1], Mn[1]}; \
      const float v_ = (g & 1) ? var * rs_ : mn_, h_ = bfhi(__float_as_uint(v_)); \
      rec = cvtpk(h_, v_ - h_); \
    } while (0)
#define LOGITS(sv, st, base_) do { \
      _Pragma("unroll") for (int r = 0; r < 4; ++r) { \
        const float s0 = half_copy<true>(sy[r]); \
        sv[r] = fmaf(fmaf(-st[1], qs[r], sa[r] + s0), st[0], -(base_)[r]); \
        sv[4 + r] = fmaf(fmaf(-st[3], qs[r], sb[r] + s0), st[2], -(base_)[r]); \
      } \
    } while (0)
#define PUBLISH(sv, st, rec, pk_, word_) do { \
      *(LAS unsigned*)(lds + ork + (pk_) * PK_B) = rec; \
      *(LAS u32x2*)(lds + opk + (pk_) * PK_B) = u32x2{cvtpk(sv[0] * st[0], sv[1] * st[0]), cvtpk(sv[2] * st[0], sv[3] * st[0])}; \
      *(LAS u32x2*)(lds + opk + (pk_) * PK_B + 128) = u32x2{cvtpk(sv[4] * st[2], sv[5] * st[2]), cvtpk(sv[6] * st[2], sv[7] * st[2])}; \
      lds_put(CT_PRDY, ctl + 4 * (pk_), (word_)); \
    } while (0)
      int s = (w + NT - 1) & 3, slot = s + 4, pk = s + 4, ncon = 0;
      {
        u32x4 yv[12], xa[12], xb[12];
        f32x4 st0, st1;
        unsigned rec0, rec1;
#pragma unroll
        for (int h = 0; h < 2; ++h) {
          f32x4 cy = z, ga = z, ca = z, gb = z, cb = z;
          AT_SLOT((s + 4 * h) * SLOT);
          lds_wait(ctl + CT_FULL, 3 * (s + 4 * h) + 3);
#pragma unroll
          for (int k = 0; k < 6; ++k) RD_K(k, 6 * h + k);
          lds_inc(CT_FREE + 16 * h, ctl + 4 * s);
          SB();
#pragma unroll
          for (int k = 0; k < 6; ++k) MF_STAT(6 * h + k);
          SB();
          if (h == 0) { STATS(st0, rec0); asm volatile("" : "+v"(st0), "+v"(rec0)); } else { STATS(st1, rec1); asm volatile("" : "+v"(st1), "+v"(rec1)); }
          SB();
        }
        lds_wait(ctl + CT_QK, 3);
#pragma unroll
        for (int k = 0; k < 6; ++k) qk[k] = *(LAS const u32x4*)(lds + LDS_QK + k * 1024 + lane * 16);
        float s0v[8], s1v[8];
        {
          f32x4 sy = z, sa = z, sb = z;
#pragma unroll
          for (int k = 0; k < 6; ++k) MF_S(k, k);
#pragma unroll
          for (int r = 0; r < 4; ++r) qs[r] = half_copy<false>(sy[r]);
          LOGITS(s0v, st0, z);
        }
        {
          f32x4 sy = z, sa = z, sb = z;
#pragma unroll
          for (int k = 0; k < 6; ++k) MF_S(k, 6 + k);
          LOGITS(s1v, st1, z);
        }
#pragma unroll
        for (int r = 0; r < 4; ++r) ref[r] = row_max(fmaxf(fmaxf(s0v[r], s0v[4 + r]), fmaxf(s1v[r], s1v[4 + r])));
        if (n < 4) __builtin_amdgcn_ds_fmaxf((LAS float*)(ctl + CT_MAX + 16 * g + 4 * n), sel4(ref, n), __ATOMIC_RELAXED, __HIP_MEMORY_SCOPE_WORKGROUP, false);
        lds_inc(CT_QR, ctl);
        lds_spin(CT_QR, ctl, 4);
        ref = *(LAS const f32x4*)(ctl + CT_MAX + 16 * g);
#pragma unroll
        for (int j = 0; j < 8; ++j) s0v[j] = __builtin_amdgcn_exp2f(s0v[j] - ref[j & 3]);
        PUBLISH(s0v, st0, rec0, s, 2 * s + 2);
#pragma unroll
        for (int j = 0; j < 8; ++j) s1v[j] = __builtin_amdgcn_exp2f(s1v[j] - ref[j & 3]);
        if (pk >= NPK) { pk -= NPK; ncon = 3; lds_spin(CT_PCON, ctl + 4 * pk, 3); }
        s += 4;
        PUBLISH(s1v, st1, rec1, pk, 2 * s + 2);
      }
      float thr = 16.f;
      f32x4 nref = -ref;
#pragma unroll 1
      for (;;) {
        s += 4; slot += 4; slot -= slot >= NS ? NS : 0; pk += 4;
        if (pk >= NPK) { pk -= NPK; ncon += 3; }
        if (s >= NT) break;
        f32x4 sy = z, cy = z, sa, ga = z, ca = z, sb, gb = z, cb = z;
        u32x4 yv[6], xa[6], xb[6];
        unsigned fl_, word = 2 * s + 2;
        AT_SLOT(slot * SLOT);
        LAS char *pw = lds + opk + pk * PK_B, *prc = lds + ork + pk * PK_B, *pfl = ctl + 4 * pk, *pfr = ctl + 4 * slot;
        asm volatile("" : "+v"(pw), "+v"(prc), "+v"(pfl), "+v"(pfr), "+v"(word));
#define RD_X "ds_read_b32 %2, %7 offset:%11\n\tds_read_b128 %3, %8 offset:%12\n\tds_read_b128 %4, %9 offset:%12\n\t" \
             "ds_read_b128 %5, %8 offset:%13\n\tds_read_b128 %6, %9 offset:%13\n"
#define RD_IN(p_) "v"(ctl), "v"(px0), "v"(px1), "s"(3 * s + (p_) + 1), "i"(CT_FULL), "i"(PART * (p_)), "i"(PART * (p_) + 2048)
#define ISSUE(p_) do { int d0_, d1_; \
        asm volatile(RD_X : "=&s"(d0_), "=&s"(d1_), "=&v"(fl_), "=&v"(xa[2 * (p_)]), "=&v"(xa[2 * (p_) + 1]), "=&v"(xb[2 * (p_)]), "=&v"(xb[2 * (p_) + 1]) \
                     : RD_IN(p_) : "memory"); } while (0)
#define CHECK(p_) do { int cnt_; \
        asm volatile("s_mov_b32 %1, 0x400000\n\ts_branch 2f\n1:\n\t" RD_X "2:\n\ts_waitcnt lgkmcnt(4)\n\tv_readfirstlane_b32 %0, %2\n\t" \
                     "s_cmp_ge_i32 %0, %10\n\ts_cbranch_scc1 3f\n\ts_cmp_ge_i32 %10, %14\n\ts_cbranch_scc1 4f\n\ts_sleep %15\n4:\n\t" \
                     "s_sub_u32 %1, %1, 1\n\ts_cbranch_scc0 1b\n3:\n\ts_waitcnt lgkmcnt(2)" \
                     : "=&s"(full), "=&s"(cnt_), "+v"(fl_), "+v"(xa[2 * (p_)]), "+v"(xa[2 * (p_) + 1]), "+v"(xb[2 * (p_)]), "+v"(xb[2 * (p_) + 1]) \
                     : RD_IN(p_), "s"(3 * NT - 2), "i"(CALM) : "memory", "scc"); \
        asm volatile("" : "+s"(full)); } while (0)
#define GOT_B(p_) do { waitlds<0>(); asm volatile("" : "+v"(xb[2 * (p_)]), "+v"(xb[2 * (p_) + 1])); } while (0)
#define MF_A(k_) do { ca = mfma(yv[k_], xa[k_], ca); ga = mfma(xa[k_], xa[k_], ga); sa = mfma(qk[k_], xa[k_], sa); } while (0)
#define MF_B(k_) do { cb = mfma(yv[k_], xb[k_], cb); gb = mfma(xb[k_], xb[k_], gb); sb = mfma(qk[k_], xb[k_], sb); } while (0)
        const unsigned pcv = *(volatile LAS unsigned*)(pfl + CT_PCON);
        NEED(3 * s);
#pragma unroll
        for (int k = 0; k < 6; ++k) yv[k] = *(LAS const u32x4*)(((k & 1) ? py1 : py0) + (k >> 1) * 128);
        int con = __builtin_amdgcn_readfirstlane(pcv);
        SB();
#pragma unroll
        for (int k = 0; k < 6; ++k) sy = mfma(qk[k], yv[k], sy);
        asm volatile("" : "+v"(sy));
        SB();
        lds_need(CT_PCON, con, pfl, ncon);
        ISSUE(0);
        SB();
#pragma unroll
        for (int k = 0; k < 6; ++k) cy = mfma(yv[k], yv[k], cy);
#pragma unroll
        for (int r = 0; r < 4; ++r) sa[r] = half_copy<true>(sy[r]);
        const float yy = half_copy<true>(sel4(cy, n & 3));
        f32x2 Y = swap32(yy, yy);
        asm volatile("" : "+v"(Y), "+v"(sa));
        SB();
        CHECK(0);
        sb = sa;
        MF_A(0);
        asm volatile("" : "+v"(ca), "+v"(ga), "+v"(sa));
        SB();
        GOT_B(0);
        ISSUE(1);
        SB();
        MF_B(0); MF_A(1); MF_B(1);
        asm volatile("" : "+v"(ca), "+v"(ga), "+v"(sa), "+v"(cb), "+v"(gb), "+v"(sb));
        SB();
        CHECK(1);
        MF_A(2);
        asm volatile("" : "+v"(ca), "+v"(ga), "+v"(sa));
        SB();
        GOT_B(1);
        ISSUE(2);
        SB();
        MF_B(2); MF_A(3); MF_B(3);
        asm volatile("" : "+v"(ca), "+v"(ga), "+v"(sa), "+v"(cb), "+v"(gb), "+v"(sb));
        SB();
        CHECK(2);
        ca = mfma(yv[5], xa[5], mfma(yv[4], xa[4], ca));
        ga = mfma(xa[5], xa[5], mfma(xa[4], xa[4], ga));
        asm volatile("" : "+v"(ca), "+v"(ga));
        SB();
        GOT_B(2);
        cb = mfma(yv[5], xb[5], mfma(yv[4], xb[4], cb));
        asm volatile("" : "+v"(cb));
        SB();
        lds_inc(CT_FREE, pfr);
        gb = mfma(xb[4], xb[4], gb);
        float ua = sel4(ca, n & 3);
        asm volatile("" : "+v"(gb), "+v"(ua));
        SB();
        gb = mfma(xb[5], xb[5], gb);
        float va = sel4(ga, n & 3);
        asm volatile("" : "+v"(gb), "+v"(va));
        SB();
        sa = mfma(qk[4], xa[4], sa);
        float ub = sel4(cb, n & 3);
        asm volatile("" : "+v"(sa), "+v"(ub));
        SB();
        sa = mfma(qk[5], xa[5], sa);
        const f32x2 X = swap32(ua, ub);
        float mn_ = (X[1] + Y[1]) * (1.f / C), q1_ = fmaf(2.f, X[0], Y[0]);
        asm volatile("" : "+v"(sa), "+v"(mn_), "+v"(q1_));
        SB();
        sb = mfma(qk[4], xb[4], sb);
        float vb = sel4(gb, n & 3);
        asm volatile("" : "+v"(sb), "+v"(vb));
        SB();
        sb = mfma(qk[5], xb[5], sb);
        const f32x2 Mn = swap32(mn_, mn_), G = swap32(va, vb);
        q1_ += n < 8 ? G[0] : G[1];
        const f32x2 Q = swap16(q1_, q1_);
        sa -= Mn[0] * qs;
        const float var = fmaxf(fmaf(-mn_, mn_, fmaf((n & 4) ? Q[1] : Q[0], 1.f / C, EPS)), EPS), rs_ = __builtin_amdgcn_rsqf(var);
        sb -= Mn[1] * qs;
        const float v_ = (g & 1) ? var * rs_ : mn_, h_ = bfhi(__float_as_uint(v_));
        *(LAS unsigned*)prc = cvtpk(h_, v_ - h_);
        const f32x2 R = swap32(rs_, rs_);
        sa = sa * R[0] + nref; sb = sb * R[1] + nref;
        const float tm = fmaxf(fmaxf(fmaxf(fmaxf(sa[0], sa[1]), sa[2]), fmaxf(fmaxf(sa[3], sb[0]), sb[1])), fmaxf(sb[2], sb[3]));
        if (__builtin_expect(__builtin_amdgcn_ballot_w64(!(tm <= thr)) != 0ull, 0)) {
#pragma unroll
          for (int r = 0; r < 4; ++r) {
            const float mx = row_max(fmaxf(sa[r], sb[r])), dm = mx <= 16.f ? 0.f : mx;
            nref[r] -= dm; sa[r] -= dm; sb[r] -= dm;
          }
          thr = __builtin_nanf("");
          word |= 1u;
          if (n == 0) *(LAS f32x4*)(lds + LDS_PK + pk * PK_B + 1280 + 16 * g) = -nref;
        }
#pragma unroll
        for (int r = 0; r < 4; ++r) { sa[r] = __builtin_amdgcn_exp2f(sa[r]); sb[r] = __builtin_amdgcn_exp2f(sb[r]); }
        float ra = R[0], rb = R[1];
        asm volatile("" : "+v"(ra), "+v"(rb));
        sa *= ra; sb *= rb;
        *(LAS u32x2*)pw = u32x2{cvtpk(sa[0], sa[1]), cvtpk(sa[2], sa[3])};
        *(LAS u32x2*)(pw + 128) = u32x2{cvtpk(sb[0], sb[1]), cvtpk(sb[2], sb[3])};
        lds_put(CT_PRDY, pfl, word);
        SB();
      }
      GET_TAG();
      LAS char* fin = lds + xslot + 8192;
      unsigned qres = (unsigned)__builtin_amdgcn_raw_buffer_load_b16(mkrs(q, NQ * C * 2), dout * 2, 0, 0) << 16;
      unsigned ao = ((tid & 255) >> 1) * 8192 + (tid & 1) * 16 + dst * 32, tw = (unsigned)(tg >> (20 * (tid & 1))), sh = 12 * (tid & 1), oo = dout * 2;
      LAS char *fw = fin + (((lane & 8) | ((g & 1) << 2) | (g & 2)) + (lane & 1)) * 16 + 4 * (w - 4), *fr = fin + (2 * (lane & 7) + ((lane >> 3) & 1)) * 16;
      float v7 = 0.f;
      unsigned fi_ = 8u;
      const unsigned t0s = lds_get(ctl + CT_T0);
      unsigned gate = (t0s ? t0s : (unsigned)tstart) + RGATE;
      u32x4 ro = mkrs4(out, NQ * C * 2), rm4 = mkrs4(ws, WS_BYTES);
      unsigned half = 128 * 8192;
      unsigned long long wm = 0x0303030303030303ull;
      asm volatile("" : "+v"(ao), "+v"(tw), "+v"(sh), "+v"(oo), "+v"(fw), "+v"(fr), "+v"(fin), "+v"(v7), "+v"(qres), "+v"(fi_), "+s"(ro), "+s"(wm), "+s"(rm4), "+s"(half));
      if (w == 4) asm volatile("s_mov_b64 exec, 0x1515\n\tbuffer_store_dword %0, %1, %2, 0 offen sc1\n\ts_mov_b64 exec, -1" :: "v"(qres), "v"(oo), "s"(ro) : "memory");
      lds_wait(ctl + CT_XD, 1);
      lds_spin(CT_ST, ctl, 4);
      gate += max((int)((unsigned)wall_clock64() + OWNG - gate), 0);
      float org = *(LAS const float*)(lds + xslot + 6144 + 4 * dq);
      *(LAS float*)(fin + 256 + 4 * (w - 4)) = org;
#pragma unroll 1
      for (int spin = 0; spin < 1024; ++spin)
        if ((int)((unsigned)wall_clock64() - gate) >= 0) break;
      u32x4 A, B;
      asm volatile("buffer_load_dwordx4 %0, %2, %3, 0 offen sc1\n\tbuffer_load_dwordx4 %1, %2, %3, %4 offen sc1\n\ts_waitcnt vmcnt(0)"
                   : "=&v"(A), "=&v"(B) : "v"(ao), "s"(rm4), "s"(half) : "memory");
      if (__builtin_expect(__builtin_amdgcn_ballot_w64((((A[3] ^ tw) | (B[3] ^ tw)) & 0xfffffu) != 0u) != 0ull, 1)) {
#pragma unroll 1
        for (int spin = 0; spin < (1 << 18); ++spin) {
          unsigned xa_ = (A[3] ^ tw) << 12, xb_ = (B[3] ^ tw) << 12;
          if (__builtin_amdgcn_ballot_w64((xa_ | xb_) != 0u) == 0ull) break;
          xa_ |= __builtin_amdgcn_mov_dpp(xa_, 0xB1, 0xf, 0xf, true); xb_ |= __builtin_amdgcn_mov_dpp(xb_, 0xB1, 0xf, 0xf, true);
          asm volatile("" ::: "memory");
          if (xa_ != 0u) A = ldb128<16>(rws, ao, 0);
          if (xb_ != 0u) B = ldb128<16>(rws, ao, 128 * 8192);
        }
      }
      unsigned la = (A[3] & 0xfff00000u) >> sh, lb_ = (B[3] & 0xfff00000u) >> sh;
      la |= __builtin_amdgcn_mov_dpp(la, 0xB1, 0xf, 0xf, true); lb_ |= __builtin_amdgcn_mov_dpp(lb_, 0xB1, 0xf, 0xf, true);
      float da = __uint_as_float(la) - org, db = __uint_as_float(lb_) - org;
      f32x2 ua_[3], ub_[3];
#pragma unroll
      for (int i = 0; i < 3; ++i) { ua_[i] = f32x2{bflo(A[i]), bfhi(A[i])}; ub_[i] = f32x2{bflo(B[i]), bfhi(B[i])}; asm volatile("" : "+v"(ua_[i]), "+v"(ub_[i])); }
      if (__builtin_expect(__builtin_amdgcn_ballot_w64(!(fmaxf(da, db) <= 64.f)) != 0ull, 0)) {
        org = xrow16_max(row_max(fmaxf(__uint_as_float(la), __uint_as_float(lb_))));
        da = __uint_as_float(la) - org; db = __uint_as_float(lb_) - org;
        *(LAS float*)(fin + 256 + 4 * (w - 4)) = org;
        fi_ = 9u;
      }
      const float wa = __builtin_amdgcn_exp2f(da), wb = __builtin_amdgcn_exp2f(db);
#pragma unroll
      for (int i = 0; i < 3; ++i) ua_[i] = ua_[i] * wa + ub_[i] * wb;
      const f32x2 p0 = swap32(ua_[0][0], ua_[0][1]), p1 = swap32(ua_[1][0], ua_[1][1]), p2 = swap32(ua_[2][0], ua_[2][1]), p3 = swap32(wa + wb, v7);
      const f32x2 r0 = swap16(p0[0] + p0[1], p1[0] + p1[1]), r1 = swap16(p2[0] + p2[1], p3[0] + p3[1]);
      float c0 = r0[0] + r0[1], c1 = r1[0] + r1[1];
      asm volatile("s_nop 1\n\tv_add_f32_dpp %0, %0, %0 row_ror:8 row_mask:0xf bank_mask:0x3\n\tv_add_f32_dpp %0, %1, %1 row_ror:8 row_mask:0xf bank_mask:0xc\n\t"
                   "s_nop 1\n\tv_add_f32_dpp %0, %0, %0 row_shl:4 row_mask:0xf bank_mask:0x5\n\t"
                   "s_nop 1\n\tv_add_f32_dpp %0, %0, %0 quad_perm:[2,3,0,1] row_mask:0xf bank_mask:0x5\n\t"
                   "s_mov_b64 exec, %3\n\tds_write_b32 %2, %0\n\ts_mov_b64 exec, 1\n\tds_add_u32 %4, %5 offset:%6\n\ts_mov_b64 exec, -1"
                   : "+v"(c0) : "v"(c1), "v"(fw), "s"(wm), "v"(ctl), "v"(fi_), "i"(CT_FIN) : "memory");
      if (w == 4) {
        f32x4 a0, l0;
        unsigned cf;
        int cnt_, c_;
        asm volatile("s_mov_b32 %1, 0x4000000\n1:\n\tds_read_b32 %2, %5 offset:%8\n\tds_read_b128 %3, %6\n\tds_read_b128 %4, %7 offset:192\n\t"
                     "s_waitcnt lgkmcnt(2)\n\tv_readfirstlane_b32 %0, %2\n\ts_cmp_ge_i32 %0, 32\n\ts_cbranch_scc1 2f\n\t"
                     "s_sub_u32 %1, %1, 1\n\ts_cbranch_scc0 1b\n2:\n\ts_waitcnt lgkmcnt(0)"
                     : "=&s"(c_), "=&s"(cnt_), "=&v"(cf), "=&v"(a0), "=&v"(l0) : "v"(ctl), "v"(fr), "v"(fin), "i"(CT_FIN) : "memory", "scc");
        asm volatile("" : "+s"(c_));
        if (__builtin_expect(c_ != 32, 0)) {
          f32x4 m0 = *(LAS const f32x4*)(fin + 256);
          const float mm = fmaxf(fmaxf(m0[0], m0[1]), fmaxf(m0[2], m0[3]));
#pragma unroll
          for (int p = 0; p < 4; ++p) m0[p] = __builtin_amdgcn_exp2f(m0[p] - mm);
          a0 *= m0; l0 *= m0;
        }
        const float o = __uint_as_float(qres) + ((a0[0] + a0[2]) + (a0[1] + a0[3])) * __builtin_amdgcn_rcpf((l0[0] + l0[2]) + (l0[1] + l0[3]));
        const unsigned ob = cvtpk(o, __uint_as_float(__builtin_amdgcn_mov_dpp(__float_as_uint(o), 0xB1, 0xf, 0xf, true)));
        asm volatile("s_mov_b64 exec, 0x1515\n\tbuffer_store_dword %0, %1, %2, 0 offen sc1\n\ts_mov_b64 exec, -1" :: "v"(ob), "v"(oo), "s"(ro) : "memory");
        if (wg == 0 && lane == 0) stb64(u32x2{(unsigned)tg, (unsigned)(tg >> 32)}, rws, EP_OFF);
      }
    } else {
      const int ftr = ((g & 1) << 2) | ((tq >> 1) << 1) | (g >> 1);
      const int fty = (g & 1) * 4 + (tq >> 1) * 3;
      int otr[4], oty[4];
#pragma unroll
      for (int t = 0; t < 4; ++t) {
        otr[t] = (w - 1) * PART + 128 * (4 * g + tq) + 16 * ((2 * t + (tp >> 1)) ^ ftr) + 8 * (tp & 1);
        oty[t] = CRS + (w - 1) * 128 + 384 * (4 * (g & 1) + tq) + 16 * ((2 * t + (tp >> 1)) ^ fty) + 8 * (tp & 1);
      }
      int opk = LDS_PK + 320 * tp + 8 * (4 * g + tq), ork = LDS_PK + 320 * (g >> 1) + 256 + 8 * (4 * (g & 1) + tq);
      asm volatile("" : "+v"(opk), "+v"(ork), "+v"(oty[0]), "+v"(oty[1]), "+v"(oty[2]), "+v"(oty[3]));
#define RD_T(ta, ty, so_) _Pragma("unroll") for (int t_ = 0; t_ < 4; ++t_) { \
    ta[t_] = vtr2(lds + otr[t_] + (so_), lds + otr[t_] + (so_) + 2048); ty[t_] = vtr(lds + oty[t_] + (so_)); }
      f32x4 acc[4] = {z, z, z, z}, tac = z;
#define RD_P(o_) \
      pf = vtr2(lds + opk + (o_), lds + opk + (o_) + 128); \
      sf = vtr2(lds + ork + (o_), lds + ork + (o_) + 640)
#define TWICE(ty) asm volatile("s_nop 2"); _Pragma("unroll") for (int t = 0; t < 4; ++t) { u32x2 lo_, hi_; \
      asm volatile("v_mov_b64 %0, %2\n\tv_mov_b64 %1, %2" : "=&v"(lo_), "=&v"(hi_) : "v"(ty[t])); t2[t] = u32x4{lo_[0], lo_[1], hi_[0], hi_[1]}; } \
      asm volatile("s_nop 1" : "+v"(t2[0]), "+v"(t2[1]), "+v"(t2[2]), "+v"(t2[3]))
#define MF_PX(ta) \
      tac = mfma(sf, pf, tac); \
      _Pragma("unroll") for (int t = 0; t < 4; ++t) acc[t] = mfma(ta[t], pf, acc[t])
#define MF_PY() \
      _Pragma("unroll") for (int t = 0; t < 4; ++t) acc[t] = mfma(t2[t], pf, acc[t])
#define MOVE_REF(word_, o_) do { moved |= (word_) & 1; if (__builtin_expect(moved, 0)) { \
            const float m_ = *(LAS const float*)(lds + (((word_) & 1) ? LDS_PK + (o_) + 1280 : LDS_CTL + CT_MAX) + 4 * n); \
            Mtop = fmaxf(Mtop, m_); \
            const bool drop = m_ < Mtop - 64.f; \
            const float mn = drop ? M : m_, fa = __builtin_amdgcn_exp2f(M - mn); \
            M = mn; \
            _Pragma("unroll") for (int t = 0; t < 4; ++t) acc[t] *= fa; \
            tac *= fa; \
            if (drop) pf = u32x4{0u, 0u, 0u, 0u}; \
          } } while (0)
      const unsigned flo = FLAG_OFF + (wg & 7) * 768 + (w - 1) * 144 + (lane >= 16 ? min(lane - 16, 35) : (lane >> 3) * 6 + (lane & 7) + ((lane & 7) < 6 ? 0 : 18)) * 8;
      float M, Mtop;
      int moved = 0;
      u32x4 wvv[18], pf, sf, t2[4];
      {
        u32x4 ta[8][4], xx[6];
        u32x2 ty[8][4], fl;
        unsigned cc[6];
        const int qo = QK_OFF + (wg & 3) * QK_COPY + (2 * w - 2) * QK_REC;
#define QK_LOAD() _Pragma("unroll") for (int j = 0; j < 6; ++j) { \
          xx[j] = ldb128<16>(rws, lane * 16, qo + ((j >> 1) * 6 + (j & 1)) * QK_REC); \
          cc[j] = __builtin_amdgcn_raw_buffer_load_b32(rws, lane * 4, qo + ((j >> 1) * 6 + (j & 1)) * QK_REC + 1024, 16); }
#pragma unroll
        for (int u = 0; u < 8; ++u) {
          if (u == QKAT) {
            GET_TAG();
#pragma unroll 1
            for (int r = 0; r < 8; ++r) {
#pragma unroll 1
              for (int spin = 0; spin < 128; ++spin) {
                if ((int)(wall_clock64() - tstart) >= GATE + r * HSTEP) break;
                __builtin_amdgcn_s_sleep(1);
              }
              u32x8 hv;
              asm volatile("s_load_dwordx8 %0, %1, %2\n\ts_waitcnt lgkmcnt(0)" : "=&s"(hv) : "s"(ws), "s"(HINT_OFF + ((wg & 7) + 8 * r) * 128 + (w - 1) * 32) : "memory");
              const unsigned t_ = (unsigned)tg;
              if ((((hv[0] ^ t_) | (hv[1] ^ t_) | (hv[2] ^ t_)) | ((hv[3] ^ t_) | (hv[4] ^ t_) | (hv[5] ^ t_))) == 0u) break;
            }
            if (wg < NPR) lds_wait(ctl + CT_PD, 4);
            QK_LOAD();
#pragma unroll 1
            for (int spin = 0; spin < (1 << 18); ++spin) {
              unsigned df = 0u;
#pragma unroll
              for (int j = 0; j < 6; ++j) df |= qk_check(xx[j], (unsigned)tg) ^ cc[j];
              if (__all(df == 0u)) break;
              asm volatile("" ::: "memory");
              QK_LOAD();
            }
#pragma unroll
            for (int h = 0; h < 2; ++h) {
              u32x4 sx;
#pragma unroll
              for (int i = 0; i < 4; ++i) sx[i] = add3(xx[h][i], xx[2 + h][i], xx[4 + h][i]);
              *(LAS u32x4*)(lds + LDS_QK + (2 * w - 2 + h) * 1024 + lane * 16) = sx;
            }
            lds_inc(CT_QK, ctl);
            SB();
          }
          lds_wait(ctl + CT_FULL, 3 * u + w);
          RD_T(ta[u], ty[u], u * SLOT);
          lds_inc(CT_FREE + 4 * u, ctl);
          SB();
        }
        fl = __builtin_bit_cast(u32x2, __builtin_amdgcn_raw_buffer_load_b64(rws, flo, 0, 16));
#pragma unroll
        for (int u = 0; u < 12; ++u) {
          const int word = lds_spin(CT_PRDY + 4 * (u % NPK), ctl, 2 * u + 2);
          if (u == 0) Mtop = M = *(LAS const float*)(ctl + CT_MAX + 4 * n);
          RD_P((u % NPK) * PK_B);
          if (u >= 8) MOVE_REF(word, (u % NPK) * PK_B);
          lds_inc(CT_PCON + 4 * (u % NPK), ctl);
          SB();
          MF_PX(ta[u & 7]);
          SB();
          TWICE(ty[u & 7]);
          MF_PY();
          asm volatile("" : "+v"(acc[0]), "+v"(acc[1]), "+v"(acc[2]), "+v"(acc[3]), "+v"(tac));
          SB();
#define FLAGS_UP() __all(fl[0] == (unsigned)tg && (fl[1] & 255u) == (unsigned)(tg >> 32))
          if (u == T0AT && w == 1 && FLAGS_UP()) {
            int d = (int)(fl[1] - ((unsigned)tstart << 8)) >> 8;
            d += __builtin_amdgcn_mov_dpp(d, XOR8, 0xf, 0xf, true); d += __builtin_amdgcn_mov_dpp(d, ROR4, 0xf, 0xf, true);
            d += __builtin_amdgcn_mov_dpp(d, 0x122, 0xf, 0xf, true); d += __builtin_amdgcn_mov_dpp(d, 0x121, 0xf, 0xf, true);
            lds_put(CT_T0, ctl, ((unsigned)tstart + min(max(d >> 4, -300), 300)) | 1u);
          }
          if (u < 4) {
            NEED(3 * (u + 8) + w);
            RD_T(ta[u], ty[u], ((u + 8) % NS) * SLOT);
            lds_inc(CT_FREE + 4 * ((u + 8) % NS), ctl);
            SB();
          }
        }
#pragma unroll 1
        for (int spin = 0; spin < (1 << 18); ++spin) {
          if (FLAGS_UP()) break;
          asm volatile("" ::: "memory");
          fl = __builtin_bit_cast(u32x2, __builtin_amdgcn_raw_buffer_load_b64(rws, flo, 0, 16));
        }
        asm volatile("" ::: "memory");
        if (w == 3) lds_put(CT_WV, ctl, 1);
      }
      int slot = 3, pk = 12 % NPK;
      unsigned rdy = 0u;
      const int kro[3] = {(w - 1) * 2048, (w == 3 ? 0 : w) * 2048, (w == 1 ? 2 : w - 2) * 2048};
#define RD_PACKET(z_) do { int cnt_; u32x2 p0_, p1_, p2_, p3_; \
        word = __builtin_amdgcn_readfirstlane(rdy); \
        asm volatile("s_mov_b32 %1, 0x400000\n1:\n\tds_read_b32 %2, %7 offset:%11\n\t" \
                     "ds_read_b64_tr_b16 %5, %9\n\tds_read_b64_tr_b16 %6, %9 offset:640\n\t" \
                     "ds_read_b64_tr_b16 %3, %8\n\tds_read_b64_tr_b16 %4, %8 offset:128\n\t" \
                     "s_cmp_ge_i32 %0, %10\n\ts_cbranch_scc1 2f\n\ts_waitcnt lgkmcnt(4)\n\tv_readfirstlane_b32 %0, %2\n\t" \
                     "s_cmp_ge_i32 %0, %10\n\ts_cbranch_scc1 2f\n\t" z_ "s_sub_u32 %1, %1, 1\n\ts_cbranch_scc0 1b\n2:" \
                     : "+s"(word), "=&s"(cnt_), "=&v"(rdy), "=&v"(p0_), "=&v"(p1_), "=&v"(p2_), "=&v"(p3_) \
                     : "v"(ctl + 4 * pk), "v"(lds + opk + pk * PK_B), "v"(lds + ork + pk * PK_B), "s"(2 * s + 2), "i"(CT_PRDY), "i"(CALM) : "memory", "scc"); \
        asm volatile("" : "+s"(word)); \
        pf = u32x4{p0_[0], p0_[1], p1_[0], p1_[1]}; sf = u32x4{p2_[0], p2_[1], p3_[0], p3_[1]}; } while (0)
#define GOT_PACKET() do { waitlds<0>(); asm volatile("" : "+v"(pf), "+v"(sf) : "v"(rdy)); MOVE_REF(word, pk * PK_B); } while (0)
#define RD_Y(t_, p_) do { const u32x2 lo_ = vtr(p_); asm volatile("" ::: "memory"); const u32x2 hi_ = vtr(p_); \
        t2[t_] = u32x4{lo_[0], lo_[1], hi_[0], hi_[1]}; } while (0)
      u32x4 ta[4];
      int s = 12, word;
      NEED(36 + w);
#pragma unroll
      for (int t = 0; t < 4; ++t) { ta[t] = vtr2(lds + otr[t] + 2 * SLOT, lds + otr[t] + 2 * SLOT + 2048); RD_Y(t, lds + oty[t] + 2 * SLOT); }
      lds_inc(CT_FREE + 8, ctl);
#pragma unroll 1
      for (; s < NT - 1; ++s) {
        RD_PACKET("s_sleep %12\n\t");
        LAS const char *px[4], *py[4];
#pragma unroll
        for (int t = 0; t < 4; ++t) { px[t] = lds + otr[t] + slot * SLOT; py[t] = lds + oty[t] + slot * SLOT; asm volatile("" : "+v"(px[t]), "+v"(py[t])); }
        NEED(3 * s + 3 + w);
        GOT_PACKET();
        SB();
        tac = mfma(sf, pf, tac);
        asm volatile("" : "+v"(tac));
        SB();
        lds_inc(CT_PCON, ctl + 4 * pk);
        pk = pk == NPK - 1 ? 0 : pk + 1;
#pragma unroll
        for (int t = 0; t < 4; ++t) { acc[t] = mfma(ta[t], pf, acc[t]); SB(); ta[t] = vtr2(px[t], px[t] + 2048); SB(); }
        rdy = *(volatile LAS unsigned*)(ctl + CT_PRDY + 4 * pk);
#pragma unroll
        for (int t = 0; t < 4; ++t) { acc[t] = mfma(t2[t], pf, acc[t]); SB(); RD_Y(t, py[t]); SB(); }
        lds_inc(CT_FREE, ctl + 4 * slot);
#define WV_LOAD(j_) wvv[j_] = ldb128<16>(rws, lane * 16 + ((j_) & 1) * 1024, WVO_OFF + (w - 1) * 18432 + ((j_) / 6) * 6144 + kro[((j_) % 6) >> 1])
        if ((unsigned)(s - WVS) < WV1) {
#pragma unroll
          for (int u = 0; u < WV1; ++u)
            if (s - WVS == u) WV_LOAD(u);
        }
        if (s == NT - 3) {
#pragma unroll
          for (int j = WV1; j < 18; ++j) WV_LOAD(j);
        }
        slot = slot == NS - 1 ? 0 : slot + 1;
      }
      LAS char *xw = lds + xslot + lane * 16 + kro[0], *xl = lds + xslot + 6144 + 4 * n;
      LAS const char *xr1 = lds + xslot + lane * 16 + kro[1], *xr2 = lds + xslot + lane * 16 + kro[2];
      unsigned mo = wg * 8192 + (w - 1) * 2048 + n * 64 + g * 16, tw = (unsigned)(tg >> (20 * (g & 1))) & 0xfffffu, sh = 12 * (g & 1);
      asm volatile("" : "+v"(xw), "+v"(xl), "+v"(xr1), "+v"(xr2), "+v"(mo), "+v"(tw), "+v"(sh));
      __builtin_amdgcn_s_setprio(2);
      RD_PACKET("");
      GOT_PACKET();
      SB();
      tac = mfma(sf, pf, tac);
      asm volatile("" : "+v"(tac));
      SB();
#pragma unroll
      for (int t = 0; t < 2; ++t) acc[t] = mfma(t2[t], pf, mfma(ta[t], pf, acc[t]));
      SB();
#pragma unroll
      for (int t = 2; t < 4; ++t) acc[t] = mfma(t2[t], pf, mfma(ta[t], pf, acc[t]));
      const float L = tac[2] + tac[3], rl = __builtin_amdgcn_rcpf(L), ct = -(tac[0] + tac[1]) * rl;
      const float lse = M + __builtin_amdgcn_logf(L);
      u32x4 b[6];
      b[0] = pack8(acc[0] * rl + ct, acc[1] * rl + ct);
      *(LAS u32x4*)xw = b[0];
      *(LAS float*)xl = lse;
#pragma unroll
      for (int j = 0; j < 4; ++j) { __builtin_amdgcn_sched_group_barrier(0x008, 1, 0); __builtin_amdgcn_sched_group_barrier(0x002, 3, 0); }
      SB();
      b[1] = pack8(acc[2] * rl + ct, acc[3] * rl + ct);
      *(LAS u32x4*)(xw + 1024) = b[1];
      lds_inc(CT_XD, ctl);
      SB();
      f32x4 o[3];
      unsigned xd;
#pragma unroll
      for (int t = 0; t < 3; ++t) o[t] = mfma(wvv[6 * t], b[0], z);
      asm volatile("" : "+v"(o[0]), "+v"(o[1]), "+v"(o[2]));
      SB();
#define RD_SLICES "ds_read_b32 %0, %7 offset:%10\n\tds_read_b128 %1, %8\n\tds_read_b128 %2, %8 offset:1024\n\tds_read_b128 %3, %9\n\tds_read_b128 %4, %9 offset:1024\n"
      asm volatile(RD_SLICES : "=&v"(xd), "=&v"(b[2]), "=&v"(b[3]), "=&v"(b[4]), "=&v"(b[5]) : "i"(0), "i"(0), "v"(ctl), "v"(xr1), "v"(xr2), "i"(CT_XD) : "memory");
      SB();
#pragma unroll
      for (int t = 0; t < 3; ++t) o[t] = mfma(wvv[6 * t + 1], b[1], o[t]);
      tw |= (__float_as_uint(lse) << sh) & 0xfff00000u;
      asm volatile("" : "+v"(tw), "+v"(o[0]), "+v"(o[1]), "+v"(o[2]));
      SB();
      {
        int cnt_, c_;
        asm volatile("s_mov_b32 %6, 0x400000\n\ts_branch 2f\n1:\n\t" RD_SLICES "2:\n\ts_waitcnt lgkmcnt(4)\n\tv_readfirstlane_b32 %5, %0\n\t"
                     "s_cmp_ge_i32 %5, 3\n\ts_cbranch_scc1 3f\n\ts_sub_u32 %6, %6, 1\n\ts_cbranch_scc0 1b\n3:\n\ts_waitcnt lgkmcnt(0)"
                     : "+v"(xd), "+v"(b[2]), "+v"(b[3]), "+v"(b[4]), "+v"(b[5]), "=&s"(c_), "=&s"(cnt_)
                     : "v"(ctl), "v"(xr1), "v"(xr2), "i"(CT_XD) : "memory", "scc");
      }
      SB();
      PROJECT(2);
      lds_inc(CT_ST, ctl);
    }
  }
}

#undef rws
#define WS_AT(size_) ((size_) > (64u << 20) ? ((size_) - (64u << 20)) & ~(size_t)0x1fffff : 0)
extern "C" void kernel_launch(void* const* d_in, const int* in_sizes, int n_in, void* d_out, int out_size, void* d_ws, size_t ws_size,
                              hipStream_t stream) {
  dino_main<<<NWG, 512, 0, stream>>>((const u16*)d_in[0], (const u16*)d_in[1], (const u16*)d_in[2], (const u16*)d_in[3], (const u16*)d_in[4],
                                     (const u16*)d_in[5], (const u16*)d_in[6], (char*)d_ws + WS_AT(ws_size) + 4096, (u16*)d_out);
}
```

```cpp
#include <hip/hip_runtime.h>
#include <hip/hip_bf16.h>
#include <stdint.h>

typedef unsigned short u16;
using u32x2 = __attribute__((ext_vector_type(2))) unsigned;
using u32x4 = __attribute__((ext_vector_type(4))) unsigned;
using u32x8 = __attribute__((ext_vector_type(8))) unsigned;
using f32x2 = __attribute__((ext_vector_type(2))) float;
using f32x4 = __attribute__((ext_vector_type(4))) float;
using s16x8 = __attribute__((ext_vector_type(8))) short;
using bf16x2_t = __attribute__((ext_vector_type(2))) __bf16;
typedef short v4i16_t __attribute__((ext_vector_type(4)));
#define LAS __attribute__((address_space(3)))

constexpr int C = 192, NQ = 16, W0 = 512, W1 = 256, NWG = 256;
constexpr float EPS = 1e-5f;
constexpr float QSCALE = 0.07216878364870322f * 1.4426950408889634f;

constexpr int PART = 4096, CRS = 3 * PART;
constexpr int SLOT = CRS + 3072, NS = 10;
constexpr int LDS_QK = NS * SLOT;
constexpr int NPK = 7;
constexpr int LDS_PK = LDS_QK, PK_B = 1344;
constexpr int LDS_CTL = LDS_PK + NPK * PK_B;
constexpr int LDS_SINK = LDS_CTL + 384;
constexpr int LDS_BYTES = LDS_SINK + 256;
constexpr int PSCR = 8 * SLOT;
constexpr int CT_FULL = 8, CT_WV = 4, CT_PA = 0, CT_PB = 12, CT_QR = 16, CT_PD = 20, CT_T0 = 24, CT_XD = 28, CT_TG = 32, CT_QK = 40, CT_ST = 44,
              CT_FREE = 48, CT_FIN = 88, CT_ONE = 112, CT_PRDY = 128, CT_PCON = 160, CT_MAX = 256;

constexpr int MB_BYTES = NWG * NWG * 32;
constexpr int EP_OFF = MB_BYTES;
constexpr int FLAG_OFF = EP_OFF + 512;
constexpr int WVO_OFF = EP_OFF + 8192;
constexpr int QK_OFF = WVO_OFF + C * C * 2;
constexpr int QK_REC = 1280, QK_COPY = 18 * QK_REC;
constexpr int HINT_OFF = QK_OFF + 4 * QK_COPY;
constexpr int WS_BYTES = HINT_OFF + 64 * 128;

#define GIVE 2
constexpr int NPR = 36;
struct Tabs { unsigned seq[112]; };
constexpr Tabs make_tabs() {
  Tabs t{};
  for (int i = 0; i < 112; ++i) t.seq[i] = i + 1;
  return t;
}
__device__ const Tabs tabs = make_tabs();
#define QKAT 3
#define WV1 6
#define WMIN 28
#define WSTEP 5
#define LSTEP 40
#define OWNG 30
#define PACE0 30
#define PACE1 1738
#define RGATE 1918
#define T0AT 3
#define WVS 12
#define CALM 2
#define QSW 12
#define QSJ 12
#define HSTEP 40
#define GATE 350
#define CODE_BYTES 25984

__device__ __forceinline__ unsigned cvtpk(float lo, float hi) {
  bf16x2_t v = {(__bf16)lo, (__bf16)hi};
  return __builtin_bit_cast(unsigned, v);
}
__device__ __forceinline__ float bflo(unsigned x) { return __builtin_bit_cast(float, x << 16); }
__device__ __forceinline__ float bfhi(unsigned x) { return __builtin_bit_cast(float, x & 0xffff0000u); }
template <int CTRL> __device__ __forceinline__ float dpp(float x) {
  return __builtin_bit_cast(float, __builtin_amdgcn_mov_dpp(__builtin_bit_cast(int, x), CTRL, 0xf, 0xf, true));
}
constexpr int XOR8 = 0x128, ROR4 = 0x124;
template <bool HI8> __device__ __forceinline__ float half_copy(float x) {
  const int v = __builtin_bit_cast(int, x);
  return __builtin_bit_cast(float, __builtin_amdgcn_update_dpp(v, v, XOR8, 0xf, HI8 ? 0xc : 0x3, false));
}
__device__ __forceinline__ float row_max(float x) {
  x = fmaxf(x, dpp<XOR8>(x)); x = fmaxf(x, dpp<ROR4>(x)); x = fmaxf(x, dpp<0x122>(x));
  return fmaxf(x, dpp<0x121>(x));
}
__device__ __forceinline__ f32x2 swap32(float a, float b) {
  auto s = __builtin_amdgcn_permlane32_swap(__float_as_uint(a), __float_as_uint(b), false, false);
  return f32x2{__uint_as_float(s[0]), __uint_as_float(s[1])};
}
__device__ __forceinline__ f32x2 swap16(float a, float b) {
  auto s = __builtin_amdgcn_permlane16_swap(__float_as_uint(a), __float_as_uint(b), false, false);
  return f32x2{__uint_as_float(s[0]), __uint_as_float(s[1])};
}
__device__ __forceinline__ float xrow16_max(float x) {
  auto s = __builtin_amdgcn_permlane16_swap(__float_as_uint(x), __float_as_uint(x), false, false);
  x = fmaxf(__uint_as_float(s[0]), __uint_as_float(s[1]));
  auto t = __builtin_amdgcn_permlane32_swap(__float_as_uint(x), __float_as_uint(x), false, false);
  return fmaxf(__uint_as_float(t[0]), __uint_as_float(t[1]));
}
__device__ __forceinline__ float xrow16_sum(float x) {
  auto s = __builtin_amdgcn_permlane16_swap(__float_as_uint(x), __float_as_uint(x), false, false);
  x = __uint_as_float(s[0]) + __uint_as_float(s[1]);
  auto t = __builtin_amdgcn_permlane32_swap(__float_as_uint(x), __float_as_uint(x), false, false);
  return __uint_as_float(t[0]) + __uint_as_float(t[1]);
}
__device__ __forceinline__ f32x4 mfma(u32x4 a, u32x4 b, f32x4 c) {
  return __builtin_amdgcn_mfma_f32_16x16x32_bf16(__builtin_bit_cast(s16x8, a), __builtin_bit_cast(s16x8, b), c, 0, 0, 0);
}
__device__ __forceinline__ u32x2 vtr(LAS const char* p) {
  return __builtin_bit_cast(u32x2, __builtin_amdgcn_ds_read_tr16_b64_v4i16((LAS v4i16_t*)p));
}
__device__ __forceinline__ u32x4 vtr2(LAS const char* p, LAS const char* q_) {
  const u32x2 a = vtr(p), b = vtr(q_);
  return u32x4{a[0], a[1], b[0], b[1]};
}
__device__ __forceinline__ float sel4(f32x4 v, int r) {
  const float a = (r & 1) ? v[1] : v[0], b = (r & 1) ? v[3] : v[2];
  return (r & 2) ? b : a;
}
using RSRC = __amdgpu_buffer_rsrc_t;
__device__ __forceinline__ RSRC mkrs(const void* p, int bytes) { return __builtin_amdgcn_make_buffer_rsrc(const_cast<void*>(p), (short)0, bytes, 0x00020000); }
__device__ __forceinline__ u32x4 mkrs4(const void* p, unsigned bytes) {
  const unsigned long long b = (unsigned long long)p;
  return u32x4{(unsigned)b, (unsigned)(b >> 32) & 0xffffu, bytes, 0x00020000u};
}
template <int AUX> __device__ __forceinline__ u32x4 ldb128(RSRC r, unsigned vo, int so) { return __builtin_bit_cast(u32x4, __builtin_amdgcn_raw_buffer_load_b128(r, vo, so, AUX)); }
#define SB() __builtin_amdgcn_sched_barrier(0)
#define RAW_BARRIER() asm volatile("s_waitcnt lgkmcnt(0)\n\ts_barrier" ::: "memory")
#define DMA(m0_, vo_, rs_, so_) asm volatile("s_mov_b32 m0, %0\n\ts_nop 0\n\tbuffer_load_dwordx4 %1, %2, %3 offen lds" :: "s"(m0_), "v"(vo_), "s"(rs_), "s"(so_) : "memory")
template <int N> __device__ __forceinline__ void waitlds() { __builtin_amdgcn_s_waitcnt(0xc07f | (N << 8)); }
template <int N> __device__ __forceinline__ void waitvm() { asm volatile("s_waitcnt vmcnt(%0)" :: "i"(N) : "memory"); }


__device__ __forceinline__ void lds_inc(int O, LAS char* p) {
  asm volatile("s_mov_b64 exec, 1\n\tds_add_u32 %0, %1 offset:%2\n\ts_mov_b64 exec, -1" :: "v"(p), "v"(1u), "i"(O) : "memory");
}
__device__ __forceinline__ void lds_put(int O, LAS char* p, unsigned v) {
  asm volatile("s_mov_b64 exec, 1\n\tds_write_b32 %0, %1 offset:%2\n\ts_mov_b64 exec, -1" :: "v"(p), "v"(v), "i"(O) : "memory");
}
__device__ __forceinline__ int lds_get(LAS char* p) {
  asm volatile("" ::: "memory");
  const unsigned v = __hip_atomic_load((LAS unsigned*)p, __ATOMIC_RELAXED, __HIP_MEMORY_SCOPE_WORKGROUP);
  asm volatile("" ::: "memory");
  return __builtin_amdgcn_readfirstlane(v);
}
__device__ __forceinline__ void lds_wait(LAS char* p, int target) {
#pragma unroll 1
  for (int spin = 0; spin < (1 << 22); ++spin) {
    if (lds_get(p) >= target) break;
    __builtin_amdgcn_s_sleep(1);
  }
}
__device__ __forceinline__ int lds_spin(int O, LAS char* p, int target) {
  int r, cnt = 1 << 22;
  unsigned t;
  asm volatile("1:\n\tds_read_b32 %1, %3 offset:%5\n\ts_waitcnt lgkmcnt(0)\n\tv_readfirstlane_b32 %0, %1\n\ts_cmp_ge_i32 %0, %4\n\ts_cbranch_scc1 2f\n\t"
               "s_sub_u32 %2, %2, 1\n\ts_cbranch_scc0 1b\n2:"
               : "=&s"(r), "=&v"(t), "+s"(cnt) : "v"(p), "s"(target), "i"(O) : "memory", "scc");
  return r;
}
__device__ __forceinline__ void lds_need(int O, int& have, LAS char* p, int target) {
  int cnt;
  unsigned t;
  asm volatile("s_cmp_ge_i32 %0, %4\n\ts_cbranch_scc1 2f\n\ts_mov_b32 %2, 0x400000\n"
               "1:\n\tds_read_b32 %1, %3 offset:%5\n\ts_waitcnt lgkmcnt(0)\n\tv_readfirstlane_b32 %0, %1\n\ts_cmp_ge_i32 %0, %4\n\ts_cbranch_scc1 2f\n\t"
               "s_sleep %6\n\ts_sub_u32 %2, %2, 1\n\ts_cbranch_scc0 1b\n2:"
               : "+s"(have), "=&v"(t), "=&s"(cnt) : "v"(p), "s"(target), "i"(O), "i"(CALM) : "memory", "scc");
  asm volatile("" : "+s"(have));
}
__device__ __forceinline__ u32x4 pack8(f32x4 a, f32x4 b) { return u32x4{cvtpk(a[0], a[1]), cvtpk(a[2], a[3]), cvtpk(b[0], b[1]), cvtpk(b[2], b[3])}; }

__device__ __forceinline__ void stb128(u32x4 v, RSRC r, unsigned o) { __builtin_amdgcn_raw_buffer_store_b128(v, r, o, 0, 16); }
__device__ __forceinline__ void stb64(u32x2 v, RSRC r, unsigned o) { __builtin_amdgcn_raw_buffer_store_b64(v, r, o, 0, 16); }
__device__ __forceinline__ void stb32(unsigned v, RSRC r, unsigned o) { __builtin_amdgcn_raw_buffer_store_b32(v, r, o, 0, 16); }
__device__ __forceinline__ unsigned qk_check(u32x4 x, unsigned tg) {
  const unsigned a = (x[0] ^ __builtin_amdgcn_alignbit(x[1], x[1], 11)) * 0x9e3779b1u + tg;
  const unsigned b = (x[2] + __builtin_amdgcn_alignbit(x[3], x[3], 19)) * 0x85ebca77u;
  return __builtin_amdgcn_alignbit(a, a, 15) ^ b ^ (x[1] + x[3]);
}
__device__ __forceinline__ unsigned add3(unsigned a, unsigned b, unsigned c) {
  return cvtpk((bflo(a) + bflo(b)) + bflo(c), (bfhi(a) + bfhi(b)) + bfhi(c));
}
__global__ __launch_bounds__(512, 2) void dino_main(const u16* q, const u16* f0, const u16* f1, const u16* wq, const u16* wk, const u16* wv,
                                                     const u16* wo, char* ws, u16* out) {
  __shared__ __attribute__((aligned(128))) char smem[LDS_BYTES];
  LAS char* lds = (LAS char*)smem;
  LAS char* ctl = lds + LDS_CTL;
  const unsigned lb = (unsigned)(unsigned long long)lds;
  const int w = __builtin_amdgcn_readfirstlane(threadIdx.x >> 6);
  unsigned long long pc0;
  asm volatile("s_getpc_b64 %0" : "=s"(pc0));
#define COORDS() \
  int tid = threadIdx.x, wg = blockIdx.x; \
  asm volatile("" : "+v"(tid), "+s"(wg)); \
  __builtin_assume(tid >= 0 && tid < 512); __builtin_assume(wg >= 0 && wg < NWG); \
  const int lane = tid & 63, n = lane & 15, g = lane >> 4;       \
    \
  const int dq = (wg & 7) * 2 + ((wg >> 4) & 1), dst = (wg & ~31) + (wg & 7) * 4 + ((wg >> 3) & 3); \
  const int dj = 6 * ((wg >> 5) & 1) + min(lane & 7, 5); \
  const int dout = dq * C + 48 * (wg >> 6) + 16 * (dj >> 2) + (wg & 8) + ((lane & 8) >> 1) + (dj & 3); \
  const int NT = wg < NPR ? 32 - GIVE : wg >= 40 && !(wg & 1) && (wg >> 3) < 5 + ((wg & 4) ? 8 : 10) * GIVE ? 33 : 32; \
  const int xslot = (NT >= 30 ? NT - 30 : NT - 20) * SLOT;       \
  (void)xslot; (void)n; (void)g; (void)dst; (void)dout; (void)NT
#define ARGS_IN() asm volatile("s_waitcnt lgkmcnt(0)" :: "s"(q), "s"(f0), "s"(f1), "s"(wq), "s"(wk), "s"(wv), "s"(wo), "s"(ws), "s"(out) : "memory")
#define rws mkrs(ws, WS_BYTES)
#define PROJECT(k0_) do { \
      _Pragma("unroll") for (int k = k0_; k < 6; ++k) { o[0] = mfma(wvv[k], b[k], o[0]); o[1] = mfma(wvv[6 + k], b[k], o[1]); } \
      asm volatile("" : "+v"(o[0]), "+v"(o[1])); \
      SB(); \
      _Pragma("unroll") for (int k = k0_; k < 6; ++k) o[2] = mfma(wvv[12 + k], b[k], o[2]); \
      asm volatile("" : "+v"(o[2])); \
      SB(); \
      stb128(u32x4{cvtpk(o[0][0], o[0][1]), cvtpk(o[0][2], o[0][3]), cvtpk(o[1][0], o[1][1]), tw}, rws, mo); \
      SB(); \
      stb128(u32x4{cvtpk(o[1][2], o[1][3]), cvtpk(o[2][0], o[2][1]), cvtpk(o[2][2], o[2][3]), tw}, rws, mo + 1024); \
    } while (0)
  unsigned long long tg;
#define GET_TAG() tg = ((unsigned long long)((unsigned)lds_spin(CT_TG + 4, ctl, 1) & 0xffu) << 32) | (unsigned)lds_get(ctl + CT_TG)
  const unsigned long long tstart = wall_clock64();
  if (w != 0) {
    const unsigned l0 = threadIdx.x & 63;
    if (blockIdx.x < NPR) {
      const unsigned p6 = (blockIdx.x * 43) >> 8, pu = blockIdx.x - 6 * p6;
      unsigned vo = ((l0 * 16) ^ (((l0 * 4) & 0x40) | (l0 & 0x20))) + (l0 >> 3) * 256;
      const bool pv = p6 >= 3, wa = w >= 4, isq = !pv && (w & 6) == 2;
      const unsigned a6 = 6 * w - 24, pj = pv ? p6 - 3 : p6;
      unsigned so = (wa ? a6 : isq ? w - 2 : 4 * pu) * 3072 + (isq ? 0 : pv && !wa ? w - 1 : pj) * 128, ga = isq ? 128 : 3072;
      unsigned lo = lb + PSCR + (wa ? a6 : pv ? 4 * w + 20 : (0x1b181e00u >> (8 * w)) & 255u) * 1024, hi = lo + (wa ? 6144 : isq ? 3072 : 4096);
      asm volatile("" : "+v"(vo), "+s"(lo), "+s"(so), "+s"(hi), "+s"(ga));
      ARGS_IN();
      const u32x4 rw = mkrs4(wa ? (pv ? wo : wq) : pv ? wv : isq ? q : wk, isq ? NQ * C * 2 : C * C * 2);
#pragma unroll 1
      do { DMA(lo, vo, rw, so); lo += 1024; so += ga; } while (lo != hi);
    } else if (w == 7 && blockIdx.x >= 56 && blockIdx.x < 72) {
      const u32x4 rc = mkrs4((const void*)(pc0 & ~255ull), CODE_BYTES);
#pragma unroll 1
      for (int i = 0; i < CODE_BYTES; i += 4096)
        asm volatile("s_mov_b32 m0, %0\n\ts_nop 0\n\tbuffer_load_dword %1, %2, %3 offen lds" :: "s"(lb + LDS_SINK), "v"(l0 * 64), "s"(rc), "s"(i) : "memory");
    }
    if (w == 3) {
      unsigned long long ep;
      asm volatile("s_waitcnt lgkmcnt(0)\n\ts_load_dwordx2 %0, %1, %2\n\ts_barrier\n\ts_waitcnt lgkmcnt(0)" : "=&s"(ep) : "s"(ws), "s"(EP_OFF) : "memory");
      *(LAS unsigned long long*)(ctl + CT_TG) = ((ep + 0x100001ull) & 0xffffffffffull) | 0x10000000000ull;
    } else RAW_BARRIER();
  }
  SB();
  if (__builtin_expect(w == 0, 1)) {
    const unsigned l0 = threadIdx.x & 63, g0 = blockIdx.x;
    unsigned y0 = (l0 * 16) ^ (l0 >= 48 ? 48 : 0), y1 = (l0 * 16) ^ (l0 >= 32 ? 64 : 48), y2 = (l0 * 16) ^ (l0 >= 16 ? 112 : 64);
    unsigned v0 = ((l0 * 16) ^ ((l0 * 2) & 0x60)) + (l0 >> 3) * 640 + 1024, v1 = (v0 ^ 16) - 640;
    unsigned v2 = v0 + W0 * C * 2 - 2048, v3 = v1 + W0 * C * 2 - 2048;
    unsigned b0 = (g0 & 7) * (2 * W0 * C * 2) + (g0 >> 3) * 6144, b1 = (g0 & 7) * (W1 * C * 2) + (g0 >> 3) * 3072;
    {
      const unsigned iv = (l0 & 15) >= CT_ONE / 8 ? 0x3f803f80u : (l0 >> 3) == CT_MAX / 64 ? 0xff800000u : 0u;
      *(LAS u32x2*)(ctl + 8 * min(l0, 47u)) = u32x2{l0 >= 47 ? 0x3f803f80u : iv, l0 >= 47 ? 0x3f803f80u : iv};
    }
    constexpr unsigned st0 = 16 * W0 * C * 2, st1 = 8 * W1 * C * 2;
    unsigned sb = lb, fo = CT_FREE, fv = 0u;
    int lap = 0;
    u32x4 rt = mkrs4(tabs.seq, 112 * 4);
    unsigned vz = 0u;
    asm volatile("" : "+v"(v0), "+v"(v1), "+v"(v2), "+v"(v3), "+v"(y0), "+v"(y1), "+v"(y2), "+v"(vz), "+s"(b0), "+s"(b1), "+s"(rt));
    ARGS_IN();
    const u32x4 r0 = mkrs4((const char*)f0 - 1024, W0 * W0 * C * 2 + 1024), r1 = mkrs4(f1, W1 * W1 * C * 2);
    if (g0 < NPR) __builtin_amdgcn_s_barrier();
#define UNIT(p_) do { unsigned so_; \
      asm volatile("s_add_u32 m0, %1, %10\n\ts_add_u32 %0, %2, %11\n\t" \
                   "buffer_load_dwordx4 %3, %7, %0 offen nt lds\n\tbuffer_load_dwordx4 %4, %7, %0 offen offset:1024 nt lds\n\t" \
                   "buffer_load_dwordx4 %5, %7, %0 offen offset:2048 nt lds\n\tbuffer_load_dwordx4 %6, %7, %0 offen offset:3072 nt lds\n\t" \
                   "s_mov_b32 m0, %12\n\ts_mov_b64 exec, 1\n\tbuffer_load_dword %13, %8, %9 offen offset:%14 lds\n\ts_mov_b64 exec, -1" \
                   : "=&s"(so_) : "s"(sb), "s"(b0), "v"(v0), "v"(v1), "v"(v2), "v"(v3), "s"(r0), "s"(rt), "s"(12 * s), "i"((p_) * PART), "i"((p_) * 128), \
                     "s"(lb + LDS_CTL + CT_FULL - 4 * (p_)), "v"(vz), "i"(4 * (p_)) : "memory", "scc"); } while (0)
#define WINDOW() \
      asm volatile("s_waitcnt vmcnt(%1)\n\ts_cmp_ge_i32 %0, %6\n\ts_cbranch_scc1 1f\n\ts_waitcnt vmcnt(%2)\n\ts_cmp_ge_i32 %0, %7\n\ts_cbranch_scc1 1f\n\t" \
                   "s_waitcnt vmcnt(%3)\n\ts_cmp_ge_i32 %0, %8\n\ts_cbranch_scc1 1f\n\ts_waitcnt vmcnt(%4)\n\ts_cmp_ge_i32 %0, %9\n\ts_cbranch_scc1 1f\n\t" \
                   "s_waitcnt vmcnt(%5)\n1:" :: "s"(late), "i"(WMIN + 4 * WSTEP), "i"(WMIN + 3 * WSTEP), "i"(WMIN + 2 * WSTEP), "i"(WMIN + WSTEP), "i"(WMIN), \
                   "i"(4 * LSTEP * 256), "i"(3 * LSTEP * 256), "i"(2 * LSTEP * 256), "i"(LSTEP * 256) : "memory", "scc")
#define COARSE(sb_) \
      asm volatile("s_add_u32 m0, %0, %6\n\ts_nop 0\n\tbuffer_load_dwordx4 %1, %4, %5 offen nt lds\n\t" \
                   "buffer_load_dwordx4 %2, %4, %5 offen offset:1024 nt lds\n\tbuffer_load_dwordx4 %3, %4, %5 offen offset:2048 nt lds" \
                   :: "s"(sb_), "v"(y0), "v"(y1), "v"(y2), "s"(r1), "s"(b1), "i"(CRS) : "memory", "scc")
    COARSE(sb);
#pragma unroll 1
    for (int s = 0; s < 2; ++s) {
      asm volatile("s_add_u32 %0, %0, %1" : "+s"(b1) : "i"(st1) : "scc");
      UNIT(0); UNIT(1); COARSE(sb + SLOT); UNIT(2);
      sb += SLOT; b0 += st0;
    }
    if (g0 >= NPR) __builtin_amdgcn_s_barrier();
    __builtin_amdgcn_s_setprio(3);
    COORDS();
    fo += 12;
    const int tau = __builtin_amdgcn_readfirstlane((int)((PACE1 - PACE0) * 256.f * __builtin_amdgcn_rcpf((float)NT)));
    unsigned due = PACE0 * 256u + 2 * tau, t0v = 0u;
    unsigned long long now = tstart;
    const unsigned xi = (wg >> 3) - 5, xrow = 8 * (31 - xi % GIVE) + (wg & 7) + (xi / GIVE & 1), xcol = xi / GIVE >> 1;
    const unsigned x0 = xrow * (2 * W0 * C * 2) + xcol * 6144, x1 = xrow * (W1 * C * 2) + xcol * 3072;
#pragma unroll 1
    for (int s = 2; s < NT; ++s) {
      asm volatile("" : "+s"(now));
      const unsigned t0s = __builtin_amdgcn_readfirstlane(t0v), t0 = t0s ? t0s : (unsigned)tstart;
      if ((int)(((unsigned)now - t0) * 256u - due) < 0) {
#pragma unroll 1
        for (int spin = 0; spin < 256; ++spin) {
          if ((int)(((unsigned)wall_clock64() - t0) * 256u - due) >= 0) break;
          __builtin_amdgcn_s_sleep(1);
        }
      }
      fv = *(volatile LAS unsigned*)(ctl + (s == 7 ? CT_PD : fo));
      now = wall_clock64();
      UNIT(0);
      asm volatile("" : "+s"(now));
      const int late = (int)(((unsigned)now - t0) * 256u - due);
      due += tau;
      WINDOW();
      UNIT(1);
      WINDOW();
      const unsigned nsb = fo == CT_FREE ? lb : sb + SLOT;
      const unsigned n0 = s == 31 ? x0 : b0 + st0;
      b1 = s == 31 ? x1 : b1 + st1;
      if (s + 1 < NT) {
        const int need = s == 7 && wg < NPR ? 4 : lap;
        if (__builtin_expect((int)__builtin_amdgcn_readfirstlane(fv) < need, 0)) {
          lds_wait(ctl + (s == 7 ? CT_PD : fo), need);
        }
        COARSE(nsb);
      }
      now = wall_clock64();
      t0v = *(volatile LAS unsigned*)(ctl + CT_T0);
      UNIT(2);
      WINDOW();
      if (fo == CT_FREE + 4 * (NS - 1)) { fo = CT_FREE; lap += 4; } else fo += 4;
      sb = nsb; b0 = n0;
    }
    __builtin_amdgcn_s_setprio(0);
    u32x4 wvv[18], b[6];
    asm volatile("s_mov_b32 m0, %0\n\ts_mov_b64 exec, 1\n\tbuffer_load_dword %1, %2, 0 offen lds\n\ts_mov_b64 exec, -1"
                 :: "s"(lb + LDS_SINK), "v"(vz), "s"(mkrs4(ws - 4096, 4096)) : "memory");
    lds_wait(ctl + CT_WV, 1);
#pragma unroll
    for (int j = 0; j < 18; ++j) wvv[j] = ldb128<16>(rws, lane * 16 + (j & 3) * 1024, WVO_OFF + 54 * 1024 + (j >> 2) * 4096);
    GET_TAG();
    LAS const char *xr = lds + xslot + lane * 16, *xl = lds + xslot + 6144 + 4 * n;
    unsigned mo = wg * 8192 + 6144 + n * 64 + g * 16, tw = (unsigned)(tg >> (20 * (g & 1))) & 0xfffffu, sh = 12 * (g & 1), lse;
    asm volatile("" : "+v"(xr), "+v"(xl), "+v"(mo), "+v"(tw), "+v"(sh));
    __builtin_amdgcn_s_waitcnt(0x0f70);
    lds_spin(CT_PRDY, ctl + 4 * ((NT - 1) % NPK), 2 * NT);
    __builtin_amdgcn_s_sleep(2);
    __builtin_amdgcn_s_setprio(2);
    {
      int cnt_, c_;
      unsigned t_;
      asm volatile("s_mov_b32 %1, 0x400000\n1:\n\tds_read_b32 %2, %10 offset:%13\n\tds_read_b128 %3, %11\n\tds_read_b128 %4, %11 offset:1024\n\t"
                   "ds_read_b128 %5, %11 offset:2048\n\tds_read_b128 %6, %11 offset:3072\n\tds_read_b128 %7, %11 offset:4096\n\t"
                   "ds_read_b128 %8, %11 offset:5120\n\tds_read_b32 %9, %12\n\ts_waitcnt lgkmcnt(7)\n\tv_readfirstlane_b32 %0, %2\n\t"
                   "s_cmp_ge_i32 %0, 3\n\ts_cbranch_scc1 2f\n\ts_sub_u32 %1, %1, 1\n\ts_cbranch_scc0 1b\n2:\n\ts_waitcnt lgkmcnt(5)"
                   : "=&s"(c_), "=&s"(cnt_), "=&v"(t_), "=&v"(b[0]), "=&v"(b[1]), "=&v"(b[2]), "=&v"(b[3]), "=&v"(b[4]), "=&v"(b[5]), "=&v"(lse)
                   : "v"(ctl), "v"(xr), "v"(xl), "i"(CT_XD) : "memory", "scc");
    }
    SB();
    f32x4 o[3] = {{0.f, 0.f, 0.f, 0.f}, {0.f, 0.f, 0.f, 0.f}, {0.f, 0.f, 0.f, 0.f}};
#pragma unroll
    for (int k = 0; k < 2; ++k)
#pragma unroll
      for (int t = 0; t < 3; ++t) o[t] = mfma(wvv[6 * t + k], b[k], o[t]);
    asm volatile("s_waitcnt lgkmcnt(0)" : "+v"(o[0]), "+v"(o[1]), "+v"(o[2]), "+v"(b[2]), "+v"(b[3]), "+v"(b[4]), "+v"(b[5]), "+v"(lse));
    SB();
    tw |= (lse << sh) & 0xfff00000u;
    PROJECT(2);
    lds_inc(CT_ST, ctl);
  } else {
    COORDS();
    const int tq = (lane >> 2) & 3, tp = lane & 3;
    const bool pq = wg < 18;
    const bool pv = wg >= 18 && wg < NPR;
    const int pj = wg / 6, pu = wg % 6;
    if (w >= 4 && ((w + NT) & 3) == 0 && !pq) {
      unsigned k0, k1, k2;
      asm volatile("s_load_dword %0, %3, %5\n\ts_load_dword %1, %3, %6\n\ts_load_dword %2, %4, %7\n\ts_waitcnt lgkmcnt(0)"
                   : "=&s"(k0), "=&s"(k1), "=&s"(k2) : "s"(f0), "s"(f1), "s"((wg >> 3) * 3145728 + 1572860), "s"((wg >> 3) * 3145728 + 3145724),
                     "s"((wg >> 3) * 786432 + 786428) : "memory");
    }
    const u32x4 one = {0x3f803f80u, 0x3f803f80u, 0x3f803f80u, 0x3f803f80u};
    const int fq = ((n & 2) << 1) | ((n & 4) >> 1);
    LAS const char* rq0 = lds + PSCR + 24576 + 3072 * (n >> 3) + 128 * (n & 7) + 16 * (g ^ fq);
    LAS const char* rq1 = lds + PSCR + 24576 + 3072 * (n >> 3) + 128 * (n & 7) + 16 * ((4 + g) ^ fq);
    if (pq && w >= 2) {
      {
        waitvm<0>();
        lds_inc(CT_PA, ctl);
        if (w >= 4) {
          f32x4 sq = {0.f, 0.f, 0.f, 0.f}, rr = sq, cs = sq;
          const int ro = PSCR + 128 * (8 * g + tq) + 16 * (((w & 2) * 2 + tp) ^ ((tq & 2) << 1)) + 8 * (w & 1);
          u32x4 qv[6], af[6];
          lds_spin(CT_PA, ctl, 6);
#pragma unroll
          for (int s = 0; s < 6; ++s) {
            qv[s] = *(LAS const u32x4*)(((s & 1) ? rq1 : rq0) + (s >> 1) * 1024);
            af[s] = vtr2(lds + ro + s * 4096, lds + (ro ^ 32) + s * 4096 + 512);
          }
          SB();
#pragma unroll
          for (int s = 0; s < 6; ++s) { sq = mfma(one, qv[s], sq); rr = mfma(af[s], qv[s], rr); cs = mfma(af[s], one, cs); }
          const f32x4 qq = rr - (sq[0] * (1.f / C)) * cs;
          *(LAS u32x2*)(lds + LDS_QK + 4096 + ((w >> 1) & 1) * 1024 + lane * 16 + 8 * (w & 1)) = u32x2{cvtpk(qq[0], qq[1]), cvtpk(qq[2], qq[3])};
          lds_inc(CT_PB, ctl);
        }
      }
    }
    if (pq && w == 1) {
      {
        waitvm<0>();
        u32x4 kf4[4], qv[6];
        const int ko = LDS_QK + 128 * (((n & 4) << 2) | (n & 11)) + 16 * (g ^ ((n & 2) << 1));
#pragma unroll
        for (int i = 0; i < 4; ++i) kf4[i] = *(LAS const u32x4*)(lds + (ko ^ ((i & 1) * 64 + (i >> 1) * 32)) + (i >> 1) * 512);
        lds_spin(CT_PA, ctl, 6);
#pragma unroll
        for (int s = 0; s < 6; ++s) qv[s] = *(LAS const u32x4*)(((s & 1) ? rq1 : rq0) + (s >> 1) * 1024);
        const f32x4 z = {0.f, 0.f, 0.f, 0.f};
        f32x4 gq = z, sq = z;
#pragma unroll
        for (int s = 0; s < 6; ++s) { gq = mfma(qv[s], qv[s], gq); sq = mfma(one, qv[s], sq); }
        const float mean = sq[0] * (1.f / C);
        const float s2 = xrow16_sum(g == (n >> 2) ? sel4(gq, n & 3) : 0.f);
        float sc = __builtin_amdgcn_rsqf(fmaxf(fmaf(-mean, mean, s2 * (1.f / C)), 0.f) + EPS) * QSCALE;
        asm volatile("" : "+v"(sc));
        GET_TAG();
        lds_spin(CT_PB, ctl, 4);
        const u32x4 q0 = *(LAS const u32x4*)(lds + LDS_QK + 4096 + lane * 16), q1 = *(LAS const u32x4*)(lds + LDS_QK + 5120 + lane * 16);
        const f32x4 d0 = mfma(kf4[1], q1, mfma(kf4[0], q0, z)), d1 = mfma(kf4[3], q1, mfma(kf4[2], q0, z));
        const u32x4 xq = pack8(d0 * sc, d1 * sc);
        const unsigned ck = qk_check(xq, (unsigned)tg);
        asm volatile("" :: "v"(ck));
        {
          const unsigned so = QK_OFF + wg * QK_REC;
#pragma unroll
          for (int cp = 0; cp < 4; ++cp) {
            stb128(xq, rws, lane * 16 + so + cp * QK_COPY);
            stb32(ck, rws, lane * 4 + so + 1024 + cp * QK_COPY);
          }
          stb32((unsigned)tg, rws, HINT_OFF + lane * 128 + (pu >> 1) * 32 + pj * 8 + (pu & 1) * 4);
          lds_put(CT_PD, ctl, 4);
        }
      }
    }
    if (pv) {
      waitvm<0>();
      lds_inc(CT_PA, ctl);
      if (w >= 4) {
        const int ro = PSCR + 128 * (8 * g + tq) + 16 * ((2 * (w - 4) + (tp >> 1)) ^ ((tq & 2) << 1)) + 8 * (tp & 1);
        const int rv = PSCR + 24576 + 128 * n + 16 * (g ^ fq);
        f32x4 d0 = {0.f, 0.f, 0.f, 0.f}, d1 = d0;
        lds_spin(CT_PA, ctl, 7);
#pragma unroll
        for (int s = 0; s < 6; ++s) {
          const u32x4 p = vtr2(lds + ro + s * 4096, lds + (ro ^ 32) + s * 4096 + 512);
          LAS const char* pd = lds + (rv ^ ((s & 1) * 64)) + (s >> 1) * 4096;
          d0 = mfma(*(LAS const u32x4*)pd, p, d0); d1 = mfma(*(LAS const u32x4*)(pd + 2048), p, d1);
        }
        const int fi = (pj - 3) * 24 + (w - 4) * 6 + pu;
        stb128(pack8(d0, d1), rws, lane * 16 + WVO_OFF + fi * 1024);
        lds_inc(CT_PD, ctl);
        GET_TAG();
        waitvm<0>();
        if (lane < 8) stb64(u32x2{(unsigned)tg, (unsigned)(tg >> 32) | (unsigned)tstart << 8}, rws, FLAG_OFF + lane * 768 + fi * 8);
      }
    }
    const f32x4 z = {0.f, 0.f, 0.f, 0.f};
    int full = 0;
#define NEED(u_) lds_need(CT_FULL, full, ctl, (u_))
    if (w >= 4) {
      const int brg = ((g & 1) << 1) | (g >> 1);
      const int fn = (((n >> 1) & 3) << 1) | ((n >> 3) & 1);
      const int fy = ((n >> 2) & 1) * 4 + ((n >> 1) & 1) * 3;
      const int orr0 = 128 * n + 16 * (brg ^ fn), orr1 = 128 * n + 16 * ((4 + brg) ^ fn);
      const int ory0 = n < 8 ? CRS + 384 * n + 16 * (brg ^ fy) : LDS_CTL + CT_ONE, ory1 = n < 8 ? CRS + 384 * n + 16 * ((4 + brg) ^ fy) : LDS_CTL + CT_ONE;
      const int yon = n < 8 ? 1 : 0;
      const int opk = LDS_PK + 320 * g + 8 * n, ork = LDS_PK + 320 * (2 * (g >> 1) + (n >> 3)) + 256 + 8 * (n & 7) + 4 * (g & 1);
      u32x4 qk[6];
      f32x4 ref, qs;
      LAS const char *px0, *px1, *py0, *py1;
#define AT_SLOT(o_) do { px0 = lds + orr0 + (o_); px1 = lds + orr1 + (o_); py0 = lds + ory0 + __mul24(yon, (o_)); py1 = lds + ory1 + __mul24(yon, (o_)); \
      asm volatile("" : "+v"(px0), "+v"(px1), "+v"(py0), "+v"(py1)); } while (0)
#define RD_K(k_, i_) do { LAS const char* p_ = (((k_) & 1) ? px1 : px0) + ((k_) >> 1) * PART; \
    yv[i_] = *(LAS const u32x4*)((((k_) & 1) ? py1 : py0) + ((k_) >> 1) * 128); \
    xa[i_] = *(LAS const u32x4*)(p_); xb[i_] = *(LAS const u32x4*)(p_ + 2048); } while (0)
#define MF_STAT(i_) do { cy = mfma(yv[i_], yv[i_], cy); ca = mfma(yv[i_], xa[i_], ca); cb = mfma(yv[i_], xb[i_], cb); \
    ga = mfma(xa[i_], xa[i_], ga); gb = mfma(xb[i_], xb[i_], gb); } while (0)
#define MF_S(k_, i_) do { sy = mfma(qk[k_], yv[i_], sy); sa = mfma(qk[k_], xa[i_], sa); sb = mfma(qk[k_], xb[i_], sb); } while (0)
#define STATS(st, rec) do { \
      const int r = n & 3; \
      const float yy = half_copy<true>(sel4(cy, r)); \
      const f32x2 Y = swap32(yy, yy), X = swap32(sel4(ca, r), sel4(cb, r)), G = swap32(sel4(ga, r), sel4(gb, r)); \
      const float q1_ = fmaf(2.f, X[0], Y[0]) + (n < 8 ? G[0] : G[1]);       \
      const f32x2 Q = swap16(q1_, q1_); \
      const float mn_ = (X[1] + Y[1]) * (1.f / C); \
      const float var = fmaxf(fmaf(-mn_, mn_, fmaf((n & 4) ? Q[1] : Q[0], 1.f / C, EPS)), EPS), rs_ = __builtin_amdgcn_rsqf(var); \
      const f32x2 R = swap32(rs_, rs_), Mn = swap32(mn_, mn_); \
      st = f32x4{R[0], Mn[0], R[1], Mn[1]}; \
      const float v_ = (g & 1) ? var * rs_ : mn_, h_ = bfhi(__float_as_uint(v_)); \
      rec = cvtpk(h_, v_ - h_); \
    } while (0)
#define LOGITS(sv, st, base_) do { \
      _Pragma("unroll") for (int r = 0; r < 4; ++r) { \
        const float s0 = half_copy<true>(sy[r]); \
        sv[r] = fmaf(fmaf(-st[1], qs[r], sa[r] + s0), st[0], -(base_)[r]); \
        sv[4 + r] = fmaf(fmaf(-st[3], qs[r], sb[r] + s0), st[2], -(base_)[r]); \
      } \
    } while (0)
#define PUBLISH(sv, st, rec, pk_, word_) do { \
      *(LAS unsigned*)(lds + ork + (pk_) * PK_B) = rec; \
      *(LAS u32x2*)(lds + opk + (pk_) * PK_B) = u32x2{cvtpk(sv[0] * st[0], sv[1] * st[0]), cvtpk(sv[2] * st[0], sv[3] * st[0])}; \
      *(LAS u32x2*)(lds + opk + (pk_) * PK_B + 128) = u32x2{cvtpk(sv[4] * st[2], sv[5] * st[2]), cvtpk(sv[6] * st[2], sv[7] * st[2])}; \
      lds_put(CT_PRDY, ctl + 4 * (pk_), (word_)); \
    } while (0)
      int s = (w + NT - 1) & 3, slot = s + 4, pk = s + 4, ncon = 0;
      {
        u32x4 yv[12], xa[12], xb[12];
        f32x4 st0, st1;
        unsigned rec0, rec1;
#pragma unroll
        for (int h = 0; h < 2; ++h) {
          f32x4 cy = z, ga = z, ca = z, gb = z, cb = z;
          AT_SLOT((s + 4 * h) * SLOT);
          lds_wait(ctl + CT_FULL, 3 * (s + 4 * h) + 3);
#pragma unroll
          for (int k = 0; k < 6; ++k) RD_K(k, 6 * h + k);
          lds_inc(CT_FREE + 16 * h, ctl + 4 * s);
          SB();
#pragma unroll
          for (int k = 0; k < 6; ++k) MF_STAT(6 * h + k);
          SB();
          if (h == 0) { STATS(st0, rec0); asm volatile("" : "+v"(st0), "+v"(rec0)); } else { STATS(st1, rec1); asm volatile("" : "+v"(st1), "+v"(rec1)); }
          SB();
        }
        lds_wait(ctl + CT_QK, 3);
#pragma unroll
        for (int k = 0; k < 6; ++k) qk[k] = *(LAS const u32x4*)(lds + LDS_QK + k * 1024 + lane * 16);
        float s0v[8], s1v[8];
        {
          f32x4 sy = z, sa = z, sb = z;
#pragma unroll
          for (int k = 0; k < 6; ++k) MF_S(k, k);
#pragma unroll
          for (int r = 0; r < 4; ++r) qs[r] = half_copy<false>(sy[r]);
          LOGITS(s0v, st0, z);
        }
        {
          f32x4 sy = z, sa = z, sb = z;
#pragma unroll
          for (int k = 0; k < 6; ++k) MF_S(k, 6 + k);
          LOGITS(s1v, st1, z);
        }
#pragma unroll
        for (int r = 0; r < 4; ++r) ref[r] = row_max(fmaxf(fmaxf(s0v[r], s0v[4 + r]), fmaxf(s1v[r], s1v[4 + r])));
        if (n < 4) __builtin_amdgcn_ds_fmaxf((LAS float*)(ctl + CT_MAX + 16 * g + 4 * n), sel4(ref, n), __ATOMIC_RELAXED, __HIP_MEMORY_SCOPE_WORKGROUP, false);
        lds_inc(CT_QR, ctl);
        lds_spin(CT_QR, ctl, 4);
        ref = *(LAS const f32x4*)(ctl + CT_MAX + 16 * g);
#pragma unroll
        for (int j = 0; j < 8; ++j) s0v[j] = __builtin_amdgcn_exp2f(s0v[j] - ref[j & 3]);
        PUBLISH(s0v, st0, rec0, s, 2 * s + 2);
#pragma unroll
        for (int j = 0; j < 8; ++j) s1v[j] = __builtin_amdgcn_exp2f(s1v[j] - ref[j & 3]);
        if (pk >= NPK) { pk -= NPK; ncon = 3; lds_spin(CT_PCON, ctl + 4 * pk, 3); }
        s += 4;
        PUBLISH(s1v, st1, rec1, pk, 2 * s + 2);
      }
      float thr = 16.f;
      f32x4 nref = -ref;
#pragma unroll 1
      for (;;) {
        s += 4; slot += 4; slot -= slot >= NS ? NS : 0; pk += 4;
        if (pk >= NPK) { pk -= NPK; ncon += 3; }
        if (s >= NT) break;
        f32x4 sy = z, cy = z, sa, ga = z, ca = z, sb, gb = z, cb = z;
        u32x4 yv[6], xa[6], xb[6];
        unsigned fl_, word = 2 * s + 2;
        AT_SLOT(slot * SLOT);
        LAS char *pw = lds + opk + pk * PK_B, *prc = lds + ork + pk * PK_B, *pfl = ctl + 4 * pk, *pfr = ctl + 4 * slot;
        asm volatile("" : "+v"(pw), "+v"(prc), "+v"(pfl), "+v"(pfr), "+v"(word));
#define RD_X "ds_read_b32 %2, %7 offset:%11\n\tds_read_b128 %3, %8 offset:%12\n\tds_read_b128 %4, %9 offset:%12\n\t" \
             "ds_read_b128 %5, %8 offset:%13\n\tds_read_b128 %6, %9 offset:%13\n"
#define RD_IN(p_) "v"(ctl), "v"(px0), "v"(px1), "s"(3 * s + (p_) + 1), "i"(CT_FULL), "i"(PART * (p_)), "i"(PART * (p_) + 2048)
#define ISSUE(p_) do { int d0_, d1_; \
        asm volatile(RD_X : "=&s"(d0_), "=&s"(d1_), "=&v"(fl_), "=&v"(xa[2 * (p_)]), "=&v"(xa[2 * (p_) + 1]), "=&v"(xb[2 * (p_)]), "=&v"(xb[2 * (p_) + 1]) \
                     : RD_IN(p_) : "memory"); } while (0)
#define CHECK(p_) do { int cnt_; \
        asm volatile("s_mov_b32 %1, 0x400000\n\ts_branch 2f\n1:\n\t" RD_X "2:\n\ts_waitcnt lgkmcnt(4)\n\tv_readfirstlane_b32 %0, %2\n\t" \
                     "s_cmp_ge_i32 %0, %10\n\ts_cbranch_scc1 3f\n\ts_cmp_ge_i32 %10, %14\n\ts_cbranch_scc1 4f\n\ts_sleep %15\n4:\n\t" \
                     "s_sub_u32 %1, %1, 1\n\ts_cbranch_scc0 1b\n3:\n\ts_waitcnt lgkmcnt(2)" \
                     : "=&s"(full), "=&s"(cnt_), "+v"(fl_), "+v"(xa[2 * (p_)]), "+v"(xa[2 * (p_) + 1]), "+v"(xb[2 * (p_)]), "+v"(xb[2 * (p_) + 1]) \
                     : RD_IN(p_), "s"(3 * NT - 2), "i"(CALM) : "memory", "scc"); \
        asm volatile("" : "+s"(full)); } while (0)
#define GOT_B(p_) do { waitlds<0>(); asm volatile("" : "+v"(xb[2 * (p_)]), "+v"(xb[2 * (p_) + 1])); } while (0)
#define MF_A(k_) do { ca = mfma(yv[k_], xa[k_], ca); ga = mfma(xa[k_], xa[k_], ga); sa = mfma(qk[k_], xa[k_], sa); } while (0)
#define MF_B(k_) do { cb = mfma(yv[k_], xb[k_], cb); gb = mfma(xb[k_], xb[k_], gb); sb = mfma(qk[k_], xb[k_], sb); } while (0)
        const unsigned pcv = *(volatile LAS unsigned*)(pfl + CT_PCON);
        NEED(3 * s);
#pragma unroll
        for (int k = 0; k < 6; ++k) yv[k] = *(LAS const u32x4*)(((k & 1) ? py1 : py0) + (k >> 1) * 128);
        int con = __builtin_amdgcn_readfirstlane(pcv);
        SB();
#pragma unroll
        for (int k = 0; k < 6; ++k) sy = mfma(qk[k], yv[k], sy);
        asm volatile("" : "+v"(sy));
        SB();
        lds_need(CT_PCON, con, pfl, ncon);
        ISSUE(0);
        SB();
#pragma unroll
        for (int k = 0; k < 6; ++k) cy = mfma(yv[k], yv[k], cy);
#pragma unroll
        for (int r = 0; r < 4; ++r) sa[r] = half_copy<true>(sy[r]);
        const float yy = half_copy<true>(sel4(cy, n & 3));
        f32x2 Y = swap32(yy, yy);
        asm volatile("" : "+v"(Y), "+v"(sa));
        SB();
        CHECK(0);
        sb = sa;
        MF_A(0);
        asm volatile("" : "+v"(ca), "+v"(ga), "+v"(sa));
        SB();
        GOT_B(0);
        ISSUE(1);
        SB();
        MF_B(0); MF_A(1); MF_B(1);
        asm volatile("" : "+v"(ca), "+v"(ga), "+v"(sa), "+v"(cb), "+v"(gb), "+v"(sb));
        SB();
        CHECK(1);
        MF_A(2);
        asm volatile("" : "+v"(ca), "+v"(ga), "+v"(sa));
        SB();
        GOT_B(1);
        ISSUE(2);
        SB();
        MF_B(2); MF_A(3); MF_B(3);
        asm volatile("" : "+v"(ca), "+v"(ga), "+v"(sa), "+v"(cb), "+v"(gb), "+v"(sb));
        SB();
        CHECK(2);
        ca = mfma(yv[5], xa[5], mfma(yv[4], xa[4], ca));
        ga = mfma(xa[5], xa[5], mfma(xa[4], xa[4], ga));
        asm volatile("" : "+v"(ca), "+v"(ga));
        SB();
        GOT_B(2);
        cb = mfma(yv[5], xb[5], mfma(yv[4], xb[4], cb));
        asm volatile("" : "+v"(cb));
        SB();
        lds_inc(CT_FREE, pfr);
        gb = mfma(xb[4], xb[4], gb);
        float ua = sel4(ca, n & 3);
        asm volatile("" : "+v"(gb), "+v"(ua));
        SB();
        gb = mfma(xb[5], xb[5], gb);
        float va = sel4(ga, n & 3);
        asm volatile("" : "+v"(gb), "+v"(va));
        SB();
        sa = mfma(qk[4], xa[4], sa);
        float ub = sel4(cb, n & 3);
        asm volatile("" : "+v"(sa), "+v"(ub));
        SB();
        sa = mfma(qk[5], xa[5], sa);
        const f32x2 X = swap32(ua, ub);
        float mn_ = (X[1] + Y[1]) * (1.f / C), q1_ = fmaf(2.f, X[0], Y[0]);
        asm volatile("" : "+v"(sa), "+v"(mn_), "+v"(q1_));
        SB();
        sb = mfma(qk[4], xb[4], sb);
        float vb = sel4(gb, n & 3);
        asm volatile("" : "+v"(sb), "+v"(vb));
        SB();
        sb = mfma(qk[5], xb[5], sb);
        const f32x2 Mn = swap32(mn_, mn_), G = swap32(va, vb);
        q1_ += n < 8 ? G[0] : G[1];
        const f32x2 Q = swap16(q1_, q1_);
        sa -= Mn[0] * qs;
        const float var = fmaxf(fmaf(-mn_, mn_, fmaf((n & 4) ? Q[1] : Q[0], 1.f / C, EPS)), EPS), rs_ = __builtin_amdgcn_rsqf(var);
        sb -= Mn[1] * qs;
        const float v_ = (g & 1) ? var * rs_ : mn_, h_ = bfhi(__float_as_uint(v_));
        *(LAS unsigned*)prc = cvtpk(h_, v_ - h_);
        const f32x2 R = swap32(rs_, rs_);
        sa = sa * R[0] + nref; sb = sb * R[1] + nref;
        const float tm = fmaxf(fmaxf(fmaxf(fmaxf(sa[0], sa[1]), sa[2]), fmaxf(fmaxf(sa[3], sb[0]), sb[1])), fmaxf(sb[2], sb[3]));
        if (__builtin_expect(__builtin_amdgcn_ballot_w64(!(tm <= thr)) != 0ull, 0)) {
#pragma unroll
          for (int r = 0; r < 4; ++r) {
            const float mx = row_max(fmaxf(sa[r], sb[r])), dm = mx <= 16.f ? 0.f : mx;
            nref[r] -= dm; sa[r] -= dm; sb[r] -= dm;
          }
          thr = __builtin_nanf("");
          word |= 1u;
          if (n == 0) *(LAS f32x4*)(lds + LDS_PK + pk * PK_B + 1280 + 16 * g) = -nref;
        }
#pragma unroll
        for (int r = 0; r < 4; ++r) { sa[r] = __builtin_amdgcn_exp2f(sa[r]); sb[r] = __builtin_amdgcn_exp2f(sb[r]); }
        float ra = R[0], rb = R[1];
        asm volatile("" : "+v"(ra), "+v"(rb));
        sa *= ra; sb *= rb;
        *(LAS u32x2*)pw = u32x2{cvtpk(sa[0], sa[1]), cvtpk(sa[2], sa[3])};
        *(LAS u32x2*)(pw + 128) = u32x2{cvtpk(sb[0], sb[1]), cvtpk(sb[2], sb[3])};
        lds_put(CT_PRDY, pfl, word);
        SB();
      }
      GET_TAG();
      LAS char* fin = lds + xslot + 8192;
      unsigned qres = (unsigned)__builtin_amdgcn_raw_buffer_load_b16(mkrs(q, NQ * C * 2), dout * 2, 0, 0) << 16;
      unsigned ao = ((tid & 255) >> 1) * 8192 + (tid & 1) * 16 + dst * 32, tw = (unsigned)(tg >> (20 * (tid & 1))), sh = 12 * (tid & 1), oo = dout * 2;
      LAS char *fw = fin + (((lane & 8) | ((g & 1) << 2) | (g & 2)) + (lane & 1)) * 16 + 4 * (w - 4), *fr = fin + (2 * (lane & 7) + ((lane >> 3) & 1)) * 16;
      float v7 = 0.f;
      unsigned fi_ = 8u;
      const unsigned t0s = lds_get(ctl + CT_T0);
      unsigned gate = (t0s ? t0s : (unsigned)tstart) + RGATE;
      u32x4 ro = mkrs4(out, NQ * C * 2), rm4 = mkrs4(ws, WS_BYTES);
      unsigned half = 128 * 8192;
      unsigned long long wm = 0x0303030303030303ull;
      asm volatile("" : "+v"(ao), "+v"(tw), "+v"(sh), "+v"(oo), "+v"(fw), "+v"(fr), "+v"(fin), "+v"(v7), "+v"(qres), "+v"(fi_), "+s"(ro), "+s"(wm), "+s"(rm4), "+s"(half));
      if (w == 4) asm volatile("s_mov_b64 exec, 0x1515\n\tbuffer_store_dword %0, %1, %2, 0 offen sc1\n\ts_mov_b64 exec, -1" :: "v"(qres), "v"(oo), "s"(ro) : "memory");
      lds_wait(ctl + CT_XD, 1);
      lds_spin(CT_ST, ctl, 4);
      gate += max((int)((unsigned)wall_clock64() + OWNG - gate), 0);
      float org = *(LAS const float*)(lds + xslot + 6144 + 4 * dq);
      *(LAS float*)(fin + 256 + 4 * (w - 4)) = org;
#pragma unroll 1
      for (int spin = 0; spin < 1024; ++spin)
        if ((int)((unsigned)wall_clock64() - gate) >= 0) break;
      u32x4 A, B;
      asm volatile("buffer_load_dwordx4 %0, %2, %3, 0 offen sc1\n\tbuffer_load_dwordx4 %1, %2, %3, %4 offen sc1\n\ts_waitcnt vmcnt(0)"
                   : "=&v"(A), "=&v"(B) : "v"(ao), "s"(rm4), "s"(half) : "memory");
      if (__builtin_expect(__builtin_amdgcn_ballot_w64((((A[3] ^ tw) | (B[3] ^ tw)) & 0xfffffu) != 0u) != 0ull, 1)) {
#pragma unroll 1
        for (int spin = 0; spin < (1 << 18); ++spin) {
          unsigned xa_ = (A[3] ^ tw) << 12, xb_ = (B[3] ^ tw) << 12;
          if (__builtin_amdgcn_ballot_w64((xa_ | xb_) != 0u) == 0ull) break;
          xa_ |= __builtin_amdgcn_mov_dpp(xa_, 0xB1, 0xf, 0xf, true); xb_ |= __builtin_amdgcn_mov_dpp(xb_, 0xB1, 0xf, 0xf, true);
          asm volatile("" ::: "memory");
          if (xa_ != 0u) A = ldb128<16>(rws, ao, 0);
          if (xb_ != 0u) B = ldb128<16>(rws, ao, 128 * 8192);
        }
      }
      unsigned la = (A[3] & 0xfff00000u) >> sh, lb_ = (B[3] & 0xfff00000u) >> sh;
      la |= __builtin_amdgcn_mov_dpp(la, 0xB1, 0xf, 0xf, true); lb_ |= __builtin_amdgcn_mov_dpp(lb_, 0xB1, 0xf, 0xf, true);
      float da = __uint_as_float(la) - org, db = __uint_as_float(lb_) - org;
      f32x2 ua_[3], ub_[3];
#pragma unroll
      for (int i = 0; i < 3; ++i) { ua_[i] = f32x2{bflo(A[i]), bfhi(A[i])}; ub_[i] = f32x2{bflo(B[i]), bfhi(B[i])}; asm volatile("" : "+v"(ua_[i]), "+v"(ub_[i])); }
      if (__builtin_expect(__builtin_amdgcn_ballot_w64(!(fmaxf(da, db) <= 64.f)) != 0ull, 0)) {
        org = xrow16_max(row_max(fmaxf(__uint_as_float(la), __uint_as_float(lb_))));
        da = __uint_as_float(la) - org; db = __uint_as_float(lb_) - org;
        *(LAS float*)(fin + 256 + 4 * (w - 4)) = org;
        fi_ = 9u;
      }
      const float wa = __builtin_amdgcn_exp2f(da), wb = __builtin_amdgcn_exp2f(db);
#pragma unroll
      for (int i = 0; i < 3; ++i) ua_[i] = ua_[i] * wa + ub_[i] * wb;
      const f32x2 p0 = swap32(ua_[0][0], ua_[0][1]), p1 = swap32(ua_[1][0], ua_[1][1]), p2 = swap32(ua_[2][0], ua_[2][1]), p3 = swap32(wa + wb, v7);
      const f32x2 r0 = swap16(p0[0] + p0[1], p1[0] + p1[1]), r1 = swap16(p2[0] + p2[1], p3[0] + p3[1]);
      float c0 = r0[0] + r0[1], c1 = r1[0] + r1[1];
      asm volatile("s_nop 1\n\tv_add_f32_dpp %0, %0, %0 row_ror:8 row_mask:0xf bank_mask:0x3\n\tv_add_f32_dpp %0, %1, %1 row_ror:8 row_mask:0xf bank_mask:0xc\n\t"
                   "s_nop 1\n\tv_add_f32_dpp %0, %0, %0 row_shl:4 row_mask:0xf bank_mask:0x5\n\t"
                   "s_nop 1\n\tv_add_f32_dpp %0, %0, %0 quad_perm:[2,3,0,1] row_mask:0xf bank_mask:0x5\n\t"
                   "s_mov_b64 exec, %3\n\tds_write_b32 %2, %0\n\ts_mov_b64 exec, 1\n\tds_add_u32 %4, %5 offset:%6\n\ts_mov_b64 exec, -1"
                   : "+v"(c0) : "v"(c1), "v"(fw), "s"(wm), "v"(ctl), "v"(fi_), "i"(CT_FIN) : "memory");
      if (w == 4) {
        f32x4 a0, l0;
        unsigned cf;
        int cnt_, c_;
        asm volatile("s_mov_b32 %1, 0x4000000\n1:\n\tds_read_b32 %2, %5 offset:%8\n\tds_read_b128 %3, %6\n\tds_read_b128 %4, %7 offset:192\n\t"
                     "s_waitcnt lgkmcnt(2)\n\tv_readfirstlane_b32 %0, %2\n\ts_cmp_ge_i32 %0, 32\n\ts_cbranch_scc1 2f\n\t"
                     "s_sub_u32 %1, %1, 1\n\ts_cbranch_scc0 1b\n2:\n\ts_waitcnt lgkmcnt(0)"
                     : "=&s"(c_), "=&s"(cnt_), "=&v"(cf), "=&v"(a0), "=&v"(l0) : "v"(ctl), "v"(fr), "v"(fin), "i"(CT_FIN) : "memory", "scc");
        asm volatile("" : "+s"(c_));
        if (__builtin_expect(c_ != 32, 0)) {
          f32x4 m0 = *(LAS const f32x4*)(fin + 256);
          const float mm = fmaxf(fmaxf(m0[0], m0[1]), fmaxf(m0[2], m0[3]));
#pragma unroll
          for (int p = 0; p < 4; ++p) m0[p] = __builtin_amdgcn_exp2f(m0[p] - mm);
          a0 *= m0; l0 *= m0;
        }
        const float o = __uint_as_float(qres) + ((a0[0] + a0[2]) + (a0[1] + a0[3])) * __builtin_amdgcn_rcpf((l0[0] + l0[2]) + (l0[1] + l0[3]));
        const unsigned ob = cvtpk(o, __uint_as_float(__builtin_amdgcn_mov_dpp(__float_as_uint(o), 0xB1, 0xf, 0xf, true)));
        asm volatile("s_mov_b64 exec, 0x1515\n\tbuffer_store_dword %0, %1, %2, 0 offen sc1\n\ts_mov_b64 exec, -1" :: "v"(ob), "v"(oo), "s"(ro) : "memory");
        if (wg == 0 && lane == 0) stb64(u32x2{(unsigned)tg, (unsigned)(tg >> 32)}, rws, EP_OFF);
      }
    } else {
      const int ftr = ((g & 1) << 2) | ((tq >> 1) << 1) | (g >> 1);
      const int fty = (g & 1) * 4 + (tq >> 1) * 3;
      int otr[4], oty[4];
#pragma unroll
      for (int t = 0; t < 4; ++t) {
        otr[t] = (w - 1) * PART + 128 * (4 * g + tq) + 16 * ((2 * t + (tp >> 1)) ^ ftr) + 8 * (tp & 1);
        oty[t] = CRS + (w - 1) * 128 + 384 * (4 * (g & 1) + tq) + 16 * ((2 * t + (tp >> 1)) ^ fty) + 8 * (tp & 1);
      }
      int opk = LDS_PK + 320 * tp + 8 * (4 * g + tq), ork = LDS_PK + 320 * (g >> 1) + 256 + 8 * (4 * (g & 1) + tq);
      asm volatile("" : "+v"(opk), "+v"(ork), "+v"(oty[0]), "+v"(oty[1]), "+v"(oty[2]), "+v"(oty[3]));
#define RD_T(ta, ty, so_) _Pragma("unroll") for (int t_ = 0; t_ < 4; ++t_) { \
    ta[t_] = vtr2(lds + otr[t_] + (so_), lds + otr[t_] + (so_) + 2048); ty[t_] = vtr(lds + oty[t_] + (so_)); }
      f32x4 acc[4] = {z, z, z, z}, tac = z;
#define RD_P(o_) \
      pf = vtr2(lds + opk + (o_), lds + opk + (o_) + 128); \
      sf = vtr2(lds + ork + (o_), lds + ork + (o_) + 640)
#define TWICE(ty) asm volatile("s_nop 2"); _Pragma("unroll") for (int t = 0; t < 4; ++t) { u32x2 lo_, hi_; \
      asm volatile("v_mov_b64 %0, %2\n\tv_mov_b64 %1, %2" : "=&v"(lo_), "=&v"(hi_) : "v"(ty[t])); t2[t] = u32x4{lo_[0], lo_[1], hi_[0], hi_[1]}; } \
      asm volatile("s_nop 1" : "+v"(t2[0]), "+v"(t2[1]), "+v"(t2[2]), "+v"(t2[3]))
#define MF_PX(ta) \
      tac = mfma(sf, pf, tac); \
      _Pragma("unroll") for (int t = 0; t < 4; ++t) acc[t] = mfma(ta[t], pf, acc[t])
#define MF_PY() \
      _Pragma("unroll") for (int t = 0; t < 4; ++t) acc[t] = mfma(t2[t], pf, acc[t])
#define MOVE_REF(word_, o_) do { moved |= (word_) & 1; if (__builtin_expect(moved, 0)) { \
            const float m_ = *(LAS const float*)(lds + (((word_) & 1) ? LDS_PK + (o_) + 1280 : LDS_CTL + CT_MAX) + 4 * n); \
            Mtop = fmaxf(Mtop, m_); \
            const bool drop = m_ < Mtop - 64.f; \
            const float mn = drop ? M : m_, fa = __builtin_amdgcn_exp2f(M - mn); \
            M = mn; \
            _Pragma("unroll") for (int t = 0; t < 4; ++t) acc[t] *= fa; \
            tac *= fa; \
            if (drop) pf = u32x4{0u, 0u, 0u, 0u}; \
          } } while (0)
      const unsigned flo = FLAG_OFF + (wg & 7) * 768 + (w - 1) * 144 + (lane >= 16 ? min(lane - 16, 35) : (lane >> 3) * 6 + (lane & 7) + ((lane & 7) < 6 ? 0 : 18)) * 8;
      float M, Mtop;
      int moved = 0;
      u32x4 wvv[18], pf, sf, t2[4];
      {
        u32x4 ta[8][4], xx[6];
        u32x2 ty[8][4], fl;
        unsigned cc[6];
        const int qo = QK_OFF + (wg & 3) * QK_COPY + (2 * w - 2) * QK_REC;
#define QK_LOAD() _Pragma("unroll") for (int j = 0; j < 6; ++j) { \
          if (j == 2 || j == 4) __builtin_amdgcn_s_sleep(QSJ); \
          xx[j] = ldb128<16>(rws, lane * 16, qo + ((j >> 1) * 6 + (j & 1)) * QK_REC); \
          cc[j] = __builtin_amdgcn_raw_buffer_load_b32(rws, lane * 4, qo + ((j >> 1) * 6 + (j & 1)) * QK_REC + 1024, 16); }
#pragma unroll
        for (int u = 0; u < 8; ++u) {
          if (u == QKAT) {
            GET_TAG();
#pragma unroll 1
            for (int r = 0; r < 8; ++r) {
#pragma unroll 1
              for (int spin = 0; spin < 128; ++spin) {
                if ((int)(wall_clock64() - tstart) >= GATE + r * HSTEP + (w - 1) * QSW) break;
                __builtin_amdgcn_s_sleep(1);
              }
              u32x8 hv;
              asm volatile("s_load_dwordx8 %0, %1, %2\n\ts_waitcnt lgkmcnt(0)" : "=&s"(hv) : "s"(ws), "s"(HINT_OFF + ((wg & 7) + 8 * r) * 128 + (w - 1) * 32) : "memory");
              const unsigned t_ = (unsigned)tg;
              if ((((hv[0] ^ t_) | (hv[1] ^ t_) | (hv[2] ^ t_)) | ((hv[3] ^ t_) | (hv[4] ^ t_) | (hv[5] ^ t_))) == 0u) break;
            }
            if (wg < NPR) lds_wait(ctl + CT_PD, 4);
            QK_LOAD();
#pragma unroll 1
            for (int spin = 0; spin < (1 << 18); ++spin) {
              unsigned df = 0u;
#pragma unroll
              for (int j = 0; j < 6; ++j) df |= qk_check(xx[j], (unsigned)tg) ^ cc[j];
              if (__all(df == 0u)) break;
              asm volatile("" ::: "memory");
              QK_LOAD();
            }
#pragma unroll
            for (int h = 0; h < 2; ++h) {
              u32x4 sx;
#pragma unroll
              for (int i = 0; i < 4; ++i) sx[i] = add3(xx[h][i], xx[2 + h][i], xx[4 + h][i]);
              *(LAS u32x4*)(lds + LDS_QK + (2 * w - 2 + h) * 1024 + lane * 16) = sx;
            }
            lds_inc(CT_QK, ctl);
            SB();
          }
          lds_wait(ctl + CT_FULL, 3 * u + w);
          RD_T(ta[u], ty[u], u * SLOT);
          lds_inc(CT_FREE + 4 * u, ctl);
          SB();
        }
        fl = __builtin_bit_cast(u32x2, __builtin_amdgcn_raw_buffer_load_b64(rws, flo, 0, 16));
#pragma unroll
        for (int u = 0; u < 12; ++u) {
          const int word = lds_spin(CT_PRDY + 4 * (u % NPK), ctl, 2 * u + 2);
          if (u == 0) Mtop = M = *(LAS const float*)(ctl + CT_MAX + 4 * n);
          RD_P((u % NPK) * PK_B);
          if (u >= 8) MOVE_REF(word, (u % NPK) * PK_B);
          lds_inc(CT_PCON + 4 * (u % NPK), ctl);
          SB();
          MF_PX(ta[u & 7]);
          SB();
          TWICE(ty[u & 7]);
          MF_PY();
          asm volatile("" : "+v"(acc[0]), "+v"(acc[1]), "+v"(acc[2]), "+v"(acc[3]), "+v"(tac));
          SB();
#define FLAGS_UP() __all(fl[0] == (unsigned)tg && (fl[1] & 255u) == (unsigned)(tg >> 32))
          if (u == T0AT && w == 1 && FLAGS_UP()) {
            int d = (int)(fl[1] - ((unsigned)tstart << 8)) >> 8;
            d += __builtin_amdgcn_mov_dpp(d, XOR8, 0xf, 0xf, true); d += __builtin_amdgcn_mov_dpp(d, ROR4, 0xf, 0xf, true);
            d += __builtin_amdgcn_mov_dpp(d, 0x122, 0xf, 0xf, true); d += __builtin_amdgcn_mov_dpp(d, 0x121, 0xf, 0xf, true);
            lds_put(CT_T0, ctl, ((unsigned)tstart + min(max(d >> 4, -300), 300)) | 1u);
          }
          if (u < 4) {
            NEED(3 * (u + 8) + w);
            RD_T(ta[u], ty[u], ((u + 8) % NS) * SLOT);
            lds_inc(CT_FREE + 4 * ((u + 8) % NS), ctl);
            SB();
          }
        }
#pragma unroll 1
        for (int spin = 0; spin < (1 << 18); ++spin) {
          if (FLAGS_UP()) break;
          asm volatile("" ::: "memory");
          fl = __builtin_bit_cast(u32x2, __builtin_amdgcn_raw_buffer_load_b64(rws, flo, 0, 16));
        }
        asm volatile("" ::: "memory");
        if (w == 3) lds_put(CT_WV, ctl, 1);
      }
      int slot = 3, pk = 12 % NPK;
      unsigned rdy = 0u;
      const int kro[3] = {(w - 1) * 2048, (w == 3 ? 0 : w) * 2048, (w == 1 ? 2 : w - 2) * 2048};
#define RD_PACKET(z_) do { int cnt_; u32x2 p0_, p1_, p2_, p3_; \
        word = __builtin_amdgcn_readfirstlane(rdy); \
        asm volatile("s_mov_b32 %1, 0x400000\n1:\n\tds_read_b32 %2, %7 offset:%11\n\t" \
                     "ds_read_b64_tr_b16 %5, %9\n\tds_read_b64_tr_b16 %6, %9 offset:640\n\t" \
                     "ds_read_b64_tr_b16 %3, %8\n\tds_read_b64_tr_b16 %4, %8 offset:128\n\t" \
                     "s_cmp_ge_i32 %0, %10\n\ts_cbranch_scc1 2f\n\ts_waitcnt lgkmcnt(4)\n\tv_readfirstlane_b32 %0, %2\n\t" \
                     "s_cmp_ge_i32 %0, %10\n\ts_cbranch_scc1 2f\n\t" z_ "s_sub_u32 %1, %1, 1\n\ts_cbranch_scc0 1b\n2:" \
                     : "+s"(word), "=&s"(cnt_), "=&v"(rdy), "=&v"(p0_), "=&v"(p1_), "=&v"(p2_), "=&v"(p3_) \
                     : "v"(ctl + 4 * pk), "v"(lds + opk + pk * PK_B), "v"(lds + ork + pk * PK_B), "s"(2 * s + 2), "i"(CT_PRDY), "i"(CALM) : "memory", "scc"); \
        asm volatile("" : "+s"(word)); \
        pf = u32x4{p0_[0], p0_[1], p1_[0], p1_[1]}; sf = u32x4{p2_[0], p2_[1], p3_[0], p3_[1]}; } while (0)
#define GOT_PACKET() do { waitlds<0>(); asm volatile("" : "+v"(pf), "+v"(sf) : "v"(rdy)); MOVE_REF(word, pk * PK_B); } while (0)
#define RD_Y(t_, p_) do { const u32x2 lo_ = vtr(p_); asm volatile("" ::: "memory"); const u32x2 hi_ = vtr(p_); \
        t2[t_] = u32x4{lo_[0], lo_[1], hi_[0], hi_[1]}; } while (0)
      u32x4 ta[4];
      int s = 12, word;
      NEED(36 + w);
#pragma unroll
      for (int t = 0; t < 4; ++t) { ta[t] = vtr2(lds + otr[t] + 2 * SLOT, lds + otr[t] + 2 * SLOT + 2048); RD_Y(t, lds + oty[t] + 2 * SLOT); }
      lds_inc(CT_FREE + 8, ctl);
#pragma unroll 1
      for (; s < NT - 1; ++s) {
        RD_PACKET("s_sleep %12\n\t");
        LAS const char *px[4], *py[4];
#pragma unroll
        for (int t = 0; t < 4; ++t) { px[t] = lds + otr[t] + slot * SLOT; py[t] = lds + oty[t] + slot * SLOT; asm volatile("" : "+v"(px[t]), "+v"(py[t])); }
        NEED(3 * s + 3 + w);
        GOT_PACKET();
        SB();
        tac = mfma(sf, pf, tac);
        asm volatile("" : "+v"(tac));
        SB();
        lds_inc(CT_PCON, ctl + 4 * pk);
        pk = pk == NPK - 1 ? 0 : pk + 1;
#pragma unroll
        for (int t = 0; t < 4; ++t) { acc[t] = mfma(ta[t], pf, acc[t]); SB(); ta[t] = vtr2(px[t], px[t] + 2048); SB(); }
        rdy = *(volatile LAS unsigned*)(ctl + CT_PRDY + 4 * pk);
#pragma unroll
        for (int t = 0; t < 4; ++t) { acc[t] = mfma(t2[t], pf, acc[t]); SB(); RD_Y(t, py[t]); SB(); }
        lds_inc(CT_FREE, ctl + 4 * slot);
#define WV_LOAD(j_) wvv[j_] = ldb128<16>(rws, lane * 16 + ((j_) & 1) * 1024, WVO_OFF + (w - 1) * 18432 + ((j_) / 6) * 6144 + kro[((j_) % 6) >> 1])
        if ((unsigned)(s - WVS) < WV1) {
#pragma unroll
          for (int u = 0; u < WV1; ++u)
            if (s - WVS == u) WV_LOAD(u);
        }
        if (s == NT - 3) {
#pragma unroll
          for (int j = WV1; j < 18; ++j) WV_LOAD(j);
        }
        slot = slot == NS - 1 ? 0 : slot + 1;
      }
      LAS char *xw = lds + xslot + lane * 16 + kro[0], *xl = lds + xslot + 6144 + 4 * n;
      LAS const char *xr1 = lds + xslot + lane * 16 + kro[1], *xr2 = lds + xslot + lane * 16 + kro[2];
      unsigned mo = wg * 8192 + (w - 1) * 2048 + n * 64 + g * 16, tw = (unsigned)(tg >> (20 * (g & 1))) & 0xfffffu, sh = 12 * (g & 1);
      asm volatile("" : "+v"(xw), "+v"(xl), "+v"(xr1), "+v"(xr2), "+v"(mo), "+v"(tw), "+v"(sh));
      __builtin_amdgcn_s_setprio(2);
      RD_PACKET("");
      GOT_PACKET();
      SB();
      tac = mfma(sf, pf, tac);
      asm volatile("" : "+v"(tac));
      SB();
#pragma unroll
      for (int t = 0; t < 2; ++t) acc[t] = mfma(t2[t], pf, mfma(ta[t], pf, acc[t]));
      SB();
#pragma unroll
      for (int t = 2; t < 4; ++t) acc[t] = mfma(t2[t], pf, mfma(ta[t], pf, acc[t]));
      const float L = tac[2] + tac[3], rl = __builtin_amdgcn_rcpf(L), ct = -(tac[0] + tac[1]) * rl;
      const float lse = M + __builtin_amdgcn_logf(L);
      u32x4 b[6];
      b[0] = pack8(acc[0] * rl + ct, acc[1] * rl + ct);
      *(LAS u32x4*)xw = b[0];
      *(LAS float*)xl = lse;
#pragma unroll
      for (int j = 0; j < 4; ++j) { __builtin_amdgcn_sched_group_barrier(0x008, 1, 0); __builtin_amdgcn_sched_group_barrier(0x002, 3, 0); }
      SB();
      b[1] = pack8(acc[2] * rl + ct, acc[3] * rl + ct);
      *(LAS u32x4*)(xw + 1024) = b[1];
      lds_inc(CT_XD, ctl);
      SB();
      f32x4 o[3];
      unsigned xd;
#pragma unroll
      for (int t = 0; t < 3; ++t) o[t] = mfma(wvv[6 * t], b[0], z);
      asm volatile("" : "+v"(o[0]), "+v"(o[1]), "+v"(o[2]));
      SB();
#define RD_SLICES "ds_read_b32 %0, %7 offset:%10\n\tds_read_b128 %1, %8\n\tds_read_b128 %2, %8 offset:1024\n\tds_read_b128 %3, %9\n\tds_read_b128 %4, %9 offset:1024\n"
      asm volatile(RD_SLICES : "=&v"(xd), "=&v"(b[2]), "=&v"(b[3]), "=&v"(b[4]), "=&v"(b[5]) : "i"(0), "i"(0), "v"(ctl), "v"(xr1), "v"(xr2), "i"(CT_XD) : "memory");
      SB();
#pragma unroll
      for (int t = 0; t < 3; ++t) o[t] = mfma(wvv[6 * t + 1], b[1], o[t]);
      tw |= (__float_as_uint(lse) << sh) & 0xfff00000u;
      asm volatile("" : "+v"(tw), "+v"(o[0]), "+v"(o[1]), "+v"(o[2]));
      SB();
      {
        int cnt_, c_;
        asm volatile("s_mov_b32 %6, 0x400000\n\ts_branch 2f\n1:\n\t" RD_SLICES "2:\n\ts_waitcnt lgkmcnt(4)\n\tv_readfirstlane_b32 %5, %0\n\t"
                     "s_cmp_ge_i32 %5, 3\n\ts_cbranch_scc1 3f\n\ts_sub_u32 %6, %6, 1\n\ts_cbranch_scc0 1b\n3:\n\ts_waitcnt lgkmcnt(0)"
                     : "+v"(xd), "+v"(b[2]), "+v"(b[3]), "+v"(b[4]), "+v"(b[5]), "=&s"(c_), "=&s"(cnt_)
                     : "v"(ctl), "v"(xr1), "v"(xr2), "i"(CT_XD) : "memory", "scc");
      }
      SB();
      PROJECT(2);
      lds_inc(CT_ST, ctl);
    }
  }
}

#undef rws
#define WS_AT(size_) ((size_) > (64u << 20) ? ((size_) - (64u << 20)) & ~(size_t)0x1fffff : 0)
extern "C" void kernel_launch(void* const* d_in, const int* in_sizes, int n_in, void* d_out, int out_size, void* d_ws, size_t ws_size,
                              hipStream_t stream) {
  dino_main<<<NWG, 512, 0, stream>>>((const u16*)d_in[0], (const u16*)d_in[1], (const u16*)d_in[2], (const u16*)d_in[3], (const u16*)d_in[4],
                                     (const u16*)d_in[5], (const u16*)d_in[6], (char*)d_ws + WS_AT(ws_size) + 4096, (u16*)d_out);
}
```

```cpp
#include <hip/hip_runtime.h>
#include <hip/hip_bf16.h>
#include <stdint.h>

typedef unsigned short u16;
using u32x2 = __attribute__((ext_vector_type(2))) unsigned;
using u32x4 = __attribute__((ext_vector_type(4))) unsigned;
using u32x8 = __attribute__((ext_vector_type(8))) unsigned;
using f32x2 = __attribute__((ext_vector_type(2))) float;
using f32x4 = __attribute__((ext_vector_type(4))) float;
using s16x8 = __attribute__((ext_vector_type(8))) short;
using bf16x2_t = __attribute__((ext_vector_type(2))) __bf16;
typedef short v4i16_t __attribute__((ext_vector_type(4)));
#define LAS __attribute__((address_space(3)))

constexpr int C = 192, NQ = 16, W0 = 512, W1 = 256, NWG = 256;
constexpr float EPS = 1e-5f;
constexpr float QSCALE = 0.07216878364870322f * 1.4426950408889634f;

constexpr int PART = 4096, CRS = 3 * PART;
constexpr int SLOT = CRS + 3072, NS = 10;
constexpr int LDS_QK = NS * SLOT;
constexpr int NPK = 7;
constexpr int LDS_PK = LDS_QK, PK_B = 1344;
constexpr int LDS_CTL = LDS_PK + NPK * PK_B;
constexpr int LDS_SINK = LDS_CTL + 384;
constexpr int LDS_BYTES = LDS_SINK + 256;
constexpr int PSCR = 8 * SLOT;
constexpr int CT_FULL = 8, CT_WV = 4, CT_PA = 0, CT_PB = 12, CT_QR = 16, CT_PD = 20, CT_T0 = 24, CT_XD = 28, CT_TG = 32, CT_QK = 40, CT_ST = 44,
              CT_FREE = 48, CT_FIN = 88, CT_ONE = 112, CT_PRDY = 128, CT_PCON = 160, CT_MAX = 256;

constexpr int MB_BYTES = NWG * NWG * 32;
constexpr int EP_OFF = MB_BYTES;
constexpr int FLAG_OFF = EP_OFF + 512;
constexpr int WVO_OFF = EP_OFF + 8192;
constexpr int QK_OFF = WVO_OFF + C * C * 2;
constexpr int QK_REC = 1280, QK_COPY = 18 * QK_REC;
constexpr int HINT_OFF = QK_OFF + 4 * QK_COPY;
constexpr int WS_BYTES = HINT_OFF + 64 * 128;

#define GIVE 2
constexpr int NPR = 36;
struct Tabs { unsigned seq[112]; };
constexpr Tabs make_tabs() {
  Tabs t{};
  for (int i = 0; i < 112; ++i) t.seq[i] = i + 1;
  return t;
}
__device__ const Tabs tabs = make_tabs();
#define QKAT 3
#define WV1 6
#define WMIN 28
#define WSTEP 5
#define LSTEP 40
#define OWNG 30
#define PACE0 30
#define PACE1 1730
#define RGATE 1911
#define T0AT 3
#define WVS 12
#define CALM 2
#define QSW 12
#define QSJ 12
#define THR 5
#define QLATE 450
#define HSTEP 40
#define GATE 350
#define CODE_BYTES 26112

__device__ __forceinline__ unsigned cvtpk(float lo, float hi) {
  bf16x2_t v = {(__bf16)lo, (__bf16)hi};
  return __builtin_bit_cast(unsigned, v);
}
__device__ __forceinline__ float bflo(unsigned x) { return __builtin_bit_cast(float, x << 16); }
__device__ __forceinline__ float bfhi(unsigned x) { return __builtin_bit_cast(float, x & 0xffff0000u); }
template <int CTRL> __device__ __forceinline__ float dpp(float x) {
  return __builtin_bit_cast(float, __builtin_amdgcn_mov_dpp(__builtin_bit_cast(int, x), CTRL, 0xf, 0xf, true));
}
constexpr int XOR8 = 0x128, ROR4 = 0x124;
template <bool HI8> __device__ __forceinline__ float half_copy(float x) {
  const int v = __builtin_bit_cast(int, x);
  return __builtin_bit_cast(float, __builtin_amdgcn_update_dpp(v, v, XOR8, 0xf, HI8 ? 0xc : 0x3, false));
}
__device__ __forceinline__ float row_max(float x) {
  x = fmaxf(x, dpp<XOR8>(x)); x = fmaxf(x, dpp<ROR4>(x)); x = fmaxf(x, dpp<0x122>(x));
  return fmaxf(x, dpp<0x121>(x));
}
__device__ __forceinline__ f32x2 swap32(float a, float b) {
  auto s = __builtin_amdgcn_permlane32_swap(__float_as_uint(a), __float_as_uint(b), false, false);
  return f32x2{__uint_as_float(s[0]), __uint_as_float(s[1])};
}
__device__ __forceinline__ f32x2 swap16(float a, float b) {
  auto s = __builtin_amdgcn_permlane16_swap(__float_as_uint(a), __float_as_uint(b), false, false);
  return f32x2{__uint_as_float(s[0]), __uint_as_float(s[1])};
}
__device__ __forceinline__ float xrow16_max(float x) {
  auto s = __builtin_amdgcn_permlane16_swap(__float_as_uint(x), __float_as_uint(x), false, false);
  x = fmaxf(__uint_as_float(s[0]), __uint_as_float(s[1]));
  auto t = __builtin_amdgcn_permlane32_swap(__float_as_uint(x), __float_as_uint(x), false, false);
  return fmaxf(__uint_as_float(t[0]), __uint_as_float(t[1]));
}
__device__ __forceinline__ float xrow16_sum(float x) {
  auto s = __builtin_amdgcn_permlane16_swap(__float_as_uint(x), __float_as_uint(x), false, false);
  x = __uint_as_float(s[0]) + __uint_as_float(s[1]);
  auto t = __builtin_amdgcn_permlane32_swap(__float_as_uint(x), __float_as_uint(x), false, false);
  return __uint_as_float(t[0]) + __uint_as_float(t[1]);
}
__device__ __forceinline__ f32x4 mfma(u32x4 a, u32x4 b, f32x4 c) {
  return __builtin_amdgcn_mfma_f32_16x16x32_bf16(__builtin_bit_cast(s16x8, a), __builtin_bit_cast(s16x8, b), c, 0, 0, 0);
}
__device__ __forceinline__ u32x2 vtr(LAS const char* p) {
  return __builtin_bit_cast(u32x2, __builtin_amdgcn_ds_read_tr16_b64_v4i16((LAS v4i16_t*)p));
}
__device__ __forceinline__ u32x4 vtr2(LAS const char* p, LAS const char* q_) {
  const u32x2 a = vtr(p), b = vtr(q_);
  return u32x4{a[0], a[1], b[0], b[1]};
}
__device__ __forceinline__ float sel4(f32x4 v, int r) {
  const float a = (r & 1) ? v[1] : v[0], b = (r & 1) ? v[3] : v[2];
  return (r & 2) ? b : a;
}
using RSRC = __amdgpu_buffer_rsrc_t;
__device__ __forceinline__ RSRC mkrs(const void* p, int bytes) { return __builtin_amdgcn_make_buffer_rsrc(const_cast<void*>(p), (short)0, bytes, 0x00020000); }
__device__ __forceinline__ u32x4 mkrs4(const void* p, unsigned bytes) {
  const unsigned long long b = (unsigned long long)p;
  return u32x4{(unsigned)b, (unsigned)(b >> 32) & 0xffffu, bytes, 0x00020000u};
}
template <int AUX> __device__ __forceinline__ u32x4 ldb128(RSRC r, unsigned vo, int so) { return __builtin_bit_cast(u32x4, __builtin_amdgcn_raw_buffer_load_b128(r, vo, so, AUX)); }
#define SB() __builtin_amdgcn_sched_barrier(0)
#define RAW_BARRIER() asm volatile("s_waitcnt lgkmcnt(0)\n\ts_barrier" ::: "memory")
#define DMA(m0_, vo_, rs_, so_) asm volatile("s_mov_b32 m0, %0\n\ts_nop 0\n\tbuffer_load_dwordx4 %1, %2, %3 offen lds" :: "s"(m0_), "v"(vo_), "s"(rs_), "s"(so_) : "memory")
template <int N> __device__ __forceinline__ void waitlds() { __builtin_amdgcn_s_waitcnt(0xc07f | (N << 8)); }
template <int N> __device__ __forceinline__ void waitvm() { asm volatile("s_waitcnt vmcnt(%0)" :: "i"(N) : "memory"); }


__device__ __forceinline__ void lds_inc(int O, LAS char* p) {
  asm volatile("s_mov_b64 exec, 1\n\tds_add_u32 %0, %1 offset:%2\n\ts_mov_b64 exec, -1" :: "v"(p), "v"(1u), "i"(O) : "memory");
}
__device__ __forceinline__ void lds_put(int O, LAS char* p, unsigned v) {
  asm volatile("s_mov_b64 exec, 1\n\tds_write_b32 %0, %1 offset:%2\n\ts_mov_b64 exec, -1" :: "v"(p), "v"(v), "i"(O) : "memory");
}
__device__ __forceinline__ int lds_get(LAS char* p) {
  asm volatile("" ::: "memory");
  const unsigned v = __hip_atomic_load((LAS unsigned*)p, __ATOMIC_RELAXED, __HIP_MEMORY_SCOPE_WORKGROUP);
  asm volatile("" ::: "memory");
  return __builtin_amdgcn_readfirstlane(v);
}
__device__ __forceinline__ void lds_wait(LAS char* p, int target) {
#pragma unroll 1
  for (int spin = 0; spin < (1 << 22); ++spin) {
    if (lds_get(p) >= target) break;
    __builtin_amdgcn_s_sleep(1);
  }
}
__device__ __forceinline__ int lds_spin(int O, LAS char* p, int target) {
  int r, cnt = 1 << 22;
  unsigned t;
  asm volatile("1:\n\tds_read_b32 %1, %3 offset:%5\n\ts_waitcnt lgkmcnt(0)\n\tv_readfirstlane_b32 %0, %1\n\ts_cmp_ge_i32 %0, %4\n\ts_cbranch_scc1 2f\n\t"
               "s_sub_u32 %2, %2, 1\n\ts_cbranch_scc0 1b\n2:"
               : "=&s"(r), "=&v"(t), "+s"(cnt) : "v"(p), "s"(target), "i"(O) : "memory", "scc");
  return r;
}
__device__ __forceinline__ void lds_need(int O, int& have, LAS char* p, int target) {
  int cnt;
  unsigned t;
  asm volatile("s_cmp_ge_i32 %0, %4\n\ts_cbranch_scc1 2f\n\ts_mov_b32 %2, 0x400000\n"
               "1:\n\tds_read_b32 %1, %3 offset:%5\n\ts_waitcnt lgkmcnt(0)\n\tv_readfirstlane_b32 %0, %1\n\ts_cmp_ge_i32 %0, %4\n\ts_cbranch_scc1 2f\n\t"
               "s_sleep %6\n\ts_sub_u32 %2, %2, 1\n\ts_cbranch_scc0 1b\n2:"
               : "+s"(have), "=&v"(t), "=&s"(cnt) : "v"(p), "s"(target), "i"(O), "i"(CALM) : "memory", "scc");
  asm volatile("" : "+s"(have));
}
__device__ __forceinline__ u32x4 pack8(f32x4 a, f32x4 b) { return u32x4{cvtpk(a[0], a[1]), cvtpk(a[2], a[3]), cvtpk(b[0], b[1]), cvtpk(b[2], b[3])}; }

__device__ __forceinline__ void stb128(u32x4 v, RSRC r, unsigned o) { __builtin_amdgcn_raw_buffer_store_b128(v, r, o, 0, 16); }
__device__ __forceinline__ void stb64(u32x2 v, RSRC r, unsigned o) { __builtin_amdgcn_raw_buffer_store_b64(v, r, o, 0, 16); }
__device__ __forceinline__ void stb32(unsigned v, RSRC r, unsigned o) { __builtin_amdgcn_raw_buffer_store_b32(v, r, o, 0, 16); }
__device__ __forceinline__ unsigned qk_check(u32x4 x, unsigned tg) {
  const unsigned a = (x[0] ^ __builtin_amdgcn_alignbit(x[1], x[1], 11)) * 0x9e3779b1u + tg;
  const unsigned b = (x[2] + __builtin_amdgcn_alignbit(x[3], x[3], 19)) * 0x85ebca77u;
  return __builtin_amdgcn_alignbit(a, a, 15) ^ b ^ (x[1] + x[3]);
}
__device__ __forceinline__ unsigned add3(unsigned a, unsigned b, unsigned c) {
  return cvtpk((bflo(a) + bflo(b)) + bflo(c), (bfhi(a) + bfhi(b)) + bfhi(c));
}
__global__ __launch_bounds__(512, 2) void dino_main(const u16* q, const u16* f0, const u16* f1, const u16* wq, const u16* wk, const u16* wv,
                                                     const u16* wo, char* ws, u16* out) {
  __shared__ __attribute__((aligned(128))) char smem[LDS_BYTES];
  LAS char* lds = (LAS char*)smem;
  LAS char* ctl = lds + LDS_CTL;
  const unsigned lb = (unsigned)(unsigned long long)lds;
  const int w = __builtin_amdgcn_readfirstlane(threadIdx.x >> 6);
  unsigned long long pc0;
  asm volatile("s_getpc_b64 %0" : "=s"(pc0));
#define COORDS() \
  int tid = threadIdx.x, wg = blockIdx.x; \
  asm volatile("" : "+v"(tid), "+s"(wg)); \
  __builtin_assume(tid >= 0 && tid < 512); __builtin_assume(wg >= 0 && wg < NWG); \
  const int lane = tid & 63, n = lane & 15, g = lane >> 4;       \
    \
  const int dq = (wg & 7) * 2 + ((wg >> 4) & 1), dst = (wg & ~31) + (wg & 7) * 4 + ((wg >> 3) & 3); \
  const int dj = 6 * ((wg >> 5) & 1) + min(lane & 7, 5); \
  const int dout = dq * C + 48 * (wg >> 6) + 16 * (dj >> 2) + (wg & 8) + ((lane & 8) >> 1) + (dj & 3); \
  const int NT = wg < NPR ? 32 - GIVE : wg >= 40 && !(wg & 1) && (wg >> 3) < 5 + ((wg & 4) ? 8 : 10) * GIVE ? 33 : 32; \
  const int xslot = (NT >= 30 ? NT - 30 : NT - 20) * SLOT;       \
  (void)xslot; (void)n; (void)g; (void)dst; (void)dout; (void)NT
#define ARGS_IN() asm volatile("s_waitcnt lgkmcnt(0)" :: "s"(q), "s"(f0), "s"(f1), "s"(wq), "s"(wk), "s"(wv), "s"(wo), "s"(ws), "s"(out) : "memory")
#define rws mkrs(ws, WS_BYTES)
#define PROJECT(k0_) do { \
      _Pragma("unroll") for (int k = k0_; k < 6; ++k) { o[0] = mfma(wvv[k], b[k], o[0]); o[1] = mfma(wvv[6 + k], b[k], o[1]); } \
      asm volatile("" : "+v"(o[0]), "+v"(o[1])); \
      SB(); \
      _Pragma("unroll") for (int k = k0_; k < 6; ++k) o[2] = mfma(wvv[12 + k], b[k], o[2]); \
      asm volatile("" : "+v"(o[2])); \
      SB(); \
      stb128(u32x4{cvtpk(o[0][0], o[0][1]), cvtpk(o[0][2], o[0][3]), cvtpk(o[1][0], o[1][1]), tw}, rws, mo); \
      SB(); \
      stb128(u32x4{cvtpk(o[1][2], o[1][3]), cvtpk(o[2][0], o[2][1]), cvtpk(o[2][2], o[2][3]), tw}, rws, mo + 1024); \
    } while (0)
  unsigned long long tg;
#define GET_TAG() tg = ((unsigned long long)((unsigned)lds_spin(CT_TG + 4, ctl, 1) & 0xffu) << 32) | (unsigned)lds_get(ctl + CT_TG)
  const unsigned long long tstart = wall_clock64();
  if (w != 0) {
    const unsigned l0 = threadIdx.x & 63;
    if (blockIdx.x < NPR) {
      const unsigned p6 = (blockIdx.x * 43) >> 8, pu = blockIdx.x - 6 * p6;
      unsigned vo = ((l0 * 16) ^ (((l0 * 4) & 0x40) | (l0 & 0x20))) + (l0 >> 3) * 256;
      const bool pv = p6 >= 3, wa = w >= 4, isq = !pv && (w & 6) == 2;
      const unsigned a6 = 6 * w - 24, pj = pv ? p6 - 3 : p6;
      unsigned so = (wa ? a6 : isq ? w - 2 : 4 * pu) * 3072 + (isq ? 0 : pv && !wa ? w - 1 : pj) * 128, ga = isq ? 128 : 3072;
      unsigned lo = lb + PSCR + (wa ? a6 : pv ? 4 * w + 20 : (0x1b181e00u >> (8 * w)) & 255u) * 1024, hi = lo + (wa ? 6144 : isq ? 3072 : 4096);
      asm volatile("" : "+v"(vo), "+s"(lo), "+s"(so), "+s"(hi), "+s"(ga));
      ARGS_IN();
      const u32x4 rw = mkrs4(wa ? (pv ? wo : wq) : pv ? wv : isq ? q : wk, isq ? NQ * C * 2 : C * C * 2);
#pragma unroll 1
      do { DMA(lo, vo, rw, so); lo += 1024; so += ga; } while (lo != hi);
    } else if (w == 7 && blockIdx.x >= 56 && blockIdx.x < 72) {
      const u32x4 rc = mkrs4((const void*)(pc0 & ~255ull), CODE_BYTES);
#pragma unroll 1
      for (int i = 0; i < CODE_BYTES; i += 4096)
        asm volatile("s_mov_b32 m0, %0\n\ts_nop 0\n\tbuffer_load_dword %1, %2, %3 offen lds" :: "s"(lb + LDS_SINK), "v"(l0 * 64), "s"(rc), "s"(i) : "memory");
    }
    if (w == 3) {
      unsigned long long ep;
      asm volatile("s_waitcnt lgkmcnt(0)\n\ts_load_dwordx2 %0, %1, %2\n\ts_barrier\n\ts_waitcnt lgkmcnt(0)" : "=&s"(ep) : "s"(ws), "s"(EP_OFF) : "memory");
      *(LAS unsigned long long*)(ctl + CT_TG) = ((ep + 0x100001ull) & 0xffffffffffull) | 0x10000000000ull;
    } else RAW_BARRIER();
  }
  SB();
  if (__builtin_expect(w == 0, 1)) {
    const unsigned l0 = threadIdx.x & 63, g0 = blockIdx.x;
    unsigned y0 = (l0 * 16) ^ (l0 >= 48 ? 48 : 0), y1 = (l0 * 16) ^ (l0 >= 32 ? 64 : 48), y2 = (l0 * 16) ^ (l0 >= 16 ? 112 : 64);
    unsigned v0 = ((l0 * 16) ^ ((l0 * 2) & 0x60)) + (l0 >> 3) * 640 + 1024, v1 = (v0 ^ 16) - 640;
    unsigned v2 = v0 + W0 * C * 2 - 2048, v3 = v1 + W0 * C * 2 - 2048;
    unsigned b0 = (g0 & 7) * (2 * W0 * C * 2) + (g0 >> 3) * 6144, b1 = (g0 & 7) * (W1 * C * 2) + (g0 >> 3) * 3072;
    {
      const unsigned iv = (l0 & 15) >= CT_ONE / 8 ? 0x3f803f80u : (l0 >> 3) == CT_MAX / 64 ? 0xff800000u : 0u;
      *(LAS u32x2*)(ctl + 8 * min(l0, 47u)) = u32x2{l0 >= 47 ? 0x3f803f80u : iv, l0 >= 47 ? 0x3f803f80u : iv};
    }
    constexpr unsigned st0 = 16 * W0 * C * 2, st1 = 8 * W1 * C * 2;
    unsigned sb = lb, fo = CT_FREE, fv = 0u;
    int lap = 0;
    u32x4 rt = mkrs4(tabs.seq, 112 * 4);
    unsigned vz = 0u;
    asm volatile("" : "+v"(v0), "+v"(v1), "+v"(v2), "+v"(v3), "+v"(y0), "+v"(y1), "+v"(y2), "+v"(vz), "+s"(b0), "+s"(b1), "+s"(rt));
    ARGS_IN();
    const u32x4 r0 = mkrs4((const char*)f0 - 1024, W0 * W0 * C * 2 + 1024), r1 = mkrs4(f1, W1 * W1 * C * 2);
    if (g0 < NPR) __builtin_amdgcn_s_barrier();
#define UNIT(p_) do { unsigned so_; \
      asm volatile("s_add_u32 m0, %1, %10\n\ts_add_u32 %0, %2, %11\n\t" \
                   "buffer_load_dwordx4 %3, %7, %0 offen nt lds\n\tbuffer_load_dwordx4 %4, %7, %0 offen offset:1024 nt lds\n\t" \
                   "buffer_load_dwordx4 %5, %7, %0 offen offset:2048 nt lds\n\tbuffer_load_dwordx4 %6, %7, %0 offen offset:3072 nt lds\n\t" \
                   "s_mov_b32 m0, %12\n\ts_mov_b64 exec, 1\n\tbuffer_load_dword %13, %8, %9 offen offset:%14 lds\n\ts_mov_b64 exec, -1" \
                   : "=&s"(so_) : "s"(sb), "s"(b0), "v"(v0), "v"(v1), "v"(v2), "v"(v3), "s"(r0), "s"(rt), "s"(12 * s), "i"((p_) * PART), "i"((p_) * 128), \
                     "s"(lb + LDS_CTL + CT_FULL - 4 * (p_)), "v"(vz), "i"(4 * (p_)) : "memory", "scc"); } while (0)
#define WINDOW() \
      asm volatile("s_waitcnt vmcnt(%1)\n\ts_cmp_ge_i32 %0, %6\n\ts_cbranch_scc1 1f\n\ts_waitcnt vmcnt(%2)\n\ts_cmp_ge_i32 %0, %7\n\ts_cbranch_scc1 1f\n\t" \
                   "s_waitcnt vmcnt(%3)\n\ts_cmp_ge_i32 %0, %8\n\ts_cbranch_scc1 1f\n\ts_waitcnt vmcnt(%4)\n\ts_cmp_ge_i32 %0, %9\n\ts_cbranch_scc1 1f\n\t" \
                   "s_waitcnt vmcnt(%5)\n1:" :: "s"(late), "i"(WMIN + 4 * WSTEP), "i"(WMIN + 3 * WSTEP), "i"(WMIN + 2 * WSTEP), "i"(WMIN + WSTEP), "i"(WMIN), \
                   "i"(4 * LSTEP * 256), "i"(3 * LSTEP * 256), "i"(2 * LSTEP * 256), "i"(LSTEP * 256) : "memory", "scc")
#define COARSE(sb_) \
      asm volatile("s_add_u32 m0, %0, %6\n\ts_nop 0\n\tbuffer_load_dwordx4 %1, %4, %5 offen nt lds\n\t" \
                   "buffer_load_dwordx4 %2, %4, %5 offen offset:1024 nt lds\n\tbuffer_load_dwordx4 %3, %4, %5 offen offset:2048 nt lds" \
                   :: "s"(sb_), "v"(y0), "v"(y1), "v"(y2), "s"(r1), "s"(b1), "i"(CRS) : "memory", "scc")
    COARSE(sb);
#pragma unroll 1
    for (int s = 0; s < 2; ++s) {
      asm volatile("s_add_u32 %0, %0, %1" : "+s"(b1) : "i"(st1) : "scc");
      UNIT(0); UNIT(1); COARSE(sb + SLOT); UNIT(2);
      sb += SLOT; b0 += st0;
    }
    if (g0 >= NPR) __builtin_amdgcn_s_barrier();
    __builtin_amdgcn_s_setprio(3);
    COORDS();
    fo += 12;
    const int tau = __builtin_amdgcn_readfirstlane((int)((PACE1 - PACE0) * 256.f * __builtin_amdgcn_rcpf((float)NT)));
    unsigned due = PACE0 * 256u + 2 * tau, t0v = 0u;
    unsigned long long now = tstart;
    const unsigned xi = (wg >> 3) - 5, xrow = 8 * (31 - xi % GIVE) + (wg & 7) + (xi / GIVE & 1), xcol = xi / GIVE >> 1;
    const unsigned x0 = xrow * (2 * W0 * C * 2) + xcol * 6144, x1 = xrow * (W1 * C * 2) + xcol * 3072;
#pragma unroll 1
    for (int s = 2; s < NT; ++s) {
      asm volatile("" : "+s"(now));
      const unsigned t0s = __builtin_amdgcn_readfirstlane(t0v), t0 = t0s ? t0s : (unsigned)tstart;
      if ((int)(((unsigned)now - t0) * 256u - due) < 0) {
#pragma unroll 1
        for (int spin = 0; spin < 256; ++spin) {
          if ((int)(((unsigned)wall_clock64() - t0) * 256u - due) >= 0) break;
          __builtin_amdgcn_s_sleep(1);
        }
      }
      fv = *(volatile LAS unsigned*)(ctl + (s == 7 ? CT_PD : fo));
      now = wall_clock64();
      UNIT(0);
      asm volatile("" : "+s"(now));
      const int late = (int)(((unsigned)now - t0) * 256u - due);
      due += tau;
      WINDOW();
      UNIT(1);
      WINDOW();
      const unsigned nsb = fo == CT_FREE ? lb : sb + SLOT;
      const unsigned n0 = s == 31 ? x0 : b0 + st0;
      b1 = s == 31 ? x1 : b1 + st1;
      if (s + 1 < NT) {
        const int need = s == 7 && wg < NPR ? 4 : lap;
        if (__builtin_expect((int)__builtin_amdgcn_readfirstlane(fv) < need, 0)) {
          lds_wait(ctl + (s == 7 ? CT_PD : fo), need);
        }
        COARSE(nsb);
      }
      now = wall_clock64();
      t0v = *(volatile LAS unsigned*)(ctl + CT_T0);
      UNIT(2);
      WINDOW();
      if (fo == CT_FREE + 4 * (NS - 1)) { fo = CT_FREE; lap += 4; } else fo += 4;
      sb = nsb; b0 = n0;
    }
    __builtin_amdgcn_s_setprio(0);
    u32x4 wvv[18], b[6];
    asm volatile("s_mov_b32 m0, %0\n\ts_mov_b64 exec, 1\n\tbuffer_load_dword %1, %2, 0 offen lds\n\ts_mov_b64 exec, -1"
                 :: "s"(lb + LDS_SINK), "v"(vz), "s"(mkrs4(ws - 4096, 4096)) : "memory");
    lds_wait(ctl + CT_WV, 1);
#pragma unroll
    for (int j = 0; j < 18; ++j) wvv[j] = ldb128<16>(rws, lane * 16 + (j & 3) * 1024, WVO_OFF + 54 * 1024 + (j >> 2) * 4096);
    GET_TAG();
    LAS const char *xr = lds + xslot + lane * 16, *xl = lds + xslot + 6144 + 4 * n;
    unsigned mo = wg * 8192 + 6144 + n * 64 + g * 16, tw = (unsigned)(tg >> (20 * (g & 1))) & 0xfffffu, sh = 12 * (g & 1), lse;
    asm volatile("" : "+v"(xr), "+v"(xl), "+v"(mo), "+v"(tw), "+v"(sh));
    __builtin_amdgcn_s_waitcnt(0x0f70);
    lds_spin(CT_PRDY, ctl + 4 * ((NT - 1) % NPK), 2 * NT);
    __builtin_amdgcn_s_sleep(2);
    __builtin_amdgcn_s_setprio(2);
    {
      int cnt_, c_;
      unsigned t_;
      asm volatile("s_mov_b32 %1, 0x400000\n1:\n\tds_read_b32 %2, %10 offset:%13\n\tds_read_b128 %3, %11\n\tds_read_b128 %4, %11 offset:1024\n\t"
                   "ds_read_b128 %5, %11 offset:2048\n\tds_read_b128 %6, %11 offset:3072\n\tds_read_b128 %7, %11 offset:4096\n\t"
                   "ds_read_b128 %8, %11 offset:5120\n\tds_read_b32 %9, %12\n\ts_waitcnt lgkmcnt(7)\n\tv_readfirstlane_b32 %0, %2\n\t"
                   "s_cmp_ge_i32 %0, 3\n\ts_cbranch_scc1 2f\n\ts_sub_u32 %1, %1, 1\n\ts_cbranch_scc0 1b\n2:\n\ts_waitcnt lgkmcnt(5)"
                   : "=&s"(c_), "=&s"(cnt_), "=&v"(t_), "=&v"(b[0]), "=&v"(b[1]), "=&v"(b[2]), "=&v"(b[3]), "=&v"(b[4]), "=&v"(b[5]), "=&v"(lse)
                   : "v"(ctl), "v"(xr), "v"(xl), "i"(CT_XD) : "memory", "scc");
    }
    SB();
    f32x4 o[3] = {{0.f, 0.f, 0.f, 0.f}, {0.f, 0.f, 0.f, 0.f}, {0.f, 0.f, 0.f, 0.f}};
#pragma unroll
    for (int k = 0; k < 2; ++k)
#pragma unroll
      for (int t = 0; t < 3; ++t) o[t] = mfma(wvv[6 * t + k], b[k], o[t]);
    asm volatile("s_waitcnt lgkmcnt(0)" : "+v"(o[0]), "+v"(o[1]), "+v"(o[2]), "+v"(b[2]), "+v"(b[3]), "+v"(b[4]), "+v"(b[5]), "+v"(lse));
    SB();
    tw |= (lse << sh) & 0xfff00000u;
    PROJECT(2);
    lds_inc(CT_ST, ctl);
  } else {
    COORDS();
    const int tq = (lane >> 2) & 3, tp = lane & 3;
    const bool pq = wg < 18;
    const bool pv = wg >= 18 && wg < NPR;
    const int pj = wg / 6, pu = wg % 6;
    if (w >= 4 && ((w + NT) & 3) == 0 && !pq) {
      unsigned k0, k1, k2;
      asm volatile("s_load_dword %0, %3, %5\n\ts_load_dword %1, %3, %6\n\ts_load_dword %2, %4, %7\n\ts_waitcnt lgkmcnt(0)"
                   : "=&s"(k0), "=&s"(k1), "=&s"(k2) : "s"(f0), "s"(f1), "s"((wg >> 3) * 3145728 + 1572860), "s"((wg >> 3) * 3145728 + 3145724),
                     "s"((wg >> 3) * 786432 + 786428) : "memory");
    }
    const u32x4 one = {0x3f803f80u, 0x3f803f80u, 0x3f803f80u, 0x3f803f80u};
    const int fq = ((n & 2) << 1) | ((n & 4) >> 1);
    LAS const char* rq0 = lds + PSCR + 24576 + 3072 * (n >> 3) + 128 * (n & 7) + 16 * (g ^ fq);
    LAS const char* rq1 = lds + PSCR + 24576 + 3072 * (n >> 3) + 128 * (n & 7) + 16 * ((4 + g) ^ fq);
    if (pq && w >= 2) {
      {
        waitvm<0>();
        lds_inc(CT_PA, ctl);
        if (w >= 4) {
          f32x4 sq = {0.f, 0.f, 0.f, 0.f}, rr = sq, cs = sq;
          const int ro = PSCR + 128 * (8 * g + tq) + 16 * (((w & 2) * 2 + tp) ^ ((tq & 2) << 1)) + 8 * (w & 1);
          u32x4 qv[6], af[6];
          lds_spin(CT_PA, ctl, 6);
#pragma unroll
          for (int s = 0; s < 6; ++s) {
            qv[s] = *(LAS const u32x4*)(((s & 1) ? rq1 : rq0) + (s >> 1) * 1024);
            af[s] = vtr2(lds + ro + s * 4096, lds + (ro ^ 32) + s * 4096 + 512);
          }
          SB();
#pragma unroll
          for (int s = 0; s < 6; ++s) { sq = mfma(one, qv[s], sq); rr = mfma(af[s], qv[s], rr); cs = mfma(af[s], one, cs); }
          const f32x4 qq = rr - (sq[0] * (1.f / C)) * cs;
          *(LAS u32x2*)(lds + LDS_QK + 4096 + ((w >> 1) & 1) * 1024 + lane * 16 + 8 * (w & 1)) = u32x2{cvtpk(qq[0], qq[1]), cvtpk(qq[2], qq[3])};
          lds_inc(CT_PB, ctl);
        }
      }
    }
    if (pq && w == 1) {
      {
        waitvm<0>();
        u32x4 kf4[4], qv[6];
        const int ko = LDS_QK + 128 * (((n & 4) << 2) | (n & 11)) + 16 * (g ^ ((n & 2) << 1));
#pragma unroll
        for (int i = 0; i < 4; ++i) kf4[i] = *(LAS const u32x4*)(lds + (ko ^ ((i & 1) * 64 + (i >> 1) * 32)) + (i >> 1) * 512);
        lds_spin(CT_PA, ctl, 6);
#pragma unroll
        for (int s = 0; s < 6; ++s) qv[s] = *(LAS const u32x4*)(((s & 1) ? rq1 : rq0) + (s >> 1) * 1024);
        const f32x4 z = {0.f, 0.f, 0.f, 0.f};
        f32x4 gq = z, sq = z;
#pragma unroll
        for (int s = 0; s < 6; ++s) { gq = mfma(qv[s], qv[s], gq); sq = mfma(one, qv[s], sq); }
        const float mean = sq[0] * (1.f / C);
        const float s2 = xrow16_sum(g == (n >> 2) ? sel4(gq, n & 3) : 0.f);
        float sc = __builtin_amdgcn_rsqf(fmaxf(fmaf(-mean, mean, s2 * (1.f / C)), 0.f) + EPS) * QSCALE;
        asm volatile("" : "+v"(sc));
        GET_TAG();
        lds_spin(CT_PB, ctl, 4);
        const u32x4 q0 = *(LAS const u32x4*)(lds + LDS_QK + 4096 + lane * 16), q1 = *(LAS const u32x4*)(lds + LDS_QK + 5120 + lane * 16);
        const f32x4 d0 = mfma(kf4[1], q1, mfma(kf4[0], q0, z)), d1 = mfma(kf4[3], q1, mfma(kf4[2], q0, z));
        const u32x4 xq = pack8(d0 * sc, d1 * sc);
        const unsigned ck = qk_check(xq, (unsigned)tg);
        asm volatile("" :: "v"(ck));
        {
          const unsigned so = QK_OFF + wg * QK_REC;
#pragma unroll
          for (int cp = 0; cp < 4; ++cp) {
            stb128(xq, rws, lane * 16 + so + cp * QK_COPY);
            stb32(ck, rws, lane * 4 + so + 1024 + cp * QK_COPY);
          }
          stb32((unsigned)tg, rws, HINT_OFF + lane * 128 + (pu >> 1) * 32 + pj * 8 + (pu & 1) * 4);
          lds_put(CT_PD, ctl, 4);
        }
      }
    }
    if (pv) {
      waitvm<0>();
      lds_inc(CT_PA, ctl);
      if (w >= 4) {
        const int ro = PSCR + 128 * (8 * g + tq) + 16 * ((2 * (w - 4) + (tp >> 1)) ^ ((tq & 2) << 1)) + 8 * (tp & 1);
        const int rv = PSCR + 24576 + 128 * n + 16 * (g ^ fq);
        f32x4 d0 = {0.f, 0.f, 0.f, 0.f}, d1 = d0;
        lds_spin(CT_PA, ctl, 7);
#pragma unroll
        for (int s = 0; s < 6; ++s) {
          const u32x4 p = vtr2(lds + ro + s * 4096, lds + (ro ^ 32) + s * 4096 + 512);
          LAS const char* pd = lds + (rv ^ ((s & 1) * 64)) + (s >> 1) * 4096;
          d0 = mfma(*(LAS const u32x4*)pd, p, d0); d1 = mfma(*(LAS const u32x4*)(pd + 2048), p, d1);
        }
        const int fi = (pj - 3) * 24 + (w - 4) * 6 + pu;
        stb128(pack8(d0, d1), rws, lane * 16 + WVO_OFF + fi * 1024);
        lds_inc(CT_PD, ctl);
        GET_TAG();
        waitvm<0>();
        if (lane < 8) stb64(u32x2{(unsigned)tg, (unsigned)(tg >> 32) | (unsigned)tstart << 8}, rws, FLAG_OFF + lane * 768 + fi * 8);
      }
    }
    const f32x4 z = {0.f, 0.f, 0.f, 0.f};
    int full = 0;
#define NEED(u_) lds_need(CT_FULL, full, ctl, (u_))
    if (w >= 4) {
      const int brg = ((g & 1) << 1) | (g >> 1);
      const int fn = (((n >> 1) & 3) << 1) | ((n >> 3) & 1);
      const int fy = ((n >> 2) & 1) * 4 + ((n >> 1) & 1) * 3;
      const int orr0 = 128 * n + 16 * (brg ^ fn), orr1 = 128 * n + 16 * ((4 + brg) ^ fn);
      const int ory0 = n < 8 ? CRS + 384 * n + 16 * (brg ^ fy) : LDS_CTL + CT_ONE, ory1 = n < 8 ? CRS + 384 * n + 16 * ((4 + brg) ^ fy) : LDS_CTL + CT_ONE;
      const int yon = n < 8 ? 1 : 0;
      const int opk = LDS_PK + 320 * g + 8 * n, ork = LDS_PK + 320 * (2 * (g >> 1) + (n >> 3)) + 256 + 8 * (n & 7) + 4 * (g & 1);
      u32x4 qk[6];
      f32x4 ref, qs;
      LAS const char *px0, *px1, *py0, *py1;
#define AT_SLOT(o_) do { px0 = lds + orr0 + (o_); px1 = lds + orr1 + (o_); py0 = lds + ory0 + __mul24(yon, (o_)); py1 = lds + ory1 + __mul24(yon, (o_)); \
      asm volatile("" : "+v"(px0), "+v"(px1), "+v"(py0), "+v"(py1)); } while (0)
#define RD_K(k_, i_) do { LAS const char* p_ = (((k_) & 1) ? px1 : px0) + ((k_) >> 1) * PART; \
    yv[i_] = *(LAS const u32x4*)((((k_) & 1) ? py1 : py0) + ((k_) >> 1) * 128); \
    xa[i_] = *(LAS const u32x4*)(p_); xb[i_] = *(LAS const u32x4*)(p_ + 2048); } while (0)
#define MF_STAT(i_) do { cy = mfma(yv[i_], yv[i_], cy); ca = mfma(yv[i_], xa[i_], ca); cb = mfma(yv[i_], xb[i_], cb); \
    ga = mfma(xa[i_], xa[i_], ga); gb = mfma(xb[i_], xb[i_], gb); } while (0)
#define MF_S(k_, i_) do { sy = mfma(qk[k_], yv[i_], sy); sa = mfma(qk[k_], xa[i_], sa); sb = mfma(qk[k_], xb[i_], sb); } while (0)
#define STATS(st, rec) do { \
      const int r = n & 3; \
      const float yy = half_copy<true>(sel4(cy, r)); \
      const f32x2 Y = swap32(yy, yy), X = swap32(sel4(ca, r), sel4(cb, r)), G = swap32(sel4(ga, r), sel4(gb, r)); \
      const float q1_ = fmaf(2.f, X[0], Y[0]) + (n < 8 ? G[0] : G[1]);       \
      const f32x2 Q = swap16(q1_, q1_); \
      const float mn_ = (X[1] + Y[1]) * (1.f / C); \
      const float var = fmaxf(fmaf(-mn_, mn_, fmaf((n & 4) ? Q[1] : Q[0], 1.f / C, EPS)), EPS), rs_ = __builtin_amdgcn_rsqf(var); \
      const f32x2 R = swap32(rs_, rs_), Mn = swap32(mn_, mn_); \
      st = f32x4{R[0], Mn[0], R[1], Mn[1]}; \
      const float v_ = (g & 1) ? var * rs_ : mn_, h_ = bfhi(__float_as_uint(v_)); \
      rec = cvtpk(h_, v_ - h_); \
    } while (0)
#define LOGITS(sv, st, base_) do { \
      _Pragma("unroll") for (int r = 0; r < 4; ++r) { \
        const float s0 = half_copy<true>(sy[r]); \
        sv[r] = fmaf(fmaf(-st[1], qs[r], sa[r] + s0), st[0], -(base_)[r]); \
        sv[4 + r] = fmaf(fmaf(-st[3], qs[r], sb[r] + s0), st[2], -(base_)[r]); \
      } \
    } while (0)
#define PUBLISH(sv, st, rec, pk_, word_) do { \
      *(LAS unsigned*)(lds + ork + (pk_) * PK_B) = rec; \
      *(LAS u32x2*)(lds + opk + (pk_) * PK_B) = u32x2{cvtpk(sv[0] * st[0], sv[1] * st[0]), cvtpk(sv[2] * st[0], sv[3] * st[0])}; \
      *(LAS u32x2*)(lds + opk + (pk_) * PK_B + 128) = u32x2{cvtpk(sv[4] * st[2], sv[5] * st[2]), cvtpk(sv[6] * st[2], sv[7] * st[2])}; \
      lds_put(CT_PRDY, ctl + 4 * (pk_), (word_)); \
    } while (0)
      int s = (w + NT - 1) & 3, slot = s + 4, pk = s + 4, ncon = 0;
      {
        u32x4 yv[12], xa[12], xb[12];
        f32x4 st0, st1;
        unsigned rec0, rec1;
#pragma unroll
        for (int h = 0; h < 2; ++h) {
          f32x4 cy = z, ga = z, ca = z, gb = z, cb = z;
          AT_SLOT((s + 4 * h) * SLOT);
          lds_wait(ctl + CT_FULL, 3 * (s + 4 * h) + 3);
#pragma unroll
          for (int k = 0; k < 6; ++k) RD_K(k, 6 * h + k);
          lds_inc(CT_FREE + 16 * h, ctl + 4 * s);
          SB();
#pragma unroll
          for (int k = 0; k < 6; ++k) MF_STAT(6 * h + k);
          SB();
          if (h == 0) { STATS(st0, rec0); asm volatile("" : "+v"(st0), "+v"(rec0)); } else { STATS(st1, rec1); asm volatile("" : "+v"(st1), "+v"(rec1)); }
          SB();
        }
        lds_wait(ctl + CT_QK, 3);
#pragma unroll
        for (int k = 0; k < 6; ++k) qk[k] = *(LAS const u32x4*)(lds + LDS_QK + k * 1024 + lane * 16);
        float s0v[8], s1v[8];
        {
          f32x4 sy = z, sa = z, sb = z;
#pragma unroll
          for (int k = 0; k < 6; ++k) MF_S(k, k);
#pragma unroll
          for (int r = 0; r < 4; ++r) qs[r] = half_copy<false>(sy[r]);
          LOGITS(s0v, st0, z);
        }
        {
          f32x4 sy = z, sa = z, sb = z;
#pragma unroll
          for (int k = 0; k < 6; ++k) MF_S(k, 6 + k);
          LOGITS(s1v, st1, z);
        }
#pragma unroll
        for (int r = 0; r < 4; ++r) ref[r] = row_max(fmaxf(fmaxf(s0v[r], s0v[4 + r]), fmaxf(s1v[r], s1v[4 + r])));
        if (n < 4) __builtin_amdgcn_ds_fmaxf((LAS float*)(ctl + CT_MAX + 16 * g + 4 * n), sel4(ref, n), __ATOMIC_RELAXED, __HIP_MEMORY_SCOPE_WORKGROUP, false);
        lds_inc(CT_QR, ctl);
        lds_spin(CT_QR, ctl, 4);
        ref = *(LAS const f32x4*)(ctl + CT_MAX + 16 * g);
#pragma unroll
        for (int j = 0; j < 8; ++j) s0v[j] = __builtin_amdgcn_exp2f(s0v[j] - ref[j & 3]);
        PUBLISH(s0v, st0, rec0, s, 2 * s + 2);
#pragma unroll
        for (int j = 0; j < 8; ++j) s1v[j] = __builtin_amdgcn_exp2f(s1v[j] - ref[j & 3]);
        if (pk >= NPK) { pk -= NPK; ncon = 3; lds_spin(CT_PCON, ctl + 4 * pk, 3); }
        s += 4;
        PUBLISH(s1v, st1, rec1, pk, 2 * s + 2);
      }
      float thr = 16.f;
      f32x4 nref = -ref;
#pragma unroll 1
      for (;;) {
        s += 4; slot += 4; slot -= slot >= NS ? NS : 0; pk += 4;
        if (pk >= NPK) { pk -= NPK; ncon += 3; }
        if (s >= NT) break;
        f32x4 sy = z, cy = z, sa, ga = z, ca = z, sb, gb = z, cb = z;
        u32x4 yv[6], xa[6], xb[6];
        unsigned fl_, word = 2 * s + 2;
        AT_SLOT(slot * SLOT);
        LAS char *pw = lds + opk + pk * PK_B, *prc = lds + ork + pk * PK_B, *pfl = ctl + 4 * pk, *pfr = ctl + 4 * slot;
        asm volatile("" : "+v"(pw), "+v"(prc), "+v"(pfl), "+v"(pfr), "+v"(word));
#define RD_X "ds_read_b32 %2, %7 offset:%11\n\tds_read_b128 %3, %8 offset:%12\n\tds_read_b128 %4, %9 offset:%12\n\t" \
             "ds_read_b128 %5, %8 offset:%13\n\tds_read_b128 %6, %9 offset:%13\n"
#define RD_IN(p_) "v"(ctl), "v"(px0), "v"(px1), "s"(3 * s + (p_) + 1), "i"(CT_FULL), "i"(PART * (p_)), "i"(PART * (p_) + 2048)
#define ISSUE(p_) do { int d0_, d1_; \
        asm volatile(RD_X : "=&s"(d0_), "=&s"(d1_), "=&v"(fl_), "=&v"(xa[2 * (p_)]), "=&v"(xa[2 * (p_) + 1]), "=&v"(xb[2 * (p_)]), "=&v"(xb[2 * (p_) + 1]) \
                     : RD_IN(p_) : "memory"); } while (0)
#define CHECK(p_) do { int cnt_; \
        asm volatile("s_mov_b32 %1, 0x400000\n\ts_branch 2f\n1:\n\t" RD_X "2:\n\ts_waitcnt lgkmcnt(4)\n\tv_readfirstlane_b32 %0, %2\n\t" \
                     "s_cmp_ge_i32 %0, %10\n\ts_cbranch_scc1 3f\n\ts_cmp_ge_i32 %10, %14\n\ts_cbranch_scc1 4f\n\ts_sleep %15\n4:\n\t" \
                     "s_sub_u32 %1, %1, 1\n\ts_cbranch_scc0 1b\n3:\n\ts_waitcnt lgkmcnt(2)" \
                     : "=&s"(full), "=&s"(cnt_), "+v"(fl_), "+v"(xa[2 * (p_)]), "+v"(xa[2 * (p_) + 1]), "+v"(xb[2 * (p_)]), "+v"(xb[2 * (p_) + 1]) \
                     : RD_IN(p_), "s"(3 * NT - 2), "i"(CALM) : "memory", "scc"); \
        asm volatile("" : "+s"(full)); } while (0)
#define GOT_B(p_) do { waitlds<0>(); asm volatile("" : "+v"(xb[2 * (p_)]), "+v"(xb[2 * (p_) + 1])); } while (0)
#define MF_A(k_) do { ca = mfma(yv[k_], xa[k_], ca); ga = mfma(xa[k_], xa[k_], ga); sa = mfma(qk[k_], xa[k_], sa); } while (0)
#define MF_B(k_) do { cb = mfma(yv[k_], xb[k_], cb); gb = mfma(xb[k_], xb[k_], gb); sb = mfma(qk[k_], xb[k_], sb); } while (0)
        const unsigned pcv = *(volatile LAS unsigned*)(pfl + CT_PCON);
        NEED(3 * s);
#pragma unroll
        for (int k = 0; k < 6; ++k) yv[k] = *(LAS const u32x4*)(((k & 1) ? py1 : py0) + (k >> 1) * 128);
        int con = __builtin_amdgcn_readfirstlane(pcv);
        SB();
#pragma unroll
        for (int k = 0; k < 6; ++k) sy = mfma(qk[k], yv[k], sy);
        asm volatile("" : "+v"(sy));
        SB();
        lds_need(CT_PCON, con, pfl, ncon);
        ISSUE(0);
        SB();
#pragma unroll
        for (int k = 0; k < 6; ++k) cy = mfma(yv[k], yv[k], cy);
#pragma unroll
        for (int r = 0; r < 4; ++r) sa[r] = half_copy<true>(sy[r]);
        const float yy = half_copy<true>(sel4(cy, n & 3));
        f32x2 Y = swap32(yy, yy);
        asm volatile("" : "+v"(Y), "+v"(sa));
        SB();
        CHECK(0);
        sb = sa;
        MF_A(0);
        asm volatile("" : "+v"(ca), "+v"(ga), "+v"(sa));
        SB();
        GOT_B(0);
        ISSUE(1);
        SB();
        MF_B(0); MF_A(1); MF_B(1);
        asm volatile("" : "+v"(ca), "+v"(ga), "+v"(sa), "+v"(cb), "+v"(gb), "+v"(sb));
        SB();
        CHECK(1);
        MF_A(2);
        asm volatile("" : "+v"(ca), "+v"(ga), "+v"(sa));
        SB();
        GOT_B(1);
        ISSUE(2);
        SB();
        MF_B(2); MF_A(3); MF_B(3);
        asm volatile("" : "+v"(ca), "+v"(ga), "+v"(sa), "+v"(cb), "+v"(gb), "+v"(sb));
        SB();
        CHECK(2);
        ca = mfma(yv[5], xa[5], mfma(yv[4], xa[4], ca));
        ga = mfma(xa[5], xa[5], mfma(xa[4], xa[4], ga));
        asm volatile("" : "+v"(ca), "+v"(ga));
        SB();
        GOT_B(2);
        cb = mfma(yv[5], xb[5], mfma(yv[4], xb[4], cb));
        asm volatile("" : "+v"(cb));
        SB();
        lds_inc(CT_FREE, pfr);
        gb = mfma(xb[4], xb[4], gb);
        float ua = sel4(ca, n & 3);
        asm volatile("" : "+v"(gb), "+v"(ua));
        SB();
        gb = mfma(xb[5], xb[5], gb);
        float va = sel4(ga, n & 3);
        asm volatile("" : "+v"(gb), "+v"(va));
        SB();
        sa = mfma(qk[4], xa[4], sa);
        float ub = sel4(cb, n & 3);
        asm volatile("" : "+v"(sa), "+v"(ub));
        SB();
        sa = mfma(qk[5], xa[5], sa);
        const f32x2 X = swap32(ua, ub);
        float mn_ = (X[1] + Y[1]) * (1.f / C), q1_ = fmaf(2.f, X[0], Y[0]);
        asm volatile("" : "+v"(sa), "+v"(mn_), "+v"(q1_));
        SB();
        sb = mfma(qk[4], xb[4], sb);
        float vb = sel4(gb, n & 3);
        asm volatile("" : "+v"(sb), "+v"(vb));
        SB();
        sb = mfma(qk[5], xb[5], sb);
        const f32x2 Mn = swap32(mn_, mn_), G = swap32(va, vb);
        q1_ += n < 8 ? G[0] : G[1];
        const f32x2 Q = swap16(q1_, q1_);
        sa -= Mn[0] * qs;
        const float var = fmaxf(fmaf(-mn_, mn_, fmaf((n & 4) ? Q[1] : Q[0], 1.f / C, EPS)), EPS), rs_ = __builtin_amdgcn_rsqf(var);
        sb -= Mn[1] * qs;
        const float v_ = (g & 1) ? var * rs_ : mn_, h_ = bfhi(__float_as_uint(v_));
        *(LAS unsigned*)prc = cvtpk(h_, v_ - h_);
        const f32x2 R = swap32(rs_, rs_);
        sa = sa * R[0] + nref; sb = sb * R[1] + nref;
        const float tm = fmaxf(fmaxf(fmaxf(fmaxf(sa[0], sa[1]), sa[2]), fmaxf(fmaxf(sa[3], sb[0]), sb[1])), fmaxf(sb[2], sb[3]));
        if (__builtin_expect(__builtin_amdgcn_ballot_w64(!(tm <= thr)) != 0ull, 0)) {
#pragma unroll
          for (int r = 0; r < 4; ++r) {
            const float mx = row_max(fmaxf(sa[r], sb[r])), dm = mx <= 16.f ? 0.f : mx;
            nref[r] -= dm; sa[r] -= dm; sb[r] -= dm;
          }
          thr = __builtin_nanf("");
          word |= 1u;
          if (n == 0) *(LAS f32x4*)(lds + LDS_PK + pk * PK_B + 1280 + 16 * g) = -nref;
        }
#pragma unroll
        for (int r = 0; r < 4; ++r) { sa[r] = __builtin_amdgcn_exp2f(sa[r]); sb[r] = __builtin_amdgcn_exp2f(sb[r]); }
        float ra = R[0], rb = R[1];
        asm volatile("" : "+v"(ra), "+v"(rb));
        sa *= ra; sb *= rb;
        *(LAS u32x2*)pw = u32x2{cvtpk(sa[0], sa[1]), cvtpk(sa[2], sa[3])};
        *(LAS u32x2*)(pw + 128) = u32x2{cvtpk(sb[0], sb[1]), cvtpk(sb[2], sb[3])};
        lds_put(CT_PRDY, pfl, word);
        SB();
        if (s < NT - 8) __builtin_amdgcn_s_sleep(THR);
      }
      GET_TAG();
      LAS char* fin = lds + xslot + 8192;
      unsigned qres = (unsigned)__builtin_amdgcn_raw_buffer_load_b16(mkrs(q, NQ * C * 2), dout * 2, 0, 0) << 16;
      unsigned ao = ((tid & 255) >> 1) * 8192 + (tid & 1) * 16 + dst * 32, tw = (unsigned)(tg >> (20 * (tid & 1))), sh = 12 * (tid & 1), oo = dout * 2;
      LAS char *fw = fin + (((lane & 8) | ((g & 1) << 2) | (g & 2)) + (lane & 1)) * 16 + 4 * (w - 4), *fr = fin + (2 * (lane & 7) + ((lane >> 3) & 1)) * 16;
      float v7 = 0.f;
      unsigned fi_ = 8u;
      const unsigned t0s = lds_get(ctl + CT_T0);
      unsigned gate = (t0s ? t0s : (unsigned)tstart) + RGATE;
      u32x4 ro = mkrs4(out, NQ * C * 2), rm4 = mkrs4(ws, WS_BYTES);
      unsigned half = 128 * 8192;
      unsigned long long wm = 0x0303030303030303ull;
      asm volatile("" : "+v"(ao), "+v"(tw), "+v"(sh), "+v"(oo), "+v"(fw), "+v"(fr), "+v"(fin), "+v"(v7), "+v"(qres), "+v"(fi_), "+s"(ro), "+s"(wm), "+s"(rm4), "+s"(half));
      if (w == 4) asm volatile("s_mov_b64 exec, 0x1515\n\tbuffer_store_dword %0, %1, %2, 0 offen sc1\n\ts_mov_b64 exec, -1" :: "v"(qres), "v"(oo), "s"(ro) : "memory");
      lds_wait(ctl + CT_XD, 1);
      lds_spin(CT_ST, ctl, 4);
      gate += max((int)((unsigned)wall_clock64() + OWNG - gate), 0);
      float org = *(LAS const float*)(lds + xslot + 6144 + 4 * dq);
      *(LAS float*)(fin + 256 + 4 * (w - 4)) = org;
#pragma unroll 1
      for (int spin = 0; spin < 1024; ++spin)
        if ((int)((unsigned)wall_clock64() - gate) >= 0) break;
      u32x4 A, B;
      asm volatile("buffer_load_dwordx4 %0, %2, %3, 0 offen sc1\n\tbuffer_load_dwordx4 %1, %2, %3, %4 offen sc1\n\ts_waitcnt vmcnt(0)"
                   : "=&v"(A), "=&v"(B) : "v"(ao), "s"(rm4), "s"(half) : "memory");
      if (__builtin_expect(__builtin_amdgcn_ballot_w64((((A[3] ^ tw) | (B[3] ^ tw)) & 0xfffffu) != 0u) != 0ull, 1)) {
#pragma unroll 1
        for (int spin = 0; spin < (1 << 18); ++spin) {
          unsigned xa_ = (A[3] ^ tw) << 12, xb_ = (B[3] ^ tw) << 12;
          if (__builtin_amdgcn_ballot_w64((xa_ | xb_) != 0u) == 0ull) break;
          xa_ |= __builtin_amdgcn_mov_dpp(xa_, 0xB1, 0xf, 0xf, true); xb_ |= __builtin_amdgcn_mov_dpp(xb_, 0xB1, 0xf, 0xf, true);
          asm volatile("" ::: "memory");
          if (xa_ != 0u) A = ldb128<16>(rws, ao, 0);
          if (xb_ != 0u) B = ldb128<16>(rws, ao, 128 * 8192);
        }
      }
      unsigned la = (A[3] & 0xfff00000u) >> sh, lb_ = (B[3] & 0xfff00000u) >> sh;
      la |= __builtin_amdgcn_mov_dpp(la, 0xB1, 0xf, 0xf, true); lb_ |= __builtin_amdgcn_mov_dpp(lb_, 0xB1, 0xf, 0xf, true);
      float da = __uint_as_float(la) - org, db = __uint_as_float(lb_) - org;
      f32x2 ua_[3], ub_[3];
#pragma unroll
      for (int i = 0; i < 3; ++i) { ua_[i] = f32x2{bflo(A[i]), bfhi(A[i])}; ub_[i] = f32x2{bflo(B[i]), bfhi(B[i])}; asm volatile("" : "+v"(ua_[i]), "+v"(ub_[i])); }
      if (__builtin_expect(__builtin_amdgcn_ballot_w64(!(fmaxf(da, db) <= 64.f)) != 0ull, 0)) {
        org = xrow16_max(row_max(fmaxf(__uint_as_float(la), __uint_as_float(lb_))));
        da = __uint_as_float(la) - org; db = __uint_as_float(lb_) - org;
        *(LAS float*)(fin + 256 + 4 * (w - 4)) = org;
        fi_ = 9u;
      }
      const float wa = __builtin_amdgcn_exp2f(da), wb = __builtin_amdgcn_exp2f(db);
#pragma unroll
      for (int i = 0; i < 3; ++i) ua_[i] = ua_[i] * wa + ub_[i] * wb;
      const f32x2 p0 = swap32(ua_[0][0], ua_[0][1]), p1 = swap32(ua_[1][0], ua_[1][1]), p2 = swap32(ua_[2][0], ua_[2][1]), p3 = swap32(wa + wb, v7);
      const f32x2 r0 = swap16(p0[0] + p0[1], p1[0] + p1[1]), r1 = swap16(p2[0] + p2[1], p3[0] + p3[1]);
      float c0 = r0[0] + r0[1], c1 = r1[0] + r1[1];
      asm volatile("s_nop 1\n\tv_add_f32_dpp %0, %0, %0 row_ror:8 row_mask:0xf bank_mask:0x3\n\tv_add_f32_dpp %0, %1, %1 row_ror:8 row_mask:0xf bank_mask:0xc\n\t"
                   "s_nop 1\n\tv_add_f32_dpp %0, %0, %0 row_shl:4 row_mask:0xf bank_mask:0x5\n\t"
                   "s_nop 1\n\tv_add_f32_dpp %0, %0, %0 quad_perm:[2,3,0,1] row_mask:0xf bank_mask:0x5\n\t"
                   "s_mov_b64 exec, %3\n\tds_write_b32 %2, %0\n\ts_mov_b64 exec, 1\n\tds_add_u32 %4, %5 offset:%6\n\ts_mov_b64 exec, -1"
                   : "+v"(c0) : "v"(c1), "v"(fw), "s"(wm), "v"(ctl), "v"(fi_), "i"(CT_FIN) : "memory");
      if (w == 4) {
        f32x4 a0, l0;
        unsigned cf;
        int cnt_, c_;
        asm volatile("s_mov_b32 %1, 0x4000000\n1:\n\tds_read_b32 %2, %5 offset:%8\n\tds_read_b128 %3, %6\n\tds_read_b128 %4, %7 offset:192\n\t"
                     "s_waitcnt lgkmcnt(2)\n\tv_readfirstlane_b32 %0, %2\n\ts_cmp_ge_i32 %0, 32\n\ts_cbranch_scc1 2f\n\t"
                     "s_sub_u32 %1, %1, 1\n\ts_cbranch_scc0 1b\n2:\n\ts_waitcnt lgkmcnt(0)"
                     : "=&s"(c_), "=&s"(cnt_), "=&v"(cf), "=&v"(a0), "=&v"(l0) : "v"(ctl), "v"(fr), "v"(fin), "i"(CT_FIN) : "memory", "scc");
        asm volatile("" : "+s"(c_));
        if (__builtin_expect(c_ != 32, 0)) {
          f32x4 m0 = *(LAS const f32x4*)(fin + 256);
          const float mm = fmaxf(fmaxf(m0[0], m0[1]), fmaxf(m0[2], m0[3]));
#pragma unroll
          for (int p = 0; p < 4; ++p) m0[p] = __builtin_amdgcn_exp2f(m0[p] - mm);
          a0 *= m0; l0 *= m0;
        }
        const float o = __uint_as_float(qres) + ((a0[0] + a0[2]) + (a0[1] + a0[3])) * __builtin_amdgcn_rcpf((l0[0] + l0[2]) + (l0[1] + l0[3]));
        const unsigned ob = cvtpk(o, __uint_as_float(__builtin_amdgcn_mov_dpp(__float_as_uint(o), 0xB1, 0xf, 0xf, true)));
        asm volatile("s_mov_b64 exec, 0x1515\n\tbuffer_store_dword %0, %1, %2, 0 offen sc1\n\ts_mov_b64 exec, -1" :: "v"(ob), "v"(oo), "s"(ro) : "memory");
        if (wg == 0 && lane == 0) stb64(u32x2{(unsigned)tg, (unsigned)(tg >> 32)}, rws, EP_OFF);
      }
    } else {
      const int ftr = ((g & 1) << 2) | ((tq >> 1) << 1) | (g >> 1);
      const int fty = (g & 1) * 4 + (tq >> 1) * 3;
      int otr[4], oty[4];
#pragma unroll
      for (int t = 0; t < 4; ++t) {
        otr[t] = (w - 1) * PART + 128 * (4 * g + tq) + 16 * ((2 * t + (tp >> 1)) ^ ftr) + 8 * (tp & 1);
        oty[t] = CRS + (w - 1) * 128 + 384 * (4 * (g & 1) + tq) + 16 * ((2 * t + (tp >> 1)) ^ fty) + 8 * (tp & 1);
      }
      int opk = LDS_PK + 320 * tp + 8 * (4 * g + tq), ork = LDS_PK + 320 * (g >> 1) + 256 + 8 * (4 * (g & 1) + tq);
      asm volatile("" : "+v"(opk), "+v"(ork), "+v"(oty[0]), "+v"(oty[1]), "+v"(oty[2]), "+v"(oty[3]));
#define RD_T(ta, ty, so_) _Pragma("unroll") for (int t_ = 0; t_ < 4; ++t_) { \
    ta[t_] = vtr2(lds + otr[t_] + (so_), lds + otr[t_] + (so_) + 2048); ty[t_] = vtr(lds + oty[t_] + (so_)); }
      f32x4 acc[4] = {z, z, z, z}, tac = z;
#define RD_P(o_) \
      pf = vtr2(lds + opk + (o_), lds + opk + (o_) + 128); \
      sf = vtr2(lds + ork + (o_), lds + ork + (o_) + 640)
#define TWICE(ty) asm volatile("s_nop 2"); _Pragma("unroll") for (int t = 0; t < 4; ++t) { u32x2 lo_, hi_; \
      asm volatile("v_mov_b64 %0, %2\n\tv_mov_b64 %1, %2" : "=&v"(lo_), "=&v"(hi_) : "v"(ty[t])); t2[t] = u32x4{lo_[0], lo_[1], hi_[0], hi_[1]}; } \
      asm volatile("s_nop 1" : "+v"(t2[0]), "+v"(t2[1]), "+v"(t2[2]), "+v"(t2[3]))
#define MF_PX(ta) \
      tac = mfma(sf, pf, tac); \
      _Pragma("unroll") for (int t = 0; t < 4; ++t) acc[t] = mfma(ta[t], pf, acc[t])
#define MF_PY() \
      _Pragma("unroll") for (int t = 0; t < 4; ++t) acc[t] = mfma(t2[t], pf, acc[t])
#define MOVE_REF(word_, o_) do { moved |= (word_) & 1; if (__builtin_expect(moved, 0)) { \
            const float m_ = *(LAS const float*)(lds + (((word_) & 1) ? LDS_PK + (o_) + 1280 : LDS_CTL + CT_MAX) + 4 * n); \
            Mtop = fmaxf(Mtop, m_); \
            const bool drop = m_ < Mtop - 64.f; \
            const float mn = drop ? M : m_, fa = __builtin_amdgcn_exp2f(M - mn); \
            M = mn; \
            _Pragma("unroll") for (int t = 0; t < 4; ++t) acc[t] *= fa; \
            tac *= fa; \
            if (drop) pf = u32x4{0u, 0u, 0u, 0u}; \
          } } while (0)
      const unsigned flo = FLAG_OFF + (wg & 7) * 768 + (w - 1) * 144 + (lane >= 16 ? min(lane - 16, 35) : (lane >> 3) * 6 + (lane & 7) + ((lane & 7) < 6 ? 0 : 18)) * 8;
      float M, Mtop;
      int moved = 0;
      u32x4 wvv[18], pf, sf, t2[4];
      {
        u32x4 ta[8][4], xx[6];
        u32x2 ty[8][4], fl;
        unsigned cc[6];
        const int qo = QK_OFF + (wg & 3) * QK_COPY + (2 * w - 2) * QK_REC;
#define QK_LOAD() _Pragma("unroll") for (int j = 0; j < 6; ++j) { \
          if ((j == 2 || j == 4) && early) __builtin_amdgcn_s_sleep(QSJ); \
          xx[j] = ldb128<16>(rws, lane * 16, qo + ((j >> 1) * 6 + (j & 1)) * QK_REC); \
          cc[j] = __builtin_amdgcn_raw_buffer_load_b32(rws, lane * 4, qo + ((j >> 1) * 6 + (j & 1)) * QK_REC + 1024, 16); }
#pragma unroll
        for (int u = 0; u < 8; ++u) {
          if (u == QKAT) {
            GET_TAG();
#pragma unroll 1
            for (int r = 0; r < 8; ++r) {
#pragma unroll 1
              for (int spin = 0; spin < 128; ++spin) {
                if ((int)(wall_clock64() - tstart) >= GATE + r * HSTEP + (w - 1) * QSW) break;
                __builtin_amdgcn_s_sleep(1);
              }
              u32x8 hv;
              asm volatile("s_load_dwordx8 %0, %1, %2\n\ts_waitcnt lgkmcnt(0)" : "=&s"(hv) : "s"(ws), "s"(HINT_OFF + ((wg & 7) + 8 * r) * 128 + (w - 1) * 32) : "memory");
              const unsigned t_ = (unsigned)tg;
              if ((((hv[0] ^ t_) | (hv[1] ^ t_) | (hv[2] ^ t_)) | ((hv[3] ^ t_) | (hv[4] ^ t_) | (hv[5] ^ t_))) == 0u) break;
            }
            bool early = (int)(wall_clock64() - tstart) < QLATE;
            if (wg < NPR) lds_wait(ctl + CT_PD, 4);
            QK_LOAD();
#pragma unroll 1
            for (int spin = 0; spin < (1 << 18); ++spin) {
              unsigned df = 0u;
#pragma unroll
              for (int j = 0; j < 6; ++j) df |= qk_check(xx[j], (unsigned)tg) ^ cc[j];
              if (__all(df == 0u)) break;
              asm volatile("" ::: "memory");
              early = false;
              QK_LOAD();
            }
#pragma unroll
            for (int h = 0; h < 2; ++h) {
              u32x4 sx;
#pragma unroll
              for (int i = 0; i < 4; ++i) sx[i] = add3(xx[h][i], xx[2 + h][i], xx[4 + h][i]);
              *(LAS u32x4*)(lds + LDS_QK + (2 * w - 2 + h) * 1024 + lane * 16) = sx;
            }
            lds_inc(CT_QK, ctl);
            SB();
          }
          lds_wait(ctl + CT_FULL, 3 * u + w);
          RD_T(ta[u], ty[u], u * SLOT);
          lds_inc(CT_FREE + 4 * u, ctl);
          SB();
        }
        fl = __builtin_bit_cast(u32x2, __builtin_amdgcn_raw_buffer_load_b64(rws, flo, 0, 16));
#pragma unroll
        for (int u = 0; u < 12; ++u) {
          const int word = lds_spin(CT_PRDY + 4 * (u % NPK), ctl, 2 * u + 2);
          if (u == 0) Mtop = M = *(LAS const float*)(ctl + CT_MAX + 4 * n);
          RD_P((u % NPK) * PK_B);
          if (u >= 8) MOVE_REF(word, (u % NPK) * PK_B);
          lds_inc(CT_PCON + 4 * (u % NPK), ctl);
          SB();
          MF_PX(ta[u & 7]);
          SB();
          TWICE(ty[u & 7]);
          MF_PY();
          asm volatile("" : "+v"(acc[0]), "+v"(acc[1]), "+v"(acc[2]), "+v"(acc[3]), "+v"(tac));
          SB();
#define FLAGS_UP() __all(fl[0] == (unsigned)tg && (fl[1] & 255u) == (unsigned)(tg >> 32))
          if (u == T0AT && w == 1 && FLAGS_UP()) {
            int d = (int)(fl[1] - ((unsigned)tstart << 8)) >> 8;
            d += __builtin_amdgcn_mov_dpp(d, XOR8, 0xf, 0xf, true); d += __builtin_amdgcn_mov_dpp(d, ROR4, 0xf, 0xf, true);
            d += __builtin_amdgcn_mov_dpp(d, 0x122, 0xf, 0xf, true); d += __builtin_amdgcn_mov_dpp(d, 0x121, 0xf, 0xf, true);
            lds_put(CT_T0, ctl, ((unsigned)tstart + min(max(d >> 4, -300), 300)) | 1u);
          }
          if (u < 4) {
            NEED(3 * (u + 8) + w);
            RD_T(ta[u], ty[u], ((u + 8) % NS) * SLOT);
            lds_inc(CT_FREE + 4 * ((u + 8) % NS), ctl);
            SB();
          }
        }
#pragma unroll 1
        for (int spin = 0; spin < (1 << 18); ++spin) {
          if (FLAGS_UP()) break;
          asm volatile("" ::: "memory");
          fl = __builtin_bit_cast(u32x2, __builtin_amdgcn_raw_buffer_load_b64(rws, flo, 0, 16));
        }
        asm volatile("" ::: "memory");
        if (w == 3) lds_put(CT_WV, ctl, 1);
      }
      int slot = 3, pk = 12 % NPK;
      unsigned rdy = 0u;
      const int kro[3] = {(w - 1) * 2048, (w == 3 ? 0 : w) * 2048, (w == 1 ? 2 : w - 2) * 2048};
#define RD_PACKET(z_) do { int cnt_; u32x2 p0_, p1_, p2_, p3_; \
        word = __builtin_amdgcn_readfirstlane(rdy); \
        asm volatile("s_mov_b32 %1, 0x400000\n1:\n\tds_read_b32 %2, %7 offset:%11\n\t" \
                     "ds_read_b64_tr_b16 %5, %9\n\tds_read_b64_tr_b16 %6, %9 offset:640\n\t" \
                     "ds_read_b64_tr_b16 %3, %8\n\tds_read_b64_tr_b16 %4, %8 offset:128\n\t" \
                     "s_cmp_ge_i32 %0, %10\n\ts_cbranch_scc1 2f\n\ts_waitcnt lgkmcnt(4)\n\tv_readfirstlane_b32 %0, %2\n\t" \
                     "s_cmp_ge_i32 %0, %10\n\ts_cbranch_scc1 2f\n\t" z_ "s_sub_u32 %1, %1, 1\n\ts_cbranch_scc0 1b\n2:" \
                     : "+s"(word), "=&s"(cnt_), "=&v"(rdy), "=&v"(p0_), "=&v"(p1_), "=&v"(p2_), "=&v"(p3_) \
                     : "v"(ctl + 4 * pk), "v"(lds + opk + pk * PK_B), "v"(lds + ork + pk * PK_B), "s"(2 * s + 2), "i"(CT_PRDY), "i"(CALM) : "memory", "scc"); \
        asm volatile("" : "+s"(word)); \
        pf = u32x4{p0_[0], p0_[1], p1_[0], p1_[1]}; sf = u32x4{p2_[0], p2_[1], p3_[0], p3_[1]}; } while (0)
#define GOT_PACKET() do { waitlds<0>(); asm volatile("" : "+v"(pf), "+v"(sf) : "v"(rdy)); MOVE_REF(word, pk * PK_B); } while (0)
#define RD_Y(t_, p_) do { const u32x2 lo_ = vtr(p_); asm volatile("" ::: "memory"); const u32x2 hi_ = vtr(p_); \
        t2[t_] = u32x4{lo_[0], lo_[1], hi_[0], hi_[1]}; } while (0)
      u32x4 ta[4];
      int s = 12, word;
      NEED(36 + w);
#pragma unroll
      for (int t = 0; t < 4; ++t) { ta[t] = vtr2(lds + otr[t] + 2 * SLOT, lds + otr[t] + 2 * SLOT + 2048); RD_Y(t, lds + oty[t] + 2 * SLOT); }
      lds_inc(CT_FREE + 8, ctl);
#pragma unroll 1
      for (; s < NT - 1; ++s) {
        RD_PACKET("s_sleep %12\n\t");
        LAS const char *px[4], *py[4];
#pragma unroll
        for (int t = 0; t < 4; ++t) { px[t] = lds + otr[t] + slot * SLOT; py[t] = lds + oty[t] + slot * SLOT; asm volatile("" : "+v"(px[t]), "+v"(py[t])); }
        NEED(3 * s + 3 + w);
        GOT_PACKET();
        SB();
        tac = mfma(sf, pf, tac);
        asm volatile("" : "+v"(tac));
        SB();
        lds_inc(CT_PCON, ctl + 4 * pk);
        pk = pk == NPK - 1 ? 0 : pk + 1;
#pragma unroll
        for (int t = 0; t < 4; ++t) { acc[t] = mfma(ta[t], pf, acc[t]); SB(); ta[t] = vtr2(px[t], px[t] + 2048); SB(); }
        rdy = *(volatile LAS unsigned*)(ctl + CT_PRDY + 4 * pk);
#pragma unroll
        for (int t = 0; t < 4; ++t) { acc[t] = mfma(t2[t], pf, acc[t]); SB(); RD_Y(t, py[t]); SB(); }
        lds_inc(CT_FREE, ctl + 4 * slot);
#define WV_LOAD(j_) wvv[j_] = ldb128<16>(rws, lane * 16 + ((j_) & 1) * 1024, WVO_OFF + (w - 1) * 18432 + ((j_) / 6) * 6144 + kro[((j_) % 6) >> 1])
        if ((unsigned)(s - WVS) < WV1) {
#pragma unroll
          for (int u = 0; u < WV1; ++u)
            if (s - WVS == u) WV_LOAD(u);
        }
        if (s == NT - 3) {
#pragma unroll
          for (int j = WV1; j < 18; ++j) WV_LOAD(j);
        }
        slot = slot == NS - 1 ? 0 : slot + 1;
      }
      LAS char *xw = lds + xslot + lane * 16 + kro[0], *xl = lds + xslot + 6144 + 4 * n;
      LAS const char *xr1 = lds + xslot + lane * 16 + kro[1], *xr2 = lds + xslot + lane * 16 + kro[2];
      unsigned mo = wg * 8192 + (w - 1) * 2048 + n * 64 + g * 16, tw = (unsigned)(tg >> (20 * (g & 1))) & 0xfffffu, sh = 12 * (g & 1);
      asm volatile("" : "+v"(xw), "+v"(xl), "+v"(xr1), "+v"(xr2), "+v"(mo), "+v"(tw), "+v"(sh));
      __builtin_amdgcn_s_setprio(2);
      RD_PACKET("");
      GOT_PACKET();
      SB();
      tac = mfma(sf, pf, tac);
      asm volatile("" : "+v"(tac));
      SB();
#pragma unroll
      for (int t = 0; t < 2; ++t) acc[t] = mfma(t2[t], pf, mfma(ta[t], pf, acc[t]));
      SB();
#pragma unroll
      for (int t = 2; t < 4; ++t) acc[t] = mfma(t2[t], pf, mfma(ta[t], pf, acc[t]));
      const float L = tac[2] + tac[3], rl = __builtin_amdgcn_rcpf(L), ct = -(tac[0] + tac[1]) * rl;
      const float lse = M + __builtin_amdgcn_logf(L);
      u32x4 b[6];
      b[0] = pack8(acc[0] * rl + ct, acc[1] * rl + ct);
      *(LAS u32x4*)xw = b[0];
      *(LAS float*)xl = lse;
#pragma unroll
      for (int j = 0; j < 4; ++j) { __builtin_amdgcn_sched_group_barrier(0x008, 1, 0); __builtin_amdgcn_sched_group_barrier(0x002, 3, 0); }
      SB();
      b[1] = pack8(acc[2] * rl + ct, acc[3] * rl + ct);
      *(LAS u32x4*)(xw + 1024) = b[1];
      lds_inc(CT_XD, ctl);
      SB();
      f32x4 o[3];
      unsigned xd;
#pragma unroll
      for (int t = 0; t < 3; ++t) o[t] = mfma(wvv[6 * t], b[0], z);
      asm volatile("" : "+v"(o[0]), "+v"(o[1]), "+v"(o[2]));
      SB();
#define RD_SLICES "ds_read_b32 %0, %7 offset:%10\n\tds_read_b128 %1, %8\n\tds_read_b128 %2, %8 offset:1024\n\tds_read_b128 %3, %9\n\tds_read_b128 %4, %9 offset:1024\n"
      asm volatile(RD_SLICES : "=&v"(xd), "=&v"(b[2]), "=&v"(b[3]), "=&v"(b[4]), "=&v"(b[5]) : "i"(0), "i"(0), "v"(ctl), "v"(xr1), "v"(xr2), "i"(CT_XD) : "memory");
      SB();
#pragma unroll
      for (int t = 0; t < 3; ++t) o[t] = mfma(wvv[6 * t + 1], b[1], o[t]);
      tw |= (__float_as_uint(lse) << sh) & 0xfff00000u;
      asm volatile("" : "+v"(tw), "+v"(o[0]), "+v"(o[1]), "+v"(o[2]));
      SB();
      {
        int cnt_, c_;
        asm volatile("s_mov_b32 %6, 0x400000\n\ts_branch 2f\n1:\n\t" RD_SLICES "2:\n\ts_waitcnt lgkmcnt(4)\n\tv_readfirstlane_b32 %5, %0\n\t"
                     "s_cmp_ge_i32 %5, 3\n\ts_cbranch_scc1 3f\n\ts_sub_u32 %6, %6, 1\n\ts_cbranch_scc0 1b\n3:\n\ts_waitcnt lgkmcnt(0)"
                     : "+v"(xd), "+v"(b[2]), "+v"(b[3]), "+v"(b[4]), "+v"(b[5]), "=&s"(c_), "=&s"(cnt_)
                     : "v"(ctl), "v"(xr1), "v"(xr2), "i"(CT_XD) : "memory", "scc");
      }
      SB();
      PROJECT(2);
      lds_inc(CT_ST, ctl);
    }
  }
}

#undef rws
#define WS_AT(size_) ((size_) > (64u << 20) ? ((size_) - (64u << 20)) & ~(size_t)0x1fffff : 0)
extern "C" void kernel_launch(void* const* d_in, const int* in_sizes, int n_in, void* d_out, int out_size, void* d_ws, size_t ws_size,
                              hipStream_t stream) {
  dino_main<<<NWG, 512, 0, stream>>>((const u16*)d_in[0], (const u16*)d_in[1], (const u16*)d_in[2], (const u16*)d_in[3], (const u16*)d_in[4],
                                     (const u16*)d_in[5], (const u16*)d_in[6], (char*)d_ws + WS_AT(ws_size) + 4096, (u16*)d_out);
}
```

```cpp
#include <hip/hip_runtime.h>
#include <hip/hip_bf16.h>
#include <stdint.h>

typedef unsigned short u16;
using u32x2 = __attribute__((ext_vector_type(2))) unsigned;
using u32x4 = __attribute__((ext_vector_type(4))) unsigned;
using u32x8 = __attribute__((ext_vector_type(8))) unsigned;
using f32x2 = __attribute__((ext_vector_type(2))) float;
using f32x4 = __attribute__((ext_vector_type(4))) float;
using s16x8 = __attribute__((ext_vector_type(8))) short;
using bf16x2_t = __attribute__((ext_vector_type(2))) __bf16;
typedef short v4i16_t __attribute__((ext_vector_type(4)));
#define LAS __attribute__((address_space(3)))

constexpr int C = 192, NQ = 16, W0 = 512, W1 = 256, NWG = 256;
constexpr float EPS = 1e-5f;
constexpr float QSCALE = 0.07216878364870322f * 1.4426950408889634f;

constexpr int PART = 4096, CRS = 3 * PART;
constexpr int SLOT = CRS + 3072, NS = 10;
constexpr int LDS_QK = NS * SLOT;
constexpr int NPK = 7;
constexpr int LDS_PK = LDS_QK, PK_B = 1344;
constexpr int LDS_CTL = LDS_PK + NPK * PK_B;
constexpr int LDS_SINK = LDS_CTL + 384;
constexpr int LDS_BYTES = LDS_SINK + 256;
constexpr int PSCR = 8 * SLOT;
constexpr int CT_FULL = 8, CT_WV = 4, CT_PA = 0, CT_PB = 12, CT_QR = 16, CT_PD = 20, CT_T0 = 24, CT_XD = 28, CT_TG = 32, CT_QK = 40, CT_ST = 44,
              CT_FREE = 48, CT_FIN = 88, CT_ONE = 112, CT_PRDY = 128, CT_PCON = 160, CT_MAX = 256;

constexpr int MB_BYTES = NWG * NWG * 32;
constexpr int EP_OFF = MB_BYTES;
constexpr int FLAG_OFF = EP_OFF + 512;
constexpr int WVO_OFF = EP_OFF + 8192;
constexpr int QK_OFF = WVO_OFF + C * C * 2;
constexpr int QK_REC = 1280, QK_COPY = 18 * QK_REC;
constexpr int HINT_OFF = QK_OFF + 4 * QK_COPY;
constexpr int WS_BYTES = HINT_OFF + 64 * 128;

#define GIVE 2
constexpr int NPR = 36;
struct Tabs { unsigned seq[112]; };
constexpr Tabs make_tabs() {
  Tabs t{};
  for (int i = 0; i < 112; ++i) t.seq[i] = i + 1;
  return t;
}
__device__ const Tabs tabs = make_tabs();
#define QKAT 3
#define WV1 6
#define WMIN 28
#define WSTEP 5
#define LSTEP 40
#define OWNG 30
#define PACE0 30
#define PACE1 1742
#define RGATE 1922
#define T0AT 3
#define WVS 12
#define CALM 2
#define QSW 12
#define QSJ 12
#define THR 6
#define QLATE 450
#define COLD0 108
#define COLDG 8
#define COLDM 48
#define HSTEP 40
#define GATE 350
#define CODE_BYTES 26176

__device__ __forceinline__ unsigned cvtpk(float lo, float hi) {
  bf16x2_t v = {(__bf16)lo, (__bf16)hi};
  return __builtin_bit_cast(unsigned, v);
}
__device__ __forceinline__ float bflo(unsigned x) { return __builtin_bit_cast(float, x << 16); }
__device__ __forceinline__ float bfhi(unsigned x) { return __builtin_bit_cast(float, x & 0xffff0000u); }
template <int CTRL> __device__ __forceinline__ float dpp(float x) {
  return __builtin_bit_cast(float, __builtin_amdgcn_mov_dpp(__builtin_bit_cast(int, x), CTRL, 0xf, 0xf, true));
}
constexpr int XOR8 = 0x128, ROR4 = 0x124;
template <bool HI8> __device__ __forceinline__ float half_copy(float x) {
  const int v = __builtin_bit_cast(int, x);
  return __builtin_bit_cast(float, __builtin_amdgcn_update_dpp(v, v, XOR8, 0xf, HI8 ? 0xc : 0x3, false));
}
__device__ __forceinline__ float row_max(float x) {
  x = fmaxf(x, dpp<XOR8>(x)); x = fmaxf(x, dpp<ROR4>(x)); x = fmaxf(x, dpp<0x122>(x));
  return fmaxf(x, dpp<0x121>(x));
}
__device__ __forceinline__ f32x2 swap32(float a, float b) {
  auto s = __builtin_amdgcn_permlane32_swap(__float_as_uint(a), __float_as_uint(b), false, false);
  return f32x2{__uint_as_float(s[0]), __uint_as_float(s[1])};
}
__device__ __forceinline__ f32x2 swap16(float a, float b) {
  auto s = __builtin_amdgcn_permlane16_swap(__float_as_uint(a), __float_as_uint(b), false, false);
  return f32x2{__uint_as_float(s[0]), __uint_as_float(s[1])};
}
__device__ __forceinline__ float xrow16_max(float x) {
  auto s = __builtin_amdgcn_permlane16_swap(__float_as_uint(x), __float_as_uint(x), false, false);
  x = fmaxf(__uint_as_float(s[0]), __uint_as_float(s[1]));
  auto t = __builtin_amdgcn_permlane32_swap(__float_as_uint(x), __float_as_uint(x), false, false);
  return fmaxf(__uint_as_float(t[0]), __uint_as_float(t[1]));
}
__device__ __forceinline__ float xrow16_sum(float x) {
  auto s = __builtin_amdgcn_permlane16_swap(__float_as_uint(x), __float_as_uint(x), false, false);
  x = __uint_as_float(s[0]) + __uint_as_float(s[1]);
  auto t = __builtin_amdgcn_permlane32_swap(__float_as_uint(x), __float_as_uint(x), false, false);
  return __uint_as_float(t[0]) + __uint_as_float(t[1]);
}
__device__ __forceinline__ f32x4 mfma(u32x4 a, u32x4 b, f32x4 c) {
  return __builtin_amdgcn_mfma_f32_16x16x32_bf16(__builtin_bit_cast(s16x8, a), __builtin_bit_cast(s16x8, b), c, 0, 0, 0);
}
__device__ __forceinline__ u32x2 vtr(LAS const char* p) {
  return __builtin_bit_cast(u32x2, __builtin_amdgcn_ds_read_tr16_b64_v4i16((LAS v4i16_t*)p));
}
__device__ __forceinline__ u32x4 vtr2(LAS const char* p, LAS const char* q_) {
  const u32x2 a = vtr(p), b = vtr(q_);
  return u32x4{a[0], a[1], b[0], b[1]};
}
__device__ __forceinline__ float sel4(f32x4 v, int r) {
  const float a = (r & 1) ? v[1] : v[0], b = (r & 1) ? v[3] : v[2];
  return (r & 2) ? b : a;
}
using RSRC = __amdgpu_buffer_rsrc_t;
__device__ __forceinline__ RSRC mkrs(const void* p, int bytes) { return __builtin_amdgcn_make_buffer_rsrc(const_cast<void*>(p), (short)0, bytes, 0x00020000); }
__device__ __forceinline__ u32x4 mkrs4(const void* p, unsigned bytes) {
  const unsigned long long b = (unsigned long long)p;
  return u32x4{(unsigned)b, (unsigned)(b >> 32) & 0xffffu, bytes, 0x00020000u};
}
template <int AUX> __device__ __forceinline__ u32x4 ldb128(RSRC r, unsigned vo, int so) { return __builtin_bit_cast(u32x4, __builtin_amdgcn_raw_buffer_load_b128(r, vo, so, AUX)); }
#define SB() __builtin_amdgcn_sched_barrier(0)
#define RAW_BARRIER() asm volatile("s_waitcnt lgkmcnt(0)\n\ts_barrier" ::: "memory")
#define DMA(m0_, vo_, rs_, so_) asm volatile("s_mov_b32 m0, %0\n\ts_nop 0\n\tbuffer_load_dwordx4 %1, %2, %3 offen lds" :: "s"(m0_), "v"(vo_), "s"(rs_), "s"(so_) : "memory")
template <int N> __device__ __forceinline__ void waitlds() { __builtin_amdgcn_s_waitcnt(0xc07f | (N << 8)); }
template <int N> __device__ __forceinline__ void waitvm() { asm volatile("s_waitcnt vmcnt(%0)" :: "i"(N) : "memory"); }


__device__ __forceinline__ void lds_inc(int O, LAS char* p) {
  asm volatile("s_mov_b64 exec, 1\n\tds_add_u32 %0, %1 offset:%2\n\ts_mov_b64 exec, -1" :: "v"(p), "v"(1u), "i"(O) : "memory");
}
__device__ __forceinline__ void lds_put(int O, LAS char* p, unsigned v) {
  asm volatile("s_mov_b64 exec, 1\n\tds_write_b32 %0, %1 offset:%2\n\ts_mov_b64 exec, -1" :: "v"(p), "v"(v), "i"(O) : "memory");
}
__device__ __forceinline__ int lds_get(LAS char* p) {
  asm volatile("" ::: "memory");
  const unsigned v = __hip_atomic_load((LAS unsigned*)p, __ATOMIC_RELAXED, __HIP_MEMORY_SCOPE_WORKGROUP);
  asm volatile("" ::: "memory");
  return __builtin_amdgcn_readfirstlane(v);
}
__device__ __forceinline__ void lds_wait(LAS char* p, int target) {
#pragma unroll 1
  for (int spin = 0; spin < (1 << 22); ++spin) {
    if (lds_get(p) >= target) break;
    __builtin_amdgcn_s_sleep(1);
  }
}
__device__ __forceinline__ int lds_spin(int O, LAS char* p, int target) {
  int r, cnt = 1 << 22;
  unsigned t;
  asm volatile("1:\n\tds_read_b32 %1, %3 offset:%5\n\ts_waitcnt lgkmcnt(0)\n\tv_readfirstlane_b32 %0, %1\n\ts_cmp_ge_i32 %0, %4\n\ts_cbranch_scc1 2f\n\t"
               "s_sub_u32 %2, %2, 1\n\ts_cbranch_scc0 1b\n2:"
               : "=&s"(r), "=&v"(t), "+s"(cnt) : "v"(p), "s"(target), "i"(O) : "memory", "scc");
  return r;
}
__device__ __forceinline__ void lds_need(int O, int& have, LAS char* p, int target) {
  int cnt;
  unsigned t;
  asm volatile("s_cmp_ge_i32 %0, %4\n\ts_cbranch_scc1 2f\n\ts_mov_b32 %2, 0x400000\n"
               "1:\n\tds_read_b32 %1, %3 offset:%5\n\ts_waitcnt lgkmcnt(0)\n\tv_readfirstlane_b32 %0, %1\n\ts_cmp_ge_i32 %0, %4\n\ts_cbranch_scc1 2f\n\t"
               "s_sleep %6\n\ts_sub_u32 %2, %2, 1\n\ts_cbranch_scc0 1b\n2:"
               : "+s"(have), "=&v"(t), "=&s"(cnt) : "v"(p), "s"(target), "i"(O), "i"(CALM) : "memory", "scc");
  asm volatile("" : "+s"(have));
}
__device__ __forceinline__ u32x4 pack8(f32x4 a, f32x4 b) { return u32x4{cvtpk(a[0], a[1]), cvtpk(a[2], a[3]), cvtpk(b[0], b[1]), cvtpk(b[2], b[3])}; }

__device__ __forceinline__ void stb128(u32x4 v, RSRC r, unsigned o) { __builtin_amdgcn_raw_buffer_store_b128(v, r, o, 0, 16); }
__device__ __forceinline__ void stb64(u32x2 v, RSRC r, unsigned o) { __builtin_amdgcn_raw_buffer_store_b64(v, r, o, 0, 16); }
__device__ __forceinline__ void stb32(unsigned v, RSRC r, unsigned o) { __builtin_amdgcn_raw_buffer_store_b32(v, r, o, 0, 16); }
__device__ __forceinline__ unsigned qk_check(u32x4 x, unsigned tg) {
  const unsigned a = (x[0] ^ __builtin_amdgcn_alignbit(x[1], x[1], 11)) * 0x9e3779b1u + tg;
  const unsigned b = (x[2] + __builtin_amdgcn_alignbit(x[3], x[3], 19)) * 0x85ebca77u;
  return __builtin_amdgcn_alignbit(a, a, 15) ^ b ^ (x[1] + x[3]);
}
__device__ __forceinline__ unsigned add3(unsigned a, unsigned b, unsigned c) {
  return cvtpk((bflo(a) + bflo(b)) + bflo(c), (bfhi(a) + bfhi(b)) + bfhi(c));
}
__global__ __launch_bounds__(512, 2) void dino_main(const u16* q, const u16* f0, const u16* f1, const u16* wq, const u16* wk, const u16* wv,
                                                     const u16* wo, char* ws, u16* out) {
  __shared__ __attribute__((aligned(128))) char smem[LDS_BYTES];
  LAS char* lds = (LAS char*)smem;
  LAS char* ctl = lds + LDS_CTL;
  const unsigned lb = (unsigned)(unsigned long long)lds;
  const int w = __builtin_amdgcn_readfirstlane(threadIdx.x >> 6);
  unsigned long long pc0;
  asm volatile("s_getpc_b64 %0" : "=s"(pc0));
#define COORDS() \
  int tid = threadIdx.x, wg = blockIdx.x; \
  asm volatile("" : "+v"(tid), "+s"(wg)); \
  __builtin_assume(tid >= 0 && tid < 512); __builtin_assume(wg >= 0 && wg < NWG); \
  const int lane = tid & 63, n = lane & 15, g = lane >> 4;       \
    \
  const int dq = (wg & 7) * 2 + ((wg >> 4) & 1), dst = (wg & ~31) + (wg & 7) * 4 + ((wg >> 3) & 3); \
  const int dj = 6 * ((wg >> 5) & 1) + min(lane & 7, 5); \
  const int dout = dq * C + 48 * (wg >> 6) + 16 * (dj >> 2) + (wg & 8) + ((lane & 8) >> 1) + (dj & 3); \
  const int NT = wg < NPR ? 32 - GIVE : wg >= 40 && !(wg & 1) && (wg >> 3) < 5 + ((wg & 4) ? 8 : 10) * GIVE ? 33 : 32; \
  const int xslot = (NT >= 30 ? NT - 30 : NT - 20) * SLOT;       \
  (void)xslot; (void)n; (void)g; (void)dst; (void)dout; (void)NT
#define ARGS_IN() asm volatile("s_waitcnt lgkmcnt(0)" :: "s"(q), "s"(f0), "s"(f1), "s"(wq), "s"(wk), "s"(wv), "s"(wo), "s"(ws), "s"(out) : "memory")
#define rws mkrs(ws, WS_BYTES)
#define PROJECT(k0_) do { \
      _Pragma("unroll") for (int k = k0_; k < 6; ++k) { o[0] = mfma(wvv[k], b[k], o[0]); o[1] = mfma(wvv[6 + k], b[k], o[1]); } \
      asm volatile("" : "+v"(o[0]), "+v"(o[1])); \
      SB(); \
      _Pragma("unroll") for (int k = k0_; k < 6; ++k) o[2] = mfma(wvv[12 + k], b[k], o[2]); \
      asm volatile("" : "+v"(o[2])); \
      SB(); \
      stb128(u32x4{cvtpk(o[0][0], o[0][1]), cvtpk(o[0][2], o[0][3]), cvtpk(o[1][0], o[1][1]), tw}, rws, mo); \
      SB(); \
      stb128(u32x4{cvtpk(o[1][2], o[1][3]), cvtpk(o[2][0], o[2][1]), cvtpk(o[2][2], o[2][3]), tw}, rws, mo + 1024); \
    } while (0)
  unsigned long long tg;
#define GET_TAG() tg = ((unsigned long long)((unsigned)lds_spin(CT_TG + 4, ctl, 256) & 0xffu) << 32) | (unsigned)lds_get(ctl + CT_TG)
  const unsigned long long tstart = wall_clock64();
  if (w != 0) {
    const unsigned l0 = threadIdx.x & 63;
    if (blockIdx.x < NPR) {
      const unsigned p6 = (blockIdx.x * 43) >> 8, pu = blockIdx.x - 6 * p6;
      unsigned vo = ((l0 * 16) ^ (((l0 * 4) & 0x40) | (l0 & 0x20))) + (l0 >> 3) * 256;
      const bool pv = p6 >= 3, wa = w >= 4, isq = !pv && (w & 6) == 2;
      const unsigned a6 = 6 * w - 24, pj = pv ? p6 - 3 : p6;
      unsigned so = (wa ? a6 : isq ? w - 2 : 4 * pu) * 3072 + (isq ? 0 : pv && !wa ? w - 1 : pj) * 128, ga = isq ? 128 : 3072;
      unsigned lo = lb + PSCR + (wa ? a6 : pv ? 4 * w + 20 : (0x1b181e00u >> (8 * w)) & 255u) * 1024, hi = lo + (wa ? 6144 : isq ? 3072 : 4096);
      asm volatile("" : "+v"(vo), "+s"(lo), "+s"(so), "+s"(hi), "+s"(ga));
      ARGS_IN();
      const u32x4 rw = mkrs4(wa ? (pv ? wo : wq) : pv ? wv : isq ? q : wk, isq ? NQ * C * 2 : C * C * 2);
#pragma unroll 1
      do { DMA(lo, vo, rw, so); lo += 1024; so += ga; } while (lo != hi);
    } else if (w == 7 && blockIdx.x >= 56 && blockIdx.x < 72) {
      const u32x4 rc = mkrs4((const void*)(pc0 & ~255ull), CODE_BYTES);
#pragma unroll 1
      for (int i = 0; i < CODE_BYTES; i += 4096)
        asm volatile("s_mov_b32 m0, %0\n\ts_nop 0\n\tbuffer_load_dword %1, %2, %3 offen lds" :: "s"(lb + LDS_SINK), "v"(l0 * 64), "s"(rc), "s"(i) : "memory");
    }
    if (w == 3) {
      unsigned long long ep;
      asm volatile("s_waitcnt lgkmcnt(0)\n\ts_load_dwordx2 %0, %1, %2\n\ts_barrier\n\ts_waitcnt lgkmcnt(0)" : "=&s"(ep) : "s"(ws), "s"(EP_OFF) : "memory");
      *(LAS unsigned long long*)(ctl + CT_TG) = ((ep + 0x100001ull) & 0xffffffffffull) | (((wall_clock64() - tstart) & 0x3fffffull) | 1ull) << 40;
    } else RAW_BARRIER();
  }
  SB();
  if (__builtin_expect(w == 0, 1)) {
    const unsigned l0 = threadIdx.x & 63, g0 = blockIdx.x;
    unsigned y0 = (l0 * 16) ^ (l0 >= 48 ? 48 : 0), y1 = (l0 * 16) ^ (l0 >= 32 ? 64 : 48), y2 = (l0 * 16) ^ (l0 >= 16 ? 112 : 64);
    unsigned v0 = ((l0 * 16) ^ ((l0 * 2) & 0x60)) + (l0 >> 3) * 640 + 1024, v1 = (v0 ^ 16) - 640;
    unsigned v2 = v0 + W0 * C * 2 - 2048, v3 = v1 + W0 * C * 2 - 2048;
    unsigned b0 = (g0 & 7) * (2 * W0 * C * 2) + (g0 >> 3) * 6144, b1 = (g0 & 7) * (W1 * C * 2) + (g0 >> 3) * 3072;
    {
      const unsigned iv = (l0 & 15) >= CT_ONE / 8 ? 0x3f803f80u : (l0 >> 3) == CT_MAX / 64 ? 0xff800000u : 0u;
      *(LAS u32x2*)(ctl + 8 * min(l0, 47u)) = u32x2{l0 >= 47 ? 0x3f803f80u : iv, l0 >= 47 ? 0x3f803f80u : iv};
    }
    constexpr unsigned st0 = 16 * W0 * C * 2, st1 = 8 * W1 * C * 2;
    unsigned sb = lb, fo = CT_FREE, fv = 0u;
    int lap = 0;
    u32x4 rt = mkrs4(tabs.seq, 112 * 4);
    unsigned vz = 0u;
    asm volatile("" : "+v"(v0), "+v"(v1), "+v"(v2), "+v"(v3), "+v"(y0), "+v"(y1), "+v"(y2), "+v"(vz), "+s"(b0), "+s"(b1), "+s"(rt));
    ARGS_IN();
    const u32x4 r0 = mkrs4((const char*)f0 - 1024, W0 * W0 * C * 2 + 1024), r1 = mkrs4(f1, W1 * W1 * C * 2);
    if (g0 < NPR) __builtin_amdgcn_s_barrier();
#define UNIT(p_) do { unsigned so_; \
      asm volatile("s_add_u32 m0, %1, %10\n\ts_add_u32 %0, %2, %11\n\t" \
                   "buffer_load_dwordx4 %3, %7, %0 offen nt lds\n\tbuffer_load_dwordx4 %4, %7, %0 offen offset:1024 nt lds\n\t" \
                   "buffer_load_dwordx4 %5, %7, %0 offen offset:2048 nt lds\n\tbuffer_load_dwordx4 %6, %7, %0 offen offset:3072 nt lds\n\t" \
                   "s_mov_b32 m0, %12\n\ts_mov_b64 exec, 1\n\tbuffer_load_dword %13, %8, %9 offen offset:%14 lds\n\ts_mov_b64 exec, -1" \
                   : "=&s"(so_) : "s"(sb), "s"(b0), "v"(v0), "v"(v1), "v"(v2), "v"(v3), "s"(r0), "s"(rt), "s"(12 * s), "i"((p_) * PART), "i"((p_) * 128), \
                     "s"(lb + LDS_CTL + CT_FULL - 4 * (p_)), "v"(vz), "i"(4 * (p_)) : "memory", "scc"); } while (0)
#define WINDOW() \
      asm volatile("s_waitcnt vmcnt(%1)\n\ts_cmp_ge_i32 %0, %6\n\ts_cbranch_scc1 1f\n\ts_waitcnt vmcnt(%2)\n\ts_cmp_ge_i32 %0, %7\n\ts_cbranch_scc1 1f\n\t" \
                   "s_waitcnt vmcnt(%3)\n\ts_cmp_ge_i32 %0, %8\n\ts_cbranch_scc1 1f\n\ts_waitcnt vmcnt(%4)\n\ts_cmp_ge_i32 %0, %9\n\ts_cbranch_scc1 1f\n\t" \
                   "s_waitcnt vmcnt(%5)\n1:" :: "s"(late), "i"(WMIN + 4 * WSTEP), "i"(WMIN + 3 * WSTEP), "i"(WMIN + 2 * WSTEP), "i"(WMIN + WSTEP), "i"(WMIN), \
                   "i"(4 * LSTEP * 256), "i"(3 * LSTEP * 256), "i"(2 * LSTEP * 256), "i"(LSTEP * 256) : "memory", "scc")
#define COARSE(sb_) \
      asm volatile("s_add_u32 m0, %0, %6\n\ts_nop 0\n\tbuffer_load_dwordx4 %1, %4, %5 offen nt lds\n\t" \
                   "buffer_load_dwordx4 %2, %4, %5 offen offset:1024 nt lds\n\tbuffer_load_dwordx4 %3, %4, %5 offen offset:2048 nt lds" \
                   :: "s"(sb_), "v"(y0), "v"(y1), "v"(y2), "s"(r1), "s"(b1), "i"(CRS) : "memory", "scc")
    COARSE(sb);
#pragma unroll 1
    for (int s = 0; s < 2; ++s) {
      asm volatile("s_add_u32 %0, %0, %1" : "+s"(b1) : "i"(st1) : "scc");
      UNIT(0); UNIT(1); COARSE(sb + SLOT); UNIT(2);
      sb += SLOT; b0 += st0;
    }
    if (g0 >= NPR) __builtin_amdgcn_s_barrier();
    __builtin_amdgcn_s_setprio(3);
    COORDS();
    fo += 12;
    const int tau = __builtin_amdgcn_readfirstlane((int)((PACE1 - PACE0) * 256.f * __builtin_amdgcn_rcpf((float)NT)));
    unsigned due = PACE0 * 256u + 2 * tau, t0v = 0u;
    unsigned long long now = tstart;
    const unsigned xi = (wg >> 3) - 5, xrow = 8 * (31 - xi % GIVE) + (wg & 7) + (xi / GIVE & 1), xcol = xi / GIVE >> 1;
    const unsigned x0 = xrow * (2 * W0 * C * 2) + xcol * 6144, x1 = xrow * (W1 * C * 2) + xcol * 3072;
#pragma unroll 1
    for (int s = 2; s < NT; ++s) {
      asm volatile("" : "+s"(now));
      const unsigned t0s = __builtin_amdgcn_readfirstlane(t0v), t0 = t0s ? t0s : (unsigned)tstart;
      if ((int)(((unsigned)now - t0) * 256u - due) < 0) {
#pragma unroll 1
        for (int spin = 0; spin < 256; ++spin) {
          if ((int)(((unsigned)wall_clock64() - t0) * 256u - due) >= 0) break;
          __builtin_amdgcn_s_sleep(1);
        }
      }
      fv = *(volatile LAS unsigned*)(ctl + (s == 7 ? CT_PD : fo));
      now = wall_clock64();
      UNIT(0);
      asm volatile("" : "+s"(now));
      const int late = (int)(((unsigned)now - t0) * 256u - due);
      due += tau;
      WINDOW();
      UNIT(1);
      WINDOW();
      const unsigned nsb = fo == CT_FREE ? lb : sb + SLOT;
      const unsigned n0 = s == 31 ? x0 : b0 + st0;
      b1 = s == 31 ? x1 : b1 + st1;
      if (s + 1 < NT) {
        const int need = s == 7 && wg < NPR ? 4 : lap;
        if (__builtin_expect((int)__builtin_amdgcn_readfirstlane(fv) < need, 0)) {
          lds_wait(ctl + (s == 7 ? CT_PD : fo), need);
        }
        COARSE(nsb);
      }
      now = wall_clock64();
      t0v = *(volatile LAS unsigned*)(ctl + CT_T0);
      UNIT(2);
      WINDOW();
      if (fo == CT_FREE + 4 * (NS - 1)) { fo = CT_FREE; lap += 4; } else fo += 4;
      sb = nsb; b0 = n0;
    }
    __builtin_amdgcn_s_setprio(0);
    u32x4 wvv[18], b[6];
    asm volatile("s_mov_b32 m0, %0\n\ts_mov_b64 exec, 1\n\tbuffer_load_dword %1, %2, 0 offen lds\n\ts_mov_b64 exec, -1"
                 :: "s"(lb + LDS_SINK), "v"(vz), "s"(mkrs4(ws - 4096, 4096)) : "memory");
    lds_wait(ctl + CT_WV, 1);
#pragma unroll
    for (int j = 0; j < 18; ++j) wvv[j] = ldb128<16>(rws, lane * 16 + (j & 3) * 1024, WVO_OFF + 54 * 1024 + (j >> 2) * 4096);
    GET_TAG();
    LAS const char *xr = lds + xslot + lane * 16, *xl = lds + xslot + 6144 + 4 * n;
    unsigned mo = wg * 8192 + 6144 + n * 64 + g * 16, tw = (unsigned)(tg >> (20 * (g & 1))) & 0xfffffu, sh = 12 * (g & 1), lse;
    asm volatile("" : "+v"(xr), "+v"(xl), "+v"(mo), "+v"(tw), "+v"(sh));
    __builtin_amdgcn_s_waitcnt(0x0f70);
    lds_spin(CT_PRDY, ctl + 4 * ((NT - 1) % NPK), 2 * NT);
    __builtin_amdgcn_s_sleep(2);
    __builtin_amdgcn_s_setprio(2);
    {
      int cnt_, c_;
      unsigned t_;
      asm volatile("s_mov_b32 %1, 0x400000\n1:\n\tds_read_b32 %2, %10 offset:%13\n\tds_read_b128 %3, %11\n\tds_read_b128 %4, %11 offset:1024\n\t"
                   "ds_read_b128 %5, %11 offset:2048\n\tds_read_b128 %6, %11 offset:3072\n\tds_read_b128 %7, %11 offset:4096\n\t"
                   "ds_read_b128 %8, %11 offset:5120\n\tds_read_b32 %9, %12\n\ts_waitcnt lgkmcnt(7)\n\tv_readfirstlane_b32 %0, %2\n\t"
                   "s_cmp_ge_i32 %0, 3\n\ts_cbranch_scc1 2f\n\ts_sub_u32 %1, %1, 1\n\ts_cbranch_scc0 1b\n2:\n\ts_waitcnt lgkmcnt(5)"
                   : "=&s"(c_), "=&s"(cnt_), "=&v"(t_), "=&v"(b[0]), "=&v"(b[1]), "=&v"(b[2]), "=&v"(b[3]), "=&v"(b[4]), "=&v"(b[5]), "=&v"(lse)
                   : "v"(ctl), "v"(xr), "v"(xl), "i"(CT_XD) : "memory", "scc");
    }
    SB();
    f32x4 o[3] = {{0.f, 0.f, 0.f, 0.f}, {0.f, 0.f, 0.f, 0.f}, {0.f, 0.f, 0.f, 0.f}};
#pragma unroll
    for (int k = 0; k < 2; ++k)
#pragma unroll
      for (int t = 0; t < 3; ++t) o[t] = mfma(wvv[6 * t + k], b[k], o[t]);
    asm volatile("s_waitcnt lgkmcnt(0)" : "+v"(o[0]), "+v"(o[1]), "+v"(o[2]), "+v"(b[2]), "+v"(b[3]), "+v"(b[4]), "+v"(b[5]), "+v"(lse));
    SB();
    tw |= (lse << sh) & 0xfff00000u;
    PROJECT(2);
    lds_inc(CT_ST, ctl);
  } else {
    COORDS();
    const int tq = (lane >> 2) & 3, tp = lane & 3;
    const bool pq = wg < 18;
    const bool pv = wg >= 18 && wg < NPR;
    const int pj = wg / 6, pu = wg % 6;
    if (w >= 4 && ((w + NT) & 3) == 0 && !pq) {
      unsigned k0, k1, k2;
      asm volatile("s_load_dword %0, %3, %5\n\ts_load_dword %1, %3, %6\n\ts_load_dword %2, %4, %7\n\ts_waitcnt lgkmcnt(0)"
                   : "=&s"(k0), "=&s"(k1), "=&s"(k2) : "s"(f0), "s"(f1), "s"((wg >> 3) * 3145728 + 1572860), "s"((wg >> 3) * 3145728 + 3145724),
                     "s"((wg >> 3) * 786432 + 786428) : "memory");
    }
    const u32x4 one = {0x3f803f80u, 0x3f803f80u, 0x3f803f80u, 0x3f803f80u};
    const int fq = ((n & 2) << 1) | ((n & 4) >> 1);
    LAS const char* rq0 = lds + PSCR + 24576 + 3072 * (n >> 3) + 128 * (n & 7) + 16 * (g ^ fq);
    LAS const char* rq1 = lds + PSCR + 24576 + 3072 * (n >> 3) + 128 * (n & 7) + 16 * ((4 + g) ^ fq);
    if (pq && w >= 2) {
      {
        waitvm<0>();
        lds_inc(CT_PA, ctl);
        if (w >= 4) {
          f32x4 sq = {0.f, 0.f, 0.f, 0.f}, rr = sq, cs = sq;
          const int ro = PSCR + 128 * (8 * g + tq) + 16 * (((w & 2) * 2 + tp) ^ ((tq & 2) << 1)) + 8 * (w & 1);
          u32x4 qv[6], af[6];
          lds_spin(CT_PA, ctl, 6);
#pragma unroll
          for (int s = 0; s < 6; ++s) {
            qv[s] = *(LAS const u32x4*)(((s & 1) ? rq1 : rq0) + (s >> 1) * 1024);
            af[s] = vtr2(lds + ro + s * 4096, lds + (ro ^ 32) + s * 4096 + 512);
          }
          SB();
#pragma unroll
          for (int s = 0; s < 6; ++s) { sq = mfma(one, qv[s], sq); rr = mfma(af[s], qv[s], rr); cs = mfma(af[s], one, cs); }
          const f32x4 qq = rr - (sq[0] * (1.f / C)) * cs;
          *(LAS u32x2*)(lds + LDS_QK + 4096 + ((w >> 1) & 1) * 1024 + lane * 16 + 8 * (w & 1)) = u32x2{cvtpk(qq[0], qq[1]), cvtpk(qq[2], qq[3])};
          lds_inc(CT_PB, ctl);
        }
      }
    }
    if (pq && w == 1) {
      {
        waitvm<0>();
        u32x4 kf4[4], qv[6];
        const int ko = LDS_QK + 128 * (((n & 4) << 2) | (n & 11)) + 16 * (g ^ ((n & 2) << 1));
#pragma unroll
        for (int i = 0; i < 4; ++i) kf4[i] = *(LAS const u32x4*)(lds + (ko ^ ((i & 1) * 64 + (i >> 1) * 32)) + (i >> 1) * 512);
        lds_spin(CT_PA, ctl, 6);
#pragma unroll
        for (int s = 0; s < 6; ++s) qv[s] = *(LAS const u32x4*)(((s & 1) ? rq1 : rq0) + (s >> 1) * 1024);
        const f32x4 z = {0.f, 0.f, 0.f, 0.f};
        f32x4 gq = z, sq = z;
#pragma unroll
        for (int s = 0; s < 6; ++s) { gq = mfma(qv[s], qv[s], gq); sq = mfma(one, qv[s], sq); }
        const float mean = sq[0] * (1.f / C);
        const float s2 = xrow16_sum(g == (n >> 2) ? sel4(gq, n & 3) : 0.f);
        float sc = __builtin_amdgcn_rsqf(fmaxf(fmaf(-mean, mean, s2 * (1.f / C)), 0.f) + EPS) * QSCALE;
        asm volatile("" : "+v"(sc));
        GET_TAG();
        lds_spin(CT_PB, ctl, 4);
        const u32x4 q0 = *(LAS const u32x4*)(lds + LDS_QK + 4096 + lane * 16), q1 = *(LAS const u32x4*)(lds + LDS_QK + 5120 + lane * 16);
        const f32x4 d0 = mfma(kf4[1], q1, mfma(kf4[0], q0, z)), d1 = mfma(kf4[3], q1, mfma(kf4[2], q0, z));
        const u32x4 xq = pack8(d0 * sc, d1 * sc);
        const unsigned ck = qk_check(xq, (unsigned)tg);
        asm volatile("" :: "v"(ck));
        {
          const unsigned so = QK_OFF + wg * QK_REC;
#pragma unroll
          for (int cp = 0; cp < 4; ++cp) {
            stb128(xq, rws, lane * 16 + so + cp * QK_COPY);
            stb32(ck, rws, lane * 4 + so + 1024 + cp * QK_COPY);
          }
          stb32((unsigned)tg, rws, HINT_OFF + lane * 128 + (pu >> 1) * 32 + pj * 8 + (pu & 1) * 4);
          lds_put(CT_PD, ctl, 4);
        }
      }
    }
    if (pv) {
      waitvm<0>();
      lds_inc(CT_PA, ctl);
      if (w >= 4) {
        const int ro = PSCR + 128 * (8 * g + tq) + 16 * ((2 * (w - 4) + (tp >> 1)) ^ ((tq & 2) << 1)) + 8 * (tp & 1);
        const int rv = PSCR + 24576 + 128 * n + 16 * (g ^ fq);
        f32x4 d0 = {0.f, 0.f, 0.f, 0.f}, d1 = d0;
        lds_spin(CT_PA, ctl, 7);
#pragma unroll
        for (int s = 0; s < 6; ++s) {
          const u32x4 p = vtr2(lds + ro + s * 4096, lds + (ro ^ 32) + s * 4096 + 512);
          LAS const char* pd = lds + (rv ^ ((s & 1) * 64)) + (s >> 1) * 4096;
          d0 = mfma(*(LAS const u32x4*)pd, p, d0); d1 = mfma(*(LAS const u32x4*)(pd + 2048), p, d1);
        }
        const int fi = (pj - 3) * 24 + (w - 4) * 6 + pu;
        stb128(pack8(d0, d1), rws, lane * 16 + WVO_OFF + fi * 1024);
        lds_inc(CT_PD, ctl);
        GET_TAG();
        const unsigned org = (unsigned)tstart + min(max((lds_get(ctl + CT_TG + 4) >> 8) - COLD0, 0) * COLDG >> 2, COLDM);
        waitvm<0>();
        if (lane < 8) stb64(u32x2{(unsigned)tg, (unsigned)(tg >> 32) | org << 8}, rws, FLAG_OFF + lane * 768 + fi * 8);
      }
    }
    const f32x4 z = {0.f, 0.f, 0.f, 0.f};
    int full = 0;
#define NEED(u_) lds_need(CT_FULL, full, ctl, (u_))
    if (w >= 4) {
      const int brg = ((g & 1) << 1) | (g >> 1);
      const int fn = (((n >> 1) & 3) << 1) | ((n >> 3) & 1);
      const int fy = ((n >> 2) & 1) * 4 + ((n >> 1) & 1) * 3;
      const int orr0 = 128 * n + 16 * (brg ^ fn), orr1 = 128 * n + 16 * ((4 + brg) ^ fn);
      const int ory0 = n < 8 ? CRS + 384 * n + 16 * (brg ^ fy) : LDS_CTL + CT_ONE, ory1 = n < 8 ? CRS + 384 * n + 16 * ((4 + brg) ^ fy) : LDS_CTL + CT_ONE;
      const int yon = n < 8 ? 1 : 0;
      const int opk = LDS_PK + 320 * g + 8 * n, ork = LDS_PK + 320 * (2 * (g >> 1) + (n >> 3)) + 256 + 8 * (n & 7) + 4 * (g & 1);
      u32x4 qk[6];
      f32x4 ref, qs;
      LAS const char *px0, *px1, *py0, *py1;
#define AT_SLOT(o_) do { px0 = lds + orr0 + (o_); px1 = lds + orr1 + (o_); py0 = lds + ory0 + __mul24(yon, (o_)); py1 = lds + ory1 + __mul24(yon, (o_)); \
      asm volatile("" : "+v"(px0), "+v"(px1), "+v"(py0), "+v"(py1)); } while (0)
#define RD_K(k_, i_) do { LAS const char* p_ = (((k_) & 1) ? px1 : px0) + ((k_) >> 1) * PART; \
    yv[i_] = *(LAS const u32x4*)((((k_) & 1) ? py1 : py0) + ((k_) >> 1) * 128); \
    xa[i_] = *(LAS const u32x4*)(p_); xb[i_] = *(LAS const u32x4*)(p_ + 2048); } while (0)
#define MF_STAT(i_) do { cy = mfma(yv[i_], yv[i_], cy); ca = mfma(yv[i_], xa[i_], ca); cb = mfma(yv[i_], xb[i_], cb); \
    ga = mfma(xa[i_], xa[i_], ga); gb = mfma(xb[i_], xb[i_], gb); } while (0)
#define MF_S(k_, i_) do { sy = mfma(qk[k_], yv[i_], sy); sa = mfma(qk[k_], xa[i_], sa); sb = mfma(qk[k_], xb[i_], sb); } while (0)
#define STATS(st, rec) do { \
      const int r = n & 3; \
      const float yy = half_copy<true>(sel4(cy, r)); \
      const f32x2 Y = swap32(yy, yy), X = swap32(sel4(ca, r), sel4(cb, r)), G = swap32(sel4(ga, r), sel4(gb, r)); \
      const float q1_ = fmaf(2.f, X[0], Y[0]) + (n < 8 ? G[0] : G[1]);       \
      const f32x2 Q = swap16(q1_, q1_); \
      const float mn_ = (X[1] + Y[1]) * (1.f / C); \
      const float var = fmaxf(fmaf(-mn_, mn_, fmaf((n & 4) ? Q[1] : Q[0], 1.f / C, EPS)), EPS), rs_ = __builtin_amdgcn_rsqf(var); \
      const f32x2 R = swap32(rs_, rs_), Mn = swap32(mn_, mn_); \
      st = f32x4{R[0], Mn[0], R[1], Mn[1]}; \
      const float v_ = (g & 1) ? var * rs_ : mn_, h_ = bfhi(__float_as_uint(v_)); \
      rec = cvtpk(h_, v_ - h_); \
    } while (0)
#define LOGITS(sv, st, base_) do { \
      _Pragma("unroll") for (int r = 0; r < 4; ++r) { \
        const float s0 = half_copy<true>(sy[r]); \
        sv[r] = fmaf(fmaf(-st[1], qs[r], sa[r] + s0), st[0], -(base_)[r]); \
        sv[4 + r] = fmaf(fmaf(-st[3], qs[r], sb[r] + s0), st[2], -(base_)[r]); \
      } \
    } while (0)
#define PUBLISH(sv, st, rec, pk_, word_) do { \
      *(LAS unsigned*)(lds + ork + (pk_) * PK_B) = rec; \
      *(LAS u32x2*)(lds + opk + (pk_) * PK_B) = u32x2{cvtpk(sv[0] * st[0], sv[1] * st[0]), cvtpk(sv[2] * st[0], sv[3] * st[0])}; \
      *(LAS u32x2*)(lds + opk + (pk_) * PK_B + 128) = u32x2{cvtpk(sv[4] * st[2], sv[5] * st[2]), cvtpk(sv[6] * st[2], sv[7] * st[2])}; \
      lds_put(CT_PRDY, ctl + 4 * (pk_), (word_)); \
    } while (0)
      int s = (w + NT - 1) & 3, slot = s + 4, pk = s + 4, ncon = 0;
      {
        u32x4 yv[12], xa[12], xb[12];
        f32x4 st0, st1;
        unsigned rec0, rec1;
#pragma unroll
        for (int h = 0; h < 2; ++h) {
          f32x4 cy = z, ga = z, ca = z, gb = z, cb = z;
          AT_SLOT((s + 4 * h) * SLOT);
          lds_wait(ctl + CT_FULL, 3 * (s + 4 * h) + 3);
#pragma unroll
          for (int k = 0; k < 6; ++k) RD_K(k, 6 * h + k);
          lds_inc(CT_FREE + 16 * h, ctl + 4 * s);
          SB();
#pragma unroll
          for (int k = 0; k < 6; ++k) MF_STAT(6 * h + k);
          SB();
          if (h == 0) { STATS(st0, rec0); asm volatile("" : "+v"(st0), "+v"(rec0)); } else { STATS(st1, rec1); asm volatile("" : "+v"(st1), "+v"(rec1)); }
          SB();
        }
        lds_wait(ctl + CT_QK, 3);
#pragma unroll
        for (int k = 0; k < 6; ++k) qk[k] = *(LAS const u32x4*)(lds + LDS_QK + k * 1024 + lane * 16);
        float s0v[8], s1v[8];
        {
          f32x4 sy = z, sa = z, sb = z;
#pragma unroll
          for (int k = 0; k < 6; ++k) MF_S(k, k);
#pragma unroll
          for (int r = 0; r < 4; ++r) qs[r] = half_copy<false>(sy[r]);
          LOGITS(s0v, st0, z);
        }
        {
          f32x4 sy = z, sa = z, sb = z;
#pragma unroll
          for (int k = 0; k < 6; ++k) MF_S(k, 6 + k);
          LOGITS(s1v, st1, z);
        }
#pragma unroll
        for (int r = 0; r < 4; ++r) ref[r] = row_max(fmaxf(fmaxf(s0v[r], s0v[4 + r]), fmaxf(s1v[r], s1v[4 + r])));
        if (n < 4) __builtin_amdgcn_ds_fmaxf((LAS float*)(ctl + CT_MAX + 16 * g + 4 * n), sel4(ref, n), __ATOMIC_RELAXED, __HIP_MEMORY_SCOPE_WORKGROUP, false);
        lds_inc(CT_QR, ctl);
        lds_spin(CT_QR, ctl, 4);
        ref = *(LAS const f32x4*)(ctl + CT_MAX + 16 * g);
#pragma unroll
        for (int j = 0; j < 8; ++j) s0v[j] = __builtin_amdgcn_exp2f(s0v[j] - ref[j & 3]);
        PUBLISH(s0v, st0, rec0, s, 2 * s + 2);
#pragma unroll
        for (int j = 0; j < 8; ++j) s1v[j] = __builtin_amdgcn_exp2f(s1v[j] - ref[j & 3]);
        if (pk >= NPK) { pk -= NPK; ncon = 3; lds_spin(CT_PCON, ctl + 4 * pk, 3); }
        s += 4;
        PUBLISH(s1v, st1, rec1, pk, 2 * s + 2);
      }
      float thr = 16.f;
      f32x4 nref = -ref;
#pragma unroll 1
      for (;;) {
        s += 4; slot += 4; slot -= slot >= NS ? NS : 0; pk += 4;
        if (pk >= NPK) { pk -= NPK; ncon += 3; }
        if (s >= NT) break;
        f32x4 sy = z, cy = z, sa, ga = z, ca = z, sb, gb = z, cb = z;
        u32x4 yv[6], xa[6], xb[6];
        unsigned fl_, word = 2 * s + 2;
        AT_SLOT(slot * SLOT);
        LAS char *pw = lds + opk + pk * PK_B, *prc = lds + ork + pk * PK_B, *pfl = ctl + 4 * pk, *pfr = ctl + 4 * slot;
        asm volatile("" : "+v"(pw), "+v"(prc), "+v"(pfl), "+v"(pfr), "+v"(word));
#define RD_X "ds_read_b32 %2, %7 offset:%11\n\tds_read_b128 %3, %8 offset:%12\n\tds_read_b128 %4, %9 offset:%12\n\t" \
             "ds_read_b128 %5, %8 offset:%13\n\tds_read_b128 %6, %9 offset:%13\n"
#define RD_IN(p_) "v"(ctl), "v"(px0), "v"(px1), "s"(3 * s + (p_) + 1), "i"(CT_FULL), "i"(PART * (p_)), "i"(PART * (p_) + 2048)
#define ISSUE(p_) do { int d0_, d1_; \
        asm volatile(RD_X : "=&s"(d0_), "=&s"(d1_), "=&v"(fl_), "=&v"(xa[2 * (p_)]), "=&v"(xa[2 * (p_) + 1]), "=&v"(xb[2 * (p_)]), "=&v"(xb[2 * (p_) + 1]) \
                     : RD_IN(p_) : "memory"); } while (0)
#define CHECK(p_) do { int cnt_; \
        asm volatile("s_mov_b32 %1, 0x400000\n\ts_branch 2f\n1:\n\t" RD_X "2:\n\ts_waitcnt lgkmcnt(4)\n\tv_readfirstlane_b32 %0, %2\n\t" \
                     "s_cmp_ge_i32 %0, %10\n\ts_cbranch_scc1 3f\n\ts_cmp_ge_i32 %10, %14\n\ts_cbranch_scc1 4f\n\ts_sleep %15\n4:\n\t" \
                     "s_sub_u32 %1, %1, 1\n\ts_cbranch_scc0 1b\n3:\n\ts_waitcnt lgkmcnt(2)" \
                     : "=&s"(full), "=&s"(cnt_), "+v"(fl_), "+v"(xa[2 * (p_)]), "+v"(xa[2 * (p_) + 1]), "+v"(xb[2 * (p_)]), "+v"(xb[2 * (p_) + 1]) \
                     : RD_IN(p_), "s"(3 * NT - 2), "i"(CALM) : "memory", "scc"); \
        asm volatile("" : "+s"(full)); } while (0)
#define GOT_B(p_) do { waitlds<0>(); asm volatile("" : "+v"(xb[2 * (p_)]), "+v"(xb[2 * (p_) + 1])); } while (0)
#define MF_A(k_) do { ca = mfma(yv[k_], xa[k_], ca); ga = mfma(xa[k_], xa[k_], ga); sa = mfma(qk[k_], xa[k_], sa); } while (0)
#define MF_B(k_) do { cb = mfma(yv[k_], xb[k_], cb); gb = mfma(xb[k_], xb[k_], gb); sb = mfma(qk[k_], xb[k_], sb); } while (0)
        const unsigned pcv = *(volatile LAS unsigned*)(pfl + CT_PCON);
        NEED(3 * s);
#pragma unroll
        for (int k = 0; k < 6; ++k) yv[k] = *(LAS const u32x4*)(((k & 1) ? py1 : py0) + (k >> 1) * 128);
        int con = __builtin_amdgcn_readfirstlane(pcv);
        SB();
#pragma unroll
        for (int k = 0; k < 6; ++k) sy = mfma(qk[k], yv[k], sy);
        asm volatile("" : "+v"(sy));
        SB();
        lds_need(CT_PCON, con, pfl, ncon);
        ISSUE(0);
        SB();
#pragma unroll
        for (int k = 0; k < 6; ++k) cy = mfma(yv[k], yv[k], cy);
#pragma unroll
        for (int r = 0; r < 4; ++r) sa[r] = half_copy<true>(sy[r]);
        const float yy = half_copy<true>(sel4(cy, n & 3));
        f32x2 Y = swap32(yy, yy);
        asm volatile("" : "+v"(Y), "+v"(sa));
        SB();
        CHECK(0);
        sb = sa;
        MF_A(0);
        asm volatile("" : "+v"(ca), "+v"(ga), "+v"(sa));
        SB();
        GOT_B(0);
        ISSUE(1);
        SB();
        MF_B(0); MF_A(1); MF_B(1);
        asm volatile("" : "+v"(ca), "+v"(ga), "+v"(sa), "+v"(cb), "+v"(gb), "+v"(sb));
        SB();
        CHECK(1);
        MF_A(2);
        asm volatile("" : "+v"(ca), "+v"(ga), "+v"(sa));
        SB();
        GOT_B(1);
        ISSUE(2);
        SB();
        MF_B(2); MF_A(3); MF_B(3);
        asm volatile("" : "+v"(ca), "+v"(ga), "+v"(sa), "+v"(cb), "+v"(gb), "+v"(sb));
        SB();
        CHECK(2);
        ca = mfma(yv[5], xa[5], mfma(yv[4], xa[4], ca));
        ga = mfma(xa[5], xa[5], mfma(xa[4], xa[4], ga));
        asm volatile("" : "+v"(ca), "+v"(ga));
        SB();
        GOT_B(2);
        cb = mfma(yv[5], xb[5], mfma(yv[4], xb[4], cb));
        asm volatile("" : "+v"(cb));
        SB();
        lds_inc(CT_FREE, pfr);
        gb = mfma(xb[4], xb[4], gb);
        float ua = sel4(ca, n & 3);
        asm volatile("" : "+v"(gb), "+v"(ua));
        SB();
        gb = mfma(xb[5], xb[5], gb);
        float va = sel4(ga, n & 3);
        asm volatile("" : "+v"(gb), "+v"(va));
        SB();
        sa = mfma(qk[4], xa[4], sa);
        float ub = sel4(cb, n & 3);
        asm volatile("" : "+v"(sa), "+v"(ub));
        SB();
        sa = mfma(qk[5], xa[5], sa);
        const f32x2 X = swap32(ua, ub);
        float mn_ = (X[1] + Y[1]) * (1.f / C), q1_ = fmaf(2.f, X[0], Y[0]);
        asm volatile("" : "+v"(sa), "+v"(mn_), "+v"(q1_));
        SB();
        sb = mfma(qk[4], xb[4], sb);
        float vb = sel4(gb, n & 3);
        asm volatile("" : "+v"(sb), "+v"(vb));
        SB();
        sb = mfma(qk[5], xb[5], sb);
        const f32x2 Mn = swap32(mn_, mn_), G = swap32(va, vb);
        q1_ += n < 8 ? G[0] : G[1];
        const f32x2 Q = swap16(q1_, q1_);
        sa -= Mn[0] * qs;
        const float var = fmaxf(fmaf(-mn_, mn_, fmaf((n & 4) ? Q[1] : Q[0], 1.f / C, EPS)), EPS), rs_ = __builtin_amdgcn_rsqf(var);
        sb -= Mn[1] * qs;
        const float v_ = (g & 1) ? var * rs_ : mn_, h_ = bfhi(__float_as_uint(v_));
        *(LAS unsigned*)prc = cvtpk(h_, v_ - h_);
        const f32x2 R = swap32(rs_, rs_);
        sa = sa * R[0] + nref; sb = sb * R[1] + nref;
        const float tm = fmaxf(fmaxf(fmaxf(fmaxf(sa[0], sa[1]), sa[2]), fmaxf(fmaxf(sa[3], sb[0]), sb[1])), fmaxf(sb[2], sb[3]));
        if (__builtin_expect(__builtin_amdgcn_ballot_w64(!(tm <= thr)) != 0ull, 0)) {
#pragma unroll
          for (int r = 0; r < 4; ++r) {
            const float mx = row_max(fmaxf(sa[r], sb[r])), dm = mx <= 16.f ? 0.f : mx;
            nref[r] -= dm; sa[r] -= dm; sb[r] -= dm;
          }
          thr = __builtin_nanf("");
          word |= 1u;
          if (n == 0) *(LAS f32x4*)(lds + LDS_PK + pk * PK_B + 1280 + 16 * g) = -nref;
        }
#pragma unroll
        for (int r = 0; r < 4; ++r) { sa[r] = __builtin_amdgcn_exp2f(sa[r]); sb[r] = __builtin_amdgcn_exp2f(sb[r]); }
        float ra = R[0], rb = R[1];
        asm volatile("" : "+v"(ra), "+v"(rb));
        sa *= ra; sb *= rb;
        *(LAS u32x2*)pw = u32x2{cvtpk(sa[0], sa[1]), cvtpk(sa[2], sa[3])};
        *(LAS u32x2*)(pw + 128) = u32x2{cvtpk(sb[0], sb[1]), cvtpk(sb[2], sb[3])};
        lds_put(CT_PRDY, pfl, word);
        SB();
        if (s < NT - 8) __builtin_amdgcn_s_sleep(THR);
      }
      GET_TAG();
      LAS char* fin = lds + xslot + 8192;
      unsigned qres = (unsigned)__builtin_amdgcn_raw_buffer_load_b16(mkrs(q, NQ * C * 2), dout * 2, 0, 0) << 16;
      unsigned ao = ((tid & 255) >> 1) * 8192 + (tid & 1) * 16 + dst * 32, tw = (unsigned)(tg >> (20 * (tid & 1))), sh = 12 * (tid & 1), oo = dout * 2;
      LAS char *fw = fin + (((lane & 8) | ((g & 1) << 2) | (g & 2)) + (lane & 1)) * 16 + 4 * (w - 4), *fr = fin + (2 * (lane & 7) + ((lane >> 3) & 1)) * 16;
      float v7 = 0.f;
      unsigned fi_ = 8u;
      const unsigned t0s = lds_get(ctl + CT_T0);
      unsigned gate = (t0s ? t0s : (unsigned)tstart) + RGATE;
      u32x4 ro = mkrs4(out, NQ * C * 2), rm4 = mkrs4(ws, WS_BYTES);
      unsigned half = 128 * 8192;
      unsigned long long wm = 0x0303030303030303ull;
      asm volatile("" : "+v"(ao), "+v"(tw), "+v"(sh), "+v"(oo), "+v"(fw), "+v"(fr), "+v"(fin), "+v"(v7), "+v"(qres), "+v"(fi_), "+s"(ro), "+s"(wm), "+s"(rm4), "+s"(half));
      if (w == 4) asm volatile("s_mov_b64 exec, 0x1515\n\tbuffer_store_dword %0, %1, %2, 0 offen sc1\n\ts_mov_b64 exec, -1" :: "v"(qres), "v"(oo), "s"(ro) : "memory");
      lds_wait(ctl + CT_XD, 1);
      lds_spin(CT_ST, ctl, 4);
      gate += max((int)((unsigned)wall_clock64() + OWNG - gate), 0);
      float org = *(LAS const float*)(lds + xslot + 6144 + 4 * dq);
      *(LAS float*)(fin + 256 + 4 * (w - 4)) = org;
#pragma unroll 1
      for (int spin = 0; spin < 1024; ++spin)
        if ((int)((unsigned)wall_clock64() - gate) >= 0) break;
      u32x4 A, B;
      asm volatile("buffer_load_dwordx4 %0, %2, %3, 0 offen sc1\n\tbuffer_load_dwordx4 %1, %2, %3, %4 offen sc1\n\ts_waitcnt vmcnt(0)"
                   : "=&v"(A), "=&v"(B) : "v"(ao), "s"(rm4), "s"(half) : "memory");
      if (__builtin_expect(__builtin_amdgcn_ballot_w64((((A[3] ^ tw) | (B[3] ^ tw)) & 0xfffffu) != 0u) != 0ull, 1)) {
#pragma unroll 1
        for (int spin = 0; spin < (1 << 18); ++spin) {
          unsigned xa_ = (A[3] ^ tw) << 12, xb_ = (B[3] ^ tw) << 12;
          if (__builtin_amdgcn_ballot_w64((xa_ | xb_) != 0u) == 0ull) break;
          xa_ |= __builtin_amdgcn_mov_dpp(xa_, 0xB1, 0xf, 0xf, true); xb_ |= __builtin_amdgcn_mov_dpp(xb_, 0xB1, 0xf, 0xf, true);
          asm volatile("" ::: "memory");
          if (xa_ != 0u) A = ldb128<16>(rws, ao, 0);
          if (xb_ != 0u) B = ldb128<16>(rws, ao, 128 * 8192);
        }
      }
      unsigned la = (A[3] & 0xfff00000u) >> sh, lb_ = (B[3] & 0xfff00000u) >> sh;
      la |= __builtin_amdgcn_mov_dpp(la, 0xB1, 0xf, 0xf, true); lb_ |= __builtin_amdgcn_mov_dpp(lb_, 0xB1, 0xf, 0xf, true);
      float da = __uint_as_float(la) - org, db = __uint_as_float(lb_) - org;
      f32x2 ua_[3], ub_[3];
#pragma unroll
      for (int i = 0; i < 3; ++i) { ua_[i] = f32x2{bflo(A[i]), bfhi(A[i])}; ub_[i] = f32x2{bflo(B[i]), bfhi(B[i])}; asm volatile("" : "+v"(ua_[i]), "+v"(ub_[i])); }
      if (__builtin_expect(__builtin_amdgcn_ballot_w64(!(fmaxf(da, db) <= 64.f)) != 0ull, 0)) {
        org = xrow16_max(row_max(fmaxf(__uint_as_float(la), __uint_as_float(lb_))));
        da = __uint_as_float(la) - org; db = __uint_as_float(lb_) - org;
        *(LAS float*)(fin + 256 + 4 * (w - 4)) = org;
        fi_ = 9u;
      }
      const float wa = __builtin_amdgcn_exp2f(da), wb = __builtin_amdgcn_exp2f(db);
#pragma unroll
      for (int i = 0; i < 3; ++i) ua_[i] = ua_[i] * wa + ub_[i] * wb;
      const f32x2 p0 = swap32(ua_[0][0], ua_[0][1]), p1 = swap32(ua_[1][0], ua_[1][1]), p2 = swap32(ua_[2][0], ua_[2][1]), p3 = swap32(wa + wb, v7);
      const f32x2 r0 = swap16(p0[0] + p0[1], p1[0] + p1[1]), r1 = swap16(p2[0] + p2[1], p3[0] + p3[1]);
      float c0 = r0[0] + r0[1], c1 = r1[0] + r1[1];
      asm volatile("s_nop 1\n\tv_add_f32_dpp %0, %0, %0 row_ror:8 row_mask:0xf bank_mask:0x3\n\tv_add_f32_dpp %0, %1, %1 row_ror:8 row_mask:0xf bank_mask:0xc\n\t"
                   "s_nop 1\n\tv_add_f32_dpp %0, %0, %0 row_shl:4 row_mask:0xf bank_mask:0x5\n\t"
                   "s_nop 1\n\tv_add_f32_dpp %0, %0, %0 quad_perm:[2,3,0,1] row_mask:0xf bank_mask:0x5\n\t"
                   "s_mov_b64 exec, %3\n\tds_write_b32 %2, %0\n\ts_mov_b64 exec, 1\n\tds_add_u32 %4, %5 offset:%6\n\ts_mov_b64 exec, -1"
                   : "+v"(c0) : "v"(c1), "v"(fw), "s"(wm), "v"(ctl), "v"(fi_), "i"(CT_FIN) : "memory");
      if (w == 4) {
        f32x4 a0, l0;
        unsigned cf;
        int cnt_, c_;
        asm volatile("s_mov_b32 %1, 0x4000000\n1:\n\tds_read_b32 %2, %5 offset:%8\n\tds_read_b128 %3, %6\n\tds_read_b128 %4, %7 offset:192\n\t"
                     "s_waitcnt lgkmcnt(2)\n\tv_readfirstlane_b32 %0, %2\n\ts_cmp_ge_i32 %0, 32\n\ts_cbranch_scc1 2f\n\t"
                     "s_sub_u32 %1, %1, 1\n\ts_cbranch_scc0 1b\n2:\n\ts_waitcnt lgkmcnt(0)"
                     : "=&s"(c_), "=&s"(cnt_), "=&v"(cf), "=&v"(a0), "=&v"(l0) : "v"(ctl), "v"(fr), "v"(fin), "i"(CT_FIN) : "memory", "scc");
        asm volatile("" : "+s"(c_));
        if (__builtin_expect(c_ != 32, 0)) {
          f32x4 m0 = *(LAS const f32x4*)(fin + 256);
          const float mm = fmaxf(fmaxf(m0[0], m0[1]), fmaxf(m0[2], m0[3]));
#pragma unroll
          for (int p = 0; p < 4; ++p) m0[p] = __builtin_amdgcn_exp2f(m0[p] - mm);
          a0 *= m0; l0 *= m0;
        }
        const float o = __uint_as_float(qres) + ((a0[0] + a0[2]) + (a0[1] + a0[3])) * __builtin_amdgcn_rcpf((l0[0] + l0[2]) + (l0[1] + l0[3]));
        const unsigned ob = cvtpk(o, __uint_as_float(__builtin_amdgcn_mov_dpp(__float_as_uint(o), 0xB1, 0xf, 0xf, true)));
        asm volatile("s_mov_b64 exec, 0x1515\n\tbuffer_store_dword %0, %1, %2, 0 offen sc1\n\ts_mov_b64 exec, -1" :: "v"(ob), "v"(oo), "s"(ro) : "memory");
        if (wg == 0 && lane == 0) stb64(u32x2{(unsigned)tg, (unsigned)(tg >> 32)}, rws, EP_OFF);
      }
    } else {
      const int ftr = ((g & 1) << 2) | ((tq >> 1) << 1) | (g >> 1);
      const int fty = (g & 1) * 4 + (tq >> 1) * 3;
      int otr[4], oty[4];
#pragma unroll
      for (int t = 0; t < 4; ++t) {
        otr[t] = (w - 1) * PART + 128 * (4 * g + tq) + 16 * ((2 * t + (tp >> 1)) ^ ftr) + 8 * (tp & 1);
        oty[t] = CRS + (w - 1) * 128 + 384 * (4 * (g & 1) + tq) + 16 * ((2 * t + (tp >> 1)) ^ fty) + 8 * (tp & 1);
      }
      int opk = LDS_PK + 320 * tp + 8 * (4 * g + tq), ork = LDS_PK + 320 * (g >> 1) + 256 + 8 * (4 * (g & 1) + tq);
      asm volatile("" : "+v"(opk), "+v"(ork), "+v"(oty[0]), "+v"(oty[1]), "+v"(oty[2]), "+v"(oty[3]));
#define RD_T(ta, ty, so_) _Pragma("unroll") for (int t_ = 0; t_ < 4; ++t_) { \
    ta[t_] = vtr2(lds + otr[t_] + (so_), lds + otr[t_] + (so_) + 2048); ty[t_] = vtr(lds + oty[t_] + (so_)); }
      f32x4 acc[4] = {z, z, z, z}, tac = z;
#define RD_P(o_) \
      pf = vtr2(lds + opk + (o_), lds + opk + (o_) + 128); \
      sf = vtr2(lds + ork + (o_), lds + ork + (o_) + 640)
#define TWICE(ty) asm volatile("s_nop 2"); _Pragma("unroll") for (int t = 0; t < 4; ++t) { u32x2 lo_, hi_; \
      asm volatile("v_mov_b64 %0, %2\n\tv_mov_b64 %1, %2" : "=&v"(lo_), "=&v"(hi_) : "v"(ty[t])); t2[t] = u32x4{lo_[0], lo_[1], hi_[0], hi_[1]}; } \
      asm volatile("s_nop 1" : "+v"(t2[0]), "+v"(t2[1]), "+v"(t2[2]), "+v"(t2[3]))
#define MF_PX(ta) \
      tac = mfma(sf, pf, tac); \
      _Pragma("unroll") for (int t = 0; t < 4; ++t) acc[t] = mfma(ta[t], pf, acc[t])
#define MF_PY() \
      _Pragma("unroll") for (int t = 0; t < 4; ++t) acc[t] = mfma(t2[t], pf, acc[t])
#define MOVE_REF(word_, o_) do { moved |= (word_) & 1; if (__builtin_expect(moved, 0)) { \
            const float m_ = *(LAS const float*)(lds + (((word_) & 1) ? LDS_PK + (o_) + 1280 : LDS_CTL + CT_MAX) + 4 * n); \
            Mtop = fmaxf(Mtop, m_); \
            const bool drop = m_ < Mtop - 64.f; \
            const float mn = drop ? M : m_, fa = __builtin_amdgcn_exp2f(M - mn); \
            M = mn; \
            _Pragma("unroll") for (int t = 0; t < 4; ++t) acc[t] *= fa; \
            tac *= fa; \
            if (drop) pf = u32x4{0u, 0u, 0u, 0u}; \
          } } while (0)
      const unsigned flo = FLAG_OFF + (wg & 7) * 768 + (w - 1) * 144 + (lane >= 16 ? min(lane - 16, 35) : (lane >> 3) * 6 + (lane & 7) + ((lane & 7) < 6 ? 0 : 18)) * 8;
      float M, Mtop;
      int moved = 0;
      u32x4 wvv[18], pf, sf, t2[4];
      {
        u32x4 ta[8][4], xx[6];
        u32x2 ty[8][4], fl;
        unsigned cc[6];
        const int qo = QK_OFF + (wg & 3) * QK_COPY + (2 * w - 2) * QK_REC;
#define QK_LOAD() _Pragma("unroll") for (int j = 0; j < 6; ++j) { \
          if ((j == 2 || j == 4) && early) __builtin_amdgcn_s_sleep(QSJ); \
          xx[j] = ldb128<16>(rws, lane * 16, qo + ((j >> 1) * 6 + (j & 1)) * QK_REC); \
          cc[j] = __builtin_amdgcn_raw_buffer_load_b32(rws, lane * 4, qo + ((j >> 1) * 6 + (j & 1)) * QK_REC + 1024, 16); }
#pragma unroll
        for (int u = 0; u < 8; ++u) {
          if (u == QKAT) {
            GET_TAG();
#pragma unroll 1
            for (int r = 0; r < 8; ++r) {
#pragma unroll 1
              for (int spin = 0; spin < 128; ++spin) {
                if ((int)(wall_clock64() - tstart) >= GATE + r * HSTEP + (w - 1) * QSW) break;
                __builtin_amdgcn_s_sleep(1);
              }
              u32x8 hv;
              asm volatile("s_load_dwordx8 %0, %1, %2\n\ts_waitcnt lgkmcnt(0)" : "=&s"(hv) : "s"(ws), "s"(HINT_OFF + ((wg & 7) + 8 * r) * 128 + (w - 1) * 32) : "memory");
              const unsigned t_ = (unsigned)tg;
              if ((((hv[0] ^ t_) | (hv[1] ^ t_) | (hv[2] ^ t_)) | ((hv[3] ^ t_) | (hv[4] ^ t_) | (hv[5] ^ t_))) == 0u) break;
            }
            bool early = (int)(wall_clock64() - tstart) < QLATE;
            if (wg < NPR) lds_wait(ctl + CT_PD, 4);
            QK_LOAD();
#pragma unroll 1
            for (int spin = 0; spin < (1 << 18); ++spin) {
              unsigned df = 0u;
#pragma unroll
              for (int j = 0; j < 6; ++j) df |= qk_check(xx[j], (unsigned)tg) ^ cc[j];
              if (__all(df == 0u)) break;
              asm volatile("" ::: "memory");
              early = false;
              QK_LOAD();
            }
#pragma unroll
            for (int h = 0; h < 2; ++h) {
              u32x4 sx;
#pragma unroll
              for (int i = 0; i < 4; ++i) sx[i] = add3(xx[h][i], xx[2 + h][i], xx[4 + h][i]);
              *(LAS u32x4*)(lds + LDS_QK + (2 * w - 2 + h) * 1024 + lane * 16) = sx;
            }
            lds_inc(CT_QK, ctl);
            SB();
          }
          lds_wait(ctl + CT_FULL, 3 * u + w);
          RD_T(ta[u], ty[u], u * SLOT);
          lds_inc(CT_FREE + 4 * u, ctl);
          SB();
        }
        fl = __builtin_bit_cast(u32x2, __builtin_amdgcn_raw_buffer_load_b64(rws, flo, 0, 16));
#pragma unroll
        for (int u = 0; u < 12; ++u) {
          const int word = lds_spin(CT_PRDY + 4 * (u % NPK), ctl, 2 * u + 2);
          if (u == 0) Mtop = M = *(LAS const float*)(ctl + CT_MAX + 4 * n);
          RD_P((u % NPK) * PK_B);
          if (u >= 8) MOVE_REF(word, (u % NPK) * PK_B);
          lds_inc(CT_PCON + 4 * (u % NPK), ctl);
          SB();
          MF_PX(ta[u & 7]);
          SB();
          TWICE(ty[u & 7]);
          MF_PY();
          asm volatile("" : "+v"(acc[0]), "+v"(acc[1]), "+v"(acc[2]), "+v"(acc[3]), "+v"(tac));
          SB();
#define FLAGS_UP() __all(fl[0] == (unsigned)tg && (fl[1] & 255u) == (unsigned)(tg >> 32))
          if (u == T0AT && w == 1 && FLAGS_UP()) {
            int d = (int)(fl[1] - ((unsigned)tstart << 8)) >> 8;
            d += __builtin_amdgcn_mov_dpp(d, XOR8, 0xf, 0xf, true); d += __builtin_amdgcn_mov_dpp(d, ROR4, 0xf, 0xf, true);
            d += __builtin_amdgcn_mov_dpp(d, 0x122, 0xf, 0xf, true); d += __builtin_amdgcn_mov_dpp(d, 0x121, 0xf, 0xf, true);
            lds_put(CT_T0, ctl, ((unsigned)tstart + min(max(d >> 4, -300), 300)) | 1u);
          }
          if (u < 4) {
            NEED(3 * (u + 8) + w);
            RD_T(ta[u], ty[u], ((u + 8) % NS) * SLOT);
            lds_inc(CT_FREE + 4 * ((u + 8) % NS), ctl);
            SB();
          }
        }
#pragma unroll 1
        for (int spin = 0; spin < (1 << 18); ++spin) {
          if (FLAGS_UP()) break;
          asm volatile("" ::: "memory");
          fl = __builtin_bit_cast(u32x2, __builtin_amdgcn_raw_buffer_load_b64(rws, flo, 0, 16));
        }
        asm volatile("" ::: "memory");
        if (w == 3) lds_put(CT_WV, ctl, 1);
      }
      int slot = 3, pk = 12 % NPK;
      unsigned rdy = 0u;
      const int kro[3] = {(w - 1) * 2048, (w == 3 ? 0 : w) * 2048, (w == 1 ? 2 : w - 2) * 2048};
#define RD_PACKET(z_) do { int cnt_; u32x2 p0_, p1_, p2_, p3_; \
        word = __builtin_amdgcn_readfirstlane(rdy); \
        asm volatile("s_mov_b32 %1, 0x400000\n1:\n\tds_read_b32 %2, %7 offset:%11\n\t" \
                     "ds_read_b64_tr_b16 %5, %9\n\tds_read_b64_tr_b16 %6, %9 offset:640\n\t" \
                     "ds_read_b64_tr_b16 %3, %8\n\tds_read_b64_tr_b16 %4, %8 offset:128\n\t" \
                     "s_cmp_ge_i32 %0, %10\n\ts_cbranch_scc1 2f\n\ts_waitcnt lgkmcnt(4)\n\tv_readfirstlane_b32 %0, %2\n\t" \
                     "s_cmp_ge_i32 %0, %10\n\ts_cbranch_scc1 2f\n\t" z_ "s_sub_u32 %1, %1, 1\n\ts_cbranch_scc0 1b\n2:" \
                     : "+s"(word), "=&s"(cnt_), "=&v"(rdy), "=&v"(p0_), "=&v"(p1_), "=&v"(p2_), "=&v"(p3_) \
                     : "v"(ctl + 4 * pk), "v"(lds + opk + pk * PK_B), "v"(lds + ork + pk * PK_B), "s"(2 * s + 2), "i"(CT_PRDY), "i"(CALM) : "memory", "scc"); \
        asm volatile("" : "+s"(word)); \
        pf = u32x4{p0_[0], p0_[1], p1_[0], p1_[1]}; sf = u32x4{p2_[0], p2_[1], p3_[0], p3_[1]}; } while (0)
#define GOT_PACKET() do { waitlds<0>(); asm volatile("" : "+v"(pf), "+v"(sf) : "v"(rdy)); MOVE_REF(word, pk * PK_B); } while (0)
#define RD_Y(t_, p_) do { const u32x2 lo_ = vtr(p_); asm volatile("" ::: "memory"); const u32x2 hi_ = vtr(p_); \
        t2[t_] = u32x4{lo_[0], lo_[1], hi_[0], hi_[1]}; } while (0)
      u32x4 ta[4];
      int s = 12, word;
      NEED(36 + w);
#pragma unroll
      for (int t = 0; t < 4; ++t) { ta[t] = vtr2(lds + otr[t] + 2 * SLOT, lds + otr[t] + 2 * SLOT + 2048); RD_Y(t, lds + oty[t] + 2 * SLOT); }
      lds_inc(CT_FREE + 8, ctl);
#pragma unroll 1
      for (; s < NT - 1; ++s) {
        RD_PACKET("s_sleep %12\n\t");
        LAS const char *px[4], *py[4];
#pragma unroll
        for (int t = 0; t < 4; ++t) { px[t] = lds + otr[t] + slot * SLOT; py[t] = lds + oty[t] + slot * SLOT; asm volatile("" : "+v"(px[t]), "+v"(py[t])); }
        NEED(3 * s + 3 + w);
        GOT_PACKET();
        SB();
        tac = mfma(sf, pf, tac);
        asm volatile("" : "+v"(tac));
        SB();
        lds_inc(CT_PCON, ctl + 4 * pk);
        pk = pk == NPK - 1 ? 0 : pk + 1;
#pragma unroll
        for (int t = 0; t < 4; ++t) { acc[t] = mfma(ta[t], pf, acc[t]); SB(); ta[t] = vtr2(px[t], px[t] + 2048); SB(); }
        rdy = *(volatile LAS unsigned*)(ctl + CT_PRDY + 4 * pk);
#pragma unroll
        for (int t = 0; t < 4; ++t) { acc[t] = mfma(t2[t], pf, acc[t]); SB(); RD_Y(t, py[t]); SB(); }
        lds_inc(CT_FREE, ctl + 4 * slot);
#define WV_LOAD(j_) wvv[j_] = ldb128<16>(rws, lane * 16 + ((j_) & 1) * 1024, WVO_OFF + (w - 1) * 18432 + ((j_) / 6) * 6144 + kro[((j_) % 6) >> 1])
        if ((unsigned)(s - WVS) < WV1) {
#pragma unroll
          for (int u = 0; u < WV1; ++u)
            if (s - WVS == u) WV_LOAD(u);
        }
        if (s == NT - 3) {
#pragma unroll
          for (int j = WV1; j < 18; ++j) WV_LOAD(j);
        }
        slot = slot == NS - 1 ? 0 : slot + 1;
      }
      LAS char *xw = lds + xslot + lane * 16 + kro[0], *xl = lds + xslot + 6144 + 4 * n;
      LAS const char *xr1 = lds + xslot + lane * 16 + kro[1], *xr2 = lds + xslot + lane * 16 + kro[2];
      unsigned mo = wg * 8192 + (w - 1) * 2048 + n * 64 + g * 16, tw = (unsigned)(tg >> (20 * (g & 1))) & 0xfffffu, sh = 12 * (g & 1);
      asm volatile("" : "+v"(xw), "+v"(xl), "+v"(xr1), "+v"(xr2), "+v"(mo), "+v"(tw), "+v"(sh));
      __builtin_amdgcn_s_setprio(2);
      RD_PACKET("");
      GOT_PACKET();
      SB();
      tac = mfma(sf, pf, tac);
      asm volatile("" : "+v"(tac));
      SB();
#pragma unroll
      for (int t = 0; t < 2; ++t) acc[t] = mfma(t2[t], pf, mfma(ta[t], pf, acc[t]));
      SB();
#pragma unroll
      for (int t = 2; t < 4; ++t) acc[t] = mfma(t2[t], pf, mfma(ta[t], pf, acc[t]));
      const float L = tac[2] + tac[3], rl = __builtin_amdgcn_rcpf(L), ct = -(tac[0] + tac[1]) * rl;
      const float lse = M + __builtin_amdgcn_logf(L);
      u32x4 b[6];
      b[0] = pack8(acc[0] * rl + ct, acc[1] * rl + ct);
      *(LAS u32x4*)xw = b[0];
      *(LAS float*)xl = lse;
#pragma unroll
      for (int j = 0; j < 4; ++j) { __builtin_amdgcn_sched_group_barrier(0x008, 1, 0); __builtin_amdgcn_sched_group_barrier(0x002, 3, 0); }
      SB();
      b[1] = pack8(acc[2] * rl + ct, acc[3] * rl + ct);
      *(LAS u32x4*)(xw + 1024) = b[1];
      lds_inc(CT_XD, ctl);
      SB();
      f32x4 o[3];
      unsigned xd;
#pragma unroll
      for (int t = 0; t < 3; ++t) o[t] = mfma(wvv[6 * t], b[0], z);
      asm volatile("" : "+v"(o[0]), "+v"(o[1]), "+v"(o[2]));
      SB();
#define RD_SLICES "ds_read_b32 %0, %7 offset:%10\n\tds_read_b128 %1, %8\n\tds_read_b128 %2, %8 offset:1024\n\tds_read_b128 %3, %9\n\tds_read_b128 %4, %9 offset:1024\n"
      asm volatile(RD_SLICES : "=&v"(xd), "=&v"(b[2]), "=&v"(b[3]), "=&v"(b[4]), "=&v"(b[5]) : "i"(0), "i"(0), "v"(ctl), "v"(xr1), "v"(xr2), "i"(CT_XD) : "memory");
      SB();
#pragma unroll
      for (int t = 0; t < 3; ++t) o[t] = mfma(wvv[6 * t + 1], b[1], o[t]);
      tw |= (__float_as_uint(lse) << sh) & 0xfff00000u;
      asm volatile("" : "+v"(tw), "+v"(o[0]), "+v"(o[1]), "+v"(o[2]));
      SB();
      {
        int cnt_, c_;
        asm volatile("s_mov_b32 %6, 0x400000\n\ts_branch 2f\n1:\n\t" RD_SLICES "2:\n\ts_waitcnt lgkmcnt(4)\n\tv_readfirstlane_b32 %5, %0\n\t"
                     "s_cmp_ge_i32 %5, 3\n\ts_cbranch_scc1 3f\n\ts_sub_u32 %6, %6, 1\n\ts_cbranch_scc0 1b\n3:\n\ts_waitcnt lgkmcnt(0)"
                     : "+v"(xd), "+v"(b[2]), "+v"(b[3]), "+v"(b[4]), "+v"(b[5]), "=&s"(c_), "=&s"(cnt_)
                     : "v"(ctl), "v"(xr1), "v"(xr2), "i"(CT_XD) : "memory", "scc");
      }
      SB();
      PROJECT(2);
      lds_inc(CT_ST, ctl);
    }
  }
}

#undef rws
#define WS_AT(size_) ((size_) > (64u << 20) ? ((size_) - (64u << 20)) & ~(size_t)0x1fffff : 0)
extern "C" void kernel_launch(void* const* d_in, const int* in_sizes, int n_in, void* d_out, int out_size, void* d_ws, size_t ws_size,
                              hipStream_t stream) {
  dino_main<<<NWG, 512, 0, stream>>>((const u16*)d_in[0], (const u16*)d_in[1], (const u16*)d_in[2], (const u16*)d_in[3], (const u16*)d_in[4],
                                     (const u16*)d_in[5], (const u16*)d_in[6], (char*)d_ws + WS_AT(ws_size) + 4096, (u16*)d_out);
}
```

```cpp
#include <hip/hip_runtime.h>
#include <hip/hip_bf16.h>
#include <stdint.h>

typedef unsigned short u16;
using u32x2 = __attribute__((ext_vector_type(2))) unsigned;
using u32x4 = __attribute__((ext_vector_type(4))) unsigned;
using u32x8 = __attribute__((ext_vector_type(8))) unsigned;
using f32x2 = __attribute__((ext_vector_type(2))) float;
using f32x4 = __attribute__((ext_vector_type(4))) float;
using s16x8 = __attribute__((ext_vector_type(8))) short;
using bf16x2_t = __attribute__((ext_vector_type(2))) __bf16;
typedef short v4i16_t __attribute__((ext_vector_type(4)));
#define LAS __attribute__((address_space(3)))

constexpr int C = 192, NQ = 16, W0 = 512, W1 = 256, NWG = 256;
constexpr float EPS = 1e-5f;
constexpr float QSCALE = 0.07216878364870322f * 1.4426950408889634f;

constexpr int PART = 4096, CRS = 3 * PART;
constexpr int SLOT = CRS + 3072, NS = 10;
constexpr int LDS_QK = NS * SLOT;
constexpr int NPK = 7;
constexpr int LDS_PK = LDS_QK, PK_B = 1344;
constexpr int LDS_CTL = LDS_PK + NPK * PK_B;
constexpr int LDS_SINK = LDS_CTL + 384;
constexpr int LDS_BYTES = LDS_SINK + 256;
constexpr int PSCR = 8 * SLOT;
constexpr int CT_FULL = 8, CT_WV = 4, CT_PA = 0, CT_PB = 12, CT_QR = 16, CT_PD = 20, CT_T0 = 24, CT_XD = 28, CT_TG = 32, CT_QK = 40, CT_ST = 44,
              CT_FREE = 48, CT_FIN = 88, CT_ONE = 112, CT_PRDY = 128, CT_PCON = 160, CT_MAX = 256;

constexpr int MB_BYTES = NWG * NWG * 32;
constexpr int EP_OFF = MB_BYTES;
constexpr int FLAG_OFF = EP_OFF + 512;
constexpr int WVO_OFF = EP_OFF + 8192;
constexpr int QK_OFF = WVO_OFF + C * C * 2;
constexpr int QK_REC = 1280, QK_COPY = 18 * QK_REC;
constexpr int HINT_OFF = QK_OFF + 4 * QK_COPY;
constexpr int WS_BYTES = HINT_OFF + 64 * 128;

#define GIVE 2
constexpr int NPR = 36;
struct Tabs { unsigned seq[112]; };
constexpr Tabs make_tabs() {
  Tabs t{};
  for (int i = 0; i < 112; ++i) t.seq[i] = i + 1;
  return t;
}
__device__ const Tabs tabs = make_tabs();
#define QKAT 3
#define WV1 6
#define WMIN 28
#define WSTEP 5
#define LSTEP 40
#define OWNG 30
#define PACE0 30
#define PACE1 1742
#define RGATE 1922
#define T0AT 3
#define WVS 12
#define CALM 2
#define QSW 12
#define QSJ 12
#define THR 6
#define QLATE 450
#define COLD0 108
#define COLDG 8
#define COLDM 48
#define THL 800
#define THS 3
#define HSTEP 40
#define GATE 350
#define CODE_BYTES 26240

__device__ __forceinline__ unsigned cvtpk(float lo, float hi) {
  bf16x2_t v = {(__bf16)lo, (__bf16)hi};
  return __builtin_bit_cast(unsigned, v);
}
__device__ __forceinline__ float bflo(unsigned x) { return __builtin_bit_cast(float, x << 16); }
__device__ __forceinline__ float bfhi(unsigned x) { return __builtin_bit_cast(float, x & 0xffff0000u); }
template <int CTRL> __device__ __forceinline__ float dpp(float x) {
  return __builtin_bit_cast(float, __builtin_amdgcn_mov_dpp(__builtin_bit_cast(int, x), CTRL, 0xf, 0xf, true));
}
constexpr int XOR8 = 0x128, ROR4 = 0x124;
template <bool HI8> __device__ __forceinline__ float half_copy(float x) {
  const int v = __builtin_bit_cast(int, x);
  return __builtin_bit_cast(float, __builtin_amdgcn_update_dpp(v, v, XOR8, 0xf, HI8 ? 0xc : 0x3, false));
}
__device__ __forceinline__ float row_max(float x) {
  x = fmaxf(x, dpp<XOR8>(x)); x = fmaxf(x, dpp<ROR4>(x)); x = fmaxf(x, dpp<0x122>(x));
  return fmaxf(x, dpp<0x121>(x));
}
__device__ __forceinline__ f32x2 swap32(float a, float b) {
  auto s = __builtin_amdgcn_permlane32_swap(__float_as_uint(a), __float_as_uint(b), false, false);
  return f32x2{__uint_as_float(s[0]), __uint_as_float(s[1])};
}
__device__ __forceinline__ f32x2 swap16(float a, float b) {
  auto s = __builtin_amdgcn_permlane16_swap(__float_as_uint(a), __float_as_uint(b), false, false);
  return f32x2{__uint_as_float(s[0]), __uint_as_float(s[1])};
}
__device__ __forceinline__ float xrow16_max(float x) {
  auto s = __builtin_amdgcn_permlane16_swap(__float_as_uint(x), __float_as_uint(x), false, false);
  x = fmaxf(__uint_as_float(s[0]), __uint_as_float(s[1]));
  auto t = __builtin_amdgcn_permlane32_swap(__float_as_uint(x), __float_as_uint(x), false, false);
  return fmaxf(__uint_as_float(t[0]), __uint_as_float(t[1]));
}
__device__ __forceinline__ float xrow16_sum(float x) {
  auto s = __builtin_amdgcn_permlane16_swap(__float_as_uint(x), __float_as_uint(x), false, false);
  x = __uint_as_float(s[0]) + __uint_as_float(s[1]);
  auto t = __builtin_amdgcn_permlane32_swap(__float_as_uint(x), __float_as_uint(x), false, false);
  return __uint_as_float(t[0]) + __uint_as_float(t[1]);
}
__device__ __forceinline__ f32x4 mfma(u32x4 a, u32x4 b, f32x4 c) {
  return __builtin_amdgcn_mfma_f32_16x16x32_bf16(__builtin_bit_cast(s16x8, a), __builtin_bit_cast(s16x8, b), c, 0, 0, 0);
}
__device__ __forceinline__ u32x2 vtr(LAS const char* p) {
  return __builtin_bit_cast(u32x2, __builtin_amdgcn_ds_read_tr16_b64_v4i16((LAS v4i16_t*)p));
}
__device__ __forceinline__ u32x4 vtr2(LAS const char* p, LAS const char* q_) {
  const u32x2 a = vtr(p), b = vtr(q_);
  return u32x4{a[0], a[1], b[0], b[1]};
}
__device__ __forceinline__ float sel4(f32x4 v, int r) {
  const float a = (r & 1) ? v[1] : v[0], b = (r & 1) ? v[3] : v[2];
  return (r & 2) ? b : a;
}
using RSRC = __amdgpu_buffer_rsrc_t;
__device__ __forceinline__ RSRC mkrs(const void* p, int bytes) { return __builtin_amdgcn_make_buffer_rsrc(const_cast<void*>(p), (short)0, bytes, 0x00020000); }
__device__ __forceinline__ u32x4 mkrs4(const void* p, unsigned bytes) {
  const unsigned long long b = (unsigned long long)p;
  return u32x4{(unsigned)b, (unsigned)(b >> 32) & 0xffffu, bytes, 0x00020000u};
}
template <int AUX> __device__ __forceinline__ u32x4 ldb128(RSRC r, unsigned vo, int so) { return __builtin_bit_cast(u32x4, __builtin_amdgcn_raw_buffer_load_b128(r, vo, so, AUX)); }
#define SB() __builtin_amdgcn_sched_barrier(0)
#define RAW_BARRIER() asm volatile("s_waitcnt lgkmcnt(0)\n\ts_barrier" ::: "memory")
#define DMA(m0_, vo_, rs_, so_) asm volatile("s_mov_b32 m0, %0\n\ts_nop 0\n\tbuffer_load_dwordx4 %1, %2, %3 offen lds" :: "s"(m0_), "v"(vo_), "s"(rs_), "s"(so_) : "memory")
template <int N> __device__ __forceinline__ void waitlds() { __builtin_amdgcn_s_waitcnt(0xc07f | (N << 8)); }
template <int N> __device__ __forceinline__ void waitvm() { asm volatile("s_waitcnt vmcnt(%0)" :: "i"(N) : "memory"); }


__device__ __forceinline__ void lds_inc(int O, LAS char* p) {
  asm volatile("s_mov_b64 exec, 1\n\tds_add_u32 %0, %1 offset:%2\n\ts_mov_b64 exec, -1" :: "v"(p), "v"(1u), "i"(O) : "memory");
}
__device__ __forceinline__ void lds_put(int O, LAS char* p, unsigned v) {
  asm volatile("s_mov_b64 exec, 1\n\tds_write_b32 %0, %1 offset:%2\n\ts_mov_b64 exec, -1" :: "v"(p), "v"(v), "i"(O) : "memory");
}
__device__ __forceinline__ int lds_get(LAS char* p) {
  asm volatile("" ::: "memory");
  const unsigned v = __hip_atomic_load((LAS unsigned*)p, __ATOMIC_RELAXED, __HIP_MEMORY_SCOPE_WORKGROUP);
  asm volatile("" ::: "memory");
  return __builtin_amdgcn_readfirstlane(v);
}
__device__ __forceinline__ void lds_wait(LAS char* p, int target) {
#pragma unroll 1
  for (int spin = 0; spin < (1 << 22); ++spin) {
    if (lds_get(p) >= target) break;
    __builtin_amdgcn_s_sleep(1);
  }
}
__device__ __forceinline__ int lds_spin(int O, LAS char* p, int target) {
  int r, cnt = 1 << 22;
  unsigned t;
  asm volatile("1:\n\tds_read_b32 %1, %3 offset:%5\n\ts_waitcnt lgkmcnt(0)\n\tv_readfirstlane_b32 %0, %1\n\ts_cmp_ge_i32 %0, %4\n\ts_cbranch_scc1 2f\n\t"
               "s_sub_u32 %2, %2, 1\n\ts_cbranch_scc0 1b\n2:"
               : "=&s"(r), "=&v"(t), "+s"(cnt) : "v"(p), "s"(target), "i"(O) : "memory", "scc");
  return r;
}
__device__ __forceinline__ void lds_need(int O, int& have, LAS char* p, int target) {
  int cnt;
  unsigned t;
  asm volatile("s_cmp_ge_i32 %0, %4\n\ts_cbranch_scc1 2f\n\ts_mov_b32 %2, 0x400000\n"
               "1:\n\tds_read_b32 %1, %3 offset:%5\n\ts_waitcnt lgkmcnt(0)\n\tv_readfirstlane_b32 %0, %1\n\ts_cmp_ge_i32 %0, %4\n\ts_cbranch_scc1 2f\n\t"
               "s_sleep %6\n\ts_sub_u32 %2, %2, 1\n\ts_cbranch_scc0 1b\n2:"
               : "+s"(have), "=&v"(t), "=&s"(cnt) : "v"(p), "s"(target), "i"(O), "i"(CALM) : "memory", "scc");
  asm volatile("" : "+s"(have));
}
__device__ __forceinline__ u32x4 pack8(f32x4 a, f32x4 b) { return u32x4{cvtpk(a[0], a[1]), cvtpk(a[2], a[3]), cvtpk(b[0], b[1]), cvtpk(b[2], b[3])}; }

__device__ __forceinline__ void stb128(u32x4 v, RSRC r, unsigned o) { __builtin_amdgcn_raw_buffer_store_b128(v, r, o, 0, 16); }
__device__ __forceinline__ void stb64(u32x2 v, RSRC r, unsigned o) { __builtin_amdgcn_raw_buffer_store_b64(v, r, o, 0, 16); }
__device__ __forceinline__ void stb32(unsigned v, RSRC r, unsigned o) { __builtin_amdgcn_raw_buffer_store_b32(v, r, o, 0, 16); }
__device__ __forceinline__ unsigned qk_check(u32x4 x, unsigned tg) {
  const unsigned a = (x[0] ^ __builtin_amdgcn_alignbit(x[1], x[1], 11)) * 0x9e3779b1u + tg;
  const unsigned b = (x[2] + __builtin_amdgcn_alignbit(x[3], x[3], 19)) * 0x85ebca77u;
  return __builtin_amdgcn_alignbit(a, a, 15) ^ b ^ (x[1] + x[3]);
}
__device__ __forceinline__ unsigned add3(unsigned a, unsigned b, unsigned c) {
  return cvtpk((bflo(a) + bflo(b)) + bflo(c), (bfhi(a) + bfhi(b)) + bfhi(c));
}
__global__ __launch_bounds__(512, 2) void dino_main(const u16* q, const u16* f0, const u16* f1, const u16* wq, const u16* wk, const u16* wv,
                                                     const u16* wo, char* ws, u16* out) {
  __shared__ __attribute__((aligned(128))) char smem[LDS_BYTES];
  LAS char* lds = (LAS char*)smem;
  LAS char* ctl = lds + LDS_CTL;
  const unsigned lb = (unsigned)(unsigned long long)lds;
  const int w = __builtin_amdgcn_readfirstlane(threadIdx.x >> 6);
  unsigned long long pc0;
  asm volatile("s_getpc_b64 %0" : "=s"(pc0));
#define COORDS() \
  int tid = threadIdx.x, wg = blockIdx.x; \
  asm volatile("" : "+v"(tid), "+s"(wg)); \
  __builtin_assume(tid >= 0 && tid < 512); __builtin_assume(wg >= 0 && wg < NWG); \
  const int lane = tid & 63, n = lane & 15, g = lane >> 4;       \
    \
  const int dq = (wg & 7) * 2 + ((wg >> 4) & 1), dst = (wg & ~31) + (wg & 7) * 4 + ((wg >> 3) & 3); \
  const int dj = 6 * ((wg >> 5) & 1) + min(lane & 7, 5); \
  const int dout = dq * C + 48 * (wg >> 6) + 16 * (dj >> 2) + (wg & 8) + ((lane & 8) >> 1) + (dj & 3); \
  const int NT = wg < NPR ? 32 - GIVE : wg >= 40 && !(wg & 1) && (wg >> 3) < 5 + ((wg & 4) ? 8 : 10) * GIVE ? 33 : 32; \
  const int xslot = (NT >= 30 ? NT - 30 : NT - 20) * SLOT;       \
  (void)xslot; (void)n; (void)g; (void)dst; (void)dout; (void)NT
#define ARGS_IN() asm volatile("s_waitcnt lgkmcnt(0)" :: "s"(q), "s"(f0), "s"(f1), "s"(wq), "s"(wk), "s"(wv), "s"(wo), "s"(ws), "s"(out) : "memory")
#define rws mkrs(ws, WS_BYTES)
#define PROJECT(k0_) do { \
      _Pragma("unroll") for (int k = k0_; k < 6; ++k) { o[0] = mfma(wvv[k], b[k], o[0]); o[1] = mfma(wvv[6 + k], b[k], o[1]); } \
      asm volatile("" : "+v"(o[0]), "+v"(o[1])); \
      SB(); \
      _Pragma("unroll") for (int k = k0_; k < 6; ++k) o[2] = mfma(wvv[12 + k], b[k], o[2]); \
      asm volatile("" : "+v"(o[2])); \
      SB(); \
      stb128(u32x4{cvtpk(o[0][0], o[0][1]), cvtpk(o[0][2], o[0][3]), cvtpk(o[1][0], o[1][1]), tw}, rws, mo); \
      SB(); \
      stb128(u32x4{cvtpk(o[1][2], o[1][3]), cvtpk(o[2][0], o[2][1]), cvtpk(o[2][2], o[2][3]), tw}, rws, mo + 1024); \
    } while (0)
  unsigned long long tg;
#define GET_TAG() tg = ((unsigned long long)((unsigned)lds_spin(CT_TG + 4, ctl, 256) & 0xffu) << 32) | (unsigned)lds_get(ctl + CT_TG)
  const unsigned long long tstart = wall_clock64();
  if (w != 0) {
    const unsigned l0 = threadIdx.x & 63;
    if (blockIdx.x < NPR) {
      const unsigned p6 = (blockIdx.x * 43) >> 8, pu = blockIdx.x - 6 * p6;
      unsigned vo = ((l0 * 16) ^ (((l0 * 4) & 0x40) | (l0 & 0x20))) + (l0 >> 3) * 256;
      const bool pv = p6 >= 3, wa = w >= 4, isq = !pv && (w & 6) == 2;
      const unsigned a6 = 6 * w - 24, pj = pv ? p6 - 3 : p6;
      unsigned so = (wa ? a6 : isq ? w - 2 : 4 * pu) * 3072 + (isq ? 0 : pv && !wa ? w - 1 : pj) * 128, ga = isq ? 128 : 3072;
      unsigned lo = lb + PSCR + (wa ? a6 : pv ? 4 * w + 20 : (0x1b181e00u >> (8 * w)) & 255u) * 1024, hi = lo + (wa ? 6144 : isq ? 3072 : 4096);
      asm volatile("" : "+v"(vo), "+s"(lo), "+s"(so), "+s"(hi), "+s"(ga));
      ARGS_IN();
      const u32x4 rw = mkrs4(wa ? (pv ? wo : wq) : pv ? wv : isq ? q : wk, isq ? NQ * C * 2 : C * C * 2);
#pragma unroll 1
      do { DMA(lo, vo, rw, so); lo += 1024; so += ga; } while (lo != hi);
    } else if (w == 7 && blockIdx.x >= 56 && blockIdx.x < 72) {
      const u32x4 rc = mkrs4((const void*)(pc0 & ~255ull), CODE_BYTES);
#pragma unroll 1
      for (int i = 0; i < CODE_BYTES; i += 4096)
        asm volatile("s_mov_b32 m0, %0\n\ts_nop 0\n\tbuffer_load_dword %1, %2, %3 offen lds" :: "s"(lb + LDS_SINK), "v"(l0 * 64), "s"(rc), "s"(i) : "memory");
    }
    if (w == 3) {
      unsigned long long ep;
      asm volatile("s_waitcnt lgkmcnt(0)\n\ts_load_dwordx2 %0, %1, %2\n\ts_barrier\n\ts_waitcnt lgkmcnt(0)" : "=&s"(ep) : "s"(ws), "s"(EP_OFF) : "memory");
      *(LAS unsigned long long*)(ctl + CT_TG) = ((ep + 0x100001ull) & 0xffffffffffull) | (((wall_clock64() - tstart) & 0x3fffffull) | 1ull) << 40;
    } else RAW_BARRIER();
  }
  SB();
  if (__builtin_expect(w == 0, 1)) {
    const unsigned l0 = threadIdx.x & 63, g0 = blockIdx.x;
    unsigned y0 = (l0 * 16) ^ (l0 >= 48 ? 48 : 0), y1 = (l0 * 16) ^ (l0 >= 32 ? 64 : 48), y2 = (l0 * 16) ^ (l0 >= 16 ? 112 : 64);
    unsigned v0 = ((l0 * 16) ^ ((l0 * 2) & 0x60)) + (l0 >> 3) * 640 + 1024, v1 = (v0 ^ 16) - 640;
    unsigned v2 = v0 + W0 * C * 2 - 2048, v3 = v1 + W0 * C * 2 - 2048;
    unsigned b0 = (g0 & 7) * (2 * W0 * C * 2) + (g0 >> 3) * 6144, b1 = (g0 & 7) * (W1 * C * 2) + (g0 >> 3) * 3072;
    {
      const unsigned iv = (l0 & 15) >= CT_ONE / 8 ? 0x3f803f80u : (l0 >> 3) == CT_MAX / 64 ? 0xff800000u : 0u;
      *(LAS u32x2*)(ctl + 8 * min(l0, 47u)) = u32x2{l0 >= 47 ? 0x3f803f80u : iv, l0 >= 47 ? 0x3f803f80u : iv};
    }
    constexpr unsigned st0 = 16 * W0 * C * 2, st1 = 8 * W1 * C * 2;
    unsigned sb = lb, fo = CT_FREE, fv = 0u;
    int lap = 0;
    u32x4 rt = mkrs4(tabs.seq, 112 * 4);
    unsigned vz = 0u;
    asm volatile("" : "+v"(v0), "+v"(v1), "+v"(v2), "+v"(v3), "+v"(y0), "+v"(y1), "+v"(y2), "+v"(vz), "+s"(b0), "+s"(b1), "+s"(rt));
    ARGS_IN();
    const u32x4 r0 = mkrs4((const char*)f0 - 1024, W0 * W0 * C * 2 + 1024), r1 = mkrs4(f1, W1 * W1 * C * 2);
    if (g0 < NPR) __builtin_amdgcn_s_barrier();
#define UNIT(p_) do { unsigned so_; \
      asm volatile("s_add_u32 m0, %1, %10\n\ts_add_u32 %0, %2, %11\n\t" \
                   "buffer_load_dwordx4 %3, %7, %0 offen nt lds\n\tbuffer_load_dwordx4 %4, %7, %0 offen offset:1024 nt lds\n\t" \
                   "buffer_load_dwordx4 %5, %7, %0 offen offset:2048 nt lds\n\tbuffer_load_dwordx4 %6, %7, %0 offen offset:3072 nt lds\n\t" \
                   "s_mov_b32 m0, %12\n\ts_mov_b64 exec, 1\n\tbuffer_load_dword %13, %8, %9 offen offset:%14 lds\n\ts_mov_b64 exec, -1" \
                   : "=&s"(so_) : "s"(sb), "s"(b0), "v"(v0), "v"(v1), "v"(v2), "v"(v3), "s"(r0), "s"(rt), "s"(12 * s), "i"((p_) * PART), "i"((p_) * 128), \
                     "s"(lb + LDS_CTL + CT_FULL - 4 * (p_)), "v"(vz), "i"(4 * (p_)) : "memory", "scc"); } while (0)
#define WINDOW() \
      asm volatile("s_waitcnt vmcnt(%1)\n\ts_cmp_ge_i32 %0, %6\n\ts_cbranch_scc1 1f\n\ts_waitcnt vmcnt(%2)\n\ts_cmp_ge_i32 %0, %7\n\ts_cbranch_scc1 1f\n\t" \
                   "s_waitcnt vmcnt(%3)\n\ts_cmp_ge_i32 %0, %8\n\ts_cbranch_scc1 1f\n\ts_waitcnt vmcnt(%4)\n\ts_cmp_ge_i32 %0, %9\n\ts_cbranch_scc1 1f\n\t" \
                   "s_waitcnt vmcnt(%5)\n1:" :: "s"(late), "i"(WMIN + 4 * WSTEP), "i"(WMIN + 3 * WSTEP), "i"(WMIN + 2 * WSTEP), "i"(WMIN + WSTEP), "i"(WMIN), \
                   "i"(4 * LSTEP * 256), "i"(3 * LSTEP * 256), "i"(2 * LSTEP * 256), "i"(LSTEP * 256) : "memory", "scc")
#define COARSE(sb_) \
      asm volatile("s_add_u32 m0, %0, %6\n\ts_nop 0\n\tbuffer_load_dwordx4 %1, %4, %5 offen nt lds\n\t" \
                   "buffer_load_dwordx4 %2, %4, %5 offen offset:1024 nt lds\n\tbuffer_load_dwordx4 %3, %4, %5 offen offset:2048 nt lds" \
                   :: "s"(sb_), "v"(y0), "v"(y1), "v"(y2), "s"(r1), "s"(b1), "i"(CRS) : "memory", "scc")
    COARSE(sb);
#pragma unroll 1
    for (int s = 0; s < 2; ++s) {
      asm volatile("s_add_u32 %0, %0, %1" : "+s"(b1) : "i"(st1) : "scc");
      UNIT(0); UNIT(1); COARSE(sb + SLOT); UNIT(2);
      sb += SLOT; b0 += st0;
    }
    if (g0 >= NPR) __builtin_amdgcn_s_barrier();
    __builtin_amdgcn_s_setprio(3);
    COORDS();
    fo += 12;
    const int tau = __builtin_amdgcn_readfirstlane((int)((PACE1 - PACE0) * 256.f * __builtin_amdgcn_rcpf((float)NT)));
    unsigned due = PACE0 * 256u + 2 * tau, t0v = 0u;
    unsigned long long now = tstart;
    const unsigned xi = (wg >> 3) - 5, xrow = 8 * (31 - xi % GIVE) + (wg & 7) + (xi / GIVE & 1), xcol = xi / GIVE >> 1;
    const unsigned x0 = xrow * (2 * W0 * C * 2) + xcol * 6144, x1 = xrow * (W1 * C * 2) + xcol * 3072;
#pragma unroll 1
    for (int s = 2; s < NT; ++s) {
      asm volatile("" : "+s"(now));
      const unsigned t0s = __builtin_amdgcn_readfirstlane(t0v), t0 = t0s ? t0s : (unsigned)tstart;
      if ((int)(((unsigned)now - t0) * 256u - due) < 0) {
#pragma unroll 1
        for (int spin = 0; spin < 256; ++spin) {
          if ((int)(((unsigned)wall_clock64() - t0) * 256u - due) >= 0) break;
          __builtin_amdgcn_s_sleep(1);
        }
      }
      fv = *(volatile LAS unsigned*)(ctl + (s == 7 ? CT_PD : fo));
      now = wall_clock64();
      UNIT(0);
      asm volatile("" : "+s"(now));
      const int late = (int)(((unsigned)now - t0) * 256u - due);
      due += tau;
      WINDOW();
      UNIT(1);
      WINDOW();
      const unsigned nsb = fo == CT_FREE ? lb : sb + SLOT;
      const unsigned n0 = s == 31 ? x0 : b0 + st0;
      b1 = s == 31 ? x1 : b1 + st1;
      if (s + 1 < NT) {
        const int need = s == 7 && wg < NPR ? 4 : lap;
        if (__builtin_expect((int)__builtin_amdgcn_readfirstlane(fv) < need, 0)) {
          lds_wait(ctl + (s == 7 ? CT_PD : fo), need);
        }
        COARSE(nsb);
      }
      now = wall_clock64();
      t0v = *(volatile LAS unsigned*)(ctl + CT_T0);
      UNIT(2);
      WINDOW();
      if (fo == CT_FREE + 4 * (NS - 1)) { fo = CT_FREE; lap += 4; } else fo += 4;
      sb = nsb; b0 = n0;
    }
    __builtin_amdgcn_s_setprio(0);
    u32x4 wvv[18], b[6];
    asm volatile("s_mov_b32 m0, %0\n\ts_mov_b64 exec, 1\n\tbuffer_load_dword %1, %2, 0 offen lds\n\ts_mov_b64 exec, -1"
                 :: "s"(lb + LDS_SINK), "v"(vz), "s"(mkrs4(ws - 4096, 4096)) : "memory");
    lds_wait(ctl + CT_WV, 1);
#pragma unroll
    for (int j = 0; j < 18; ++j) wvv[j] = ldb128<16>(rws, lane * 16 + (j & 3) * 1024, WVO_OFF + 54 * 1024 + (j >> 2) * 4096);
    GET_TAG();
    LAS const char *xr = lds + xslot + lane * 16, *xl = lds + xslot + 6144 + 4 * n;
    unsigned mo = wg * 8192 + 6144 + n * 64 + g * 16, tw = (unsigned)(tg >> (20 * (g & 1))) & 0xfffffu, sh = 12 * (g & 1), lse;
    asm volatile("" : "+v"(xr), "+v"(xl), "+v"(mo), "+v"(tw), "+v"(sh));
    __builtin_amdgcn_s_waitcnt(0x0f70);
    lds_spin(CT_PRDY, ctl + 4 * ((NT - 1) % NPK), 2 * NT);
    __builtin_amdgcn_s_sleep(2);
    __builtin_amdgcn_s_setprio(2);
    {
      int cnt_, c_;
      unsigned t_;
      asm volatile("s_mov_b32 %1, 0x400000\n1:\n\tds_read_b32 %2, %10 offset:%13\n\tds_read_b128 %3, %11\n\tds_read_b128 %4, %11 offset:1024\n\t"
                   "ds_read_b128 %5, %11 offset:2048\n\tds_read_b128 %6, %11 offset:3072\n\tds_read_b128 %7, %11 offset:4096\n\t"
                   "ds_read_b128 %8, %11 offset:5120\n\tds_read_b32 %9, %12\n\ts_waitcnt lgkmcnt(7)\n\tv_readfirstlane_b32 %0, %2\n\t"
                   "s_cmp_ge_i32 %0, 3\n\ts_cbranch_scc1 2f\n\ts_sub_u32 %1, %1, 1\n\ts_cbranch_scc0 1b\n2:\n\ts_waitcnt lgkmcnt(5)"
                   : "=&s"(c_), "=&s"(cnt_), "=&v"(t_), "=&v"(b[0]), "=&v"(b[1]), "=&v"(b[2]), "=&v"(b[3]), "=&v"(b[4]), "=&v"(b[5]), "=&v"(lse)
                   : "v"(ctl), "v"(xr), "v"(xl), "i"(CT_XD) : "memory", "scc");
    }
    SB();
    f32x4 o[3] = {{0.f, 0.f, 0.f, 0.f}, {0.f, 0.f, 0.f, 0.f}, {0.f, 0.f, 0.f, 0.f}};
#pragma unroll
    for (int k = 0; k < 2; ++k)
#pragma unroll
      for (int t = 0; t < 3; ++t) o[t] = mfma(wvv[6 * t + k], b[k], o[t]);
    asm volatile("s_waitcnt lgkmcnt(0)" : "+v"(o[0]), "+v"(o[1]), "+v"(o[2]), "+v"(b[2]), "+v"(b[3]), "+v"(b[4]), "+v"(b[5]), "+v"(lse));
    SB();
    tw |= (lse << sh) & 0xfff00000u;
    PROJECT(2);
    lds_inc(CT_ST, ctl);
  } else {
    COORDS();
    const int tq = (lane >> 2) & 3, tp = lane & 3;
    const bool pq = wg < 18;
    const bool pv = wg >= 18 && wg < NPR;
    const int pj = wg / 6, pu = wg % 6;
    if (w >= 4 && ((w + NT) & 3) == 0 && !pq) {
      unsigned k0, k1, k2;
      asm volatile("s_load_dword %0, %3, %5\n\ts_load_dword %1, %3, %6\n\ts_load_dword %2, %4, %7\n\ts_waitcnt lgkmcnt(0)"
                   : "=&s"(k0), "=&s"(k1), "=&s"(k2) : "s"(f0), "s"(f1), "s"((wg >> 3) * 3145728 + 1572860), "s"((wg >> 3) * 3145728 + 3145724),
                     "s"((wg >> 3) * 786432 + 786428) : "memory");
    }
    const u32x4 one = {0x3f803f80u, 0x3f803f80u, 0x3f803f80u, 0x3f803f80u};
    const int fq = ((n & 2) << 1) | ((n & 4) >> 1);
    LAS const char* rq0 = lds + PSCR + 24576 + 3072 * (n >> 3) + 128 * (n & 7) + 16 * (g ^ fq);
    LAS const char* rq1 = lds + PSCR + 24576 + 3072 * (n >> 3) + 128 * (n & 7) + 16 * ((4 + g) ^ fq);
    if (pq && w >= 2) {
      {
        waitvm<0>();
        lds_inc(CT_PA, ctl);
        if (w >= 4) {
          f32x4 sq = {0.f, 0.f, 0.f, 0.f}, rr = sq, cs = sq;
          const int ro = PSCR + 128 * (8 * g + tq) + 16 * (((w & 2) * 2 + tp) ^ ((tq & 2) << 1)) + 8 * (w & 1);
          u32x4 qv[6], af[6];
          lds_spin(CT_PA, ctl, 6);
#pragma unroll
          for (int s = 0; s < 6; ++s) {
            qv[s] = *(LAS const u32x4*)(((s & 1) ? rq1 : rq0) + (s >> 1) * 1024);
            af[s] = vtr2(lds + ro + s * 4096, lds + (ro ^ 32) + s * 4096 + 512);
          }
          SB();
#pragma unroll
          for (int s = 0; s < 6; ++s) { sq = mfma(one, qv[s], sq); rr = mfma(af[s], qv[s], rr); cs = mfma(af[s], one, cs); }
          const f32x4 qq = rr - (sq[0] * (1.f / C)) * cs;
          *(LAS u32x2*)(lds + LDS_QK + 4096 + ((w >> 1) & 1) * 1024 + lane * 16 + 8 * (w & 1)) = u32x2{cvtpk(qq[0], qq[1]), cvtpk(qq[2], qq[3])};
          lds_inc(CT_PB, ctl);
        }
      }
    }
    if (pq && w == 1) {
      {
        waitvm<0>();
        u32x4 kf4[4], qv[6];
        const int ko = LDS_QK + 128 * (((n & 4) << 2) | (n & 11)) + 16 * (g ^ ((n & 2) << 1));
#pragma unroll
        for (int i = 0; i < 4; ++i) kf4[i] = *(LAS const u32x4*)(lds + (ko ^ ((i & 1) * 64 + (i >> 1) * 32)) + (i >> 1) * 512);
        lds_spin(CT_PA, ctl, 6);
#pragma unroll
        for (int s = 0; s < 6; ++s) qv[s] = *(LAS const u32x4*)(((s & 1) ? rq1 : rq0) + (s >> 1) * 1024);
        const f32x4 z = {0.f, 0.f, 0.f, 0.f};
        f32x4 gq = z, sq = z;
#pragma unroll
        for (int s = 0; s < 6; ++s) { gq = mfma(qv[s], qv[s], gq); sq = mfma(one, qv[s], sq); }
        const float mean = sq[0] * (1.f / C);
        const float s2 = xrow16_sum(g == (n >> 2) ? sel4(gq, n & 3) : 0.f);
        float sc = __builtin_amdgcn_rsqf(fmaxf(fmaf(-mean, mean, s2 * (1.f / C)), 0.f) + EPS) * QSCALE;
        asm volatile("" : "+v"(sc));
        GET_TAG();
        lds_spin(CT_PB, ctl, 4);
        const u32x4 q0 = *(LAS const u32x4*)(lds + LDS_QK + 4096 + lane * 16), q1 = *(LAS const u32x4*)(lds + LDS_QK + 5120 + lane * 16);
        const f32x4 d0 = mfma(kf4[1], q1, mfma(kf4[0], q0, z)), d1 = mfma(kf4[3], q1, mfma(kf4[2], q0, z));
        const u32x4 xq = pack8(d0 * sc, d1 * sc);
        const unsigned ck = qk_check(xq, (unsigned)tg);
        asm volatile("" :: "v"(ck));
        {
          const unsigned so = QK_OFF + wg * QK_REC;
#pragma unroll
          for (int cp = 0; cp < 4; ++cp) {
            stb128(xq, rws, lane * 16 + so + cp * QK_COPY);
            stb32(ck, rws, lane * 4 + so + 1024 + cp * QK_COPY);
          }
          stb32((unsigned)tg, rws, HINT_OFF + lane * 128 + (pu >> 1) * 32 + pj * 8 + (pu & 1) * 4);
          lds_put(CT_PD, ctl, 4);
        }
      }
    }
    if (pv) {
      waitvm<0>();
      lds_inc(CT_PA, ctl);
      if (w >= 4) {
        const int ro = PSCR + 128 * (8 * g + tq) + 16 * ((2 * (w - 4) + (tp >> 1)) ^ ((tq & 2) << 1)) + 8 * (tp & 1);
        const int rv = PSCR + 24576 + 128 * n + 16 * (g ^ fq);
        f32x4 d0 = {0.f, 0.f, 0.f, 0.f}, d1 = d0;
        lds_spin(CT_PA, ctl, 7);
#pragma unroll
        for (int s = 0; s < 6; ++s) {
          const u32x4 p = vtr2(lds + ro + s * 4096, lds + (ro ^ 32) + s * 4096 + 512);
          LAS const char* pd = lds + (rv ^ ((s & 1) * 64)) + (s >> 1) * 4096;
          d0 = mfma(*(LAS const u32x4*)pd, p, d0); d1 = mfma(*(LAS const u32x4*)(pd + 2048), p, d1);
        }
        const int fi = (pj - 3) * 24 + (w - 4) * 6 + pu;
        stb128(pack8(d0, d1), rws, lane * 16 + WVO_OFF + fi * 1024);
        lds_inc(CT_PD, ctl);
        GET_TAG();
        const unsigned org = (unsigned)tstart + min(max((lds_get(ctl + CT_TG + 4) >> 8) - COLD0, 0) * COLDG >> 2, COLDM);
        waitvm<0>();
        if (lane < 8) stb64(u32x2{(unsigned)tg, (unsigned)(tg >> 32) | org << 8}, rws, FLAG_OFF + lane * 768 + fi * 8);
      }
    }
    const f32x4 z = {0.f, 0.f, 0.f, 0.f};
    int full = 0;
#define NEED(u_) lds_need(CT_FULL, full, ctl, (u_))
    if (w >= 4) {
      const int brg = ((g & 1) << 1) | (g >> 1);
      const int fn = (((n >> 1) & 3) << 1) | ((n >> 3) & 1);
      const int fy = ((n >> 2) & 1) * 4 + ((n >> 1) & 1) * 3;
      const int orr0 = 128 * n + 16 * (brg ^ fn), orr1 = 128 * n + 16 * ((4 + brg) ^ fn);
      const int ory0 = n < 8 ? CRS + 384 * n + 16 * (brg ^ fy) : LDS_CTL + CT_ONE, ory1 = n < 8 ? CRS + 384 * n + 16 * ((4 + brg) ^ fy) : LDS_CTL + CT_ONE;
      const int yon = n < 8 ? 1 : 0;
      const int opk = LDS_PK + 320 * g + 8 * n, ork = LDS_PK + 320 * (2 * (g >> 1) + (n >> 3)) + 256 + 8 * (n & 7) + 4 * (g & 1);
      u32x4 qk[6];
      f32x4 ref, qs;
      LAS const char *px0, *px1, *py0, *py1;
#define AT_SLOT(o_) do { px0 = lds + orr0 + (o_); px1 = lds + orr1 + (o_); py0 = lds + ory0 + __mul24(yon, (o_)); py1 = lds + ory1 + __mul24(yon, (o_)); \
      asm volatile("" : "+v"(px0), "+v"(px1), "+v"(py0), "+v"(py1)); } while (0)
#define RD_K(k_, i_) do { LAS const char* p_ = (((k_) & 1) ? px1 : px0) + ((k_) >> 1) * PART; \
    yv[i_] = *(LAS const u32x4*)((((k_) & 1) ? py1 : py0) + ((k_) >> 1) * 128); \
    xa[i_] = *(LAS const u32x4*)(p_); xb[i_] = *(LAS const u32x4*)(p_ + 2048); } while (0)
#define MF_STAT(i_) do { cy = mfma(yv[i_], yv[i_], cy); ca = mfma(yv[i_], xa[i_], ca); cb = mfma(yv[i_], xb[i_], cb); \
    ga = mfma(xa[i_], xa[i_], ga); gb = mfma(xb[i_], xb[i_], gb); } while (0)
#define MF_S(k_, i_) do { sy = mfma(qk[k_], yv[i_], sy); sa = mfma(qk[k_], xa[i_], sa); sb = mfma(qk[k_], xb[i_], sb); } while (0)
#define STATS(st, rec) do { \
      const int r = n & 3; \
      const float yy = half_copy<true>(sel4(cy, r)); \
      const f32x2 Y = swap32(yy, yy), X = swap32(sel4(ca, r), sel4(cb, r)), G = swap32(sel4(ga, r), sel4(gb, r)); \
      const float q1_ = fmaf(2.f, X[0], Y[0]) + (n < 8 ? G[0] : G[1]);       \
      const f32x2 Q = swap16(q1_, q1_); \
      const float mn_ = (X[1] + Y[1]) * (1.f / C); \
      const float var = fmaxf(fmaf(-mn_, mn_, fmaf((n & 4) ? Q[1] : Q[0], 1.f / C, EPS)), EPS), rs_ = __builtin_amdgcn_rsqf(var); \
      const f32x2 R = swap32(rs_, rs_), Mn = swap32(mn_, mn_); \
      st = f32x4{R[0], Mn[0], R[1], Mn[1]}; \
      const float v_ = (g & 1) ? var * rs_ : mn_, h_ = bfhi(__float_as_uint(v_)); \
      rec = cvtpk(h_, v_ - h_); \
    } while (0)
#define LOGITS(sv, st, base_) do { \
      _Pragma("unroll") for (int r = 0; r < 4; ++r) { \
        const float s0 = half_copy<true>(sy[r]); \
        sv[r] = fmaf(fmaf(-st[1], qs[r], sa[r] + s0), st[0], -(base_)[r]); \
        sv[4 + r] = fmaf(fmaf(-st[3], qs[r], sb[r] + s0), st[2], -(base_)[r]); \
      } \
    } while (0)
#define PUBLISH(sv, st, rec, pk_, word_) do { \
      *(LAS unsigned*)(lds + ork + (pk_) * PK_B) = rec; \
      *(LAS u32x2*)(lds + opk + (pk_) * PK_B) = u32x2{cvtpk(sv[0] * st[0], sv[1] * st[0]), cvtpk(sv[2] * st[0], sv[3] * st[0])}; \
      *(LAS u32x2*)(lds + opk + (pk_) * PK_B + 128) = u32x2{cvtpk(sv[4] * st[2], sv[5] * st[2]), cvtpk(sv[6] * st[2], sv[7] * st[2])}; \
      lds_put(CT_PRDY, ctl + 4 * (pk_), (word_)); \
    } while (0)
      int s = (w + NT - 1) & 3, slot = s + 4, pk = s + 4, ncon = 0;
      {
        u32x4 yv[12], xa[12], xb[12];
        f32x4 st0, st1;
        unsigned rec0, rec1;
#pragma unroll
        for (int h = 0; h < 2; ++h) {
          f32x4 cy = z, ga = z, ca = z, gb = z, cb = z;
          AT_SLOT((s + 4 * h) * SLOT);
          lds_wait(ctl + CT_FULL, 3 * (s + 4 * h) + 3);
#pragma unroll
          for (int k = 0; k < 6; ++k) RD_K(k, 6 * h + k);
          lds_inc(CT_FREE + 16 * h, ctl + 4 * s);
          SB();
#pragma unroll
          for (int k = 0; k < 6; ++k) MF_STAT(6 * h + k);
          SB();
          if (h == 0) { STATS(st0, rec0); asm volatile("" : "+v"(st0), "+v"(rec0)); } else { STATS(st1, rec1); asm volatile("" : "+v"(st1), "+v"(rec1)); }
          SB();
        }
        lds_wait(ctl + CT_QK, 3);
#pragma unroll
        for (int k = 0; k < 6; ++k) qk[k] = *(LAS const u32x4*)(lds + LDS_QK + k * 1024 + lane * 16);
        float s0v[8], s1v[8];
        {
          f32x4 sy = z, sa = z, sb = z;
#pragma unroll
          for (int k = 0; k < 6; ++k) MF_S(k, k);
#pragma unroll
          for (int r = 0; r < 4; ++r) qs[r] = half_copy<false>(sy[r]);
          LOGITS(s0v, st0, z);
        }
        {
          f32x4 sy = z, sa = z, sb = z;
#pragma unroll
          for (int k = 0; k < 6; ++k) MF_S(k, 6 + k);
          LOGITS(s1v, st1, z);
        }
#pragma unroll
        for (int r = 0; r < 4; ++r) ref[r] = row_max(fmaxf(fmaxf(s0v[r], s0v[4 + r]), fmaxf(s1v[r], s1v[4 + r])));
        if (n < 4) __builtin_amdgcn_ds_fmaxf((LAS float*)(ctl + CT_MAX + 16 * g + 4 * n), sel4(ref, n), __ATOMIC_RELAXED, __HIP_MEMORY_SCOPE_WORKGROUP, false);
        lds_inc(CT_QR, ctl);
        lds_spin(CT_QR, ctl, 4);
        ref = *(LAS const f32x4*)(ctl + CT_MAX + 16 * g);
#pragma unroll
        for (int j = 0; j < 8; ++j) s0v[j] = __builtin_amdgcn_exp2f(s0v[j] - ref[j & 3]);
        PUBLISH(s0v, st0, rec0, s, 2 * s + 2);
#pragma unroll
        for (int j = 0; j < 8; ++j) s1v[j] = __builtin_amdgcn_exp2f(s1v[j] - ref[j & 3]);
        if (pk >= NPK) { pk -= NPK; ncon = 3; lds_spin(CT_PCON, ctl + 4 * pk, 3); }
        s += 4;
        PUBLISH(s1v, st1, rec1, pk, 2 * s + 2);
      }
      const int slow = (int)(wall_clock64() - tstart) < THL ? NT - 8 : 0;
      float thr = 16.f;
      f32x4 nref = -ref;
#pragma unroll 1
      for (;;) {
        s += 4; slot += 4; slot -= slot >= NS ? NS : 0; pk += 4;
        if (pk >= NPK) { pk -= NPK; ncon += 3; }
        if (s >= NT) break;
        f32x4 sy = z, cy = z, sa, ga = z, ca = z, sb, gb = z, cb = z;
        u32x4 yv[6], xa[6], xb[6];
        unsigned fl_, word = 2 * s + 2;
        AT_SLOT(slot * SLOT);
        LAS char *pw = lds + opk + pk * PK_B, *prc = lds + ork + pk * PK_B, *pfl = ctl + 4 * pk, *pfr = ctl + 4 * slot;
        asm volatile("" : "+v"(pw), "+v"(prc), "+v"(pfl), "+v"(pfr), "+v"(word));
#define RD_X "ds_read_b32 %2, %7 offset:%11\n\tds_read_b128 %3, %8 offset:%12\n\tds_read_b128 %4, %9 offset:%12\n\t" \
             "ds_read_b128 %5, %8 offset:%13\n\tds_read_b128 %6, %9 offset:%13\n"
#define RD_IN(p_) "v"(ctl), "v"(px0), "v"(px1), "s"(3 * s + (p_) + 1), "i"(CT_FULL), "i"(PART * (p_)), "i"(PART * (p_) + 2048)
#define ISSUE(p_) do { int d0_, d1_; \
        asm volatile(RD_X : "=&s"(d0_), "=&s"(d1_), "=&v"(fl_), "=&v"(xa[2 * (p_)]), "=&v"(xa[2 * (p_) + 1]), "=&v"(xb[2 * (p_)]), "=&v"(xb[2 * (p_) + 1]) \
                     : RD_IN(p_) : "memory"); } while (0)
#define CHECK(p_) do { int cnt_; \
        asm volatile("s_mov_b32 %1, 0x400000\n\ts_branch 2f\n1:\n\t" RD_X "2:\n\ts_waitcnt lgkmcnt(4)\n\tv_readfirstlane_b32 %0, %2\n\t" \
                     "s_cmp_ge_i32 %0, %10\n\ts_cbranch_scc1 3f\n\ts_cmp_ge_i32 %10, %14\n\ts_cbranch_scc1 4f\n\ts_sleep %15\n4:\n\t" \
                     "s_sub_u32 %1, %1, 1\n\ts_cbranch_scc0 1b\n3:\n\ts_waitcnt lgkmcnt(2)" \
                     : "=&s"(full), "=&s"(cnt_), "+v"(fl_), "+v"(xa[2 * (p_)]), "+v"(xa[2 * (p_) + 1]), "+v"(xb[2 * (p_)]), "+v"(xb[2 * (p_) + 1]) \
                     : RD_IN(p_), "s"(3 * NT - 2), "i"(CALM) : "memory", "scc"); \
        asm volatile("" : "+s"(full)); } while (0)
#define GOT_B(p_) do { waitlds<0>(); asm volatile("" : "+v"(xb[2 * (p_)]), "+v"(xb[2 * (p_) + 1])); } while (0)
#define MF_A(k_) do { ca = mfma(yv[k_], xa[k_], ca); ga = mfma(xa[k_], xa[k_], ga); sa = mfma(qk[k_], xa[k_], sa); } while (0)
#define MF_B(k_) do { cb = mfma(yv[k_], xb[k_], cb); gb = mfma(xb[k_], xb[k_], gb); sb = mfma(qk[k_], xb[k_], sb); } while (0)
        const unsigned pcv = *(volatile LAS unsigned*)(pfl + CT_PCON);
        NEED(3 * s);
#pragma unroll
        for (int k = 0; k < 6; ++k) yv[k] = *(LAS const u32x4*)(((k & 1) ? py1 : py0) + (k >> 1) * 128);
        int con = __builtin_amdgcn_readfirstlane(pcv);
        SB();
#pragma unroll
        for (int k = 0; k < 6; ++k) sy = mfma(qk[k], yv[k], sy);
        asm volatile("" : "+v"(sy));
        SB();
        lds_need(CT_PCON, con, pfl, ncon);
        ISSUE(0);
        SB();
#pragma unroll
        for (int k = 0; k < 6; ++k) cy = mfma(yv[k], yv[k], cy);
#pragma unroll
        for (int r = 0; r < 4; ++r) sa[r] = half_copy<true>(sy[r]);
        const float yy = half_copy<true>(sel4(cy, n & 3));
        f32x2 Y = swap32(yy, yy);
        asm volatile("" : "+v"(Y), "+v"(sa));
        SB();
        CHECK(0);
        sb = sa;
        MF_A(0);
        asm volatile("" : "+v"(ca), "+v"(ga), "+v"(sa));
        SB();
        GOT_B(0);
        ISSUE(1);
        SB();
        MF_B(0); MF_A(1); MF_B(1);
        asm volatile("" : "+v"(ca), "+v"(ga), "+v"(sa), "+v"(cb), "+v"(gb), "+v"(sb));
        SB();
        CHECK(1);
        MF_A(2);
        asm volatile("" : "+v"(ca), "+v"(ga), "+v"(sa));
        SB();
        GOT_B(1);
        ISSUE(2);
        SB();
        MF_B(2); MF_A(3); MF_B(3);
        asm volatile("" : "+v"(ca), "+v"(ga), "+v"(sa), "+v"(cb), "+v"(gb), "+v"(sb));
        SB();
        CHECK(2);
        ca = mfma(yv[5], xa[5], mfma(yv[4], xa[4], ca));
        ga = mfma(xa[5], xa[5], mfma(xa[4], xa[4], ga));
        asm volatile("" : "+v"(ca), "+v"(ga));
        SB();
        GOT_B(2);
        cb = mfma(yv[5], xb[5], mfma(yv[4], xb[4], cb));
        asm volatile("" : "+v"(cb));
        SB();
        lds_inc(CT_FREE, pfr);
        gb = mfma(xb[4], xb[4], gb);
        float ua = sel4(ca, n & 3);
        asm volatile("" : "+v"(gb), "+v"(ua));
        SB();
        gb = mfma(xb[5], xb[5], gb);
        float va = sel4(ga, n & 3);
        asm volatile("" : "+v"(gb), "+v"(va));
        SB();
        sa = mfma(qk[4], xa[4], sa);
        float ub = sel4(cb, n & 3);
        asm volatile("" : "+v"(sa), "+v"(ub));
        SB();
        sa = mfma(qk[5], xa[5], sa);
        const f32x2 X = swap32(ua, ub);
        float mn_ = (X[1] + Y[1]) * (1.f / C), q1_ = fmaf(2.f, X[0], Y[0]);
        asm volatile("" : "+v"(sa), "+v"(mn_), "+v"(q1_));
        SB();
        sb = mfma(qk[4], xb[4], sb);
        float vb = sel4(gb, n & 3);
        asm volatile("" : "+v"(sb), "+v"(vb));
        SB();
        sb = mfma(qk[5], xb[5], sb);
        const f32x2 Mn = swap32(mn_, mn_), G = swap32(va, vb);
        q1_ += n < 8 ? G[0] : G[1];
        const f32x2 Q = swap16(q1_, q1_);
        sa -= Mn[0] * qs;
        const float var = fmaxf(fmaf(-mn_, mn_, fmaf((n & 4) ? Q[1] : Q[0], 1.f / C, EPS)), EPS), rs_ = __builtin_amdgcn_rsqf(var);
        sb -= Mn[1] * qs;
        const float v_ = (g & 1) ? var * rs_ : mn_, h_ = bfhi(__float_as_uint(v_));
        *(LAS unsigned*)prc = cvtpk(h_, v_ - h_);
        const f32x2 R = swap32(rs_, rs_);
        sa = sa * R[0] + nref; sb = sb * R[1] + nref;
        const float tm = fmaxf(fmaxf(fmaxf(fmaxf(sa[0], sa[1]), sa[2]), fmaxf(fmaxf(sa[3], sb[0]), sb[1])), fmaxf(sb[2], sb[3]));
        if (__builtin_expect(__builtin_amdgcn_ballot_w64(!(tm <= thr)) != 0ull, 0)) {
#pragma unroll
          for (int r = 0; r < 4; ++r) {
            const float mx = row_max(fmaxf(sa[r], sb[r])), dm = mx <= 16.f ? 0.f : mx;
            nref[r] -= dm; sa[r] -= dm; sb[r] -= dm;
          }
          thr = __builtin_nanf("");
          word |= 1u;
          if (n == 0) *(LAS f32x4*)(lds + LDS_PK + pk * PK_B + 1280 + 16 * g) = -nref;
        }
#pragma unroll
        for (int r = 0; r < 4; ++r) { sa[r] = __builtin_amdgcn_exp2f(sa[r]); sb[r] = __builtin_amdgcn_exp2f(sb[r]); }
        float ra = R[0], rb = R[1];
        asm volatile("" : "+v"(ra), "+v"(rb));
        sa *= ra; sb *= rb;
        *(LAS u32x2*)pw = u32x2{cvtpk(sa[0], sa[1]), cvtpk(sa[2], sa[3])};
        *(LAS u32x2*)(pw + 128) = u32x2{cvtpk(sb[0], sb[1]), cvtpk(sb[2], sb[3])};
        lds_put(CT_PRDY, pfl, word);
        SB();
        if (s < slow) __builtin_amdgcn_s_sleep(THR); else if (s < NT - 8) __builtin_amdgcn_s_sleep(THS);
      }
      GET_TAG();
      LAS char* fin = lds + xslot + 8192;
      unsigned qres = (unsigned)__builtin_amdgcn_raw_buffer_load_b16(mkrs(q, NQ * C * 2), dout * 2, 0, 0) << 16;
      unsigned ao = ((tid & 255) >> 1) * 8192 + (tid & 1) * 16 + dst * 32, tw = (unsigned)(tg >> (20 * (tid & 1))), sh = 12 * (tid & 1), oo = dout * 2;
      LAS char *fw = fin + (((lane & 8) | ((g & 1) << 2) | (g & 2)) + (lane & 1)) * 16 + 4 * (w - 4), *fr = fin + (2 * (lane & 7) + ((lane >> 3) & 1)) * 16;
      float v7 = 0.f;
      unsigned fi_ = 8u;
      const unsigned t0s = lds_get(ctl + CT_T0);
      unsigned gate = (t0s ? t0s : (unsigned)tstart) + RGATE;
      u32x4 ro = mkrs4(out, NQ * C * 2), rm4 = mkrs4(ws, WS_BYTES);
      unsigned half = 128 * 8192;
      unsigned long long wm = 0x0303030303030303ull;
      asm volatile("" : "+v"(ao), "+v"(tw), "+v"(sh), "+v"(oo), "+v"(fw), "+v"(fr), "+v"(fin), "+v"(v7), "+v"(qres), "+v"(fi_), "+s"(ro), "+s"(wm), "+s"(rm4), "+s"(half));
      if (w == 4) asm volatile("s_mov_b64 exec, 0x1515\n\tbuffer_store_dword %0, %1, %2, 0 offen sc1\n\ts_mov_b64 exec, -1" :: "v"(qres), "v"(oo), "s"(ro) : "memory");
      lds_wait(ctl + CT_XD, 1);
      lds_spin(CT_ST, ctl, 4);
      gate += max((int)((unsigned)wall_clock64() + OWNG - gate), 0);
      float org = *(LAS const float*)(lds + xslot + 6144 + 4 * dq);
      *(LAS float*)(fin + 256 + 4 * (w - 4)) = org;
#pragma unroll 1
      for (int spin = 0; spin < 1024; ++spin)
        if ((int)((unsigned)wall_clock64() - gate) >= 0) break;
      u32x4 A, B;
      asm volatile("buffer_load_dwordx4 %0, %2, %3, 0 offen sc1\n\tbuffer_load_dwordx4 %1, %2, %3, %4 offen sc1\n\ts_waitcnt vmcnt(0)"
                   : "=&v"(A), "=&v"(B) : "v"(ao), "s"(rm4), "s"(half) : "memory");
      if (__builtin_expect(__builtin_amdgcn_ballot_w64((((A[3] ^ tw) | (B[3] ^ tw)) & 0xfffffu) != 0u) != 0ull, 1)) {
#pragma unroll 1
        for (int spin = 0; spin < (1 << 18); ++spin) {
          unsigned xa_ = (A[3] ^ tw) << 12, xb_ = (B[3] ^ tw) << 12;
          if (__builtin_amdgcn_ballot_w64((xa_ | xb_) != 0u) == 0ull) break;
          xa_ |= __builtin_amdgcn_mov_dpp(xa_, 0xB1, 0xf, 0xf, true); xb_ |= __builtin_amdgcn_mov_dpp(xb_, 0xB1, 0xf, 0xf, true);
          asm volatile("" ::: "memory");
          if (xa_ != 0u) A = ldb128<16>(rws, ao, 0);
          if (xb_ != 0u) B = ldb128<16>(rws, ao, 128 * 8192);
        }
      }
      unsigned la = (A[3] & 0xfff00000u) >> sh, lb_ = (B[3] & 0xfff00000u) >> sh;
      la |= __builtin_amdgcn_mov_dpp(la, 0xB1, 0xf, 0xf, true); lb_ |= __builtin_amdgcn_mov_dpp(lb_, 0xB1, 0xf, 0xf, true);
      float da = __uint_as_float(la) - org, db = __uint_as_float(lb_) - org;
      f32x2 ua_[3], ub_[3];
#pragma unroll
      for (int i = 0; i < 3; ++i) { ua_[i] = f32x2{bflo(A[i]), bfhi(A[i])}; ub_[i] = f32x2{bflo(B[i]), bfhi(B[i])}; asm volatile("" : "+v"(ua_[i]), "+v"(ub_[i])); }
      if (__builtin_expect(__builtin_amdgcn_ballot_w64(!(fmaxf(da, db) <= 64.f)) != 0ull, 0)) {
        org = xrow16_max(row_max(fmaxf(__uint_as_float(la), __uint_as_float(lb_))));
        da = __uint_as_float(la) - org; db = __uint_as_float(lb_) - org;
        *(LAS float*)(fin + 256 + 4 * (w - 4)) = org;
        fi_ = 9u;
      }
      const float wa = __builtin_amdgcn_exp2f(da), wb = __builtin_amdgcn_exp2f(db);
#pragma unroll
      for (int i = 0; i < 3; ++i) ua_[i] = ua_[i] * wa + ub_[i] * wb;
      const f32x2 p0 = swap32(ua_[0][0], ua_[0][1]), p1 = swap32(ua_[1][0], ua_[1][1]), p2 = swap32(ua_[2][0], ua_[2][1]), p3 = swap32(wa + wb, v7);
      const f32x2 r0 = swap16(p0[0] + p0[1], p1[0] + p1[1]), r1 = swap16(p2[0] + p2[1], p3[0] + p3[1]);
      float c0 = r0[0] + r0[1], c1 = r1[0] + r1[1];
      asm volatile("s_nop 1\n\tv_add_f32_dpp %0, %0, %0 row_ror:8 row_mask:0xf bank_mask:0x3\n\tv_add_f32_dpp %0, %1, %1 row_ror:8 row_mask:0xf bank_mask:0xc\n\t"
                   "s_nop 1\n\tv_add_f32_dpp %0, %0, %0 row_shl:4 row_mask:0xf bank_mask:0x5\n\t"
                   "s_nop 1\n\tv_add_f32_dpp %0, %0, %0 quad_perm:[2,3,0,1] row_mask:0xf bank_mask:0x5\n\t"
                   "s_mov_b64 exec, %3\n\tds_write_b32 %2, %0\n\ts_mov_b64 exec, 1\n\tds_add_u32 %4, %5 offset:%6\n\ts_mov_b64 exec, -1"
                   : "+v"(c0) : "v"(c1), "v"(fw), "s"(wm), "v"(ctl), "v"(fi_), "i"(CT_FIN) : "memory");
      if (w == 4) {
        f32x4 a0, l0;
        unsigned cf;
        int cnt_, c_;
        asm volatile("s_mov_b32 %1, 0x4000000\n1:\n\tds_read_b32 %2, %5 offset:%8\n\tds_read_b128 %3, %6\n\tds_read_b128 %4, %7 offset:192\n\t"
                     "s_waitcnt lgkmcnt(2)\n\tv_readfirstlane_b32 %0, %2\n\ts_cmp_ge_i32 %0, 32\n\ts_cbranch_scc1 2f\n\t"
                     "s_sub_u32 %1, %1, 1\n\ts_cbranch_scc0 1b\n2:\n\ts_waitcnt lgkmcnt(0)"
                     : "=&s"(c_), "=&s"(cnt_), "=&v"(cf), "=&v"(a0), "=&v"(l0) : "v"(ctl), "v"(fr), "v"(fin), "i"(CT_FIN) : "memory", "scc");
        asm volatile("" : "+s"(c_));
        if (__builtin_expect(c_ != 32, 0)) {
          f32x4 m0 = *(LAS const f32x4*)(fin + 256);
          const float mm = fmaxf(fmaxf(m0[0], m0[1]), fmaxf(m0[2], m0[3]));
#pragma unroll
          for (int p = 0; p < 4; ++p) m0[p] = __builtin_amdgcn_exp2f(m0[p] - mm);
          a0 *= m0; l0 *= m0;
        }
        const float o = __uint_as_float(qres) + ((a0[0] + a0[2]) + (a0[1] + a0[3])) * __builtin_amdgcn_rcpf((l0[0] + l0[2]) + (l0[1] + l0[3]));
        const unsigned ob = cvtpk(o, __uint_as_float(__builtin_amdgcn_mov_dpp(__float_as_uint(o), 0xB1, 0xf, 0xf, true)));
        asm volatile("s_mov_b64 exec, 0x1515\n\tbuffer_store_dword %0, %1, %2, 0 offen sc1\n\ts_mov_b64 exec, -1" :: "v"(ob), "v"(oo), "s"(ro) : "memory");
        if (wg == 0 && lane == 0) stb64(u32x2{(unsigned)tg, (unsigned)(tg >> 32)}, rws, EP_OFF);
      }
    } else {
      const int ftr = ((g & 1) << 2) | ((tq >> 1) << 1) | (g >> 1);
      const int fty = (g & 1) * 4 + (tq >> 1) * 3;
      int otr[4], oty[4];
#pragma unroll
      for (int t = 0; t < 4; ++t) {
        otr[t] = (w - 1) * PART + 128 * (4 * g + tq) + 16 * ((2 * t + (tp >> 1)) ^ ftr) + 8 * (tp & 1);
        oty[t] = CRS + (w - 1) * 128 + 384 * (4 * (g & 1) + tq) + 16 * ((2 * t + (tp >> 1)) ^ fty) + 8 * (tp & 1);
      }
      int opk = LDS_PK + 320 * tp + 8 * (4 * g + tq), ork = LDS_PK + 320 * (g >> 1) + 256 + 8 * (4 * (g & 1) + tq);
      asm volatile("" : "+v"(opk), "+v"(ork), "+v"(oty[0]), "+v"(oty[1]), "+v"(oty[2]), "+v"(oty[3]));
#define RD_T(ta, ty, so_) _Pragma("unroll") for (int t_ = 0; t_ < 4; ++t_) { \
    ta[t_] = vtr2(lds + otr[t_] + (so_), lds + otr[t_] + (so_) + 2048); ty[t_] = vtr(lds + oty[t_] + (so_)); }
      f32x4 acc[4] = {z, z, z, z}, tac = z;
#define RD_P(o_) \
      pf = vtr2(lds + opk + (o_), lds + opk + (o_) + 128); \
      sf = vtr2(lds + ork + (o_), lds + ork + (o_) + 640)
#define TWICE(ty) asm volatile("s_nop 2"); _Pragma("unroll") for (int t = 0; t < 4; ++t) { u32x2 lo_, hi_; \
      asm volatile("v_mov_b64 %0, %2\n\tv_mov_b64 %1, %2" : "=&v"(lo_), "=&v"(hi_) : "v"(ty[t])); t2[t] = u32x4{lo_[0], lo_[1], hi_[0], hi_[1]}; } \
      asm volatile("s_nop 1" : "+v"(t2[0]), "+v"(t2[1]), "+v"(t2[2]), "+v"(t2[3]))
#define MF_PX(ta) \
      tac = mfma(sf, pf, tac); \
      _Pragma("unroll") for (int t = 0; t < 4; ++t) acc[t] = mfma(ta[t], pf, acc[t])
#define MF_PY() \
      _Pragma("unroll") for (int t = 0; t < 4; ++t) acc[t] = mfma(t2[t], pf, acc[t])
#define MOVE_REF(word_, o_) do { moved |= (word_) & 1; if (__builtin_expect(moved, 0)) { \
            const float m_ = *(LAS const float*)(lds + (((word_) & 1) ? LDS_PK + (o_) + 1280 : LDS_CTL + CT_MAX) + 4 * n); \
            Mtop = fmaxf(Mtop, m_); \
            const bool drop = m_ < Mtop - 64.f; \
            const float mn = drop ? M : m_, fa = __builtin_amdgcn_exp2f(M - mn); \
            M = mn; \
            _Pragma("unroll") for (int t = 0; t < 4; ++t) acc[t] *= fa; \
            tac *= fa; \
            if (drop) pf = u32x4{0u, 0u, 0u, 0u}; \
          } } while (0)
      const unsigned flo = FLAG_OFF + (wg & 7) * 768 + (w - 1) * 144 + (lane >= 16 ? min(lane - 16, 35) : (lane >> 3) * 6 + (lane & 7) + ((lane & 7) < 6 ? 0 : 18)) * 8;
      float M, Mtop;
      int moved = 0;
      u32x4 wvv[18], pf, sf, t2[4];
      {
        u32x4 ta[8][4], xx[6];
        u32x2 ty[8][4], fl;
        unsigned cc[6];
        const int qo = QK_OFF + (wg & 3) * QK_COPY + (2 * w - 2) * QK_REC;
#define QK_LOAD() _Pragma("unroll") for (int j = 0; j < 6; ++j) { \
          if ((j == 2 || j == 4) && early) __builtin_amdgcn_s_sleep(QSJ); \
          xx[j] = ldb128<16>(rws, lane * 16, qo + ((j >> 1) * 6 + (j & 1)) * QK_REC); \
          cc[j] = __builtin_amdgcn_raw_buffer_load_b32(rws, lane * 4, qo + ((j >> 1) * 6 + (j & 1)) * QK_REC + 1024, 16); }
#pragma unroll
        for (int u = 0; u < 8; ++u) {
          if (u == QKAT) {
            GET_TAG();
#pragma unroll 1
            for (int r = 0; r < 8; ++r) {
#pragma unroll 1
              for (int spin = 0; spin < 128; ++spin) {
                if ((int)(wall_clock64() - tstart) >= GATE + r * HSTEP + (w - 1) * QSW) break;
                __builtin_amdgcn_s_sleep(1);
              }
              u32x8 hv;
              asm volatile("s_load_dwordx8 %0, %1, %2\n\ts_waitcnt lgkmcnt(0)" : "=&s"(hv) : "s"(ws), "s"(HINT_OFF + ((wg & 7) + 8 * r) * 128 + (w - 1) * 32) : "memory");
              const unsigned t_ = (unsigned)tg;
              if ((((hv[0] ^ t_) | (hv[1] ^ t_) | (hv[2] ^ t_)) | ((hv[3] ^ t_) | (hv[4] ^ t_) | (hv[5] ^ t_))) == 0u) break;
            }
            bool early = (int)(wall_clock64() - tstart) < QLATE;
            if (wg < NPR) lds_wait(ctl + CT_PD, 4);
            QK_LOAD();
#pragma unroll 1
            for (int spin = 0; spin < (1 << 18); ++spin) {
              unsigned df = 0u;
#pragma unroll
              for (int j = 0; j < 6; ++j) df |= qk_check(xx[j], (unsigned)tg) ^ cc[j];
              if (__all(df == 0u)) break;
              asm volatile("" ::: "memory");
              early = false;
              QK_LOAD();
            }
#pragma unroll
            for (int h = 0; h < 2; ++h) {
              u32x4 sx;
#pragma unroll
              for (int i = 0; i < 4; ++i) sx[i] = add3(xx[h][i], xx[2 + h][i], xx[4 + h][i]);
              *(LAS u32x4*)(lds + LDS_QK + (2 * w - 2 + h) * 1024 + lane * 16) = sx;
            }
            lds_inc(CT_QK, ctl);
            SB();
          }
          lds_wait(ctl + CT_FULL, 3 * u + w);
          RD_T(ta[u], ty[u], u * SLOT);
          lds_inc(CT_FREE + 4 * u, ctl);
          SB();
        }
        fl = __builtin_bit_cast(u32x2, __builtin_amdgcn_raw_buffer_load_b64(rws, flo, 0, 16));
#pragma unroll
        for (int u = 0; u < 12; ++u) {
          const int word = lds_spin(CT_PRDY + 4 * (u % NPK), ctl, 2 * u + 2);
          if (u == 0) Mtop = M = *(LAS const float*)(ctl + CT_MAX + 4 * n);
          RD_P((u % NPK) * PK_B);
          if (u >= 8) MOVE_REF(word, (u % NPK) * PK_B);
          lds_inc(CT_PCON + 4 * (u % NPK), ctl);
          SB();
          MF_PX(ta[u & 7]);
          SB();
          TWICE(ty[u & 7]);
          MF_PY();
          asm volatile("" : "+v"(acc[0]), "+v"(acc[1]), "+v"(acc[2]), "+v"(acc[3]), "+v"(tac));
          SB();
#define FLAGS_UP() __all(fl[0] == (unsigned)tg && (fl[1] & 255u) == (unsigned)(tg >> 32))
          if (u == T0AT && w == 1 && FLAGS_UP()) {
            int d = (int)(fl[1] - ((unsigned)tstart << 8)) >> 8;
            d += __builtin_amdgcn_mov_dpp(d, XOR8, 0xf, 0xf, true); d += __builtin_amdgcn_mov_dpp(d, ROR4, 0xf, 0xf, true);
            d += __builtin_amdgcn_mov_dpp(d, 0x122, 0xf, 0xf, true); d += __builtin_amdgcn_mov_dpp(d, 0x121, 0xf, 0xf, true);
            lds_put(CT_T0, ctl, ((unsigned)tstart + min(max(d >> 4, -300), 300)) | 1u);
          }
          if (u < 4) {
            NEED(3 * (u + 8) + w);
            RD_T(ta[u], ty[u], ((u + 8) % NS) * SLOT);
            lds_inc(CT_FREE + 4 * ((u + 8) % NS), ctl);
            SB();
          }
        }
#pragma unroll 1
        for (int spin = 0; spin < (1 << 18); ++spin) {
          if (FLAGS_UP()) break;
          asm volatile("" ::: "memory");
          fl = __builtin_bit_cast(u32x2, __builtin_amdgcn_raw_buffer_load_b64(rws, flo, 0, 16));
        }
        asm volatile("" ::: "memory");
        if (w == 3) lds_put(CT_WV, ctl, 1);
      }
      int slot = 3, pk = 12 % NPK;
      unsigned rdy = 0u;
      const int kro[3] = {(w - 1) * 2048, (w == 3 ? 0 : w) * 2048, (w == 1 ? 2 : w - 2) * 2048};
#define RD_PACKET(z_) do { int cnt_; u32x2 p0_, p1_, p2_, p3_; \
        word = __builtin_amdgcn_readfirstlane(rdy); \
        asm volatile("s_mov_b32 %1, 0x400000\n1:\n\tds_read_b32 %2, %7 offset:%11\n\t" \
                     "ds_read_b64_tr_b16 %5, %9\n\tds_read_b64_tr_b16 %6, %9 offset:640\n\t" \
                     "ds_read_b64_tr_b16 %3, %8\n\tds_read_b64_tr_b16 %4, %8 offset:128\n\t" \
                     "s_cmp_ge_i32 %0, %10\n\ts_cbranch_scc1 2f\n\ts_waitcnt lgkmcnt(4)\n\tv_readfirstlane_b32 %0, %2\n\t" \
                     "s_cmp_ge_i32 %0, %10\n\ts_cbranch_scc1 2f\n\t" z_ "s_sub_u32 %1, %1, 1\n\ts_cbranch_scc0 1b\n2:" \
                     : "+s"(word), "=&s"(cnt_), "=&v"(rdy), "=&v"(p0_), "=&v"(p1_), "=&v"(p2_), "=&v"(p3_) \
                     : "v"(ctl + 4 * pk), "v"(lds + opk + pk * PK_B), "v"(lds + ork + pk * PK_B), "s"(2 * s + 2), "i"(CT_PRDY), "i"(CALM) : "memory", "scc"); \
        asm volatile("" : "+s"(word)); \
        pf = u32x4{p0_[0], p0_[1], p1_[0], p1_[1]}; sf = u32x4{p2_[0], p2_[1], p3_[0], p3_[1]}; } while (0)
#define GOT_PACKET() do { waitlds<0>(); asm volatile("" : "+v"(pf), "+v"(sf) : "v"(rdy)); MOVE_REF(word, pk * PK_B); } while (0)
#define RD_Y(t_, p_) do { const u32x2 lo_ = vtr(p_); asm volatile("" ::: "memory"); const u32x2 hi_ = vtr(p_); \
        t2[t_] = u32x4{lo_[0], lo_[1], hi_[0], hi_[1]}; } while (0)
      u32x4 ta[4];
      int s = 12, word;
      NEED(36 + w);
#pragma unroll
      for (int t = 0; t < 4; ++t) { ta[t] = vtr2(lds + otr[t] + 2 * SLOT, lds + otr[t] + 2 * SLOT + 2048); RD_Y(t, lds + oty[t] + 2 * SLOT); }
      lds_inc(CT_FREE + 8, ctl);
#pragma unroll 1
      for (; s < NT - 1; ++s) {
        RD_PACKET("s_sleep %12\n\t");
        LAS const char *px[4], *py[4];
#pragma unroll
        for (int t = 0; t < 4; ++t) { px[t] = lds + otr[t] + slot * SLOT; py[t] = lds + oty[t] + slot * SLOT; asm volatile("" : "+v"(px[t]), "+v"(py[t])); }
        NEED(3 * s + 3 + w);
        GOT_PACKET();
        SB();
        tac = mfma(sf, pf, tac);
        asm volatile("" : "+v"(tac));
        SB();
        lds_inc(CT_PCON, ctl + 4 * pk);
        pk = pk == NPK - 1 ? 0 : pk + 1;
#pragma unroll
        for (int t = 0; t < 4; ++t) { acc[t] = mfma(ta[t], pf, acc[t]); SB(); ta[t] = vtr2(px[t], px[t] + 2048); SB(); }
        rdy = *(volatile LAS unsigned*)(ctl + CT_PRDY + 4 * pk);
#pragma unroll
        for (int t = 0; t < 4; ++t) { acc[t] = mfma(t2[t], pf, acc[t]); SB(); RD_Y(t, py[t]); SB(); }
        lds_inc(CT_FREE, ctl + 4 * slot);
#define WV_LOAD(j_) wvv[j_] = ldb128<16>(rws, lane * 16 + ((j_) & 1) * 1024, WVO_OFF + (w - 1) * 18432 + ((j_) / 6) * 6144 + kro[((j_) % 6) >> 1])
        if ((unsigned)(s - WVS) < WV1) {
#pragma unroll
          for (int u = 0; u < WV1; ++u)
            if (s - WVS == u) WV_LOAD(u);
        }
        if (s == NT - 3) {
#pragma unroll
          for (int j = WV1; j < 18; ++j) WV_LOAD(j);
        }
        slot = slot == NS - 1 ? 0 : slot + 1;
      }
      LAS char *xw = lds + xslot + lane * 16 + kro[0], *xl = lds + xslot + 6144 + 4 * n;
      LAS const char *xr1 = lds + xslot + lane * 16 + kro[1], *xr2 = lds + xslot + lane * 16 + kro[2];
      unsigned mo = wg * 8192 + (w - 1) * 2048 + n * 64 + g * 16, tw = (unsigned)(tg >> (20 * (g & 1))) & 0xfffffu, sh = 12 * (g & 1);
      asm volatile("" : "+v"(xw), "+v"(xl), "+v"(xr1), "+v"(xr2), "+v"(mo), "+v"(tw), "+v"(sh));
      __builtin_amdgcn_s_setprio(2);
      RD_PACKET("");
      GOT_PACKET();
      SB();
      tac = mfma(sf, pf, tac);
      asm volatile("" : "+v"(tac));
      SB();
#pragma unroll
      for (int t = 0; t < 2; ++t) acc[t] = mfma(t2[t], pf, mfma(ta[t], pf, acc[t]));
      SB();
#pragma unroll
      for (int t = 2; t < 4; ++t) acc[t] = mfma(t2[t], pf, mfma(ta[t], pf, acc[t]));
      const float L = tac[2] + tac[3], rl = __builtin_amdgcn_rcpf(L), ct = -(tac[0] + tac[1]) * rl;
      const float lse = M + __builtin_amdgcn_logf(L);
      u32x4 b[6];
      b[0] = pack8(acc[0] * rl + ct, acc[1] * rl + ct);
      *(LAS u32x4*)xw = b[0];
      *(LAS float*)xl = lse;
#pragma unroll
      for (int j = 0; j < 4; ++j) { __builtin_amdgcn_sched_group_barrier(0x008, 1, 0); __builtin_amdgcn_sched_group_barrier(0x002, 3, 0); }
      SB();
      b[1] = pack8(acc[2] * rl + ct, acc[3] * rl + ct);
      *(LAS u32x4*)(xw + 1024) = b[1];
      lds_inc(CT_XD, ctl);
      SB();
      f32x4 o[3];
      unsigned xd;
#pragma unroll
      for (int t = 0; t < 3; ++t) o[t] = mfma(wvv[6 * t], b[0], z);
      asm volatile("" : "+v"(o[0]), "+v"(o[1]), "+v"(o[2]));
      SB();
#define RD_SLICES "ds_read_b32 %0, %7 offset:%10\n\tds_read_b128 %1, %8\n\tds_read_b128 %2, %8 offset:1024\n\tds_read_b128 %3, %9\n\tds_read_b128 %4, %9 offset:1024\n"
      asm volatile(RD_SLICES : "=&v"(xd), "=&v"(b[2]), "=&v"(b[3]), "=&v"(b[4]), "=&v"(b[5]) : "i"(0), "i"(0), "v"(ctl), "v"(xr1), "v"(xr2), "i"(CT_XD) : "memory");
      SB();
#pragma unroll
      for (int t = 0; t < 3; ++t) o[t] = mfma(wvv[6 * t + 1], b[1], o[t]);
      tw |= (__float_as_uint(lse) << sh) & 0xfff00000u;
      asm volatile("" : "+v"(tw), "+v"(o[0]), "+v"(o[1]), "+v"(o[2]));
      SB();
      {
        int cnt_, c_;
        asm volatile("s_mov_b32 %6, 0x400000\n\ts_branch 2f\n1:\n\t" RD_SLICES "2:\n\ts_waitcnt lgkmcnt(4)\n\tv_readfirstlane_b32 %5, %0\n\t"
                     "s_cmp_ge_i32 %5, 3\n\ts_cbranch_scc1 3f\n\ts_sub_u32 %6, %6, 1\n\ts_cbranch_scc0 1b\n3:\n\ts_waitcnt lgkmcnt(0)"
                     : "+v"(xd), "+v"(b[2]), "+v"(b[3]), "+v"(b[4]), "+v"(b[5]), "=&s"(c_), "=&s"(cnt_)
                     : "v"(ctl), "v"(xr1), "v"(xr2), "i"(CT_XD) : "memory", "scc");
      }
      SB();
      PROJECT(2);
      lds_inc(CT_ST, ctl);
    }
  }
}

#undef rws
#define WS_AT(size_) ((size_) > (64u << 20) ? ((size_) - (64u << 20)) & ~(size_t)0x1fffff : 0)
extern "C" void kernel_launch(void* const* d_in, const int* in_sizes, int n_in, void* d_out, int out_size, void* d_ws, size_t ws_size,
                              hipStream_t stream) {
  dino_main<<<NWG, 512, 0, stream>>>((const u16*)d_in[0], (const u16*)d_in[1], (const u16*)d_in[2], (const u16*)d_in[3], (const u16*)d_in[4],
                                     (const u16*)d_in[5], (const u16*)d_in[6], (char*)d_ws + WS_AT(ws_size) + 4096, (u16*)d_out);
}
```
